```python
import torch

def _softmax(x, axis=-1):
    return torch.softmax(x, dim=axis)
def _gelu(x):
    return torch.nn.functional.gelu(x, approximate="tanh")
def _silu(x):
    return torch.nn.functional.silu(x)
def _sigmoid(x):
    return torch.sigmoid(x)
def _layernorm(x, axis=-1, eps=1e-5):
    m = x.mean(dim=axis, keepdim=True)
    v = x.var(dim=axis, keepdim=True, unbiased=False)
    return (x - m) / torch.sqrt(v + eps)
def _scatter_add(tgt, idx, src, axis=0):
    return tgt.clone().index_add_(axis, idx, src)
def _segment_sum(src, seg, n):
    out = torch.zeros((n,) + src.shape[1:], dtype=src.dtype)
    return out.index_add_(0, seg, src)
def _pad(x, pw):
    flat = tuple(v for pair in reversed(pw) for v in pair)
    return torch.nn.functional.pad(x, flat)

kernel_name = 'parallel_sum__compressed_kv_cla__mixer_channel_mlp__prompt'

def setup_inputs():

    _w = (lambda a: (lambda t: t.to(torch.bfloat16) if t.is_floating_point() else t)(torch.as_tensor(a)))
    def _A_setup():
        B, H, Sq, Sk, D, M = 1, 32, 2048, 2048, 128, 640
        Dc = 4 * D
        return {
            "x": _w((lambda *s: torch.randn(*s, dtype=torch.bfloat16) * 0.05)(B, Sq, M)),
            "c_kv": _w((lambda *s: torch.randn(*s, dtype=torch.bfloat16) * 0.4)(B, Sk, Dc)),
            "w_q": _w((lambda *s: torch.randn(*s, dtype=torch.bfloat16) * 3.2)(M, H, D) * (M ** -0.5)),
            "w_uk": _w((lambda *s: torch.randn(*s, dtype=torch.bfloat16) * 3.2)(Dc, H, D) * (Dc ** -0.5)),
            "w_uv": _w((lambda *s: torch.randn(*s, dtype=torch.bfloat16) * 0.4)(Dc, H, D) * (Dc ** -0.5)),
            "w_o": _w((lambda *s: torch.randn(*s, dtype=torch.bfloat16) * 0.4)(H, D, M) * ((H * D) ** -0.5)),
        }
    _A_in = _A_setup()

    def _B_setup():
        B, S, D, F = 1, 2048, 640, 1728
        return {
            "x": _w((lambda *s: torch.randn(*s, dtype=torch.bfloat16) * 0.05)(B, S, D)),
            "w1": _w((lambda *s: torch.randn(*s, dtype=torch.bfloat16) * 0.8)(D, F) * (D ** -0.5)),
            "b1": _w((lambda *s: torch.randn(*s, dtype=torch.bfloat16) * 0.4)(F)),
            "w2": _w((lambda *s: torch.randn(*s, dtype=torch.bfloat16) * 0.05)(F, D) * (F ** -0.5)),
            "b2": _w((lambda *s: torch.randn(*s, dtype=torch.bfloat16) * 0.05)(D)),
        }
    _B_in = _B_setup()

    return {
        "A_x": _A_in["x"],
        "A_c_kv": _A_in["c_kv"],
        "A_w_q": _A_in["w_q"],
        "A_w_uk": _A_in["w_uk"],
        "A_w_uv": _A_in["w_uv"],
        "A_w_o": _A_in["w_o"],
        "B_x": _B_in["x"],
        "B_w1": _B_in["w1"],
        "B_b1": _B_in["b1"],
        "B_w2": _B_in["w2"],
        "B_b2": _B_in["b2"],
    }

def reference(A_x, A_c_kv, A_w_q, A_w_uk, A_w_uv, A_w_o, B_x, B_w1, B_b1, B_w2, B_b2):
    def _A_ref(x, c_kv, w_q, w_uk, w_uv, w_o):
        B, Sq, M = x.shape[0], x.shape[1], x.shape[2]
        Sk = c_kv.shape[1]
        H, D = w_q.shape[1], w_q.shape[2]
        q = torch.einsum("bqm,mhd->bhqd", x, w_q)
        k = torch.einsum("bkc,chd->bhkd", c_kv, w_uk)
        v = torch.einsum("bkc,chd->bhkd", c_kv, w_uv)
        scores = torch.einsum("bhqd,bhkd->bhqk", q, k) * (D ** -0.5)
        off = Sk - Sq
        qi = torch.arange(Sq)[:, None]
        kj = torch.arange(Sk)[None, :]
        cmask = kj <= (qi + off)
        scores = torch.where(cmask[None, None], scores, -1e9)
        p = _softmax(scores, axis=-1)
        ctx = torch.einsum("bhqk,bhkd->bhqd", p, v)
        return torch.einsum("bhqd,hdm->bqm", ctx, w_o)

    def _B_ref(x, w1, b1, w2, b2):
        y = _layernorm(x, axis=-1)
        h = _gelu(torch.einsum("bsd,df->bsf", y, w1) + b1)
        return torch.einsum("bsf,fd->bsd", h, w2) + b2

    x = A_x
    xn = _layernorm(x, axis=-1)
    return x + _A_ref(xn, A_c_kv, A_w_q, A_w_uk, A_w_uv, A_w_o) + _B_ref(xn, B_w1, B_b1, B_w2, B_b2)
```

```cpp
#include <hip/hip_runtime.h>
typedef unsigned short u16;
typedef unsigned u32x4 __attribute__((ext_vector_type(4)));
typedef unsigned u32x2 __attribute__((ext_vector_type(2)));
struct Args { const void* x; const void* ckv; const void* wq; const void* wuk; const void* wuv; const void* wo; const void* w1; const void* b1; const void* w2; const void* b2; void* out; void* ws; };
constexpr unsigned WS_XN=0, WS_ST=2621440, WS_K=2637824, WS_V=19415040, WS_Z=36192256, F_LN=67911680, WS_END=67920128;
__device__ __forceinline__ float bflo(unsigned u){ return __uint_as_float(u<<16); }
__device__ __forceinline__ float bfhi(unsigned u){ return __uint_as_float(u&0xffff0000u); }
__device__ __forceinline__ unsigned cvtpk(float lo,float hi){ unsigned r; asm("v_cvt_pk_bf16_f32 %0, %1, %2":"=v"(r):"v"(lo),"v"(hi)); return r; }
__device__ __forceinline__ float hsum32(float s){ s+=__shfl_xor(s,16); s+=__shfl_xor(s,8); s+=__shfl_xor(s,4); s+=__shfl_xor(s,2); s+=__shfl_xor(s,1); return s; }

__device__ __forceinline__ void slow_attention(const void* ka_, int bid_, int wid_){
  const Args* ap=(const Args*)ka_; asm volatile("" : "+s"(ap)); asm volatile("" : "+s"(bid_)); asm volatile("" : "+s"(wid_));
  int lane_; asm volatile("v_mbcnt_lo_u32_b32 %0, -1, 0\n\tv_mbcnt_hi_u32_b32 %0, -1, %0" : "=v"(lane_));
  const int h=(bid_&7)*4+((bid_>>3)&3), j=bid_>>5;
  struct { void* ws; const void* wq; } a; a.ws=ap->ws; a.wq=ap->wq;
  const char* ws=(const char*)a.ws; const u16* wq=(const u16*)a.wq;
  const int t=wid_*64+lane_; const int qb=(t<128)?(15-j):j; const int row=qb*128+(t&127);
  const int xc=row&15, xpi=((((xc>>2)^(xc>>3))&1)<<1)|(xc>>3);
  const char* xn=ws+WS_XN+(size_t)qb*20*8192+((row>>4)&7)*1024+xc*64;
  const char* kb=ws+WS_K+(size_t)h*524288; const char* vb=ws+WS_V+(size_t)h*524288;
  unsigned q[64];
  #pragma unroll
  for(int dc=0;dc<8;++dc){ float acc[16];
    #pragma unroll
    for(int e=0;e<16;++e) acc[e]=0.f;
    for(int m=0;m<640;++m){ float xv=__uint_as_float((unsigned)*(const u16*)(xn+(m>>5)*8192+((((m>>3)&3)^xpi)<<4)+(m&7)*2)<<16);
      const unsigned* w=(const unsigned*)(wq+(size_t)m*4096+h*128+dc*16);
      #pragma unroll
      for(int e=0;e<8;++e){ unsigned u=w[e]; acc[2*e]+=xv*bflo(u); acc[2*e+1]+=xv*bfhi(u);} }
    #pragma unroll
    for(int e=0;e<8;++e) q[dc*8+e]=cvtpk(acc[2*e]*0.12751743082f,acc[2*e+1]*0.12751743082f); }
  float mx=-3.0e38f, l=0.f;
  for(int pass=0;pass<17;++pass){
    float o[8];
    #pragma unroll
    for(int e=0;e<8;++e) o[e]=0.f;
    for(int key=0;key<=row;++key){
      const int k5=key&31, r=(k5&7)|((k5&8)<<1)|((k5&16)>>1); const char* kp=kb+(key>>5)*8192+r*16; float s=0.f;
      #pragma unroll
      for(int d4=0;d4<32;++d4){ const int g=d4&3, sidx=2*(d4>>3)+(g>>1); const u32x2 kk=*(const u32x2*)(kp+sidx*1024+(g&1)*512+((d4>>2)&1)*8);
        s+=bflo(q[2*d4])*bflo(kk[0])+bfhi(q[2*d4])*bfhi(kk[0])+bflo(q[2*d4+1])*bflo(kk[1])+bfhi(q[2*d4+1])*bfhi(kk[1]); }
      if(pass==0){ if(s>mx){ l=l*exp2f(mx-s)+1.f; mx=s; } else l+=exp2f(s-mx); }
      else { const float p=exp2f(s-mx); const int d0=(pass-1)*8; const int jj=4*(k5>>4)+(k5&3), g=(k5>>2)&3;
        const char* vp=vb+(key>>5)*8192+(g>>1)*4096+(d0>>5)*1024+((d0&31)+32*(g&1))*16+jj*2;
        #pragma unroll
        for(int e=0;e<8;++e) o[e]+=p*__uint_as_float((unsigned)*(const u16*)(vp+e*16)<<16); } }
    if(pass>0){ const int d0=(pass-1)*8; const float il=1.f/l; char* z=(char*)a.ws+WS_Z+((size_t)(qb*182+4*h+(d0>>5))*8+((row>>4)&7))*1024;
      #pragma unroll
      for(int e=0;e<8;++e){ const int d=(d0&31)+e; const int g=(d>>2)&3, jj=4*(d>>4)+(d&3); *(u16*)(z+(g*16+(row&15))*16+jj*2)=(u16)(cvtpk(o[e]*il,0.f)&0xffffu); } }
  }
  __threadfence(); __syncthreads();
}

__global__ __launch_bounds__(256,1) void fused(Args a){
  extern __shared__ __attribute__((aligned(1024))) char lds[];
  const int bid=blockIdx.x; const int wid=__builtin_amdgcn_readfirstlane(threadIdx.x>>6); const int lane=threadIdx.x&63;
  const void* ka=(const void*)__builtin_amdgcn_kernarg_segment_ptr();
  unsigned tag=0, mode=0, again;

  do {
  asm volatile(R"ASM(
fz_code_begin:
s_cmp_lg_u32 %[mode], 0
s_cbranch_scc1 L_resume%=
s_mov_b64 s[36:37], %2
s_mov_b32 s38, %3
s_mov_b32 s43, %4
s_and_b32 s44, s43, 7
s_lshl_b32 s44, s44, 2
s_bfe_u32 s45, s43, 0x20003
s_or_b32 s40, s44, s45
s_lshr_b32 s41, s43, 5
s_load_dwordx2 s[48:49], s[36:37], 88
v_mbcnt_lo_u32_b32 v8, -1, 0
v_mbcnt_hi_u32_b32 v8, -1, v8
s_load_dwordx2 s[68:69], s[36:37], 16
s_load_dwordx2 s[72:73], s[36:37], 48
s_load_dwordx2 s[64:65], s[36:37], 0
s_load_dwordx2 s[52:53], s[36:37], 8
s_load_dwordx2 s[56:57], s[36:37], 24
s_load_dwordx2 s[60:61], s[36:37], 32
v_lshrrev_b32 v10, 4, v8
v_lshrrev_b32 v9, 3, v8
v_lshlrev_b32 v9, 10, v9
v_and_b32 v11, 7, v8
v_xor_b32 v11, v11, v10
v_lshl_add_u32 v21, v11, 4, v9
v_xor_b32 v22, 64, v21
v_add_u32 v22, 0x2000, v22
v_add_u32 v23, 0x4000, v21
v_add_u32 v24, 0x4000, v22
v_bfe_u32 v9, v8, 1, 3
s_and_b32 s44, s38, 1
s_lshl_b32 s44, s44, 2
v_xor_b32 v9, s44, v9
v_lshlrev_b32 v9, 13, v9
v_lshl_add_u32 v9, v10, 5, v9
v_and_b32 v11, 1, v8
v_lshl_add_u32 v25, v11, 4, v9
s_waitcnt lgkmcnt(0)
s_lshl_b32 s44, s41, 18
s_add_u32 s52, s52, s44
s_addc_u32 s53, s53, 0
s_and_b32 s53, s53, 0xffff
s_mov_b32 s55, 0x20000
s_sub_u32 s54, 2097152, s44
s_lshl_b32 s44, s40, 8
s_add_u32 s56, s56, s44
s_addc_u32 s57, s57, 0
s_and_b32 s57, s57, 0xffff
s_mov_b32 s59, 0x20000
s_add_u32 s60, s60, s44
s_addc_u32 s61, s61, 0
s_and_b32 s61, s61, 0xffff
s_mov_b32 s63, 0x20000
s_sub_u32 s58, 4194304, s44
s_mov_b32 s62, s58
s_and_b32 s49, s49, 0xffff
s_mov_b32 s51, 0x20000
s_mov_b32 s50, 67920128
s_lshl_b32 s44, s38, 13
s_add_u32 s79, s44, 65536
s_mov_b32 s83, 0
s_lshl_b32 s44, s38, 16
s_add_u32 s80, s44, 0
s_lshl_b32 s44, s38, 12
s_add_u32 s81, s44, 0
s_lshl_b32 s44, s38, 16
s_add_u32 s82, s44, 0
s_mov_b32 m0, s79
s_nop 0
buffer_load_dwordx4 v21, s[52:55], s80 offen lds
s_add_u32 m0, s79, 1024
s_nop 0
buffer_load_dwordx4 v22, s[52:55], s80 offen lds
s_add_u32 m0, s79, 2048
s_nop 0
buffer_load_dwordx4 v23, s[52:55], s80 offen lds
s_add_u32 m0, s79, 3072
s_nop 0
buffer_load_dwordx4 v24, s[52:55], s80 offen lds
s_add_u32 s79, s79, 4096
s_add_u32 s80, s80, 32768
s_mov_b32 m0, s81
s_nop 0
buffer_load_dwordx4 v25, s[56:59], s82 offen lds
s_add_u32 m0, s81, 896
s_nop 0
buffer_load_dwordx4 v25, s[56:59], s82 offen offset:128 lds
s_add_u32 m0, s81, 2048
s_nop 0
buffer_load_dwordx4 v25, s[60:63], s82 offen lds
s_add_u32 m0, s81, 2944
s_nop 0
buffer_load_dwordx4 v25, s[60:63], s82 offen offset:128 lds
s_add_u32 s81, s81, 16384
s_and_b32 s81, s81, 0xffff
s_add_u32 s82, s82, 262144
s_mov_b32 m0, s79
s_nop 0
buffer_load_dwordx4 v21, s[52:55], s80 offen lds
s_add_u32 m0, s79, 1024
s_nop 0
buffer_load_dwordx4 v22, s[52:55], s80 offen lds
s_add_u32 m0, s79, 2048
s_nop 0
buffer_load_dwordx4 v23, s[52:55], s80 offen lds
s_add_u32 m0, s79, 3072
s_nop 0
buffer_load_dwordx4 v24, s[52:55], s80 offen lds
s_add_u32 s83, s83, 32768
s_cmp_eq_u32 s83, 98304
s_cselect_b32 s83, 0, s83
s_lshl_b32 s45, s38, 13
s_add_u32 s45, s45, s83
s_add_u32 s79, s45, 65536
s_sub_u32 s80, s80, 32640
s_mov_b32 m0, s81
s_nop 0
buffer_load_dwordx4 v25, s[56:59], s82 offen lds
s_add_u32 m0, s81, 896
s_nop 0
buffer_load_dwordx4 v25, s[56:59], s82 offen offset:128 lds
s_add_u32 m0, s81, 2048
s_nop 0
buffer_load_dwordx4 v25, s[60:63], s82 offen lds
s_add_u32 m0, s81, 2944
s_nop 0
buffer_load_dwordx4 v25, s[60:63], s82 offen offset:128 lds
s_add_u32 s81, s81, 16384
s_and_b32 s81, s81, 0xffff
s_add_u32 s82, s82, 262144
s_mov_b32 m0, s79
s_nop 0
buffer_load_dwordx4 v21, s[52:55], s80 offen lds
s_add_u32 m0, s79, 1024
s_nop 0
buffer_load_dwordx4 v22, s[52:55], s80 offen lds
s_add_u32 m0, s79, 2048
s_nop 0
buffer_load_dwordx4 v23, s[52:55], s80 offen lds
s_add_u32 m0, s79, 3072
s_nop 0
buffer_load_dwordx4 v24, s[52:55], s80 offen lds
s_add_u32 s79, s79, 4096
s_add_u32 s80, s80, 32768
s_mov_b32 m0, s81
s_nop 0
buffer_load_dwordx4 v25, s[56:59], s82 offen lds
s_add_u32 m0, s81, 896
s_nop 0
buffer_load_dwordx4 v25, s[56:59], s82 offen offset:128 lds
s_add_u32 m0, s81, 2048
s_nop 0
buffer_load_dwordx4 v25, s[60:63], s82 offen lds
s_add_u32 m0, s81, 2944
s_nop 0
buffer_load_dwordx4 v25, s[60:63], s82 offen offset:128 lds
s_add_u32 s81, s81, 16384
s_and_b32 s81, s81, 0xffff
s_add_u32 s82, s82, 262144
s_mov_b32 m0, s79
s_nop 0
buffer_load_dwordx4 v21, s[52:55], s80 offen lds
s_add_u32 m0, s79, 1024
s_nop 0
buffer_load_dwordx4 v22, s[52:55], s80 offen lds
s_add_u32 m0, s79, 2048
s_nop 0
buffer_load_dwordx4 v23, s[52:55], s80 offen lds
s_add_u32 m0, s79, 3072
s_nop 0
buffer_load_dwordx4 v24, s[52:55], s80 offen lds
s_add_u32 s83, s83, 32768
s_cmp_eq_u32 s83, 98304
s_cselect_b32 s83, 0, s83
s_lshl_b32 s45, s38, 13
s_add_u32 s45, s45, s83
s_add_u32 s79, s45, 65536
s_sub_u32 s80, s80, 32640
s_mov_b32 s44, 67919872
v_mov_b32 v9, 0
buffer_load_dword v57, v9, s[48:51], s44 offen sc0 sc1
s_lshl_b32 s44, s43, 3
s_lshl_b32 s45, s38, 1
s_add_u32 s44, s44, s45
v_lshrrev_b32 v51, 5, v8
v_add_u32 v51, s44, v51
v_and_b32 v54, 31, v8
v_mul_u32_u24 v28, 0x500, v51
v_lshl_add_u32 v28, v54, 4, v28
v_cmp_gt_u32 vcc, 16, v54
v_add_u32 v29, 0x400, v28
v_mov_b32 v55, 0x7ffffff0
v_cndmask_b32 v29, v55, v29, vcc
v_cndmask_b32_e64 v30, 0, 1.0, vcc
v_lshrrev_b32 v48, 7, v51
s_mov_b32 s45, 163840
v_mul_lo_u32 v48, s45, v48
v_bfe_u32 v56, v51, 4, 3
v_lshl_add_u32 v48, v56, 10, v48
v_and_b32 v56, 15, v51
v_lshl_add_u32 v48, v56, 6, v48
v_lshrrev_b32 v53, 3, v56
v_lshrrev_b32 v52, 2, v56
v_xor_b32 v52, v52, v53
v_and_b32 v52, 1, v52
v_lshl_add_u32 v52, v52, 1, v53
v_and_b32 v56, 3, v54
v_xor_b32 v56, v56, v52
v_lshl_add_u32 v48, v56, 4, v48
v_lshrrev_b32 v56, 2, v54
v_lshl_add_u32 v48, v56, 13, v48
v_add_u32 v49, 0x20000, v48
v_cndmask_b32 v49, v55, v49, vcc
v_lshlrev_b32 v50, 3, v51
v_add_u32 v50, 2621440, v50
v_cmp_eq_u32 vcc, 0, v54
s_nop 1
v_cndmask_b32 v50, v55, v50, vcc
s_and_b32 s65, s65, 0xffff
s_mov_b32 s66, 2621440
s_mov_b32 s67, 0x20000
buffer_load_dwordx4 v[192:195], v28, s[64:67], 0 offen
buffer_load_dwordx4 v[196:199], v28, s[64:67], 0 offen offset:512
buffer_load_dwordx4 v[200:203], v29, s[64:67], 0 offen
s_getpc_b64 s[64:65]
fz_code_pc:
s_sub_u32 s64, s64, fz_code_pc-fz_code_begin
s_subb_u32 s65, s65, 0
s_and_b32 s64, s64, 0xffffff80
s_and_b32 s65, s65, 0xffff
s_mov_b32 s67, 0x20000
s_lshr_b32 s44, s43, 3
s_lshl_b32 s44, s44, 2
s_add_u32 s44, s44, s38
s_lshl_b32 s44, s44, 10
s_add_u32 s45, s44, 1024
s_min_u32 s66, s45, fz_code_end-fz_code_begin+128
v_lshlrev_b32 v58, 7, v8
buffer_load_dword v59, v58, s[64:67], s44 offen
v_and_b32 v9, 15, v8
v_lshrrev_b32 v10, 4, v8
v_lshrrev_b32 v11, 1, v9
v_xor_b32 v11, v11, v10
v_lshlrev_b32 v11, 4, v11
v_lshl_add_u32 v13, v9, 7, v11
s_lshr_b32 s44, s38, 1
s_lshl_b32 s44, s44, 14
s_add_u32 s44, s44, 65536
v_add_u32 v13, s44, v13
v_xor_b32 v14, 64, v13
v_bfe_u32 v9, v8, 2, 2
v_and_b32 v11, 1, v10
v_lshl_add_u32 v9, v11, 2, v9
v_lshlrev_b32 v9, 5, v9
v_lshl_add_u32 v9, v10, 12, v9
v_and_b32 v11, 3, v8
v_lshl_add_u32 v17, v11, 3, v9
s_and_b32 s44, s38, 1
s_lshl_b32 s44, s44, 10
v_add_u32 v17, s44, v17
v_xor_b32 v18, 128, v17
s_lshl_b32 s44, s38, 13
s_add_u32 s79, s44, 131072
s_mov_b32 s83, 65536
s_lshl_b32 s44, s38, 16
s_add_u32 s80, s44, 256
s_lshl_b32 s44, s38, 12
s_add_u32 s81, s44, 49152
s_lshl_b32 s44, s38, 16
s_add_u32 s82, s44, 786432
s_waitcnt vmcnt(21)
s_barrier
v_mov_b32 v15, v13
v_mov_b32 v16, v14
v_mov_b32 v19, v17
v_mov_b32 v20, v18
s_mov_b32 m0, s79
s_nop 0
buffer_load_dwordx4 v21, s[52:55], s80 offen lds
s_add_u32 m0, s79, 1024
s_nop 0
buffer_load_dwordx4 v22, s[52:55], s80 offen lds
s_add_u32 m0, s79, 2048
s_nop 0
buffer_load_dwordx4 v23, s[52:55], s80 offen lds
s_add_u32 m0, s79, 3072
s_nop 0
buffer_load_dwordx4 v24, s[52:55], s80 offen lds
s_add_u32 s79, s79, 4096
s_add_u32 s80, s80, 32768
s_mov_b32 m0, s81
s_nop 0
buffer_load_dwordx4 v25, s[56:59], s82 offen lds
s_add_u32 m0, s81, 896
s_nop 0
buffer_load_dwordx4 v25, s[56:59], s82 offen offset:128 lds
s_add_u32 m0, s81, 2048
s_nop 0
buffer_load_dwordx4 v25, s[60:63], s82 offen lds
s_add_u32 m0, s81, 2944
s_nop 0
buffer_load_dwordx4 v25, s[60:63], s82 offen offset:128 lds
s_add_u32 s81, s81, 16384
s_and_b32 s81, s81, 0xffff
s_add_u32 s82, s82, 262144
ds_read_b128 v[64:67], v15 offset:0
ds_read_b64_tr_b16 v[128:129], v19 offset:0
ds_read_b64_tr_b16 v[130:131], v20 offset:0
ds_read_b64_tr_b16 v[132:133], v19 offset:256
ds_read_b64_tr_b16 v[134:135], v20 offset:256
ds_read_b64_tr_b16 v[136:137], v19 offset:512
ds_read_b64_tr_b16 v[138:139], v20 offset:512
ds_read_b64_tr_b16 v[140:141], v19 offset:768
ds_read_b64_tr_b16 v[142:143], v20 offset:768
ds_read_b64_tr_b16 v[144:145], v19 offset:2048
ds_read_b64_tr_b16 v[146:147], v20 offset:2048
ds_read_b64_tr_b16 v[148:149], v19 offset:2304
ds_read_b64_tr_b16 v[150:151], v20 offset:2304
ds_read_b64_tr_b16 v[152:153], v19 offset:2560
ds_read_b64_tr_b16 v[154:155], v20 offset:2560
ds_read_b64_tr_b16 v[156:157], v19 offset:2816
ds_read_b64_tr_b16 v[158:159], v20 offset:2816
ds_read_b128 v[68:71], v15 offset:2048
ds_read_b128 v[72:75], v15 offset:4096
ds_read_b128 v[76:79], v15 offset:6144
ds_read_b128 v[80:83], v15 offset:8192
ds_read_b128 v[84:87], v15 offset:10240
ds_read_b128 v[88:91], v15 offset:12288
ds_read_b128 v[92:95], v15 offset:14336
s_mov_b32 s77, 0
s_mov_b32 s78, 0
s_waitcnt vmcnt(25) lgkmcnt(0)
s_barrier
v_mfma_f32_16x16x32_bf16 a[0:3], v[128:131], v[64:67], 0
ds_read_b128 v[96:99], v16 offset:0
v_mfma_f32_16x16x32_bf16 a[4:7], v[132:135], v[64:67], 0
v_mfma_f32_16x16x32_bf16 a[8:11], v[136:139], v[64:67], 0
ds_read_b64_tr_b16 v[160:161], v19 offset:16384
s_mov_b32 m0, s79
v_mfma_f32_16x16x32_bf16 a[12:15], v[140:143], v[64:67], 0
v_mfma_f32_16x16x32_bf16 a[16:19], v[64:67], v[144:147], 0
buffer_load_dwordx4 v21, s[52:55], s80 offen lds
ds_read_b64_tr_b16 v[162:163], v20 offset:16384
v_mfma_f32_16x16x32_bf16 a[20:23], v[64:67], v[148:151], 0
v_mfma_f32_16x16x32_bf16 a[24:27], v[64:67], v[152:155], 0
s_add_u32 m0, s79, 1024
v_mfma_f32_16x16x32_bf16 a[28:31], v[64:67], v[156:159], 0
ds_read_b64_tr_b16 v[164:165], v19 offset:16640
v_mfma_f32_16x16x32_bf16 a[32:35], v[128:131], v[68:71], 0
buffer_load_dwordx4 v22, s[52:55], s80 offen lds
v_mfma_f32_16x16x32_bf16 a[36:39], v[132:135], v[68:71], 0
ds_read_b64_tr_b16 v[166:167], v20 offset:16640
v_mfma_f32_16x16x32_bf16 a[40:43], v[136:139], v[68:71], 0
v_mfma_f32_16x16x32_bf16 a[44:47], v[140:143], v[68:71], 0
s_add_u32 m0, s79, 2048
v_mfma_f32_16x16x32_bf16 a[48:51], v[68:71], v[144:147], 0
ds_read_b64_tr_b16 v[168:169], v19 offset:16896
v_mfma_f32_16x16x32_bf16 a[52:55], v[68:71], v[148:151], 0
buffer_load_dwordx4 v23, s[52:55], s80 offen lds
v_mfma_f32_16x16x32_bf16 a[56:59], v[68:71], v[152:155], 0
ds_read_b64_tr_b16 v[170:171], v20 offset:16896
v_mfma_f32_16x16x32_bf16 a[60:63], v[68:71], v[156:159], 0
s_add_u32 m0, s79, 3072
v_mfma_f32_16x16x32_bf16 a[64:67], v[128:131], v[72:75], 0
ds_read_b64_tr_b16 v[172:173], v19 offset:17152
v_mfma_f32_16x16x32_bf16 a[68:71], v[132:135], v[72:75], 0
v_mfma_f32_16x16x32_bf16 a[72:75], v[136:139], v[72:75], 0
buffer_load_dwordx4 v24, s[52:55], s80 offen lds
v_mfma_f32_16x16x32_bf16 a[76:79], v[140:143], v[72:75], 0
ds_read_b64_tr_b16 v[174:175], v20 offset:17152
v_mfma_f32_16x16x32_bf16 a[80:83], v[72:75], v[144:147], 0
s_add_u32 s83, s83, 32768
v_mfma_f32_16x16x32_bf16 a[84:87], v[72:75], v[148:151], 0
ds_read_b64_tr_b16 v[176:177], v19 offset:18432
v_mfma_f32_16x16x32_bf16 a[88:91], v[72:75], v[152:155], 0
s_cmp_eq_u32 s83, 98304
v_mfma_f32_16x16x32_bf16 a[92:95], v[72:75], v[156:159], 0
v_mfma_f32_16x16x32_bf16 a[96:99], v[128:131], v[76:79], 0
ds_read_b64_tr_b16 v[178:179], v20 offset:18432
v_mfma_f32_16x16x32_bf16 a[100:103], v[132:135], v[76:79], 0
s_cselect_b32 s83, 0, s83
v_mfma_f32_16x16x32_bf16 a[104:107], v[136:139], v[76:79], 0
ds_read_b64_tr_b16 v[180:181], v19 offset:18688
v_mfma_f32_16x16x32_bf16 a[108:111], v[140:143], v[76:79], 0
s_lshl_b32 s45, s38, 13
v_mfma_f32_16x16x32_bf16 a[112:115], v[76:79], v[144:147], 0
v_mfma_f32_16x16x32_bf16 a[116:119], v[76:79], v[148:151], 0
ds_read_b64_tr_b16 v[182:183], v20 offset:18688
s_add_u32 s45, s45, s83
v_mfma_f32_16x16x32_bf16 a[120:123], v[76:79], v[152:155], 0
v_mfma_f32_16x16x32_bf16 a[124:127], v[76:79], v[156:159], 0
ds_read_b64_tr_b16 v[184:185], v19 offset:18944
v_mfma_f32_16x16x32_bf16 a[128:131], v[128:131], v[80:83], 0
s_add_u32 s79, s45, 65536
v_mfma_f32_16x16x32_bf16 a[132:135], v[132:135], v[80:83], 0
ds_read_b64_tr_b16 v[186:187], v20 offset:18944
v_mfma_f32_16x16x32_bf16 a[136:139], v[136:139], v[80:83], 0
s_sub_u32 s80, s80, 32640
v_mfma_f32_16x16x32_bf16 a[140:143], v[140:143], v[80:83], 0
v_mfma_f32_16x16x32_bf16 a[144:147], v[80:83], v[144:147], 0
ds_read_b64_tr_b16 v[188:189], v19 offset:19200
s_mov_b32 m0, s81
v_mfma_f32_16x16x32_bf16 a[148:151], v[80:83], v[148:151], 0
v_mfma_f32_16x16x32_bf16 a[152:155], v[80:83], v[152:155], 0
buffer_load_dwordx4 v25, s[56:59], s82 offen lds
ds_read_b64_tr_b16 v[190:191], v20 offset:19200
v_mfma_f32_16x16x32_bf16 a[156:159], v[80:83], v[156:159], 0
v_mfma_f32_16x16x32_bf16 a[160:163], v[128:131], v[84:87], 0
v_mfma_f32_16x16x32_bf16 a[164:167], v[132:135], v[84:87], 0
ds_read_b128 v[100:103], v16 offset:2048
s_add_u32 m0, s81, 896
v_mfma_f32_16x16x32_bf16 a[168:171], v[136:139], v[84:87], 0
v_mfma_f32_16x16x32_bf16 a[172:175], v[140:143], v[84:87], 0
buffer_load_dwordx4 v25, s[56:59], s82 offen offset:128 lds
ds_read_b128 v[104:107], v16 offset:4096
v_mfma_f32_16x16x32_bf16 a[176:179], v[84:87], v[144:147], 0
v_mfma_f32_16x16x32_bf16 a[180:183], v[84:87], v[148:151], 0
ds_read_b128 v[108:111], v16 offset:6144
s_add_u32 m0, s81, 2048
v_mfma_f32_16x16x32_bf16 a[184:187], v[84:87], v[152:155], 0
v_mfma_f32_16x16x32_bf16 a[188:191], v[84:87], v[156:159], 0
v_mfma_f32_16x16x32_bf16 a[192:195], v[128:131], v[88:91], 0
buffer_load_dwordx4 v25, s[60:63], s82 offen lds
ds_read_b128 v[112:115], v16 offset:8192
v_mfma_f32_16x16x32_bf16 a[196:199], v[132:135], v[88:91], 0
v_mfma_f32_16x16x32_bf16 a[200:203], v[136:139], v[88:91], 0
ds_read_b128 v[116:119], v16 offset:10240
s_add_u32 m0, s81, 2944
v_mfma_f32_16x16x32_bf16 a[204:207], v[140:143], v[88:91], 0
v_mfma_f32_16x16x32_bf16 a[208:211], v[88:91], v[144:147], 0
buffer_load_dwordx4 v25, s[60:63], s82 offen offset:128 lds
v_mfma_f32_16x16x32_bf16 a[212:215], v[88:91], v[148:151], 0
ds_read_b128 v[120:123], v16 offset:12288
v_mfma_f32_16x16x32_bf16 a[216:219], v[88:91], v[152:155], 0
v_mfma_f32_16x16x32_bf16 a[220:223], v[88:91], v[156:159], 0
ds_read_b128 v[124:127], v16 offset:14336
s_add_u32 s81, s81, 16384
v_mfma_f32_16x16x32_bf16 a[224:227], v[128:131], v[92:95], 0
v_mfma_f32_16x16x32_bf16 a[228:231], v[132:135], v[92:95], 0
s_and_b32 s81, s81, 0xffff
v_mfma_f32_16x16x32_bf16 a[232:235], v[136:139], v[92:95], 0
v_mfma_f32_16x16x32_bf16 a[236:239], v[140:143], v[92:95], 0
s_add_u32 s82, s82, 262144
v_mfma_f32_16x16x32_bf16 a[240:243], v[92:95], v[144:147], 0
v_mfma_f32_16x16x32_bf16 a[244:247], v[92:95], v[148:151], 0
v_mfma_f32_16x16x32_bf16 a[248:251], v[92:95], v[152:155], 0
v_mfma_f32_16x16x32_bf16 a[252:255], v[92:95], v[156:159], 0
s_waitcnt vmcnt(17) lgkmcnt(0)
s_barrier
v_mfma_f32_16x16x32_bf16 a[0:3], v[160:163], v[96:99], a[0:3]
s_add_u32 s77, s77, 32768
v_mfma_f32_16x16x32_bf16 a[4:7], v[164:167], v[96:99], a[4:7]
s_cmp_eq_u32 s77, 98304
v_mfma_f32_16x16x32_bf16 a[8:11], v[168:171], v[96:99], a[8:11]
s_cselect_b32 s77, 0, s77
s_mov_b32 m0, s79
v_mfma_f32_16x16x32_bf16 a[12:15], v[172:175], v[96:99], a[12:15]
s_xor_b32 s78, s78, 32768
v_mfma_f32_16x16x32_bf16 a[16:19], v[96:99], v[176:179], a[16:19]
buffer_load_dwordx4 v21, s[52:55], s80 offen lds
v_add_u32 v15, s77, v13
v_mfma_f32_16x16x32_bf16 a[20:23], v[96:99], v[180:183], a[20:23]
v_add_u32 v19, s78, v17
v_mfma_f32_16x16x32_bf16 a[24:27], v[96:99], v[184:187], a[24:27]
v_add_u32 v20, s78, v18
v_mfma_f32_16x16x32_bf16 a[28:31], v[96:99], v[188:191], a[28:31]
v_add_u32 v16, s77, v14
s_add_u32 m0, s79, 1024
v_mfma_f32_16x16x32_bf16 a[32:35], v[160:163], v[100:103], a[32:35]
ds_read_b128 v[64:67], v15 offset:0
v_mfma_f32_16x16x32_bf16 a[36:39], v[164:167], v[100:103], a[36:39]
v_lshlrev_b32 v204, 16, v192
v_mfma_f32_16x16x32_bf16 a[40:43], v[168:171], v[100:103], a[40:43]
buffer_load_dwordx4 v22, s[52:55], s80 offen lds
ds_read_b64_tr_b16 v[128:129], v19 offset:0
v_and_b32 v205, 0xffff0000, v192
v_mfma_f32_16x16x32_bf16 a[44:47], v[172:175], v[100:103], a[44:47]
v_lshlrev_b32 v206, 16, v193
v_mfma_f32_16x16x32_bf16 a[48:51], v[100:103], v[176:179], a[48:51]
ds_read_b64_tr_b16 v[130:131], v20 offset:0
v_and_b32 v207, 0xffff0000, v193
v_mfma_f32_16x16x32_bf16 a[52:55], v[100:103], v[180:183], a[52:55]
s_add_u32 m0, s79, 2048
v_lshlrev_b32 v208, 16, v194
v_mfma_f32_16x16x32_bf16 a[56:59], v[100:103], v[184:187], a[56:59]
ds_read_b64_tr_b16 v[132:133], v19 offset:256
v_mfma_f32_16x16x32_bf16 a[60:63], v[100:103], v[188:191], a[60:63]
v_and_b32 v209, 0xffff0000, v194
v_mfma_f32_16x16x32_bf16 a[64:67], v[160:163], v[104:107], a[64:67]
buffer_load_dwordx4 v23, s[52:55], s80 offen lds
ds_read_b64_tr_b16 v[134:135], v20 offset:256
v_lshlrev_b32 v210, 16, v195
v_mfma_f32_16x16x32_bf16 a[68:71], v[164:167], v[104:107], a[68:71]
v_and_b32 v211, 0xffff0000, v195
v_mfma_f32_16x16x32_bf16 a[72:75], v[168:171], v[104:107], a[72:75]
ds_read_b64_tr_b16 v[136:137], v19 offset:512
v_lshlrev_b32 v212, 16, v196
v_mfma_f32_16x16x32_bf16 a[76:79], v[172:175], v[104:107], a[76:79]
s_add_u32 m0, s79, 3072
v_mfma_f32_16x16x32_bf16 a[80:83], v[104:107], v[176:179], a[80:83]
ds_read_b64_tr_b16 v[138:139], v20 offset:512
v_and_b32 v213, 0xffff0000, v196
v_mfma_f32_16x16x32_bf16 a[84:87], v[104:107], v[180:183], a[84:87]
v_lshlrev_b32 v214, 16, v197
v_mfma_f32_16x16x32_bf16 a[88:91], v[104:107], v[184:187], a[88:91]
buffer_load_dwordx4 v24, s[52:55], s80 offen lds
ds_read_b64_tr_b16 v[140:141], v19 offset:768
v_and_b32 v215, 0xffff0000, v197
v_mfma_f32_16x16x32_bf16 a[92:95], v[104:107], v[188:191], a[92:95]
v_lshlrev_b32 v216, 16, v198
v_mfma_f32_16x16x32_bf16 a[96:99], v[160:163], v[108:111], a[96:99]
ds_read_b64_tr_b16 v[142:143], v20 offset:768
s_add_u32 s79, s79, 4096
v_and_b32 v217, 0xffff0000, v198
v_mfma_f32_16x16x32_bf16 a[100:103], v[164:167], v[108:111], a[100:103]
v_mfma_f32_16x16x32_bf16 a[104:107], v[168:171], v[108:111], a[104:107]
ds_read_b64_tr_b16 v[144:145], v19 offset:2048
v_lshlrev_b32 v218, 16, v199
v_mfma_f32_16x16x32_bf16 a[108:111], v[172:175], v[108:111], a[108:111]
s_add_u32 s80, s80, 32768
v_and_b32 v219, 0xffff0000, v199
v_mfma_f32_16x16x32_bf16 a[112:115], v[108:111], v[176:179], a[112:115]
ds_read_b64_tr_b16 v[146:147], v20 offset:2048
v_lshlrev_b32 v220, 16, v200
v_mfma_f32_16x16x32_bf16 a[116:119], v[108:111], v[180:183], a[116:119]
v_and_b32 v221, 0xffff0000, v200
v_mfma_f32_16x16x32_bf16 a[120:123], v[108:111], v[184:187], a[120:123]
ds_read_b64_tr_b16 v[148:149], v19 offset:2304
s_mov_b32 m0, s81
v_mfma_f32_16x16x32_bf16 a[124:127], v[108:111], v[188:191], a[124:127]
v_lshlrev_b32 v222, 16, v201
v_mfma_f32_16x16x32_bf16 a[128:131], v[160:163], v[112:115], a[128:131]
v_and_b32 v223, 0xffff0000, v201
v_mfma_f32_16x16x32_bf16 a[132:135], v[164:167], v[112:115], a[132:135]
buffer_load_dwordx4 v25, s[56:59], s82 offen lds
ds_read_b64_tr_b16 v[150:151], v20 offset:2304
v_lshlrev_b32 v224, 16, v202
v_mfma_f32_16x16x32_bf16 a[136:139], v[168:171], v[112:115], a[136:139]
v_and_b32 v225, 0xffff0000, v202
v_mfma_f32_16x16x32_bf16 a[140:143], v[172:175], v[112:115], a[140:143]
ds_read_b64_tr_b16 v[152:153], v19 offset:2560
v_mfma_f32_16x16x32_bf16 a[144:147], v[112:115], v[176:179], a[144:147]
s_add_u32 m0, s81, 896
v_lshlrev_b32 v226, 16, v203
v_mfma_f32_16x16x32_bf16 a[148:151], v[112:115], v[180:183], a[148:151]
ds_read_b64_tr_b16 v[154:155], v20 offset:2560
v_and_b32 v227, 0xffff0000, v203
v_mfma_f32_16x16x32_bf16 a[152:155], v[112:115], v[184:187], a[152:155]
v_add_f32 v52, v204, v205
v_mfma_f32_16x16x32_bf16 a[156:159], v[112:115], v[188:191], a[156:159]
buffer_load_dwordx4 v25, s[56:59], s82 offen offset:128 lds
ds_read_b64_tr_b16 v[156:157], v19 offset:2816
v_add_f32 v53, v206, v207
v_mfma_f32_16x16x32_bf16 a[160:163], v[160:163], v[116:119], a[160:163]
v_add_f32 v52, v52, v208
v_mfma_f32_16x16x32_bf16 a[164:167], v[164:167], v[116:119], a[164:167]
ds_read_b64_tr_b16 v[158:159], v20 offset:2816
v_mfma_f32_16x16x32_bf16 a[168:171], v[168:171], v[116:119], a[168:171]
s_add_u32 m0, s81, 2048
v_add_f32 v53, v53, v209
v_mfma_f32_16x16x32_bf16 a[172:175], v[172:175], v[116:119], a[172:175]
ds_read_b128 v[68:71], v15 offset:2048
v_add_f32 v52, v52, v210
v_mfma_f32_16x16x32_bf16 a[176:179], v[116:119], v[176:179], a[176:179]
buffer_load_dwordx4 v25, s[60:63], s82 offen lds
v_add_f32 v53, v53, v211
v_mfma_f32_16x16x32_bf16 a[180:183], v[116:119], v[180:183], a[180:183]
ds_read_b128 v[72:75], v15 offset:4096
v_add_f32 v52, v52, v212
v_mfma_f32_16x16x32_bf16 a[184:187], v[116:119], v[184:187], a[184:187]
v_mfma_f32_16x16x32_bf16 a[188:191], v[116:119], v[188:191], a[188:191]
ds_read_b128 v[76:79], v15 offset:6144
s_add_u32 m0, s81, 2944
v_add_f32 v53, v53, v213
v_mfma_f32_16x16x32_bf16 a[192:195], v[160:163], v[120:123], a[192:195]
v_add_f32 v52, v52, v214
v_mfma_f32_16x16x32_bf16 a[196:199], v[164:167], v[120:123], a[196:199]
ds_read_b128 v[80:83], v15 offset:8192
v_add_f32 v53, v53, v215
v_mfma_f32_16x16x32_bf16 a[200:203], v[168:171], v[120:123], a[200:203]
buffer_load_dwordx4 v25, s[60:63], s82 offen offset:128 lds
v_add_f32 v52, v52, v216
v_mfma_f32_16x16x32_bf16 a[204:207], v[172:175], v[120:123], a[204:207]
ds_read_b128 v[84:87], v15 offset:10240
v_add_f32 v53, v53, v217
v_mfma_f32_16x16x32_bf16 a[208:211], v[120:123], v[176:179], a[208:211]
v_mfma_f32_16x16x32_bf16 a[212:215], v[120:123], v[180:183], a[212:215]
ds_read_b128 v[88:91], v15 offset:12288
s_add_u32 s81, s81, 16384
v_add_f32 v52, v52, v218
v_mfma_f32_16x16x32_bf16 a[216:219], v[120:123], v[184:187], a[216:219]
v_add_f32 v53, v53, v219
v_mfma_f32_16x16x32_bf16 a[220:223], v[120:123], v[188:191], a[220:223]
ds_read_b128 v[92:95], v15 offset:14336
v_add_f32 v52, v52, v220
v_mfma_f32_16x16x32_bf16 a[224:227], v[160:163], v[124:127], a[224:227]
s_and_b32 s81, s81, 0xffff
v_add_f32 v53, v53, v221
v_mfma_f32_16x16x32_bf16 a[228:231], v[164:167], v[124:127], a[228:231]
v_mfma_f32_16x16x32_bf16 a[232:235], v[168:171], v[124:127], a[232:235]
v_add_f32 v52, v52, v222
v_mfma_f32_16x16x32_bf16 a[236:239], v[172:175], v[124:127], a[236:239]
s_add_u32 s82, s82, 262144
v_add_f32 v53, v53, v223
v_mfma_f32_16x16x32_bf16 a[240:243], v[124:127], v[176:179], a[240:243]
v_add_f32 v52, v52, v224
v_mfma_f32_16x16x32_bf16 a[244:247], v[124:127], v[180:183], a[244:247]
v_add_f32 v53, v53, v225
v_mfma_f32_16x16x32_bf16 a[248:251], v[124:127], v[184:187], a[248:251]
v_mfma_f32_16x16x32_bf16 a[252:255], v[124:127], v[188:191], a[252:255]
s_waitcnt vmcnt(16) lgkmcnt(0)
s_barrier
v_readfirstlane_b32 s42, v57
s_nop 0
s_add_u32 s42, s42, 1
v_mfma_f32_16x16x32_bf16 a[0:3], v[128:131], v[64:67], a[0:3]
ds_read_b128 v[96:99], v16 offset:0
v_mfma_f32_16x16x32_bf16 a[4:7], v[132:135], v[64:67], a[4:7]
v_add_f32 v52, v52, v226
v_mfma_f32_16x16x32_bf16 a[8:11], v[136:139], v[64:67], a[8:11]
ds_read_b64_tr_b16 v[160:161], v19 offset:16384
s_mov_b32 m0, s79
v_add_f32 v53, v53, v227
v_mfma_f32_16x16x32_bf16 a[12:15], v[140:143], v[64:67], a[12:15]
v_add_f32 v52, v52, v53
v_mfma_f32_16x16x32_bf16 a[16:19], v[64:67], v[144:147], a[16:19]
buffer_load_dwordx4 v21, s[52:55], s80 offen lds
ds_read_b64_tr_b16 v[162:163], v20 offset:16384
v_mfma_f32_16x16x32_bf16 a[20:23], v[64:67], v[148:151], a[20:23]
v_add_f32_dpp v52, v52, v52 quad_perm:[1,0,3,2] row_mask:0xf bank_mask:0xf
v_mfma_f32_16x16x32_bf16 a[24:27], v[64:67], v[152:155], a[24:27]
s_add_u32 m0, s79, 1024
s_nop 0
v_mfma_f32_16x16x32_bf16 a[28:31], v[64:67], v[156:159], a[28:31]
ds_read_b64_tr_b16 v[164:165], v19 offset:16640
v_mfma_f32_16x16x32_bf16 a[32:35], v[128:131], v[68:71], a[32:35]
buffer_load_dwordx4 v22, s[52:55], s80 offen lds
v_add_f32_dpp v52, v52, v52 quad_perm:[2,3,0,1] row_mask:0xf bank_mask:0xf
v_mfma_f32_16x16x32_bf16 a[36:39], v[132:135], v[68:71], a[36:39]
ds_read_b64_tr_b16 v[166:167], v20 offset:16640
s_nop 0
v_mfma_f32_16x16x32_bf16 a[40:43], v[136:139], v[68:71], a[40:43]
v_add_f32_dpp v52, v52, v52 row_half_mirror row_mask:0xf bank_mask:0xf
v_mfma_f32_16x16x32_bf16 a[44:47], v[140:143], v[68:71], a[44:47]
s_add_u32 m0, s79, 2048
v_mfma_f32_16x16x32_bf16 a[48:51], v[68:71], v[144:147], a[48:51]
ds_read_b64_tr_b16 v[168:169], v19 offset:16896
s_nop 0
v_mfma_f32_16x16x32_bf16 a[52:55], v[68:71], v[148:151], a[52:55]
buffer_load_dwordx4 v23, s[52:55], s80 offen lds
v_add_f32_dpp v52, v52, v52 row_mirror row_mask:0xf bank_mask:0xf
v_mfma_f32_16x16x32_bf16 a[56:59], v[68:71], v[152:155], a[56:59]
ds_read_b64_tr_b16 v[170:171], v20 offset:16896
v_mfma_f32_16x16x32_bf16 a[60:63], v[68:71], v[156:159], a[60:63]
s_add_u32 m0, s79, 3072
v_mov_b32 v51, v52
v_mfma_f32_16x16x32_bf16 a[64:67], v[128:131], v[72:75], a[64:67]
ds_read_b64_tr_b16 v[172:173], v19 offset:17152
s_nop 0
v_mfma_f32_16x16x32_bf16 a[68:71], v[132:135], v[72:75], a[68:71]
v_permlane16_swap_b32 v52, v51
v_mfma_f32_16x16x32_bf16 a[72:75], v[136:139], v[72:75], a[72:75]
buffer_load_dwordx4 v24, s[52:55], s80 offen lds
v_mfma_f32_16x16x32_bf16 a[76:79], v[140:143], v[72:75], a[76:79]
ds_read_b64_tr_b16 v[174:175], v20 offset:17152
v_add_f32 v52, v52, v51
v_mfma_f32_16x16x32_bf16 a[80:83], v[72:75], v[144:147], a[80:83]
s_add_u32 s83, s83, 32768
v_mul_f32 v54, 0x3acccccd, v52
v_mfma_f32_16x16x32_bf16 a[84:87], v[72:75], v[148:151], a[84:87]
ds_read_b64_tr_b16 v[176:177], v19 offset:18432
v_mfma_f32_16x16x32_bf16 a[88:91], v[72:75], v[152:155], a[88:91]
s_cmp_eq_u32 s83, 98304
v_mul_f32 v55, v54, v30
v_mfma_f32_16x16x32_bf16 a[92:95], v[72:75], v[156:159], a[92:95]
v_sub_f32 v204, v204, v54
v_mfma_f32_16x16x32_bf16 a[96:99], v[128:131], v[76:79], a[96:99]
ds_read_b64_tr_b16 v[178:179], v20 offset:18432
v_sub_f32 v205, v205, v54
v_mfma_f32_16x16x32_bf16 a[100:103], v[132:135], v[76:79], a[100:103]
s_cselect_b32 s83, 0, s83
v_mfma_f32_16x16x32_bf16 a[104:107], v[136:139], v[76:79], a[104:107]
ds_read_b64_tr_b16 v[180:181], v19 offset:18688
v_sub_f32 v206, v206, v54
v_mfma_f32_16x16x32_bf16 a[108:111], v[140:143], v[76:79], a[108:111]
s_lshl_b32 s45, s38, 13
v_sub_f32 v207, v207, v54
v_mfma_f32_16x16x32_bf16 a[112:115], v[76:79], v[144:147], a[112:115]
v_mfma_f32_16x16x32_bf16 a[116:119], v[76:79], v[148:151], a[116:119]
ds_read_b64_tr_b16 v[182:183], v20 offset:18688
s_add_u32 s45, s45, s83
v_sub_f32 v208, v208, v54
v_mfma_f32_16x16x32_bf16 a[120:123], v[76:79], v[152:155], a[120:123]
v_sub_f32 v209, v209, v54
v_mfma_f32_16x16x32_bf16 a[124:127], v[76:79], v[156:159], a[124:127]
ds_read_b64_tr_b16 v[184:185], v19 offset:18944
v_mfma_f32_16x16x32_bf16 a[128:131], v[128:131], v[80:83], a[128:131]
s_add_u32 s79, s45, 65536
v_sub_f32 v210, v210, v54
v_mfma_f32_16x16x32_bf16 a[132:135], v[132:135], v[80:83], a[132:135]
ds_read_b64_tr_b16 v[186:187], v20 offset:18944
v_sub_f32 v211, v211, v54
v_mfma_f32_16x16x32_bf16 a[136:139], v[136:139], v[80:83], a[136:139]
s_sub_u32 s80, s80, 32640
v_sub_f32 v212, v212, v54
v_mfma_f32_16x16x32_bf16 a[140:143], v[140:143], v[80:83], a[140:143]
v_mfma_f32_16x16x32_bf16 a[144:147], v[80:83], v[144:147], a[144:147]
ds_read_b64_tr_b16 v[188:189], v19 offset:19200
s_mov_b32 m0, s81
v_sub_f32 v213, v213, v54
v_mfma_f32_16x16x32_bf16 a[148:151], v[80:83], v[148:151], a[148:151]
v_sub_f32 v214, v214, v54
v_mfma_f32_16x16x32_bf16 a[152:155], v[80:83], v[152:155], a[152:155]
buffer_load_dwordx4 v25, s[56:59], s82 offen lds
ds_read_b64_tr_b16 v[190:191], v20 offset:19200
v_mfma_f32_16x16x32_bf16 a[156:159], v[80:83], v[156:159], a[156:159]
v_sub_f32 v215, v215, v54
v_mfma_f32_16x16x32_bf16 a[160:163], v[128:131], v[84:87], a[160:163]
v_sub_f32 v216, v216, v54
v_mfma_f32_16x16x32_bf16 a[164:167], v[132:135], v[84:87], a[164:167]
ds_read_b128 v[100:103], v16 offset:2048
s_add_u32 m0, s81, 896
v_sub_f32 v217, v217, v54
v_mfma_f32_16x16x32_bf16 a[168:171], v[136:139], v[84:87], a[168:171]
v_mfma_f32_16x16x32_bf16 a[172:175], v[140:143], v[84:87], a[172:175]
buffer_load_dwordx4 v25, s[56:59], s82 offen offset:128 lds
ds_read_b128 v[104:107], v16 offset:4096
v_sub_f32 v218, v218, v54
v_mfma_f32_16x16x32_bf16 a[176:179], v[84:87], v[144:147], a[176:179]
v_sub_f32 v219, v219, v54
v_mfma_f32_16x16x32_bf16 a[180:183], v[84:87], v[148:151], a[180:183]
ds_read_b128 v[108:111], v16 offset:6144
s_add_u32 m0, s81, 2048
v_mfma_f32_16x16x32_bf16 a[184:187], v[84:87], v[152:155], a[184:187]
v_sub_f32 v220, v220, v55
v_mfma_f32_16x16x32_bf16 a[188:191], v[84:87], v[156:159], a[188:191]
v_sub_f32 v221, v221, v55
v_mfma_f32_16x16x32_bf16 a[192:195], v[128:131], v[88:91], a[192:195]
buffer_load_dwordx4 v25, s[60:63], s82 offen lds
ds_read_b128 v[112:115], v16 offset:8192
v_sub_f32 v222, v222, v55
v_mfma_f32_16x16x32_bf16 a[196:199], v[132:135], v[88:91], a[196:199]
v_mfma_f32_16x16x32_bf16 a[200:203], v[136:139], v[88:91], a[200:203]
ds_read_b128 v[116:119], v16 offset:10240
s_add_u32 m0, s81, 2944
v_sub_f32 v223, v223, v55
v_mfma_f32_16x16x32_bf16 a[204:207], v[140:143], v[88:91], a[204:207]
v_sub_f32 v224, v224, v55
v_mfma_f32_16x16x32_bf16 a[208:211], v[88:91], v[144:147], a[208:211]
buffer_load_dwordx4 v25, s[60:63], s82 offen offset:128 lds
v_mfma_f32_16x16x32_bf16 a[212:215], v[88:91], v[148:151], a[212:215]
ds_read_b128 v[120:123], v16 offset:12288
v_sub_f32 v225, v225, v55
v_mfma_f32_16x16x32_bf16 a[216:219], v[88:91], v[152:155], a[216:219]
v_sub_f32 v226, v226, v55
v_mfma_f32_16x16x32_bf16 a[220:223], v[88:91], v[156:159], a[220:223]
ds_read_b128 v[124:127], v16 offset:14336
s_add_u32 s81, s81, 16384
v_sub_f32 v227, v227, v55
v_mfma_f32_16x16x32_bf16 a[224:227], v[128:131], v[92:95], a[224:227]
v_mfma_f32_16x16x32_bf16 a[228:231], v[132:135], v[92:95], a[228:231]
s_and_b32 s81, s81, 0xffff
v_mul_f32 v52, v204, v204
v_mfma_f32_16x16x32_bf16 a[232:235], v[136:139], v[92:95], a[232:235]
v_mul_f32 v53, v205, v205
v_mfma_f32_16x16x32_bf16 a[236:239], v[140:143], v[92:95], a[236:239]
s_add_u32 s82, s82, 262144
v_mfma_f32_16x16x32_bf16 a[240:243], v[92:95], v[144:147], a[240:243]
v_fmac_f32 v52, v206, v206
v_mfma_f32_16x16x32_bf16 a[244:247], v[92:95], v[148:151], a[244:247]
v_fmac_f32 v53, v207, v207
v_mfma_f32_16x16x32_bf16 a[248:251], v[92:95], v[152:155], a[248:251]
v_mfma_f32_16x16x32_bf16 a[252:255], v[92:95], v[156:159], a[252:255]
s_waitcnt vmcnt(16) lgkmcnt(0)
s_barrier
v_mfma_f32_16x16x32_bf16 a[0:3], v[160:163], v[96:99], a[0:3]
s_add_u32 s77, s77, 32768
v_mfma_f32_16x16x32_bf16 a[4:7], v[164:167], v[96:99], a[4:7]
s_cmp_eq_u32 s77, 98304
v_mfma_f32_16x16x32_bf16 a[8:11], v[168:171], v[96:99], a[8:11]
s_cselect_b32 s77, 0, s77
s_mov_b32 m0, s79
v_mfma_f32_16x16x32_bf16 a[12:15], v[172:175], v[96:99], a[12:15]
s_xor_b32 s78, s78, 32768
v_mfma_f32_16x16x32_bf16 a[16:19], v[96:99], v[176:179], a[16:19]
buffer_load_dwordx4 v21, s[52:55], s80 offen lds
v_add_u32 v15, s77, v13
v_mfma_f32_16x16x32_bf16 a[20:23], v[96:99], v[180:183], a[20:23]
v_add_u32 v19, s78, v17
v_mfma_f32_16x16x32_bf16 a[24:27], v[96:99], v[184:187], a[24:27]
v_add_u32 v20, s78, v18
v_mfma_f32_16x16x32_bf16 a[28:31], v[96:99], v[188:191], a[28:31]
v_add_u32 v16, s77, v14
s_add_u32 m0, s79, 1024
v_mfma_f32_16x16x32_bf16 a[32:35], v[160:163], v[100:103], a[32:35]
ds_read_b128 v[64:67], v15 offset:0
v_mfma_f32_16x16x32_bf16 a[36:39], v[164:167], v[100:103], a[36:39]
v_fmac_f32 v52, v208, v208
v_mfma_f32_16x16x32_bf16 a[40:43], v[168:171], v[100:103], a[40:43]
buffer_load_dwordx4 v22, s[52:55], s80 offen lds
ds_read_b64_tr_b16 v[128:129], v19 offset:0
v_fmac_f32 v53, v209, v209
v_mfma_f32_16x16x32_bf16 a[44:47], v[172:175], v[100:103], a[44:47]
v_fmac_f32 v52, v210, v210
v_mfma_f32_16x16x32_bf16 a[48:51], v[100:103], v[176:179], a[48:51]
ds_read_b64_tr_b16 v[130:131], v20 offset:0
v_fmac_f32 v53, v211, v211
s_lshl_b32 s46, s43, 2
v_mfma_f32_16x16x32_bf16 a[52:55], v[100:103], v[180:183], a[52:55]
s_add_u32 m0, s79, 2048
v_fmac_f32 v52, v212, v212
v_mfma_f32_16x16x32_bf16 a[56:59], v[100:103], v[184:187], a[56:59]
ds_read_b64_tr_b16 v[132:133], v19 offset:256
s_add_u32 s46, s46, s38
v_mfma_f32_16x16x32_bf16 a[60:63], v[100:103], v[188:191], a[60:63]
v_fmac_f32 v53, v213, v213
v_mfma_f32_16x16x32_bf16 a[64:67], v[160:163], v[104:107], a[64:67]
buffer_load_dwordx4 v23, s[52:55], s80 offen lds
ds_read_b64_tr_b16 v[134:135], v20 offset:256
v_fmac_f32 v52, v214, v214
v_mfma_f32_16x16x32_bf16 a[68:71], v[164:167], v[104:107], a[68:71]
v_fmac_f32 v53, v215, v215
v_lshlrev_b32 v58, 7, v8
v_mfma_f32_16x16x32_bf16 a[72:75], v[168:171], v[104:107], a[72:75]
ds_read_b64_tr_b16 v[136:137], v19 offset:512
v_fmac_f32 v52, v216, v216
v_mfma_f32_16x16x32_bf16 a[76:79], v[172:175], v[104:107], a[76:79]
s_add_u32 m0, s79, 3072
v_fmac_f32 v53, v217, v217
v_mfma_f32_16x16x32_bf16 a[80:83], v[104:107], v[176:179], a[80:83]
ds_read_b64_tr_b16 v[138:139], v20 offset:512
s_mul_i32 s44, s46, 5120
v_mfma_f32_16x16x32_bf16 a[84:87], v[104:107], v[180:183], a[84:87]
v_fmac_f32 v52, v218, v218
v_mfma_f32_16x16x32_bf16 a[88:91], v[104:107], v[184:187], a[88:91]
buffer_load_dwordx4 v24, s[52:55], s80 offen lds
ds_read_b64_tr_b16 v[140:141], v19 offset:768
v_fmac_f32 v53, v219, v219
v_mfma_f32_16x16x32_bf16 a[92:95], v[104:107], v[188:191], a[92:95]
v_fmac_f32 v52, v220, v220
s_add_u32 s44, s44, 0
v_mfma_f32_16x16x32_bf16 a[96:99], v[160:163], v[108:111], a[96:99]
ds_read_b64_tr_b16 v[142:143], v20 offset:768
s_add_u32 s79, s79, 4096
v_fmac_f32 v53, v221, v221
v_mfma_f32_16x16x32_bf16 a[100:103], v[164:167], v[108:111], a[100:103]
v_fmac_f32 v52, v222, v222
v_mfma_f32_16x16x32_bf16 a[104:107], v[168:171], v[108:111], a[104:107]
ds_read_b64_tr_b16 v[144:145], v19 offset:2048
s_and_b32 s69, s69, 0xffff
v_mfma_f32_16x16x32_bf16 a[108:111], v[172:175], v[108:111], a[108:111]
s_add_u32 s80, s80, 32768
v_fmac_f32 v53, v223, v223
v_mfma_f32_16x16x32_bf16 a[112:115], v[108:111], v[176:179], a[112:115]
ds_read_b64_tr_b16 v[146:147], v20 offset:2048
v_fmac_f32 v52, v224, v224
v_mfma_f32_16x16x32_bf16 a[116:119], v[108:111], v[180:183], a[116:119]
v_fmac_f32 v53, v225, v225
s_mov_b32 s71, 0x20000
v_mfma_f32_16x16x32_bf16 a[120:123], v[108:111], v[184:187], a[120:123]
ds_read_b64_tr_b16 v[148:149], v19 offset:2304
s_mov_b32 m0, s81
v_fmac_f32 v52, v226, v226
v_mfma_f32_16x16x32_bf16 a[124:127], v[108:111], v[188:191], a[124:127]
v_fmac_f32 v53, v227, v227
v_mfma_f32_16x16x32_bf16 a[128:131], v[160:163], v[112:115], a[128:131]
s_add_u32 s70, s44, 1280
v_mfma_f32_16x16x32_bf16 a[132:135], v[164:167], v[112:115], a[132:135]
buffer_load_dwordx4 v25, s[56:59], s82 offen lds
ds_read_b64_tr_b16 v[150:151], v20 offset:2304
v_add_f32 v52, v52, v53
v_mfma_f32_16x16x32_bf16 a[136:139], v[168:171], v[112:115], a[136:139]
s_nop 0
v_add_f32_dpp v52, v52, v52 quad_perm:[1,0,3,2] row_mask:0xf bank_mask:0xf
v_mfma_f32_16x16x32_bf16 a[140:143], v[172:175], v[112:115], a[140:143]
ds_read_b64_tr_b16 v[152:153], v19 offset:2560
s_nop 0
s_min_u32 s70, s70, 5242880
v_mfma_f32_16x16x32_bf16 a[144:147], v[112:115], v[176:179], a[144:147]
s_add_u32 m0, s81, 896
v_add_f32_dpp v52, v52, v52 quad_perm:[2,3,0,1] row_mask:0xf bank_mask:0xf
v_mfma_f32_16x16x32_bf16 a[148:151], v[112:115], v[180:183], a[148:151]
buffer_load_dword v46, v58, s[68:71], s44 offen
ds_read_b64_tr_b16 v[154:155], v20 offset:2560
s_nop 0
v_mfma_f32_16x16x32_bf16 a[152:155], v[112:115], v[184:187], a[152:155]
v_mfma_f32_16x16x32_bf16 a[156:159], v[112:115], v[188:191], a[156:159]
buffer_load_dwordx4 v25, s[56:59], s82 offen offset:128 lds
ds_read_b64_tr_b16 v[156:157], v19 offset:2816
v_add_f32_dpp v52, v52, v52 row_half_mirror row_mask:0xf bank_mask:0xf
v_mfma_f32_16x16x32_bf16 a[160:163], v[160:163], v[116:119], a[160:163]
s_nop 0
s_mul_i32 s44, s46, 2176
v_mfma_f32_16x16x32_bf16 a[164:167], v[164:167], v[116:119], a[164:167]
ds_read_b64_tr_b16 v[158:159], v20 offset:2816
v_add_f32_dpp v52, v52, v52 row_mirror row_mask:0xf bank_mask:0xf
v_mfma_f32_16x16x32_bf16 a[168:171], v[168:171], v[116:119], a[168:171]
s_add_u32 m0, s81, 2048
v_mov_b32 v51, v52
v_mfma_f32_16x16x32_bf16 a[172:175], v[172:175], v[116:119], a[172:175]
ds_read_b128 v[68:71], v15 offset:2048
s_nop 0
s_add_u32 s44, s44, 0
v_mfma_f32_16x16x32_bf16 a[176:179], v[116:119], v[176:179], a[176:179]
buffer_load_dwordx4 v25, s[60:63], s82 offen lds
v_mfma_f32_16x16x32_bf16 a[180:183], v[116:119], v[180:183], a[180:183]
ds_read_b128 v[72:75], v15 offset:4096
v_permlane16_swap_b32 v52, v51
v_mfma_f32_16x16x32_bf16 a[184:187], v[116:119], v[184:187], a[184:187]
v_add_f32 v52, v52, v51
s_and_b32 s73, s73, 0xffff
v_mfma_f32_16x16x32_bf16 a[188:191], v[116:119], v[188:191], a[188:191]
ds_read_b128 v[76:79], v15 offset:6144
s_add_u32 m0, s81, 2944
v_mov_b32 v56, 0x3727c5ac
v_mfma_f32_16x16x32_bf16 a[192:195], v[160:163], v[120:123], a[192:195]
v_fmac_f32 v56, 0x3acccccd, v52
v_mfma_f32_16x16x32_bf16 a[196:199], v[164:167], v[120:123], a[196:199]
ds_read_b128 v[80:83], v15 offset:8192
v_rsq_f32 v56, v56
s_mov_b32 s75, 0x20000
v_mfma_f32_16x16x32_bf16 a[200:203], v[168:171], v[120:123], a[200:203]
buffer_load_dwordx4 v25, s[60:63], s82 offen offset:128 lds
v_mfma_f32_16x16x32_bf16 a[204:207], v[172:175], v[120:123], a[204:207]
ds_read_b128 v[84:87], v15 offset:10240
s_nop 0
v_mfma_f32_16x16x32_bf16 a[208:211], v[120:123], v[176:179], a[208:211]
v_mul_f32 v204, v204, v56
s_add_u32 s74, s44, 640
v_mfma_f32_16x16x32_bf16 a[212:215], v[120:123], v[180:183], a[212:215]
ds_read_b128 v[88:91], v15 offset:12288
s_add_u32 s81, s81, 16384
v_mul_f32 v205, v205, v56
v_mfma_f32_16x16x32_bf16 a[216:219], v[120:123], v[184:187], a[216:219]
v_mul_f32 v206, v206, v56
v_mfma_f32_16x16x32_bf16 a[220:223], v[120:123], v[188:191], a[220:223]
ds_read_b128 v[92:95], v15 offset:14336
v_mul_f32 v207, v207, v56
s_min_u32 s74, s74, 2211840
v_mfma_f32_16x16x32_bf16 a[224:227], v[160:163], v[124:127], a[224:227]
s_and_b32 s81, s81, 0xffff
v_mfma_f32_16x16x32_bf16 a[228:231], v[164:167], v[124:127], a[228:231]
v_mul_f32 v208, v208, v56
v_mfma_f32_16x16x32_bf16 a[232:235], v[168:171], v[124:127], a[232:235]
buffer_load_dword v47, v58, s[72:75], s44 offen
v_mul_f32 v209, v209, v56
v_mfma_f32_16x16x32_bf16 a[236:239], v[172:175], v[124:127], a[236:239]
s_add_u32 s82, s82, 262144
v_mul_f32 v210, v210, v56
v_mfma_f32_16x16x32_bf16 a[240:243], v[124:127], v[176:179], a[240:243]
v_mul_f32 v211, v211, v56
v_mfma_f32_16x16x32_bf16 a[244:247], v[124:127], v[180:183], a[244:247]
v_mul_f32 v212, v212, v56
v_mfma_f32_16x16x32_bf16 a[248:251], v[124:127], v[184:187], a[248:251]
v_mfma_f32_16x16x32_bf16 a[252:255], v[124:127], v[188:191], a[252:255]
s_waitcnt vmcnt(18) lgkmcnt(0)
s_barrier
v_mfma_f32_16x16x32_bf16 a[0:3], v[128:131], v[64:67], a[0:3]
ds_read_b128 v[96:99], v16 offset:0
v_mfma_f32_16x16x32_bf16 a[4:7], v[132:135], v[64:67], a[4:7]
v_mul_f32 v213, v213, v56
v_mfma_f32_16x16x32_bf16 a[8:11], v[136:139], v[64:67], a[8:11]
ds_read_b64_tr_b16 v[160:161], v19 offset:16384
s_mov_b32 m0, s79
v_mul_f32 v214, v214, v56
v_mfma_f32_16x16x32_bf16 a[12:15], v[140:143], v[64:67], a[12:15]
v_mul_f32 v215, v215, v56
v_mfma_f32_16x16x32_bf16 a[16:19], v[64:67], v[144:147], a[16:19]
buffer_load_dwordx4 v21, s[52:55], s80 offen lds
ds_read_b64_tr_b16 v[162:163], v20 offset:16384
v_mfma_f32_16x16x32_bf16 a[20:23], v[64:67], v[148:151], a[20:23]
v_mul_f32 v216, v216, v56
v_mfma_f32_16x16x32_bf16 a[24:27], v[64:67], v[152:155], a[24:27]
s_add_u32 m0, s79, 1024
v_mul_f32 v217, v217, v56
v_mfma_f32_16x16x32_bf16 a[28:31], v[64:67], v[156:159], a[28:31]
ds_read_b64_tr_b16 v[164:165], v19 offset:16640
v_mfma_f32_16x16x32_bf16 a[32:35], v[128:131], v[68:71], a[32:35]
buffer_load_dwordx4 v22, s[52:55], s80 offen lds
v_mul_f32 v218, v218, v56
v_mfma_f32_16x16x32_bf16 a[36:39], v[132:135], v[68:71], a[36:39]
ds_read_b64_tr_b16 v[166:167], v20 offset:16640
v_mul_f32 v219, v219, v56
v_mfma_f32_16x16x32_bf16 a[40:43], v[136:139], v[68:71], a[40:43]
v_mul_f32 v220, v220, v56
v_mfma_f32_16x16x32_bf16 a[44:47], v[140:143], v[68:71], a[44:47]
s_add_u32 m0, s79, 2048
v_mfma_f32_16x16x32_bf16 a[48:51], v[68:71], v[144:147], a[48:51]
ds_read_b64_tr_b16 v[168:169], v19 offset:16896
v_mul_f32 v221, v221, v56
v_mfma_f32_16x16x32_bf16 a[52:55], v[68:71], v[148:151], a[52:55]
buffer_load_dwordx4 v23, s[52:55], s80 offen lds
v_mul_f32 v222, v222, v56
v_mfma_f32_16x16x32_bf16 a[56:59], v[68:71], v[152:155], a[56:59]
ds_read_b64_tr_b16 v[170:171], v20 offset:16896
v_mfma_f32_16x16x32_bf16 a[60:63], v[68:71], v[156:159], a[60:63]
s_add_u32 m0, s79, 3072
v_mul_f32 v223, v223, v56
v_mfma_f32_16x16x32_bf16 a[64:67], v[128:131], v[72:75], a[64:67]
ds_read_b64_tr_b16 v[172:173], v19 offset:17152
v_mul_f32 v224, v224, v56
v_mfma_f32_16x16x32_bf16 a[68:71], v[132:135], v[72:75], a[68:71]
v_mul_f32 v225, v225, v56
v_mfma_f32_16x16x32_bf16 a[72:75], v[136:139], v[72:75], a[72:75]
buffer_load_dwordx4 v24, s[52:55], s80 offen lds
v_mfma_f32_16x16x32_bf16 a[76:79], v[140:143], v[72:75], a[76:79]
ds_read_b64_tr_b16 v[174:175], v20 offset:17152
v_mul_f32 v226, v226, v56
v_mfma_f32_16x16x32_bf16 a[80:83], v[72:75], v[144:147], a[80:83]
s_add_u32 s83, s83, 32768
v_mul_f32 v227, v227, v56
v_mfma_f32_16x16x32_bf16 a[84:87], v[72:75], v[148:151], a[84:87]
ds_read_b64_tr_b16 v[176:177], v19 offset:18432
v_mfma_f32_16x16x32_bf16 a[88:91], v[72:75], v[152:155], a[88:91]
s_cmp_eq_u32 s83, 98304
v_cvt_pk_bf16_f32 v240, v204, v205
v_mfma_f32_16x16x32_bf16 a[92:95], v[72:75], v[156:159], a[92:95]
v_cvt_pk_bf16_f32 v241, v206, v207
v_mfma_f32_16x16x32_bf16 a[96:99], v[128:131], v[76:79], a[96:99]
ds_read_b64_tr_b16 v[178:179], v20 offset:18432
v_cvt_pk_bf16_f32 v242, v208, v209
v_mfma_f32_16x16x32_bf16 a[100:103], v[132:135], v[76:79], a[100:103]
s_cselect_b32 s83, 0, s83
v_mfma_f32_16x16x32_bf16 a[104:107], v[136:139], v[76:79], a[104:107]
ds_read_b64_tr_b16 v[180:181], v19 offset:18688
v_cvt_pk_bf16_f32 v243, v210, v211
v_mfma_f32_16x16x32_bf16 a[108:111], v[140:143], v[76:79], a[108:111]
s_lshl_b32 s45, s38, 13
v_cvt_pk_bf16_f32 v244, v212, v213
v_mfma_f32_16x16x32_bf16 a[112:115], v[76:79], v[144:147], a[112:115]
v_mfma_f32_16x16x32_bf16 a[116:119], v[76:79], v[148:151], a[116:119]
ds_read_b64_tr_b16 v[182:183], v20 offset:18688
s_add_u32 s45, s45, s83
v_cvt_pk_bf16_f32 v245, v214, v215
v_mfma_f32_16x16x32_bf16 a[120:123], v[76:79], v[152:155], a[120:123]
v_cvt_pk_bf16_f32 v246, v216, v217
v_mfma_f32_16x16x32_bf16 a[124:127], v[76:79], v[156:159], a[124:127]
ds_read_b64_tr_b16 v[184:185], v19 offset:18944
v_mfma_f32_16x16x32_bf16 a[128:131], v[128:131], v[80:83], a[128:131]
s_add_u32 s79, s45, 65536
v_cvt_pk_bf16_f32 v247, v218, v219
v_mfma_f32_16x16x32_bf16 a[132:135], v[132:135], v[80:83], a[132:135]
ds_read_b64_tr_b16 v[186:187], v20 offset:18944
v_cvt_pk_bf16_f32 v248, v220, v221
v_mfma_f32_16x16x32_bf16 a[136:139], v[136:139], v[80:83], a[136:139]
s_sub_u32 s80, s80, 32640
v_cvt_pk_bf16_f32 v249, v222, v223
v_mfma_f32_16x16x32_bf16 a[140:143], v[140:143], v[80:83], a[140:143]
v_mfma_f32_16x16x32_bf16 a[144:147], v[80:83], v[144:147], a[144:147]
ds_read_b64_tr_b16 v[188:189], v19 offset:19200
s_mov_b32 m0, s81
v_cvt_pk_bf16_f32 v250, v224, v225
v_mfma_f32_16x16x32_bf16 a[148:151], v[80:83], v[148:151], a[148:151]
v_cvt_pk_bf16_f32 v251, v226, v227
v_mfma_f32_16x16x32_bf16 a[152:155], v[80:83], v[152:155], a[152:155]
buffer_load_dwordx4 v25, s[56:59], s82 offen lds
ds_read_b64_tr_b16 v[190:191], v20 offset:19200
v_mfma_f32_16x16x32_bf16 a[156:159], v[80:83], v[156:159], a[156:159]
buffer_store_dwordx4 v[240:243], v48, s[48:51], 0 offen sc1
v_mfma_f32_16x16x32_bf16 a[160:163], v[128:131], v[84:87], a[160:163]
v_add_u32 v48, 0x10000, v48
v_mfma_f32_16x16x32_bf16 a[164:167], v[132:135], v[84:87], a[164:167]
buffer_store_dwordx4 v[244:247], v48, s[48:51], 0 offen sc1
ds_read_b128 v[100:103], v16 offset:2048
s_add_u32 m0, s81, 896
v_mfma_f32_16x16x32_bf16 a[168:171], v[136:139], v[84:87], a[168:171]
v_mfma_f32_16x16x32_bf16 a[172:175], v[140:143], v[84:87], a[172:175]
buffer_load_dwordx4 v25, s[56:59], s82 offen offset:128 lds
buffer_store_dwordx4 v[248:251], v49, s[48:51], 0 offen sc1
ds_read_b128 v[104:107], v16 offset:4096
v_mfma_f32_16x16x32_bf16 a[176:179], v[84:87], v[144:147], a[176:179]
v_lshlrev_b32 v204, 16, v240
v_mfma_f32_16x16x32_bf16 a[180:183], v[84:87], v[148:151], a[180:183]
ds_read_b128 v[108:111], v16 offset:6144
s_add_u32 m0, s81, 2048
v_mfma_f32_16x16x32_bf16 a[184:187], v[84:87], v[152:155], a[184:187]
v_and_b32 v205, 0xffff0000, v240
v_mfma_f32_16x16x32_bf16 a[188:191], v[84:87], v[156:159], a[188:191]
v_lshlrev_b32 v206, 16, v241
v_mfma_f32_16x16x32_bf16 a[192:195], v[128:131], v[88:91], a[192:195]
buffer_load_dwordx4 v25, s[60:63], s82 offen lds
ds_read_b128 v[112:115], v16 offset:8192
v_and_b32 v207, 0xffff0000, v241
v_mfma_f32_16x16x32_bf16 a[196:199], v[132:135], v[88:91], a[196:199]
v_mfma_f32_16x16x32_bf16 a[200:203], v[136:139], v[88:91], a[200:203]
ds_read_b128 v[116:119], v16 offset:10240
s_add_u32 m0, s81, 2944
v_lshlrev_b32 v208, 16, v242
v_mfma_f32_16x16x32_bf16 a[204:207], v[140:143], v[88:91], a[204:207]
v_and_b32 v209, 0xffff0000, v242
v_mfma_f32_16x16x32_bf16 a[208:211], v[88:91], v[144:147], a[208:211]
buffer_load_dwordx4 v25, s[60:63], s82 offen offset:128 lds
v_mfma_f32_16x16x32_bf16 a[212:215], v[88:91], v[148:151], a[212:215]
ds_read_b128 v[120:123], v16 offset:12288
v_lshlrev_b32 v210, 16, v243
v_mfma_f32_16x16x32_bf16 a[216:219], v[88:91], v[152:155], a[216:219]
v_and_b32 v211, 0xffff0000, v243
v_mfma_f32_16x16x32_bf16 a[220:223], v[88:91], v[156:159], a[220:223]
ds_read_b128 v[124:127], v16 offset:14336
s_add_u32 s81, s81, 16384
v_lshlrev_b32 v212, 16, v244
v_mfma_f32_16x16x32_bf16 a[224:227], v[128:131], v[92:95], a[224:227]
v_mfma_f32_16x16x32_bf16 a[228:231], v[132:135], v[92:95], a[228:231]
s_and_b32 s81, s81, 0xffff
v_and_b32 v213, 0xffff0000, v244
v_mfma_f32_16x16x32_bf16 a[232:235], v[136:139], v[92:95], a[232:235]
v_lshlrev_b32 v214, 16, v245
v_mfma_f32_16x16x32_bf16 a[236:239], v[140:143], v[92:95], a[236:239]
s_add_u32 s82, s82, 262144
v_mfma_f32_16x16x32_bf16 a[240:243], v[92:95], v[144:147], a[240:243]
v_and_b32 v215, 0xffff0000, v245
v_mfma_f32_16x16x32_bf16 a[244:247], v[92:95], v[148:151], a[244:247]
v_lshlrev_b32 v216, 16, v246
v_mfma_f32_16x16x32_bf16 a[248:251], v[92:95], v[152:155], a[248:251]
v_mfma_f32_16x16x32_bf16 a[252:255], v[92:95], v[156:159], a[252:255]
s_waitcnt vmcnt(21) lgkmcnt(0)
s_barrier
v_mfma_f32_16x16x32_bf16 a[0:3], v[160:163], v[96:99], a[0:3]
s_add_u32 s77, s77, 32768
v_mfma_f32_16x16x32_bf16 a[4:7], v[164:167], v[96:99], a[4:7]
s_cmp_eq_u32 s77, 98304
v_mfma_f32_16x16x32_bf16 a[8:11], v[168:171], v[96:99], a[8:11]
s_cselect_b32 s77, 0, s77
s_mov_b32 m0, s79
v_mfma_f32_16x16x32_bf16 a[12:15], v[172:175], v[96:99], a[12:15]
s_xor_b32 s78, s78, 32768
v_mfma_f32_16x16x32_bf16 a[16:19], v[96:99], v[176:179], a[16:19]
buffer_load_dwordx4 v21, s[52:55], s80 offen lds
v_add_u32 v15, s77, v13
v_mfma_f32_16x16x32_bf16 a[20:23], v[96:99], v[180:183], a[20:23]
v_add_u32 v19, s78, v17
v_mfma_f32_16x16x32_bf16 a[24:27], v[96:99], v[184:187], a[24:27]
v_add_u32 v20, s78, v18
v_mfma_f32_16x16x32_bf16 a[28:31], v[96:99], v[188:191], a[28:31]
v_add_u32 v16, s77, v14
s_add_u32 m0, s79, 1024
v_mfma_f32_16x16x32_bf16 a[32:35], v[160:163], v[100:103], a[32:35]
ds_read_b128 v[64:67], v15 offset:0
v_mfma_f32_16x16x32_bf16 a[36:39], v[164:167], v[100:103], a[36:39]
v_and_b32 v217, 0xffff0000, v246
v_mfma_f32_16x16x32_bf16 a[40:43], v[168:171], v[100:103], a[40:43]
buffer_load_dwordx4 v22, s[52:55], s80 offen lds
ds_read_b64_tr_b16 v[128:129], v19 offset:0
v_lshlrev_b32 v218, 16, v247
v_mfma_f32_16x16x32_bf16 a[44:47], v[172:175], v[100:103], a[44:47]
v_and_b32 v219, 0xffff0000, v247
v_mfma_f32_16x16x32_bf16 a[48:51], v[100:103], v[176:179], a[48:51]
ds_read_b64_tr_b16 v[130:131], v20 offset:0
v_lshlrev_b32 v220, 16, v248
s_lshl_b32 s46, s43, 2
v_mfma_f32_16x16x32_bf16 a[52:55], v[100:103], v[180:183], a[52:55]
s_add_u32 m0, s79, 2048
v_and_b32 v221, 0xffff0000, v248
v_mfma_f32_16x16x32_bf16 a[56:59], v[100:103], v[184:187], a[56:59]
ds_read_b64_tr_b16 v[132:133], v19 offset:256
v_mfma_f32_16x16x32_bf16 a[60:63], v[100:103], v[188:191], a[60:63]
v_lshlrev_b32 v222, 16, v249
v_mfma_f32_16x16x32_bf16 a[64:67], v[160:163], v[104:107], a[64:67]
buffer_load_dwordx4 v23, s[52:55], s80 offen lds
ds_read_b64_tr_b16 v[134:135], v20 offset:256
v_and_b32 v223, 0xffff0000, v249
s_add_u32 s46, s46, s38
v_mfma_f32_16x16x32_bf16 a[68:71], v[164:167], v[104:107], a[68:71]
v_lshlrev_b32 v224, 16, v250
v_mfma_f32_16x16x32_bf16 a[72:75], v[168:171], v[104:107], a[72:75]
ds_read_b64_tr_b16 v[136:137], v19 offset:512
v_and_b32 v225, 0xffff0000, v250
v_mfma_f32_16x16x32_bf16 a[76:79], v[172:175], v[104:107], a[76:79]
s_add_u32 m0, s79, 3072
v_mfma_f32_16x16x32_bf16 a[80:83], v[104:107], v[176:179], a[80:83]
ds_read_b64_tr_b16 v[138:139], v20 offset:512
v_lshlrev_b32 v226, 16, v251
s_mul_i32 s44, s46, 5120
v_mfma_f32_16x16x32_bf16 a[84:87], v[104:107], v[180:183], a[84:87]
v_and_b32 v227, 0xffff0000, v251
v_mfma_f32_16x16x32_bf16 a[88:91], v[104:107], v[184:187], a[88:91]
buffer_load_dwordx4 v24, s[52:55], s80 offen lds
ds_read_b64_tr_b16 v[140:141], v19 offset:768
v_add_f32 v52, v204, v205
v_mfma_f32_16x16x32_bf16 a[92:95], v[104:107], v[188:191], a[92:95]
v_mul_f32 v53, v204, v204
v_mfma_f32_16x16x32_bf16 a[96:99], v[160:163], v[108:111], a[96:99]
ds_read_b64_tr_b16 v[142:143], v20 offset:768
s_add_u32 s79, s79, 4096
v_fmac_f32 v53, v205, v205
s_add_u32 s44, s44, 1280
v_mfma_f32_16x16x32_bf16 a[100:103], v[164:167], v[108:111], a[100:103]
v_mfma_f32_16x16x32_bf16 a[104:107], v[168:171], v[108:111], a[104:107]
ds_read_b64_tr_b16 v[144:145], v19 offset:2048
v_add_f32 v52, v52, v206
v_mfma_f32_16x16x32_bf16 a[108:111], v[172:175], v[108:111], a[108:111]
s_add_u32 s80, s80, 32768
v_fmac_f32 v53, v206, v206
v_mfma_f32_16x16x32_bf16 a[112:115], v[108:111], v[176:179], a[112:115]
ds_read_b64_tr_b16 v[146:147], v20 offset:2048
v_add_f32 v52, v52, v207
s_add_u32 s70, s44, 1280
v_mfma_f32_16x16x32_bf16 a[116:119], v[108:111], v[180:183], a[116:119]
v_fmac_f32 v53, v207, v207
v_mfma_f32_16x16x32_bf16 a[120:123], v[108:111], v[184:187], a[120:123]
ds_read_b64_tr_b16 v[148:149], v19 offset:2304
s_mov_b32 m0, s81
v_mfma_f32_16x16x32_bf16 a[124:127], v[108:111], v[188:191], a[124:127]
v_add_f32 v52, v52, v208
v_mfma_f32_16x16x32_bf16 a[128:131], v[160:163], v[112:115], a[128:131]
v_fmac_f32 v53, v208, v208
s_min_u32 s70, s70, 5242880
v_mfma_f32_16x16x32_bf16 a[132:135], v[164:167], v[112:115], a[132:135]
buffer_load_dwordx4 v25, s[56:59], s82 offen lds
ds_read_b64_tr_b16 v[150:151], v20 offset:2304
v_add_f32 v52, v52, v209
v_mfma_f32_16x16x32_bf16 a[136:139], v[168:171], v[112:115], a[136:139]
v_fmac_f32 v53, v209, v209
v_mfma_f32_16x16x32_bf16 a[140:143], v[172:175], v[112:115], a[140:143]
ds_read_b64_tr_b16 v[152:153], v19 offset:2560
v_mfma_f32_16x16x32_bf16 a[144:147], v[112:115], v[176:179], a[144:147]
buffer_load_dword v46, v58, s[68:71], s44 offen
s_add_u32 m0, s81, 896
v_add_f32 v52, v52, v210
v_mfma_f32_16x16x32_bf16 a[148:151], v[112:115], v[180:183], a[148:151]
ds_read_b64_tr_b16 v[154:155], v20 offset:2560
v_fmac_f32 v53, v210, v210
v_mfma_f32_16x16x32_bf16 a[152:155], v[112:115], v[184:187], a[152:155]
v_add_f32 v52, v52, v211
v_mfma_f32_16x16x32_bf16 a[156:159], v[112:115], v[188:191], a[156:159]
buffer_load_dwordx4 v25, s[56:59], s82 offen offset:128 lds
ds_read_b64_tr_b16 v[156:157], v19 offset:2816
v_fmac_f32 v53, v211, v211
v_mfma_f32_16x16x32_bf16 a[160:163], v[160:163], v[116:119], a[160:163]
v_add_f32 v52, v52, v212
s_mul_i32 s44, s46, 2176
v_mfma_f32_16x16x32_bf16 a[164:167], v[164:167], v[116:119], a[164:167]
ds_read_b64_tr_b16 v[158:159], v20 offset:2816
v_mfma_f32_16x16x32_bf16 a[168:171], v[168:171], v[116:119], a[168:171]
s_add_u32 m0, s81, 2048
v_fmac_f32 v53, v212, v212
v_mfma_f32_16x16x32_bf16 a[172:175], v[172:175], v[116:119], a[172:175]
ds_read_b128 v[68:71], v15 offset:2048
v_add_f32 v52, v52, v213
v_mfma_f32_16x16x32_bf16 a[176:179], v[116:119], v[176:179], a[176:179]
buffer_load_dwordx4 v25, s[60:63], s82 offen lds
v_fmac_f32 v53, v213, v213
s_add_u32 s44, s44, 640
v_mfma_f32_16x16x32_bf16 a[180:183], v[116:119], v[180:183], a[180:183]
ds_read_b128 v[72:75], v15 offset:4096
v_add_f32 v52, v52, v214
v_mfma_f32_16x16x32_bf16 a[184:187], v[116:119], v[184:187], a[184:187]
v_mfma_f32_16x16x32_bf16 a[188:191], v[116:119], v[188:191], a[188:191]
ds_read_b128 v[76:79], v15 offset:6144
s_add_u32 m0, s81, 2944
v_fmac_f32 v53, v214, v214
v_mfma_f32_16x16x32_bf16 a[192:195], v[160:163], v[120:123], a[192:195]
v_add_f32 v52, v52, v215
s_add_u32 s74, s44, 640
v_mfma_f32_16x16x32_bf16 a[196:199], v[164:167], v[120:123], a[196:199]
ds_read_b128 v[80:83], v15 offset:8192
v_fmac_f32 v53, v215, v215
v_mfma_f32_16x16x32_bf16 a[200:203], v[168:171], v[120:123], a[200:203]
buffer_load_dwordx4 v25, s[60:63], s82 offen offset:128 lds
v_add_f32 v52, v52, v216
v_mfma_f32_16x16x32_bf16 a[204:207], v[172:175], v[120:123], a[204:207]
ds_read_b128 v[84:87], v15 offset:10240
v_fmac_f32 v53, v216, v216
v_mfma_f32_16x16x32_bf16 a[208:211], v[120:123], v[176:179], a[208:211]
s_min_u32 s74, s74, 2211840
v_mfma_f32_16x16x32_bf16 a[212:215], v[120:123], v[180:183], a[212:215]
ds_read_b128 v[88:91], v15 offset:12288
s_add_u32 s81, s81, 16384
v_add_f32 v52, v52, v217
v_mfma_f32_16x16x32_bf16 a[216:219], v[120:123], v[184:187], a[216:219]
v_fmac_f32 v53, v217, v217
v_mfma_f32_16x16x32_bf16 a[220:223], v[120:123], v[188:191], a[220:223]
ds_read_b128 v[92:95], v15 offset:14336
v_add_f32 v52, v52, v218
v_mfma_f32_16x16x32_bf16 a[224:227], v[160:163], v[124:127], a[224:227]
buffer_load_dword v47, v58, s[72:75], s44 offen
s_and_b32 s81, s81, 0xffff
v_fmac_f32 v53, v218, v218
v_mfma_f32_16x16x32_bf16 a[228:231], v[164:167], v[124:127], a[228:231]
v_mfma_f32_16x16x32_bf16 a[232:235], v[168:171], v[124:127], a[232:235]
v_add_f32 v52, v52, v219
v_mfma_f32_16x16x32_bf16 a[236:239], v[172:175], v[124:127], a[236:239]
s_add_u32 s82, s82, 262144
v_fmac_f32 v53, v219, v219
v_mfma_f32_16x16x32_bf16 a[240:243], v[124:127], v[176:179], a[240:243]
v_add_f32 v52, v52, v220
v_mfma_f32_16x16x32_bf16 a[244:247], v[124:127], v[180:183], a[244:247]
v_fmac_f32 v53, v220, v220
v_mfma_f32_16x16x32_bf16 a[248:251], v[124:127], v[184:187], a[248:251]
v_mfma_f32_16x16x32_bf16 a[252:255], v[124:127], v[188:191], a[252:255]
s_waitcnt vmcnt(22) lgkmcnt(0)
s_barrier
v_mfma_f32_16x16x32_bf16 a[0:3], v[128:131], v[64:67], a[0:3]
ds_read_b128 v[96:99], v16 offset:0
v_mfma_f32_16x16x32_bf16 a[4:7], v[132:135], v[64:67], a[4:7]
v_add_f32 v52, v52, v221
v_mfma_f32_16x16x32_bf16 a[8:11], v[136:139], v[64:67], a[8:11]
ds_read_b64_tr_b16 v[160:161], v19 offset:16384
s_mov_b32 m0, s79
v_fmac_f32 v53, v221, v221
v_mfma_f32_16x16x32_bf16 a[12:15], v[140:143], v[64:67], a[12:15]
v_add_f32 v52, v52, v222
v_mfma_f32_16x16x32_bf16 a[16:19], v[64:67], v[144:147], a[16:19]
buffer_load_dwordx4 v21, s[52:55], s80 offen lds
ds_read_b64_tr_b16 v[162:163], v20 offset:16384
v_mfma_f32_16x16x32_bf16 a[20:23], v[64:67], v[148:151], a[20:23]
v_fmac_f32 v53, v222, v222
v_mfma_f32_16x16x32_bf16 a[24:27], v[64:67], v[152:155], a[24:27]
s_add_u32 m0, s79, 1024
v_add_f32 v52, v52, v223
v_mfma_f32_16x16x32_bf16 a[28:31], v[64:67], v[156:159], a[28:31]
ds_read_b64_tr_b16 v[164:165], v19 offset:16640
v_fmac_f32 v53, v223, v223
v_mfma_f32_16x16x32_bf16 a[32:35], v[128:131], v[68:71], a[32:35]
buffer_load_dwordx4 v22, s[52:55], s80 offen lds
v_mfma_f32_16x16x32_bf16 a[36:39], v[132:135], v[68:71], a[36:39]
ds_read_b64_tr_b16 v[166:167], v20 offset:16640
v_add_f32 v52, v52, v224
v_mfma_f32_16x16x32_bf16 a[40:43], v[136:139], v[68:71], a[40:43]
v_fmac_f32 v53, v224, v224
v_mfma_f32_16x16x32_bf16 a[44:47], v[140:143], v[68:71], a[44:47]
s_add_u32 m0, s79, 2048
v_mfma_f32_16x16x32_bf16 a[48:51], v[68:71], v[144:147], a[48:51]
ds_read_b64_tr_b16 v[168:169], v19 offset:16896
v_add_f32 v52, v52, v225
v_mfma_f32_16x16x32_bf16 a[52:55], v[68:71], v[148:151], a[52:55]
buffer_load_dwordx4 v23, s[52:55], s80 offen lds
v_fmac_f32 v53, v225, v225
v_mfma_f32_16x16x32_bf16 a[56:59], v[68:71], v[152:155], a[56:59]
ds_read_b64_tr_b16 v[170:171], v20 offset:16896
v_add_f32 v52, v52, v226
v_mfma_f32_16x16x32_bf16 a[60:63], v[68:71], v[156:159], a[60:63]
s_add_u32 m0, s79, 3072
v_mfma_f32_16x16x32_bf16 a[64:67], v[128:131], v[72:75], a[64:67]
ds_read_b64_tr_b16 v[172:173], v19 offset:17152
v_fmac_f32 v53, v226, v226
v_mfma_f32_16x16x32_bf16 a[68:71], v[132:135], v[72:75], a[68:71]
v_add_f32 v52, v52, v227
v_mfma_f32_16x16x32_bf16 a[72:75], v[136:139], v[72:75], a[72:75]
buffer_load_dwordx4 v24, s[52:55], s80 offen lds
v_fmac_f32 v53, v227, v227
v_mfma_f32_16x16x32_bf16 a[76:79], v[140:143], v[72:75], a[76:79]
ds_read_b64_tr_b16 v[174:175], v20 offset:17152
v_mfma_f32_16x16x32_bf16 a[80:83], v[72:75], v[144:147], a[80:83]
s_add_u32 s83, s83, 32768
v_add_f32_dpp v52, v52, v52 quad_perm:[1,0,3,2] row_mask:0xf bank_mask:0xf
v_mfma_f32_16x16x32_bf16 a[84:87], v[72:75], v[148:151], a[84:87]
ds_read_b64_tr_b16 v[176:177], v19 offset:18432
s_nop 0
v_mfma_f32_16x16x32_bf16 a[88:91], v[72:75], v[152:155], a[88:91]
s_cmp_eq_u32 s83, 98304
v_mfma_f32_16x16x32_bf16 a[92:95], v[72:75], v[156:159], a[92:95]
v_add_f32_dpp v52, v52, v52 quad_perm:[2,3,0,1] row_mask:0xf bank_mask:0xf
v_mfma_f32_16x16x32_bf16 a[96:99], v[128:131], v[76:79], a[96:99]
ds_read_b64_tr_b16 v[178:179], v20 offset:18432
s_nop 0
v_mfma_f32_16x16x32_bf16 a[100:103], v[132:135], v[76:79], a[100:103]
s_cselect_b32 s83, 0, s83
v_add_f32_dpp v52, v52, v52 row_half_mirror row_mask:0xf bank_mask:0xf
v_mfma_f32_16x16x32_bf16 a[104:107], v[136:139], v[76:79], a[104:107]
ds_read_b64_tr_b16 v[180:181], v19 offset:18688
v_mfma_f32_16x16x32_bf16 a[108:111], v[140:143], v[76:79], a[108:111]
s_lshl_b32 s45, s38, 13
s_nop 0
v_mfma_f32_16x16x32_bf16 a[112:115], v[76:79], v[144:147], a[112:115]
v_add_f32_dpp v52, v52, v52 row_mirror row_mask:0xf bank_mask:0xf
v_mfma_f32_16x16x32_bf16 a[116:119], v[76:79], v[148:151], a[116:119]
ds_read_b64_tr_b16 v[182:183], v20 offset:18688
s_add_u32 s45, s45, s83
v_mov_b32 v51, v52
v_mfma_f32_16x16x32_bf16 a[120:123], v[76:79], v[152:155], a[120:123]
v_mfma_f32_16x16x32_bf16 a[124:127], v[76:79], v[156:159], a[124:127]
ds_read_b64_tr_b16 v[184:185], v19 offset:18944
s_nop 0
v_mfma_f32_16x16x32_bf16 a[128:131], v[128:131], v[80:83], a[128:131]
s_add_u32 s79, s45, 65536
v_permlane16_swap_b32 v52, v51
v_mfma_f32_16x16x32_bf16 a[132:135], v[132:135], v[80:83], a[132:135]
ds_read_b64_tr_b16 v[186:187], v20 offset:18944
v_mfma_f32_16x16x32_bf16 a[136:139], v[136:139], v[80:83], a[136:139]
s_sub_u32 s80, s80, 32640
v_add_f32 v52, v52, v51
v_mfma_f32_16x16x32_bf16 a[140:143], v[140:143], v[80:83], a[140:143]
v_add_f32_dpp v53, v53, v53 quad_perm:[1,0,3,2] row_mask:0xf bank_mask:0xf
v_mfma_f32_16x16x32_bf16 a[144:147], v[80:83], v[144:147], a[144:147]
ds_read_b64_tr_b16 v[188:189], v19 offset:19200
s_mov_b32 m0, s81
s_nop 0
v_mfma_f32_16x16x32_bf16 a[148:151], v[80:83], v[148:151], a[148:151]
v_mfma_f32_16x16x32_bf16 a[152:155], v[80:83], v[152:155], a[152:155]
buffer_load_dwordx4 v25, s[56:59], s82 offen lds
ds_read_b64_tr_b16 v[190:191], v20 offset:19200
v_add_f32_dpp v53, v53, v53 quad_perm:[2,3,0,1] row_mask:0xf bank_mask:0xf
v_mfma_f32_16x16x32_bf16 a[156:159], v[80:83], v[156:159], a[156:159]
s_nop 0
v_mfma_f32_16x16x32_bf16 a[160:163], v[128:131], v[84:87], a[160:163]
v_add_f32_dpp v53, v53, v53 row_half_mirror row_mask:0xf bank_mask:0xf
v_mfma_f32_16x16x32_bf16 a[164:167], v[132:135], v[84:87], a[164:167]
ds_read_b128 v[100:103], v16 offset:2048
s_add_u32 m0, s81, 896
v_mfma_f32_16x16x32_bf16 a[168:171], v[136:139], v[84:87], a[168:171]
s_nop 0
v_mfma_f32_16x16x32_bf16 a[172:175], v[140:143], v[84:87], a[172:175]
buffer_load_dwordx4 v25, s[56:59], s82 offen offset:128 lds
ds_read_b128 v[104:107], v16 offset:4096
v_add_f32_dpp v53, v53, v53 row_mirror row_mask:0xf bank_mask:0xf
v_mfma_f32_16x16x32_bf16 a[176:179], v[84:87], v[144:147], a[176:179]
v_mfma_f32_16x16x32_bf16 a[180:183], v[84:87], v[148:151], a[180:183]
ds_read_b128 v[108:111], v16 offset:6144
s_add_u32 m0, s81, 2048
v_mov_b32 v51, v53
v_mfma_f32_16x16x32_bf16 a[184:187], v[84:87], v[152:155], a[184:187]
s_nop 0
v_mfma_f32_16x16x32_bf16 a[188:191], v[84:87], v[156:159], a[188:191]
v_permlane16_swap_b32 v53, v51
v_mfma_f32_16x16x32_bf16 a[192:195], v[128:131], v[88:91], a[192:195]
buffer_load_dwordx4 v25, s[60:63], s82 offen lds
ds_read_b128 v[112:115], v16 offset:8192
v_mfma_f32_16x16x32_bf16 a[196:199], v[132:135], v[88:91], a[196:199]
v_add_f32 v53, v53, v51
v_mfma_f32_16x16x32_bf16 a[200:203], v[136:139], v[88:91], a[200:203]
ds_read_b128 v[116:119], v16 offset:10240
s_add_u32 m0, s81, 2944
v_mul_f32 v54, 0x3acccccd, v52
v_mfma_f32_16x16x32_bf16 a[204:207], v[140:143], v[88:91], a[204:207]
v_mul_f32 v56, 0x3acccccd, v53
v_mfma_f32_16x16x32_bf16 a[208:211], v[88:91], v[144:147], a[208:211]
buffer_load_dwordx4 v25, s[60:63], s82 offen offset:128 lds
v_mfma_f32_16x16x32_bf16 a[212:215], v[88:91], v[148:151], a[212:215]
ds_read_b128 v[120:123], v16 offset:12288
v_fma_f32 v56, -v54, v54, v56
v_mfma_f32_16x16x32_bf16 a[216:219], v[88:91], v[152:155], a[216:219]
v_add_f32 v56, 0x3727c5ac, v56
v_mfma_f32_16x16x32_bf16 a[220:223], v[88:91], v[156:159], a[220:223]
ds_read_b128 v[124:127], v16 offset:14336
s_add_u32 s81, s81, 16384
v_mfma_f32_16x16x32_bf16 a[224:227], v[128:131], v[92:95], a[224:227]
v_rsq_f32 v55, v56
v_mfma_f32_16x16x32_bf16 a[228:231], v[132:135], v[92:95], a[228:231]
s_and_b32 s81, s81, 0xffff
s_nop 0
v_mfma_f32_16x16x32_bf16 a[232:235], v[136:139], v[92:95], a[232:235]
v_mov_b32 v52, v54
v_mfma_f32_16x16x32_bf16 a[236:239], v[140:143], v[92:95], a[236:239]
s_add_u32 s82, s82, 262144
v_mfma_f32_16x16x32_bf16 a[240:243], v[92:95], v[144:147], a[240:243]
v_mov_b32 v53, v55
v_mfma_f32_16x16x32_bf16 a[244:247], v[92:95], v[148:151], a[244:247]
buffer_store_dwordx2 v[52:53], v50, s[48:51], 0 offen sc1
v_mfma_f32_16x16x32_bf16 a[248:251], v[92:95], v[152:155], a[248:251]
v_mfma_f32_16x16x32_bf16 a[252:255], v[92:95], v[156:159], a[252:255]
s_waitcnt vmcnt(19) lgkmcnt(0)
s_barrier
v_mfma_f32_16x16x32_bf16 a[0:3], v[160:163], v[96:99], a[0:3]
s_add_u32 s77, s77, 32768
v_mfma_f32_16x16x32_bf16 a[4:7], v[164:167], v[96:99], a[4:7]
s_cmp_eq_u32 s77, 98304
v_mfma_f32_16x16x32_bf16 a[8:11], v[168:171], v[96:99], a[8:11]
s_cselect_b32 s77, 0, s77
s_mov_b32 m0, s79
v_mfma_f32_16x16x32_bf16 a[12:15], v[172:175], v[96:99], a[12:15]
s_xor_b32 s78, s78, 32768
v_mfma_f32_16x16x32_bf16 a[16:19], v[96:99], v[176:179], a[16:19]
buffer_load_dwordx4 v21, s[52:55], s80 offen lds
v_add_u32 v15, s77, v13
v_mfma_f32_16x16x32_bf16 a[20:23], v[96:99], v[180:183], a[20:23]
v_add_u32 v19, s78, v17
v_mfma_f32_16x16x32_bf16 a[24:27], v[96:99], v[184:187], a[24:27]
v_add_u32 v20, s78, v18
v_mfma_f32_16x16x32_bf16 a[28:31], v[96:99], v[188:191], a[28:31]
v_add_u32 v16, s77, v14
s_add_u32 m0, s79, 1024
v_mfma_f32_16x16x32_bf16 a[32:35], v[160:163], v[100:103], a[32:35]
ds_read_b128 v[64:67], v15 offset:0
v_mfma_f32_16x16x32_bf16 a[36:39], v[164:167], v[100:103], a[36:39]
v_mfma_f32_16x16x32_bf16 a[40:43], v[168:171], v[100:103], a[40:43]
buffer_load_dwordx4 v22, s[52:55], s80 offen lds
ds_read_b64_tr_b16 v[128:129], v19 offset:0
v_mfma_f32_16x16x32_bf16 a[44:47], v[172:175], v[100:103], a[44:47]
v_mfma_f32_16x16x32_bf16 a[48:51], v[100:103], v[176:179], a[48:51]
ds_read_b64_tr_b16 v[130:131], v20 offset:0
s_lshl_b32 s46, s43, 2
v_mfma_f32_16x16x32_bf16 a[52:55], v[100:103], v[180:183], a[52:55]
s_add_u32 m0, s79, 2048
v_mfma_f32_16x16x32_bf16 a[56:59], v[100:103], v[184:187], a[56:59]
ds_read_b64_tr_b16 v[132:133], v19 offset:256
v_mfma_f32_16x16x32_bf16 a[60:63], v[100:103], v[188:191], a[60:63]
v_mfma_f32_16x16x32_bf16 a[64:67], v[160:163], v[104:107], a[64:67]
buffer_load_dwordx4 v23, s[52:55], s80 offen lds
ds_read_b64_tr_b16 v[134:135], v20 offset:256
s_add_u32 s46, s46, s38
v_mfma_f32_16x16x32_bf16 a[68:71], v[164:167], v[104:107], a[68:71]
v_mfma_f32_16x16x32_bf16 a[72:75], v[168:171], v[104:107], a[72:75]
ds_read_b64_tr_b16 v[136:137], v19 offset:512
v_mfma_f32_16x16x32_bf16 a[76:79], v[172:175], v[104:107], a[76:79]
s_add_u32 m0, s79, 3072
v_mfma_f32_16x16x32_bf16 a[80:83], v[104:107], v[176:179], a[80:83]
ds_read_b64_tr_b16 v[138:139], v20 offset:512
s_mul_i32 s44, s46, 5120
v_mfma_f32_16x16x32_bf16 a[84:87], v[104:107], v[180:183], a[84:87]
v_mfma_f32_16x16x32_bf16 a[88:91], v[104:107], v[184:187], a[88:91]
buffer_load_dwordx4 v24, s[52:55], s80 offen lds
ds_read_b64_tr_b16 v[140:141], v19 offset:768
v_mfma_f32_16x16x32_bf16 a[92:95], v[104:107], v[188:191], a[92:95]
v_mfma_f32_16x16x32_bf16 a[96:99], v[160:163], v[108:111], a[96:99]
ds_read_b64_tr_b16 v[142:143], v20 offset:768
s_add_u32 s79, s79, 4096
s_add_u32 s44, s44, 2560
v_mfma_f32_16x16x32_bf16 a[100:103], v[164:167], v[108:111], a[100:103]
v_mfma_f32_16x16x32_bf16 a[104:107], v[168:171], v[108:111], a[104:107]
ds_read_b64_tr_b16 v[144:145], v19 offset:2048
v_mfma_f32_16x16x32_bf16 a[108:111], v[172:175], v[108:111], a[108:111]
s_add_u32 s80, s80, 32768
v_mfma_f32_16x16x32_bf16 a[112:115], v[108:111], v[176:179], a[112:115]
ds_read_b64_tr_b16 v[146:147], v20 offset:2048
s_add_u32 s70, s44, 1280
v_mfma_f32_16x16x32_bf16 a[116:119], v[108:111], v[180:183], a[116:119]
v_mfma_f32_16x16x32_bf16 a[120:123], v[108:111], v[184:187], a[120:123]
ds_read_b64_tr_b16 v[148:149], v19 offset:2304
s_mov_b32 m0, s81
v_mfma_f32_16x16x32_bf16 a[124:127], v[108:111], v[188:191], a[124:127]
v_mfma_f32_16x16x32_bf16 a[128:131], v[160:163], v[112:115], a[128:131]
s_min_u32 s70, s70, 5242880
v_mfma_f32_16x16x32_bf16 a[132:135], v[164:167], v[112:115], a[132:135]
buffer_load_dwordx4 v25, s[56:59], s82 offen lds
ds_read_b64_tr_b16 v[150:151], v20 offset:2304
v_mfma_f32_16x16x32_bf16 a[136:139], v[168:171], v[112:115], a[136:139]
v_mfma_f32_16x16x32_bf16 a[140:143], v[172:175], v[112:115], a[140:143]
ds_read_b64_tr_b16 v[152:153], v19 offset:2560
v_mfma_f32_16x16x32_bf16 a[144:147], v[112:115], v[176:179], a[144:147]
buffer_load_dword v46, v58, s[68:71], s44 offen
s_add_u32 m0, s81, 896
v_mfma_f32_16x16x32_bf16 a[148:151], v[112:115], v[180:183], a[148:151]
ds_read_b64_tr_b16 v[154:155], v20 offset:2560
v_mfma_f32_16x16x32_bf16 a[152:155], v[112:115], v[184:187], a[152:155]
v_mfma_f32_16x16x32_bf16 a[156:159], v[112:115], v[188:191], a[156:159]
buffer_load_dwordx4 v25, s[56:59], s82 offen offset:128 lds
ds_read_b64_tr_b16 v[156:157], v19 offset:2816
v_mfma_f32_16x16x32_bf16 a[160:163], v[160:163], v[116:119], a[160:163]
s_mul_i32 s44, s46, 2176
v_mfma_f32_16x16x32_bf16 a[164:167], v[164:167], v[116:119], a[164:167]
ds_read_b64_tr_b16 v[158:159], v20 offset:2816
v_mfma_f32_16x16x32_bf16 a[168:171], v[168:171], v[116:119], a[168:171]
s_add_u32 m0, s81, 2048
v_mfma_f32_16x16x32_bf16 a[172:175], v[172:175], v[116:119], a[172:175]
ds_read_b128 v[68:71], v15 offset:2048
v_mfma_f32_16x16x32_bf16 a[176:179], v[116:119], v[176:179], a[176:179]
buffer_load_dwordx4 v25, s[60:63], s82 offen lds
s_add_u32 s44, s44, 1280
v_mfma_f32_16x16x32_bf16 a[180:183], v[116:119], v[180:183], a[180:183]
ds_read_b128 v[72:75], v15 offset:4096
v_mfma_f32_16x16x32_bf16 a[184:187], v[116:119], v[184:187], a[184:187]
v_mfma_f32_16x16x32_bf16 a[188:191], v[116:119], v[188:191], a[188:191]
ds_read_b128 v[76:79], v15 offset:6144
s_add_u32 m0, s81, 2944
v_mfma_f32_16x16x32_bf16 a[192:195], v[160:163], v[120:123], a[192:195]
s_add_u32 s74, s44, 640
v_mfma_f32_16x16x32_bf16 a[196:199], v[164:167], v[120:123], a[196:199]
ds_read_b128 v[80:83], v15 offset:8192
v_mfma_f32_16x16x32_bf16 a[200:203], v[168:171], v[120:123], a[200:203]
buffer_load_dwordx4 v25, s[60:63], s82 offen offset:128 lds
v_mfma_f32_16x16x32_bf16 a[204:207], v[172:175], v[120:123], a[204:207]
ds_read_b128 v[84:87], v15 offset:10240
v_mfma_f32_16x16x32_bf16 a[208:211], v[120:123], v[176:179], a[208:211]
s_min_u32 s74, s74, 2211840
v_mfma_f32_16x16x32_bf16 a[212:215], v[120:123], v[180:183], a[212:215]
ds_read_b128 v[88:91], v15 offset:12288
s_add_u32 s81, s81, 16384
v_mfma_f32_16x16x32_bf16 a[216:219], v[120:123], v[184:187], a[216:219]
v_mfma_f32_16x16x32_bf16 a[220:223], v[120:123], v[188:191], a[220:223]
ds_read_b128 v[92:95], v15 offset:14336
v_mfma_f32_16x16x32_bf16 a[224:227], v[160:163], v[124:127], a[224:227]
buffer_load_dword v47, v58, s[72:75], s44 offen
s_and_b32 s81, s81, 0xffff
v_mfma_f32_16x16x32_bf16 a[228:231], v[164:167], v[124:127], a[228:231]
v_mfma_f32_16x16x32_bf16 a[232:235], v[168:171], v[124:127], a[232:235]
v_mfma_f32_16x16x32_bf16 a[236:239], v[172:175], v[124:127], a[236:239]
s_add_u32 s82, s82, 262144
v_mfma_f32_16x16x32_bf16 a[240:243], v[124:127], v[176:179], a[240:243]
v_mfma_f32_16x16x32_bf16 a[244:247], v[124:127], v[180:183], a[244:247]
v_mfma_f32_16x16x32_bf16 a[248:251], v[124:127], v[184:187], a[248:251]
v_mfma_f32_16x16x32_bf16 a[252:255], v[124:127], v[188:191], a[252:255]
s_waitcnt vmcnt(20) lgkmcnt(0)
s_barrier
v_mfma_f32_16x16x32_bf16 a[0:3], v[128:131], v[64:67], a[0:3]
ds_read_b128 v[96:99], v16 offset:0
v_mfma_f32_16x16x32_bf16 a[4:7], v[132:135], v[64:67], a[4:7]
v_mfma_f32_16x16x32_bf16 a[8:11], v[136:139], v[64:67], a[8:11]
ds_read_b64_tr_b16 v[160:161], v19 offset:16384
s_mov_b32 m0, s79
v_mfma_f32_16x16x32_bf16 a[12:15], v[140:143], v[64:67], a[12:15]
v_mfma_f32_16x16x32_bf16 a[16:19], v[64:67], v[144:147], a[16:19]
buffer_load_dwordx4 v21, s[52:55], s80 offen lds
ds_read_b64_tr_b16 v[162:163], v20 offset:16384
v_mfma_f32_16x16x32_bf16 a[20:23], v[64:67], v[148:151], a[20:23]
v_mfma_f32_16x16x32_bf16 a[24:27], v[64:67], v[152:155], a[24:27]
s_add_u32 m0, s79, 1024
v_mfma_f32_16x16x32_bf16 a[28:31], v[64:67], v[156:159], a[28:31]
ds_read_b64_tr_b16 v[164:165], v19 offset:16640
v_mfma_f32_16x16x32_bf16 a[32:35], v[128:131], v[68:71], a[32:35]
buffer_load_dwordx4 v22, s[52:55], s80 offen lds
v_mfma_f32_16x16x32_bf16 a[36:39], v[132:135], v[68:71], a[36:39]
ds_read_b64_tr_b16 v[166:167], v20 offset:16640
v_mfma_f32_16x16x32_bf16 a[40:43], v[136:139], v[68:71], a[40:43]
v_mfma_f32_16x16x32_bf16 a[44:47], v[140:143], v[68:71], a[44:47]
s_add_u32 m0, s79, 2048
v_mfma_f32_16x16x32_bf16 a[48:51], v[68:71], v[144:147], a[48:51]
ds_read_b64_tr_b16 v[168:169], v19 offset:16896
v_mfma_f32_16x16x32_bf16 a[52:55], v[68:71], v[148:151], a[52:55]
buffer_load_dwordx4 v23, s[52:55], s80 offen lds
v_mfma_f32_16x16x32_bf16 a[56:59], v[68:71], v[152:155], a[56:59]
ds_read_b64_tr_b16 v[170:171], v20 offset:16896
v_mfma_f32_16x16x32_bf16 a[60:63], v[68:71], v[156:159], a[60:63]
s_add_u32 m0, s79, 3072
v_mfma_f32_16x16x32_bf16 a[64:67], v[128:131], v[72:75], a[64:67]
ds_read_b64_tr_b16 v[172:173], v19 offset:17152
v_mfma_f32_16x16x32_bf16 a[68:71], v[132:135], v[72:75], a[68:71]
v_mfma_f32_16x16x32_bf16 a[72:75], v[136:139], v[72:75], a[72:75]
buffer_load_dwordx4 v24, s[52:55], s80 offen lds
v_mfma_f32_16x16x32_bf16 a[76:79], v[140:143], v[72:75], a[76:79]
ds_read_b64_tr_b16 v[174:175], v20 offset:17152
v_mfma_f32_16x16x32_bf16 a[80:83], v[72:75], v[144:147], a[80:83]
s_add_u32 s83, s83, 32768
v_mfma_f32_16x16x32_bf16 a[84:87], v[72:75], v[148:151], a[84:87]
ds_read_b64_tr_b16 v[176:177], v19 offset:18432
v_mfma_f32_16x16x32_bf16 a[88:91], v[72:75], v[152:155], a[88:91]
s_cmp_eq_u32 s83, 98304
v_mfma_f32_16x16x32_bf16 a[92:95], v[72:75], v[156:159], a[92:95]
v_mfma_f32_16x16x32_bf16 a[96:99], v[128:131], v[76:79], a[96:99]
ds_read_b64_tr_b16 v[178:179], v20 offset:18432
v_mfma_f32_16x16x32_bf16 a[100:103], v[132:135], v[76:79], a[100:103]
s_cselect_b32 s83, 0, s83
v_mfma_f32_16x16x32_bf16 a[104:107], v[136:139], v[76:79], a[104:107]
ds_read_b64_tr_b16 v[180:181], v19 offset:18688
v_mfma_f32_16x16x32_bf16 a[108:111], v[140:143], v[76:79], a[108:111]
s_lshl_b32 s45, s38, 13
v_mfma_f32_16x16x32_bf16 a[112:115], v[76:79], v[144:147], a[112:115]
v_mfma_f32_16x16x32_bf16 a[116:119], v[76:79], v[148:151], a[116:119]
ds_read_b64_tr_b16 v[182:183], v20 offset:18688
s_add_u32 s45, s45, s83
v_mfma_f32_16x16x32_bf16 a[120:123], v[76:79], v[152:155], a[120:123]
v_mfma_f32_16x16x32_bf16 a[124:127], v[76:79], v[156:159], a[124:127]
ds_read_b64_tr_b16 v[184:185], v19 offset:18944
v_mfma_f32_16x16x32_bf16 a[128:131], v[128:131], v[80:83], a[128:131]
s_add_u32 s79, s45, 65536
v_mfma_f32_16x16x32_bf16 a[132:135], v[132:135], v[80:83], a[132:135]
ds_read_b64_tr_b16 v[186:187], v20 offset:18944
v_mfma_f32_16x16x32_bf16 a[136:139], v[136:139], v[80:83], a[136:139]
s_sub_u32 s80, s80, 32640
v_mfma_f32_16x16x32_bf16 a[140:143], v[140:143], v[80:83], a[140:143]
v_mfma_f32_16x16x32_bf16 a[144:147], v[80:83], v[144:147], a[144:147]
ds_read_b64_tr_b16 v[188:189], v19 offset:19200
s_mov_b32 m0, s81
v_mfma_f32_16x16x32_bf16 a[148:151], v[80:83], v[148:151], a[148:151]
v_mfma_f32_16x16x32_bf16 a[152:155], v[80:83], v[152:155], a[152:155]
buffer_load_dwordx4 v25, s[56:59], s82 offen lds
ds_read_b64_tr_b16 v[190:191], v20 offset:19200
v_mfma_f32_16x16x32_bf16 a[156:159], v[80:83], v[156:159], a[156:159]
v_mfma_f32_16x16x32_bf16 a[160:163], v[128:131], v[84:87], a[160:163]
v_mfma_f32_16x16x32_bf16 a[164:167], v[132:135], v[84:87], a[164:167]
ds_read_b128 v[100:103], v16 offset:2048
s_add_u32 m0, s81, 896
v_mfma_f32_16x16x32_bf16 a[168:171], v[136:139], v[84:87], a[168:171]
v_mfma_f32_16x16x32_bf16 a[172:175], v[140:143], v[84:87], a[172:175]
buffer_load_dwordx4 v25, s[56:59], s82 offen offset:128 lds
ds_read_b128 v[104:107], v16 offset:4096
v_mfma_f32_16x16x32_bf16 a[176:179], v[84:87], v[144:147], a[176:179]
v_mfma_f32_16x16x32_bf16 a[180:183], v[84:87], v[148:151], a[180:183]
ds_read_b128 v[108:111], v16 offset:6144
s_add_u32 m0, s81, 2048
v_mfma_f32_16x16x32_bf16 a[184:187], v[84:87], v[152:155], a[184:187]
v_mfma_f32_16x16x32_bf16 a[188:191], v[84:87], v[156:159], a[188:191]
v_mfma_f32_16x16x32_bf16 a[192:195], v[128:131], v[88:91], a[192:195]
buffer_load_dwordx4 v25, s[60:63], s82 offen lds
ds_read_b128 v[112:115], v16 offset:8192
v_mfma_f32_16x16x32_bf16 a[196:199], v[132:135], v[88:91], a[196:199]
v_mfma_f32_16x16x32_bf16 a[200:203], v[136:139], v[88:91], a[200:203]
ds_read_b128 v[116:119], v16 offset:10240
s_add_u32 m0, s81, 2944
v_mfma_f32_16x16x32_bf16 a[204:207], v[140:143], v[88:91], a[204:207]
v_mfma_f32_16x16x32_bf16 a[208:211], v[88:91], v[144:147], a[208:211]
buffer_load_dwordx4 v25, s[60:63], s82 offen offset:128 lds
v_mfma_f32_16x16x32_bf16 a[212:215], v[88:91], v[148:151], a[212:215]
ds_read_b128 v[120:123], v16 offset:12288
v_mfma_f32_16x16x32_bf16 a[216:219], v[88:91], v[152:155], a[216:219]
v_mfma_f32_16x16x32_bf16 a[220:223], v[88:91], v[156:159], a[220:223]
ds_read_b128 v[124:127], v16 offset:14336
s_add_u32 s81, s81, 16384
v_mfma_f32_16x16x32_bf16 a[224:227], v[128:131], v[92:95], a[224:227]
v_mfma_f32_16x16x32_bf16 a[228:231], v[132:135], v[92:95], a[228:231]
s_and_b32 s81, s81, 0xffff
v_mfma_f32_16x16x32_bf16 a[232:235], v[136:139], v[92:95], a[232:235]
v_mfma_f32_16x16x32_bf16 a[236:239], v[140:143], v[92:95], a[236:239]
s_add_u32 s82, s82, 262144
v_mfma_f32_16x16x32_bf16 a[240:243], v[92:95], v[144:147], a[240:243]
v_mfma_f32_16x16x32_bf16 a[244:247], v[92:95], v[148:151], a[244:247]
v_mfma_f32_16x16x32_bf16 a[248:251], v[92:95], v[152:155], a[248:251]
v_mfma_f32_16x16x32_bf16 a[252:255], v[92:95], v[156:159], a[252:255]
s_waitcnt vmcnt(19) lgkmcnt(0)
s_barrier
v_mfma_f32_16x16x32_bf16 a[0:3], v[160:163], v[96:99], a[0:3]
s_add_u32 s77, s77, 32768
v_mfma_f32_16x16x32_bf16 a[4:7], v[164:167], v[96:99], a[4:7]
s_cmp_eq_u32 s77, 98304
v_mfma_f32_16x16x32_bf16 a[8:11], v[168:171], v[96:99], a[8:11]
s_cselect_b32 s77, 0, s77
s_mov_b32 m0, s79
v_mfma_f32_16x16x32_bf16 a[12:15], v[172:175], v[96:99], a[12:15]
s_xor_b32 s78, s78, 32768
v_mfma_f32_16x16x32_bf16 a[16:19], v[96:99], v[176:179], a[16:19]
buffer_load_dwordx4 v21, s[52:55], s80 offen lds
v_add_u32 v15, s77, v13
v_mfma_f32_16x16x32_bf16 a[20:23], v[96:99], v[180:183], a[20:23]
v_add_u32 v19, s78, v17
v_mfma_f32_16x16x32_bf16 a[24:27], v[96:99], v[184:187], a[24:27]
v_add_u32 v20, s78, v18
v_mfma_f32_16x16x32_bf16 a[28:31], v[96:99], v[188:191], a[28:31]
v_add_u32 v16, s77, v14
s_add_u32 m0, s79, 1024
v_mfma_f32_16x16x32_bf16 a[32:35], v[160:163], v[100:103], a[32:35]
ds_read_b128 v[64:67], v15 offset:0
v_mfma_f32_16x16x32_bf16 a[36:39], v[164:167], v[100:103], a[36:39]
v_mfma_f32_16x16x32_bf16 a[40:43], v[168:171], v[100:103], a[40:43]
buffer_load_dwordx4 v22, s[52:55], s80 offen lds
ds_read_b64_tr_b16 v[128:129], v19 offset:0
v_mfma_f32_16x16x32_bf16 a[44:47], v[172:175], v[100:103], a[44:47]
v_mfma_f32_16x16x32_bf16 a[48:51], v[100:103], v[176:179], a[48:51]
ds_read_b64_tr_b16 v[130:131], v20 offset:0
s_lshl_b32 s46, s43, 2
v_mfma_f32_16x16x32_bf16 a[52:55], v[100:103], v[180:183], a[52:55]
s_add_u32 m0, s79, 2048
v_mfma_f32_16x16x32_bf16 a[56:59], v[100:103], v[184:187], a[56:59]
ds_read_b64_tr_b16 v[132:133], v19 offset:256
v_mfma_f32_16x16x32_bf16 a[60:63], v[100:103], v[188:191], a[60:63]
v_mfma_f32_16x16x32_bf16 a[64:67], v[160:163], v[104:107], a[64:67]
buffer_load_dwordx4 v23, s[52:55], s80 offen lds
ds_read_b64_tr_b16 v[134:135], v20 offset:256
s_add_u32 s46, s46, s38
v_mfma_f32_16x16x32_bf16 a[68:71], v[164:167], v[104:107], a[68:71]
v_mfma_f32_16x16x32_bf16 a[72:75], v[168:171], v[104:107], a[72:75]
ds_read_b64_tr_b16 v[136:137], v19 offset:512
v_mfma_f32_16x16x32_bf16 a[76:79], v[172:175], v[104:107], a[76:79]
s_add_u32 m0, s79, 3072
v_mfma_f32_16x16x32_bf16 a[80:83], v[104:107], v[176:179], a[80:83]
ds_read_b64_tr_b16 v[138:139], v20 offset:512
s_mul_i32 s44, s46, 5120
v_mfma_f32_16x16x32_bf16 a[84:87], v[104:107], v[180:183], a[84:87]
v_mfma_f32_16x16x32_bf16 a[88:91], v[104:107], v[184:187], a[88:91]
buffer_load_dwordx4 v24, s[52:55], s80 offen lds
ds_read_b64_tr_b16 v[140:141], v19 offset:768
v_mfma_f32_16x16x32_bf16 a[92:95], v[104:107], v[188:191], a[92:95]
v_mfma_f32_16x16x32_bf16 a[96:99], v[160:163], v[108:111], a[96:99]
ds_read_b64_tr_b16 v[142:143], v20 offset:768
s_add_u32 s79, s79, 4096
s_add_u32 s44, s44, 3840
v_mfma_f32_16x16x32_bf16 a[100:103], v[164:167], v[108:111], a[100:103]
v_mfma_f32_16x16x32_bf16 a[104:107], v[168:171], v[108:111], a[104:107]
ds_read_b64_tr_b16 v[144:145], v19 offset:2048
v_mfma_f32_16x16x32_bf16 a[108:111], v[172:175], v[108:111], a[108:111]
s_add_u32 s80, s80, 32768
v_mfma_f32_16x16x32_bf16 a[112:115], v[108:111], v[176:179], a[112:115]
ds_read_b64_tr_b16 v[146:147], v20 offset:2048
s_add_u32 s70, s44, 1280
v_mfma_f32_16x16x32_bf16 a[116:119], v[108:111], v[180:183], a[116:119]
v_mfma_f32_16x16x32_bf16 a[120:123], v[108:111], v[184:187], a[120:123]
ds_read_b64_tr_b16 v[148:149], v19 offset:2304
s_mov_b32 m0, s81
v_mfma_f32_16x16x32_bf16 a[124:127], v[108:111], v[188:191], a[124:127]
v_mfma_f32_16x16x32_bf16 a[128:131], v[160:163], v[112:115], a[128:131]
s_min_u32 s70, s70, 5242880
v_mfma_f32_16x16x32_bf16 a[132:135], v[164:167], v[112:115], a[132:135]
buffer_load_dwordx4 v25, s[56:59], s82 offen lds
ds_read_b64_tr_b16 v[150:151], v20 offset:2304
v_mfma_f32_16x16x32_bf16 a[136:139], v[168:171], v[112:115], a[136:139]
v_mfma_f32_16x16x32_bf16 a[140:143], v[172:175], v[112:115], a[140:143]
ds_read_b64_tr_b16 v[152:153], v19 offset:2560
v_mfma_f32_16x16x32_bf16 a[144:147], v[112:115], v[176:179], a[144:147]
buffer_load_dword v46, v58, s[68:71], s44 offen
s_add_u32 m0, s81, 896
v_mfma_f32_16x16x32_bf16 a[148:151], v[112:115], v[180:183], a[148:151]
ds_read_b64_tr_b16 v[154:155], v20 offset:2560
v_mfma_f32_16x16x32_bf16 a[152:155], v[112:115], v[184:187], a[152:155]
v_mfma_f32_16x16x32_bf16 a[156:159], v[112:115], v[188:191], a[156:159]
buffer_load_dwordx4 v25, s[56:59], s82 offen offset:128 lds
ds_read_b64_tr_b16 v[156:157], v19 offset:2816
v_mfma_f32_16x16x32_bf16 a[160:163], v[160:163], v[116:119], a[160:163]
s_mul_i32 s44, s46, 2176
v_mfma_f32_16x16x32_bf16 a[164:167], v[164:167], v[116:119], a[164:167]
ds_read_b64_tr_b16 v[158:159], v20 offset:2816
v_mfma_f32_16x16x32_bf16 a[168:171], v[168:171], v[116:119], a[168:171]
s_add_u32 m0, s81, 2048
v_mfma_f32_16x16x32_bf16 a[172:175], v[172:175], v[116:119], a[172:175]
ds_read_b128 v[68:71], v15 offset:2048
v_mfma_f32_16x16x32_bf16 a[176:179], v[116:119], v[176:179], a[176:179]
buffer_load_dwordx4 v25, s[60:63], s82 offen lds
s_add_u32 s44, s44, 1920
v_mfma_f32_16x16x32_bf16 a[180:183], v[116:119], v[180:183], a[180:183]
ds_read_b128 v[72:75], v15 offset:4096
v_mfma_f32_16x16x32_bf16 a[184:187], v[116:119], v[184:187], a[184:187]
v_mfma_f32_16x16x32_bf16 a[188:191], v[116:119], v[188:191], a[188:191]
ds_read_b128 v[76:79], v15 offset:6144
s_add_u32 m0, s81, 2944
v_mfma_f32_16x16x32_bf16 a[192:195], v[160:163], v[120:123], a[192:195]
s_add_u32 s74, s44, 256
v_mfma_f32_16x16x32_bf16 a[196:199], v[164:167], v[120:123], a[196:199]
ds_read_b128 v[80:83], v15 offset:8192
v_mfma_f32_16x16x32_bf16 a[200:203], v[168:171], v[120:123], a[200:203]
buffer_load_dwordx4 v25, s[60:63], s82 offen offset:128 lds
v_mfma_f32_16x16x32_bf16 a[204:207], v[172:175], v[120:123], a[204:207]
ds_read_b128 v[84:87], v15 offset:10240
v_mfma_f32_16x16x32_bf16 a[208:211], v[120:123], v[176:179], a[208:211]
s_min_u32 s74, s74, 2211840
v_mfma_f32_16x16x32_bf16 a[212:215], v[120:123], v[180:183], a[212:215]
ds_read_b128 v[88:91], v15 offset:12288
s_add_u32 s81, s81, 16384
v_mfma_f32_16x16x32_bf16 a[216:219], v[120:123], v[184:187], a[216:219]
v_mfma_f32_16x16x32_bf16 a[220:223], v[120:123], v[188:191], a[220:223]
ds_read_b128 v[92:95], v15 offset:14336
v_mfma_f32_16x16x32_bf16 a[224:227], v[160:163], v[124:127], a[224:227]
buffer_load_dword v47, v58, s[72:75], s44 offen
s_and_b32 s81, s81, 0xffff
v_mfma_f32_16x16x32_bf16 a[228:231], v[164:167], v[124:127], a[228:231]
v_mfma_f32_16x16x32_bf16 a[232:235], v[168:171], v[124:127], a[232:235]
v_mfma_f32_16x16x32_bf16 a[236:239], v[172:175], v[124:127], a[236:239]
s_add_u32 s82, s82, 262144
v_mfma_f32_16x16x32_bf16 a[240:243], v[124:127], v[176:179], a[240:243]
v_mfma_f32_16x16x32_bf16 a[244:247], v[124:127], v[180:183], a[244:247]
v_mfma_f32_16x16x32_bf16 a[248:251], v[124:127], v[184:187], a[248:251]
v_mfma_f32_16x16x32_bf16 a[252:255], v[124:127], v[188:191], a[252:255]
s_waitcnt vmcnt(19) lgkmcnt(0)
s_barrier
s_lshl_b32 s44, s43, 2
s_add_u32 s44, s44, 67911680
s_cmp_lg_u32 s38, 0
s_cbranch_scc1 L_pb1%=
v_mov_b32 v9, s42
v_mov_b32 v10, 0
buffer_store_dword v9, v10, s[48:51], s44 offen sc1
L_pb1%=:
v_mfma_f32_16x16x32_bf16 a[0:3], v[128:131], v[64:67], a[0:3]
ds_read_b128 v[96:99], v16 offset:0
v_mfma_f32_16x16x32_bf16 a[4:7], v[132:135], v[64:67], a[4:7]
v_mfma_f32_16x16x32_bf16 a[8:11], v[136:139], v[64:67], a[8:11]
ds_read_b64_tr_b16 v[160:161], v19 offset:16384
s_mov_b32 m0, s79
v_mfma_f32_16x16x32_bf16 a[12:15], v[140:143], v[64:67], a[12:15]
v_mfma_f32_16x16x32_bf16 a[16:19], v[64:67], v[144:147], a[16:19]
buffer_load_dwordx4 v21, s[52:55], s80 offen lds
ds_read_b64_tr_b16 v[162:163], v20 offset:16384
v_mfma_f32_16x16x32_bf16 a[20:23], v[64:67], v[148:151], a[20:23]
v_mfma_f32_16x16x32_bf16 a[24:27], v[64:67], v[152:155], a[24:27]
s_add_u32 m0, s79, 1024
v_mfma_f32_16x16x32_bf16 a[28:31], v[64:67], v[156:159], a[28:31]
ds_read_b64_tr_b16 v[164:165], v19 offset:16640
v_mfma_f32_16x16x32_bf16 a[32:35], v[128:131], v[68:71], a[32:35]
buffer_load_dwordx4 v22, s[52:55], s80 offen lds
v_mfma_f32_16x16x32_bf16 a[36:39], v[132:135], v[68:71], a[36:39]
ds_read_b64_tr_b16 v[166:167], v20 offset:16640
v_mfma_f32_16x16x32_bf16 a[40:43], v[136:139], v[68:71], a[40:43]
v_mfma_f32_16x16x32_bf16 a[44:47], v[140:143], v[68:71], a[44:47]
s_add_u32 m0, s79, 2048
v_mfma_f32_16x16x32_bf16 a[48:51], v[68:71], v[144:147], a[48:51]
ds_read_b64_tr_b16 v[168:169], v19 offset:16896
v_mfma_f32_16x16x32_bf16 a[52:55], v[68:71], v[148:151], a[52:55]
buffer_load_dwordx4 v23, s[52:55], s80 offen lds
v_mfma_f32_16x16x32_bf16 a[56:59], v[68:71], v[152:155], a[56:59]
ds_read_b64_tr_b16 v[170:171], v20 offset:16896
v_mfma_f32_16x16x32_bf16 a[60:63], v[68:71], v[156:159], a[60:63]
s_add_u32 m0, s79, 3072
v_mfma_f32_16x16x32_bf16 a[64:67], v[128:131], v[72:75], a[64:67]
ds_read_b64_tr_b16 v[172:173], v19 offset:17152
v_mfma_f32_16x16x32_bf16 a[68:71], v[132:135], v[72:75], a[68:71]
v_mfma_f32_16x16x32_bf16 a[72:75], v[136:139], v[72:75], a[72:75]
buffer_load_dwordx4 v24, s[52:55], s80 offen lds
v_mfma_f32_16x16x32_bf16 a[76:79], v[140:143], v[72:75], a[76:79]
ds_read_b64_tr_b16 v[174:175], v20 offset:17152
v_mfma_f32_16x16x32_bf16 a[80:83], v[72:75], v[144:147], a[80:83]
s_add_u32 s83, s83, 32768
v_mfma_f32_16x16x32_bf16 a[84:87], v[72:75], v[148:151], a[84:87]
ds_read_b64_tr_b16 v[176:177], v19 offset:18432
v_mfma_f32_16x16x32_bf16 a[88:91], v[72:75], v[152:155], a[88:91]
s_cmp_eq_u32 s83, 98304
v_mfma_f32_16x16x32_bf16 a[92:95], v[72:75], v[156:159], a[92:95]
v_mfma_f32_16x16x32_bf16 a[96:99], v[128:131], v[76:79], a[96:99]
ds_read_b64_tr_b16 v[178:179], v20 offset:18432
v_mfma_f32_16x16x32_bf16 a[100:103], v[132:135], v[76:79], a[100:103]
s_cselect_b32 s83, 0, s83
v_mfma_f32_16x16x32_bf16 a[104:107], v[136:139], v[76:79], a[104:107]
ds_read_b64_tr_b16 v[180:181], v19 offset:18688
v_mfma_f32_16x16x32_bf16 a[108:111], v[140:143], v[76:79], a[108:111]
s_lshl_b32 s45, s38, 13
v_mfma_f32_16x16x32_bf16 a[112:115], v[76:79], v[144:147], a[112:115]
v_mfma_f32_16x16x32_bf16 a[116:119], v[76:79], v[148:151], a[116:119]
ds_read_b64_tr_b16 v[182:183], v20 offset:18688
s_add_u32 s45, s45, s83
v_mfma_f32_16x16x32_bf16 a[120:123], v[76:79], v[152:155], a[120:123]
v_mfma_f32_16x16x32_bf16 a[124:127], v[76:79], v[156:159], a[124:127]
ds_read_b64_tr_b16 v[184:185], v19 offset:18944
v_mfma_f32_16x16x32_bf16 a[128:131], v[128:131], v[80:83], a[128:131]
s_add_u32 s79, s45, 65536
v_mfma_f32_16x16x32_bf16 a[132:135], v[132:135], v[80:83], a[132:135]
ds_read_b64_tr_b16 v[186:187], v20 offset:18944
v_mfma_f32_16x16x32_bf16 a[136:139], v[136:139], v[80:83], a[136:139]
s_sub_u32 s80, s80, 32640
v_mfma_f32_16x16x32_bf16 a[140:143], v[140:143], v[80:83], a[140:143]
v_mfma_f32_16x16x32_bf16 a[144:147], v[80:83], v[144:147], a[144:147]
ds_read_b64_tr_b16 v[188:189], v19 offset:19200
s_mov_b32 m0, s81
v_mfma_f32_16x16x32_bf16 a[148:151], v[80:83], v[148:151], a[148:151]
v_mfma_f32_16x16x32_bf16 a[152:155], v[80:83], v[152:155], a[152:155]
buffer_load_dwordx4 v25, s[56:59], s82 offen lds
ds_read_b64_tr_b16 v[190:191], v20 offset:19200
v_mfma_f32_16x16x32_bf16 a[156:159], v[80:83], v[156:159], a[156:159]
v_mfma_f32_16x16x32_bf16 a[160:163], v[128:131], v[84:87], a[160:163]
v_mfma_f32_16x16x32_bf16 a[164:167], v[132:135], v[84:87], a[164:167]
ds_read_b128 v[100:103], v16 offset:2048
s_add_u32 m0, s81, 896
v_mfma_f32_16x16x32_bf16 a[168:171], v[136:139], v[84:87], a[168:171]
v_mfma_f32_16x16x32_bf16 a[172:175], v[140:143], v[84:87], a[172:175]
buffer_load_dwordx4 v25, s[56:59], s82 offen offset:128 lds
ds_read_b128 v[104:107], v16 offset:4096
v_mfma_f32_16x16x32_bf16 a[176:179], v[84:87], v[144:147], a[176:179]
v_mfma_f32_16x16x32_bf16 a[180:183], v[84:87], v[148:151], a[180:183]
ds_read_b128 v[108:111], v16 offset:6144
s_add_u32 m0, s81, 2048
v_mfma_f32_16x16x32_bf16 a[184:187], v[84:87], v[152:155], a[184:187]
v_mfma_f32_16x16x32_bf16 a[188:191], v[84:87], v[156:159], a[188:191]
v_mfma_f32_16x16x32_bf16 a[192:195], v[128:131], v[88:91], a[192:195]
buffer_load_dwordx4 v25, s[60:63], s82 offen lds
ds_read_b128 v[112:115], v16 offset:8192
v_mfma_f32_16x16x32_bf16 a[196:199], v[132:135], v[88:91], a[196:199]
v_mfma_f32_16x16x32_bf16 a[200:203], v[136:139], v[88:91], a[200:203]
ds_read_b128 v[116:119], v16 offset:10240
s_add_u32 m0, s81, 2944
v_mfma_f32_16x16x32_bf16 a[204:207], v[140:143], v[88:91], a[204:207]
v_mfma_f32_16x16x32_bf16 a[208:211], v[88:91], v[144:147], a[208:211]
buffer_load_dwordx4 v25, s[60:63], s82 offen offset:128 lds
v_mfma_f32_16x16x32_bf16 a[212:215], v[88:91], v[148:151], a[212:215]
ds_read_b128 v[120:123], v16 offset:12288
v_mfma_f32_16x16x32_bf16 a[216:219], v[88:91], v[152:155], a[216:219]
v_mfma_f32_16x16x32_bf16 a[220:223], v[88:91], v[156:159], a[220:223]
ds_read_b128 v[124:127], v16 offset:14336
s_add_u32 s81, s81, 16384
v_mfma_f32_16x16x32_bf16 a[224:227], v[128:131], v[92:95], a[224:227]
v_mfma_f32_16x16x32_bf16 a[228:231], v[132:135], v[92:95], a[228:231]
s_and_b32 s81, s81, 0xffff
v_mfma_f32_16x16x32_bf16 a[232:235], v[136:139], v[92:95], a[232:235]
v_mfma_f32_16x16x32_bf16 a[236:239], v[140:143], v[92:95], a[236:239]
s_add_u32 s82, s82, 262144
v_mfma_f32_16x16x32_bf16 a[240:243], v[92:95], v[144:147], a[240:243]
v_mfma_f32_16x16x32_bf16 a[244:247], v[92:95], v[148:151], a[244:247]
v_mfma_f32_16x16x32_bf16 a[248:251], v[92:95], v[152:155], a[248:251]
v_mfma_f32_16x16x32_bf16 a[252:255], v[92:95], v[156:159], a[252:255]
s_waitcnt vmcnt(18) lgkmcnt(0)
s_barrier
v_mfma_f32_16x16x32_bf16 a[0:3], v[160:163], v[96:99], a[0:3]
s_add_u32 s77, s77, 32768
v_mfma_f32_16x16x32_bf16 a[4:7], v[164:167], v[96:99], a[4:7]
s_cmp_eq_u32 s77, 98304
v_mfma_f32_16x16x32_bf16 a[8:11], v[168:171], v[96:99], a[8:11]
s_cselect_b32 s77, 0, s77
s_mov_b32 m0, s81
v_mfma_f32_16x16x32_bf16 a[12:15], v[172:175], v[96:99], a[12:15]
s_xor_b32 s78, s78, 32768
v_mfma_f32_16x16x32_bf16 a[16:19], v[96:99], v[176:179], a[16:19]
v_add_u32 v15, s77, v13
v_mfma_f32_16x16x32_bf16 a[20:23], v[96:99], v[180:183], a[20:23]
v_add_u32 v19, s78, v17
v_mfma_f32_16x16x32_bf16 a[24:27], v[96:99], v[184:187], a[24:27]
v_add_u32 v20, s78, v18
v_mfma_f32_16x16x32_bf16 a[28:31], v[96:99], v[188:191], a[28:31]
buffer_load_dwordx4 v25, s[56:59], s82 offen lds
v_add_u32 v16, s77, v14
v_mfma_f32_16x16x32_bf16 a[32:35], v[160:163], v[100:103], a[32:35]
ds_read_b128 v[64:67], v15 offset:0
v_mfma_f32_16x16x32_bf16 a[36:39], v[164:167], v[100:103], a[36:39]
v_mfma_f32_16x16x32_bf16 a[40:43], v[168:171], v[100:103], a[40:43]
ds_read_b64_tr_b16 v[128:129], v19 offset:0
v_mfma_f32_16x16x32_bf16 a[44:47], v[172:175], v[100:103], a[44:47]
v_mfma_f32_16x16x32_bf16 a[48:51], v[100:103], v[176:179], a[48:51]
ds_read_b64_tr_b16 v[130:131], v20 offset:0
s_add_u32 m0, s81, 896
v_mfma_f32_16x16x32_bf16 a[52:55], v[100:103], v[180:183], a[52:55]
v_mfma_f32_16x16x32_bf16 a[56:59], v[100:103], v[184:187], a[56:59]
ds_read_b64_tr_b16 v[132:133], v19 offset:256
v_mfma_f32_16x16x32_bf16 a[60:63], v[100:103], v[188:191], a[60:63]
v_mfma_f32_16x16x32_bf16 a[64:67], v[160:163], v[104:107], a[64:67]
ds_read_b64_tr_b16 v[134:135], v20 offset:256
v_mfma_f32_16x16x32_bf16 a[68:71], v[164:167], v[104:107], a[68:71]
v_mfma_f32_16x16x32_bf16 a[72:75], v[168:171], v[104:107], a[72:75]
buffer_load_dwordx4 v25, s[56:59], s82 offen offset:128 lds
ds_read_b64_tr_b16 v[136:137], v19 offset:512
v_mfma_f32_16x16x32_bf16 a[76:79], v[172:175], v[104:107], a[76:79]
v_mfma_f32_16x16x32_bf16 a[80:83], v[104:107], v[176:179], a[80:83]
ds_read_b64_tr_b16 v[138:139], v20 offset:512
v_mfma_f32_16x16x32_bf16 a[84:87], v[104:107], v[180:183], a[84:87]
v_mfma_f32_16x16x32_bf16 a[88:91], v[104:107], v[184:187], a[88:91]
ds_read_b64_tr_b16 v[140:141], v19 offset:768
v_mfma_f32_16x16x32_bf16 a[92:95], v[104:107], v[188:191], a[92:95]
s_add_u32 m0, s81, 2048
v_mfma_f32_16x16x32_bf16 a[96:99], v[160:163], v[108:111], a[96:99]
ds_read_b64_tr_b16 v[142:143], v20 offset:768
v_mfma_f32_16x16x32_bf16 a[100:103], v[164:167], v[108:111], a[100:103]
v_mfma_f32_16x16x32_bf16 a[104:107], v[168:171], v[108:111], a[104:107]
ds_read_b64_tr_b16 v[144:145], v19 offset:2048
v_mfma_f32_16x16x32_bf16 a[108:111], v[172:175], v[108:111], a[108:111]
v_mfma_f32_16x16x32_bf16 a[112:115], v[108:111], v[176:179], a[112:115]
ds_read_b64_tr_b16 v[146:147], v20 offset:2048
v_mfma_f32_16x16x32_bf16 a[116:119], v[108:111], v[180:183], a[116:119]
buffer_load_dwordx4 v25, s[60:63], s82 offen lds
v_mfma_f32_16x16x32_bf16 a[120:123], v[108:111], v[184:187], a[120:123]
ds_read_b64_tr_b16 v[148:149], v19 offset:2304
v_mfma_f32_16x16x32_bf16 a[124:127], v[108:111], v[188:191], a[124:127]
v_mfma_f32_16x16x32_bf16 a[128:131], v[160:163], v[112:115], a[128:131]
v_mfma_f32_16x16x32_bf16 a[132:135], v[164:167], v[112:115], a[132:135]
ds_read_b64_tr_b16 v[150:151], v20 offset:2304
v_mfma_f32_16x16x32_bf16 a[136:139], v[168:171], v[112:115], a[136:139]
s_add_u32 m0, s81, 2944
v_mfma_f32_16x16x32_bf16 a[140:143], v[172:175], v[112:115], a[140:143]
ds_read_b64_tr_b16 v[152:153], v19 offset:2560
v_mfma_f32_16x16x32_bf16 a[144:147], v[112:115], v[176:179], a[144:147]
v_mfma_f32_16x16x32_bf16 a[148:151], v[112:115], v[180:183], a[148:151]
ds_read_b64_tr_b16 v[154:155], v20 offset:2560
v_mfma_f32_16x16x32_bf16 a[152:155], v[112:115], v[184:187], a[152:155]
v_mfma_f32_16x16x32_bf16 a[156:159], v[112:115], v[188:191], a[156:159]
ds_read_b64_tr_b16 v[156:157], v19 offset:2816
v_mfma_f32_16x16x32_bf16 a[160:163], v[160:163], v[116:119], a[160:163]
buffer_load_dwordx4 v25, s[60:63], s82 offen offset:128 lds
v_mfma_f32_16x16x32_bf16 a[164:167], v[164:167], v[116:119], a[164:167]
ds_read_b64_tr_b16 v[158:159], v20 offset:2816
v_mfma_f32_16x16x32_bf16 a[168:171], v[168:171], v[116:119], a[168:171]
v_mfma_f32_16x16x32_bf16 a[172:175], v[172:175], v[116:119], a[172:175]
ds_read_b128 v[68:71], v15 offset:2048
v_mfma_f32_16x16x32_bf16 a[176:179], v[116:119], v[176:179], a[176:179]
v_mfma_f32_16x16x32_bf16 a[180:183], v[116:119], v[180:183], a[180:183]
ds_read_b128 v[72:75], v15 offset:4096
s_add_u32 s81, s81, 16384
v_mfma_f32_16x16x32_bf16 a[184:187], v[116:119], v[184:187], a[184:187]
v_mfma_f32_16x16x32_bf16 a[188:191], v[116:119], v[188:191], a[188:191]
ds_read_b128 v[76:79], v15 offset:6144
v_mfma_f32_16x16x32_bf16 a[192:195], v[160:163], v[120:123], a[192:195]
v_mfma_f32_16x16x32_bf16 a[196:199], v[164:167], v[120:123], a[196:199]
ds_read_b128 v[80:83], v15 offset:8192
v_mfma_f32_16x16x32_bf16 a[200:203], v[168:171], v[120:123], a[200:203]
v_mfma_f32_16x16x32_bf16 a[204:207], v[172:175], v[120:123], a[204:207]
ds_read_b128 v[84:87], v15 offset:10240
s_and_b32 s81, s81, 0xffff
v_mfma_f32_16x16x32_bf16 a[208:211], v[120:123], v[176:179], a[208:211]
v_mfma_f32_16x16x32_bf16 a[212:215], v[120:123], v[180:183], a[212:215]
ds_read_b128 v[88:91], v15 offset:12288
v_mfma_f32_16x16x32_bf16 a[216:219], v[120:123], v[184:187], a[216:219]
v_mfma_f32_16x16x32_bf16 a[220:223], v[120:123], v[188:191], a[220:223]
ds_read_b128 v[92:95], v15 offset:14336
v_mfma_f32_16x16x32_bf16 a[224:227], v[160:163], v[124:127], a[224:227]
s_add_u32 s82, s82, 262144
v_mfma_f32_16x16x32_bf16 a[228:231], v[164:167], v[124:127], a[228:231]
v_mfma_f32_16x16x32_bf16 a[232:235], v[168:171], v[124:127], a[232:235]
v_mfma_f32_16x16x32_bf16 a[236:239], v[172:175], v[124:127], a[236:239]
v_mfma_f32_16x16x32_bf16 a[240:243], v[124:127], v[176:179], a[240:243]
v_mfma_f32_16x16x32_bf16 a[244:247], v[124:127], v[180:183], a[244:247]
v_mfma_f32_16x16x32_bf16 a[248:251], v[124:127], v[184:187], a[248:251]
v_mfma_f32_16x16x32_bf16 a[252:255], v[124:127], v[188:191], a[252:255]
s_waitcnt vmcnt(13) lgkmcnt(0)
s_barrier
v_mfma_f32_16x16x32_bf16 a[0:3], v[128:131], v[64:67], a[0:3]
ds_read_b128 v[96:99], v16 offset:0
v_mfma_f32_16x16x32_bf16 a[4:7], v[132:135], v[64:67], a[4:7]
s_lshr_b32 s100, s38, 1
v_mfma_f32_16x16x32_bf16 a[8:11], v[136:139], v[64:67], a[8:11]
ds_read_b64_tr_b16 v[160:161], v19 offset:16384
s_and_b32 s101, s38, 1
v_mfma_f32_16x16x32_bf16 a[12:15], v[140:143], v[64:67], a[12:15]
v_mfma_f32_16x16x32_bf16 a[16:19], v[64:67], v[144:147], a[16:19]
ds_read_b64_tr_b16 v[162:163], v20 offset:16384
s_sub_u32 s44, 15, s41
v_mfma_f32_16x16x32_bf16 a[20:23], v[64:67], v[148:151], a[20:23]
v_mfma_f32_16x16x32_bf16 a[24:27], v[64:67], v[152:155], a[24:27]
s_cmp_eq_u32 s100, 0
v_mfma_f32_16x16x32_bf16 a[28:31], v[64:67], v[156:159], a[28:31]
ds_read_b64_tr_b16 v[164:165], v19 offset:16640
s_cselect_b32 s98, s44, s41
v_mfma_f32_16x16x32_bf16 a[32:35], v[128:131], v[68:71], a[32:35]
v_mfma_f32_16x16x32_bf16 a[36:39], v[132:135], v[68:71], a[36:39]
ds_read_b64_tr_b16 v[166:167], v20 offset:16640
s_min_u32 s99, s40, 26
v_mfma_f32_16x16x32_bf16 a[40:43], v[136:139], v[68:71], a[40:43]
v_mfma_f32_16x16x32_bf16 a[44:47], v[140:143], v[68:71], a[44:47]
s_mul_i32 s44, s98, 163840
v_mfma_f32_16x16x32_bf16 a[48:51], v[68:71], v[144:147], a[48:51]
ds_read_b64_tr_b16 v[168:169], v19 offset:16896
s_add_u32 s64, s48, s44
v_mfma_f32_16x16x32_bf16 a[52:55], v[68:71], v[148:151], a[52:55]
v_mfma_f32_16x16x32_bf16 a[56:59], v[68:71], v[152:155], a[56:59]
ds_read_b64_tr_b16 v[170:171], v20 offset:16896
s_addc_u32 s65, s49, 0
v_mfma_f32_16x16x32_bf16 a[60:63], v[68:71], v[156:159], a[60:63]
v_mfma_f32_16x16x32_bf16 a[64:67], v[128:131], v[72:75], a[64:67]
ds_read_b64_tr_b16 v[172:173], v19 offset:17152
s_mov_b32 s66, 163840
v_mfma_f32_16x16x32_bf16 a[68:71], v[132:135], v[72:75], a[68:71]
s_mov_b32 s67, 0x20000
v_mfma_f32_16x16x32_bf16 a[72:75], v[136:139], v[72:75], a[72:75]
v_mfma_f32_16x16x32_bf16 a[76:79], v[140:143], v[72:75], a[76:79]
ds_read_b64_tr_b16 v[174:175], v20 offset:17152
v_lshlrev_b32 v32, 4, v8
v_mfma_f32_16x16x32_bf16 a[80:83], v[72:75], v[144:147], a[80:83]
v_mfma_f32_16x16x32_bf16 a[84:87], v[72:75], v[148:151], a[84:87]
ds_read_b64_tr_b16 v[176:177], v19 offset:18432
v_lshrrev_b32 v10, 4, v8
v_mfma_f32_16x16x32_bf16 a[88:91], v[72:75], v[152:155], a[88:91]
s_and_b32 s46, s38, 1
v_mfma_f32_16x16x32_bf16 a[92:95], v[72:75], v[156:159], a[92:95]
v_mfma_f32_16x16x32_bf16 a[96:99], v[128:131], v[76:79], a[96:99]
ds_read_b64_tr_b16 v[178:179], v20 offset:18432
s_lshl_b32 s46, s46, 2
v_mfma_f32_16x16x32_bf16 a[100:103], v[132:135], v[76:79], a[100:103]
v_mfma_f32_16x16x32_bf16 a[104:107], v[136:139], v[76:79], a[104:107]
ds_read_b64_tr_b16 v[180:181], v19 offset:18688
v_bfe_u32 v12, v8, 1, 3
v_mfma_f32_16x16x32_bf16 a[108:111], v[140:143], v[76:79], a[108:111]
v_xor_b32 v12, s46, v12
v_mfma_f32_16x16x32_bf16 a[112:115], v[76:79], v[144:147], a[112:115]
v_mfma_f32_16x16x32_bf16 a[116:119], v[76:79], v[148:151], a[116:119]
ds_read_b64_tr_b16 v[182:183], v20 offset:18688
v_and_b32 v11, 1, v8
v_mfma_f32_16x16x32_bf16 a[120:123], v[76:79], v[152:155], a[120:123]
v_mfma_f32_16x16x32_bf16 a[124:127], v[76:79], v[156:159], a[124:127]
ds_read_b64_tr_b16 v[184:185], v19 offset:18944
v_lshlrev_b32 v11, 4, v11
v_mfma_f32_16x16x32_bf16 a[128:131], v[128:131], v[80:83], a[128:131]
v_lshl_add_u32 v11, v10, 5, v11
v_mfma_f32_16x16x32_bf16 a[132:135], v[132:135], v[80:83], a[132:135]
ds_read_b64_tr_b16 v[186:187], v20 offset:18944
v_mfma_f32_16x16x32_bf16 a[136:139], v[136:139], v[80:83], a[136:139]
v_lshl_add_u32 v39, v12, 13, v11
v_mfma_f32_16x16x32_bf16 a[140:143], v[140:143], v[80:83], a[140:143]
v_mfma_f32_16x16x32_bf16 a[144:147], v[80:83], v[144:147], a[144:147]
ds_read_b64_tr_b16 v[188:189], v19 offset:19200
v_add_u32 v40, 128, v39
v_mfma_f32_16x16x32_bf16 a[148:151], v[80:83], v[148:151], a[148:151]
s_mov_b32 s45, 3456
v_mfma_f32_16x16x32_bf16 a[152:155], v[80:83], v[152:155], a[152:155]
ds_read_b64_tr_b16 v[190:191], v20 offset:19200
v_mfma_f32_16x16x32_bf16 a[156:159], v[80:83], v[156:159], a[156:159]
v_mul_lo_u32 v12, s45, v12
v_mfma_f32_16x16x32_bf16 a[160:163], v[128:131], v[84:87], a[160:163]
v_mfma_f32_16x16x32_bf16 a[164:167], v[132:135], v[84:87], a[164:167]
ds_read_b128 v[100:103], v16 offset:2048
v_add_u32 v41, v12, v11
v_mfma_f32_16x16x32_bf16 a[168:171], v[136:139], v[84:87], a[168:171]
v_and_b32 v9, 15, v8
v_mfma_f32_16x16x32_bf16 a[172:175], v[140:143], v[84:87], a[172:175]
ds_read_b128 v[104:107], v16 offset:4096
v_mfma_f32_16x16x32_bf16 a[176:179], v[84:87], v[144:147], a[176:179]
v_lshrrev_b32 v12, 3, v9
v_mfma_f32_16x16x32_bf16 a[180:183], v[84:87], v[148:151], a[180:183]
ds_read_b128 v[108:111], v16 offset:6144
v_mfma_f32_16x16x32_bf16 a[184:187], v[84:87], v[152:155], a[184:187]
v_lshrrev_b32 v11, 2, v9
v_mfma_f32_16x16x32_bf16 a[188:191], v[84:87], v[156:159], a[188:191]
v_xor_b32 v11, v11, v12
v_mfma_f32_16x16x32_bf16 a[192:195], v[128:131], v[88:91], a[192:195]
ds_read_b128 v[112:115], v16 offset:8192
v_mfma_f32_16x16x32_bf16 a[196:199], v[132:135], v[88:91], a[196:199]
v_and_b32 v11, 1, v11
v_mfma_f32_16x16x32_bf16 a[200:203], v[136:139], v[88:91], a[200:203]
ds_read_b128 v[116:119], v16 offset:10240
v_mfma_f32_16x16x32_bf16 a[204:207], v[140:143], v[88:91], a[204:207]
v_lshl_add_u32 v11, v11, 1, v12
v_mfma_f32_16x16x32_bf16 a[208:211], v[88:91], v[144:147], a[208:211]
v_xor_b32 v11, v11, v10
v_mfma_f32_16x16x32_bf16 a[212:215], v[88:91], v[148:151], a[212:215]
ds_read_b128 v[120:123], v16 offset:12288
v_mfma_f32_16x16x32_bf16 a[216:219], v[88:91], v[152:155], a[216:219]
v_lshlrev_b32 v9, 6, v9
v_mfma_f32_16x16x32_bf16 a[220:223], v[88:91], v[156:159], a[220:223]
ds_read_b128 v[124:127], v16 offset:14336
v_mfma_f32_16x16x32_bf16 a[224:227], v[128:131], v[92:95], a[224:227]
v_lshl_add_u32 v33, v11, 4, v9
v_mfma_f32_16x16x32_bf16 a[228:231], v[132:135], v[92:95], a[228:231]
s_lshl_b32 s44, s100, 13
v_mfma_f32_16x16x32_bf16 a[232:235], v[136:139], v[92:95], a[232:235]
v_mfma_f32_16x16x32_bf16 a[236:239], v[140:143], v[92:95], a[236:239]
v_add_u32 v33, s44, v33
v_mfma_f32_16x16x32_bf16 a[240:243], v[92:95], v[144:147], a[240:243]
v_mfma_f32_16x16x32_bf16 a[244:247], v[92:95], v[148:151], a[244:247]
v_bfe_u32 v9, v8, 2, 2
v_mfma_f32_16x16x32_bf16 a[248:251], v[92:95], v[152:155], a[248:251]
v_mfma_f32_16x16x32_bf16 a[252:255], v[92:95], v[156:159], a[252:255]
s_waitcnt vmcnt(4) lgkmcnt(0)
s_barrier
v_mfma_f32_16x16x32_bf16 a[0:3], v[160:163], v[96:99], a[0:3]
s_add_u32 s77, s77, 32768
v_mfma_f32_16x16x32_bf16 a[4:7], v[164:167], v[96:99], a[4:7]
s_cmp_eq_u32 s77, 98304
v_mfma_f32_16x16x32_bf16 a[8:11], v[168:171], v[96:99], a[8:11]
s_cselect_b32 s77, 0, s77
v_mfma_f32_16x16x32_bf16 a[12:15], v[172:175], v[96:99], a[12:15]
s_xor_b32 s78, s78, 32768
v_mfma_f32_16x16x32_bf16 a[16:19], v[96:99], v[176:179], a[16:19]
v_add_u32 v15, s77, v13
v_mfma_f32_16x16x32_bf16 a[20:23], v[96:99], v[180:183], a[20:23]
v_add_u32 v19, s78, v17
v_mfma_f32_16x16x32_bf16 a[24:27], v[96:99], v[184:187], a[24:27]
v_add_u32 v20, s78, v18
v_mfma_f32_16x16x32_bf16 a[28:31], v[96:99], v[188:191], a[28:31]
v_add_u32 v16, s77, v14
v_mfma_f32_16x16x32_bf16 a[32:35], v[160:163], v[100:103], a[32:35]
ds_read_b128 v[64:67], v15 offset:0
v_mfma_f32_16x16x32_bf16 a[36:39], v[164:167], v[100:103], a[36:39]
v_mfma_f32_16x16x32_bf16 a[40:43], v[168:171], v[100:103], a[40:43]
ds_read_b64_tr_b16 v[128:129], v19 offset:0
v_and_b32 v12, 1, v10
v_mfma_f32_16x16x32_bf16 a[44:47], v[172:175], v[100:103], a[44:47]
v_lshl_add_u32 v12, v12, 2, v9
v_mfma_f32_16x16x32_bf16 a[48:51], v[100:103], v[176:179], a[48:51]
ds_read_b64_tr_b16 v[130:131], v20 offset:0
v_lshlrev_b32 v12, 5, v12
v_mfma_f32_16x16x32_bf16 a[52:55], v[100:103], v[180:183], a[52:55]
v_mfma_f32_16x16x32_bf16 a[56:59], v[100:103], v[184:187], a[56:59]
ds_read_b64_tr_b16 v[132:133], v19 offset:256
s_mov_b32 s45, 3072
v_mfma_f32_16x16x32_bf16 a[60:63], v[100:103], v[188:191], a[60:63]
v_mul_lo_u32 v35, s45, v10
v_mfma_f32_16x16x32_bf16 a[64:67], v[160:163], v[104:107], a[64:67]
ds_read_b64_tr_b16 v[134:135], v20 offset:256
v_mfma_f32_16x16x32_bf16 a[68:71], v[164:167], v[104:107], a[68:71]
v_add_u32 v35, v35, v12
v_mfma_f32_16x16x32_bf16 a[72:75], v[168:171], v[104:107], a[72:75]
ds_read_b64_tr_b16 v[136:137], v19 offset:512
v_and_b32 v11, 3, v8
v_mfma_f32_16x16x32_bf16 a[76:79], v[172:175], v[104:107], a[76:79]
v_mfma_f32_16x16x32_bf16 a[80:83], v[104:107], v[176:179], a[80:83]
ds_read_b64_tr_b16 v[138:139], v20 offset:512
v_lshl_add_u32 v35, v11, 3, v35
v_mfma_f32_16x16x32_bf16 a[84:87], v[104:107], v[180:183], a[84:87]
s_lshl_b32 s44, s101, 9
v_mfma_f32_16x16x32_bf16 a[88:91], v[104:107], v[184:187], a[88:91]
ds_read_b64_tr_b16 v[140:141], v19 offset:768
s_add_u32 s44, s44, 16384
v_mfma_f32_16x16x32_bf16 a[92:95], v[104:107], v[188:191], a[92:95]
v_mfma_f32_16x16x32_bf16 a[96:99], v[160:163], v[108:111], a[96:99]
ds_read_b64_tr_b16 v[142:143], v20 offset:768
v_add_u32 v35, s44, v35
v_mfma_f32_16x16x32_bf16 a[100:103], v[164:167], v[108:111], a[100:103]
v_xor_b32 v36, 128, v35
v_mfma_f32_16x16x32_bf16 a[104:107], v[168:171], v[108:111], a[104:107]
ds_read_b64_tr_b16 v[144:145], v19 offset:2048
v_mfma_f32_16x16x32_bf16 a[108:111], v[172:175], v[108:111], a[108:111]
v_mov_b32 v44, 0x3f803f80
v_mfma_f32_16x16x32_bf16 a[112:115], v[108:111], v[176:179], a[112:115]
ds_read_b64_tr_b16 v[146:147], v20 offset:2048
v_mov_b32 v45, 0x3f803f80
v_mfma_f32_16x16x32_bf16 a[116:119], v[108:111], v[180:183], a[116:119]
v_mfma_f32_16x16x32_bf16 a[120:123], v[108:111], v[184:187], a[120:123]
ds_read_b64_tr_b16 v[148:149], v19 offset:2304
v_mov_b32 v46, 0x3f803f80
v_mfma_f32_16x16x32_bf16 a[124:127], v[108:111], v[188:191], a[124:127]
v_mov_b32 v47, 0x3f803f80
v_mfma_f32_16x16x32_bf16 a[128:131], v[160:163], v[112:115], a[128:131]
s_lshl_b32 s89, s101, 12
v_mfma_f32_16x16x32_bf16 a[132:135], v[164:167], v[112:115], a[132:135]
ds_read_b64_tr_b16 v[150:151], v20 offset:2304
v_mfma_f32_16x16x32_bf16 a[136:139], v[168:171], v[112:115], a[136:139]
s_lshl_b32 s90, s38, 16
v_mfma_f32_16x16x32_bf16 a[140:143], v[172:175], v[112:115], a[140:143]
ds_read_b64_tr_b16 v[152:153], v19 offset:2560
s_mul_i32 s91, s38, 27648
v_mfma_f32_16x16x32_bf16 a[144:147], v[112:115], v[176:179], a[144:147]
v_mfma_f32_16x16x32_bf16 a[148:151], v[112:115], v[180:183], a[148:151]
ds_read_b64_tr_b16 v[154:155], v20 offset:2560
s_lshl_b32 s94, s38, 12
v_mfma_f32_16x16x32_bf16 a[152:155], v[112:115], v[184:187], a[152:155]
s_mul_i32 s44, s38, 3072
v_mfma_f32_16x16x32_bf16 a[156:159], v[112:115], v[188:191], a[156:159]
ds_read_b64_tr_b16 v[156:157], v19 offset:2816
v_mfma_f32_16x16x32_bf16 a[160:163], v[160:163], v[116:119], a[160:163]
s_add_u32 s95, s44, 16384
v_mfma_f32_16x16x32_bf16 a[164:167], v[164:167], v[116:119], a[164:167]
ds_read_b64_tr_b16 v[158:159], v20 offset:2816
s_mov_b32 s92, 0
v_mfma_f32_16x16x32_bf16 a[168:171], v[168:171], v[116:119], a[168:171]
s_mov_b32 s96, s94
v_mfma_f32_16x16x32_bf16 a[172:175], v[172:175], v[116:119], a[172:175]
ds_read_b128 v[68:71], v15 offset:2048
v_mfma_f32_16x16x32_bf16 a[176:179], v[116:119], v[176:179], a[176:179]
s_mov_b32 s97, s95
v_mfma_f32_16x16x32_bf16 a[180:183], v[116:119], v[180:183], a[180:183]
ds_read_b128 v[72:75], v15 offset:4096
s_lshl_b32 s44, s40, 8
v_mfma_f32_16x16x32_bf16 a[184:187], v[116:119], v[184:187], a[184:187]
v_mfma_f32_16x16x32_bf16 a[188:191], v[116:119], v[188:191], a[188:191]
ds_read_b128 v[76:79], v15 offset:6144
s_add_u32 s68, s68, s44
v_mfma_f32_16x16x32_bf16 a[192:195], v[160:163], v[120:123], a[192:195]
s_addc_u32 s69, s69, 0
v_mfma_f32_16x16x32_bf16 a[196:199], v[164:167], v[120:123], a[196:199]
ds_read_b128 v[80:83], v15 offset:8192
v_mfma_f32_16x16x32_bf16 a[200:203], v[168:171], v[120:123], a[200:203]
s_and_b32 s69, s69, 0xffff
v_mfma_f32_16x16x32_bf16 a[204:207], v[172:175], v[120:123], a[204:207]
ds_read_b128 v[84:87], v15 offset:10240
s_mov_b32 s71, 0x20000
v_mfma_f32_16x16x32_bf16 a[208:211], v[120:123], v[176:179], a[208:211]
s_sub_u32 s70, 5242880, s44
v_mfma_f32_16x16x32_bf16 a[212:215], v[120:123], v[180:183], a[212:215]
ds_read_b128 v[88:91], v15 offset:12288
v_mfma_f32_16x16x32_bf16 a[216:219], v[120:123], v[184:187], a[216:219]
s_lshl_b32 s44, s99, 7
v_mfma_f32_16x16x32_bf16 a[220:223], v[120:123], v[188:191], a[220:223]
ds_read_b128 v[92:95], v15 offset:14336
s_add_u32 s72, s72, s44
v_mfma_f32_16x16x32_bf16 a[224:227], v[160:163], v[124:127], a[224:227]
v_mfma_f32_16x16x32_bf16 a[228:231], v[164:167], v[124:127], a[228:231]
s_addc_u32 s73, s73, 0
v_mfma_f32_16x16x32_bf16 a[232:235], v[168:171], v[124:127], a[232:235]
s_and_b32 s73, s73, 0xffff
v_mfma_f32_16x16x32_bf16 a[236:239], v[172:175], v[124:127], a[236:239]
v_mfma_f32_16x16x32_bf16 a[240:243], v[124:127], v[176:179], a[240:243]
s_mov_b32 s75, 0x20000
v_mfma_f32_16x16x32_bf16 a[244:247], v[124:127], v[180:183], a[244:247]
s_sub_u32 s74, 2211840, s44
v_mfma_f32_16x16x32_bf16 a[248:251], v[124:127], v[184:187], a[248:251]
v_mfma_f32_16x16x32_bf16 a[252:255], v[124:127], v[188:191], a[252:255]
s_waitcnt vmcnt(0) lgkmcnt(0)
s_sub_u32 s44, 15, s41
s_lshl_b32 s44, s44, 6
s_lshl_b32 s45, s41, 6
v_and_b32 v9, 15, v8
v_lshlrev_b32 v9, 2, v9
v_mov_b32 v11, s44
v_mov_b32 v12, s45
v_cmp_gt_u32 vcc, 32, v8
s_nop 1
v_cndmask_b32 v11, v12, v11, vcc
v_add_u32 v9, v9, v11
s_mov_b32 s44, 67911680
s_cmp_lg_u32 s38, 0
s_cbranch_scc1 L_pd2%=
s_mov_b32 s45, 3000000
L_pl2%=:
buffer_load_dword v10, v9, s[48:51], s44 offen sc1
s_waitcnt vmcnt(0)
v_cmp_ne_u32 vcc, s42, v10
s_nop 3
s_cmp_eq_u64 vcc, 0
s_cbranch_scc1 L_pd2%=
s_sleep 1
s_sub_u32 s45, s45, 1
s_cmp_lg_u32 s45, 0
s_cbranch_scc1 L_pl2%=
L_pd2%=:
s_barrier
v_mfma_f32_16x16x32_bf16 a[0:3], v[128:131], v[64:67], a[0:3]
ds_read_b128 v[96:99], v16 offset:0
v_mfma_f32_16x16x32_bf16 a[4:7], v[132:135], v[64:67], a[4:7]
v_mfma_f32_16x16x32_bf16 a[8:11], v[136:139], v[64:67], a[8:11]
ds_read_b64_tr_b16 v[160:161], v19 offset:16384
v_mfma_f32_16x16x32_bf16 a[12:15], v[140:143], v[64:67], a[12:15]
v_mfma_f32_16x16x32_bf16 a[16:19], v[64:67], v[144:147], a[16:19]
ds_read_b64_tr_b16 v[162:163], v20 offset:16384
v_mfma_f32_16x16x32_bf16 a[20:23], v[64:67], v[148:151], a[20:23]
v_mfma_f32_16x16x32_bf16 a[24:27], v[64:67], v[152:155], a[24:27]
v_mfma_f32_16x16x32_bf16 a[28:31], v[64:67], v[156:159], a[28:31]
ds_read_b64_tr_b16 v[164:165], v19 offset:16640
v_mfma_f32_16x16x32_bf16 a[32:35], v[128:131], v[68:71], a[32:35]
v_mfma_f32_16x16x32_bf16 a[36:39], v[132:135], v[68:71], a[36:39]
ds_read_b64_tr_b16 v[166:167], v20 offset:16640
v_mfma_f32_16x16x32_bf16 a[40:43], v[136:139], v[68:71], a[40:43]
v_mfma_f32_16x16x32_bf16 a[44:47], v[140:143], v[68:71], a[44:47]
v_mfma_f32_16x16x32_bf16 a[48:51], v[68:71], v[144:147], a[48:51]
ds_read_b64_tr_b16 v[168:169], v19 offset:16896
v_mfma_f32_16x16x32_bf16 a[52:55], v[68:71], v[148:151], a[52:55]
v_mfma_f32_16x16x32_bf16 a[56:59], v[68:71], v[152:155], a[56:59]
ds_read_b64_tr_b16 v[170:171], v20 offset:16896
v_mfma_f32_16x16x32_bf16 a[60:63], v[68:71], v[156:159], a[60:63]
v_mfma_f32_16x16x32_bf16 a[64:67], v[128:131], v[72:75], a[64:67]
ds_read_b64_tr_b16 v[172:173], v19 offset:17152
v_mfma_f32_16x16x32_bf16 a[68:71], v[132:135], v[72:75], a[68:71]
v_mfma_f32_16x16x32_bf16 a[72:75], v[136:139], v[72:75], a[72:75]
v_mfma_f32_16x16x32_bf16 a[76:79], v[140:143], v[72:75], a[76:79]
ds_read_b64_tr_b16 v[174:175], v20 offset:17152
v_mfma_f32_16x16x32_bf16 a[80:83], v[72:75], v[144:147], a[80:83]
v_mfma_f32_16x16x32_bf16 a[84:87], v[72:75], v[148:151], a[84:87]
ds_read_b64_tr_b16 v[176:177], v19 offset:18432
v_mfma_f32_16x16x32_bf16 a[88:91], v[72:75], v[152:155], a[88:91]
v_mfma_f32_16x16x32_bf16 a[92:95], v[72:75], v[156:159], a[92:95]
v_mfma_f32_16x16x32_bf16 a[96:99], v[128:131], v[76:79], a[96:99]
ds_read_b64_tr_b16 v[178:179], v20 offset:18432
v_mfma_f32_16x16x32_bf16 a[100:103], v[132:135], v[76:79], a[100:103]
v_mfma_f32_16x16x32_bf16 a[104:107], v[136:139], v[76:79], a[104:107]
ds_read_b64_tr_b16 v[180:181], v19 offset:18688
v_mfma_f32_16x16x32_bf16 a[108:111], v[140:143], v[76:79], a[108:111]
v_mfma_f32_16x16x32_bf16 a[112:115], v[76:79], v[144:147], a[112:115]
v_mfma_f32_16x16x32_bf16 a[116:119], v[76:79], v[148:151], a[116:119]
ds_read_b64_tr_b16 v[182:183], v20 offset:18688
v_mfma_f32_16x16x32_bf16 a[120:123], v[76:79], v[152:155], a[120:123]
v_mfma_f32_16x16x32_bf16 a[124:127], v[76:79], v[156:159], a[124:127]
ds_read_b64_tr_b16 v[184:185], v19 offset:18944
v_mfma_f32_16x16x32_bf16 a[128:131], v[128:131], v[80:83], a[128:131]
v_mfma_f32_16x16x32_bf16 a[132:135], v[132:135], v[80:83], a[132:135]
ds_read_b64_tr_b16 v[186:187], v20 offset:18944
v_mfma_f32_16x16x32_bf16 a[136:139], v[136:139], v[80:83], a[136:139]
v_mfma_f32_16x16x32_bf16 a[140:143], v[140:143], v[80:83], a[140:143]
v_mfma_f32_16x16x32_bf16 a[144:147], v[80:83], v[144:147], a[144:147]
ds_read_b64_tr_b16 v[188:189], v19 offset:19200
v_mfma_f32_16x16x32_bf16 a[148:151], v[80:83], v[148:151], a[148:151]
v_mfma_f32_16x16x32_bf16 a[152:155], v[80:83], v[152:155], a[152:155]
ds_read_b64_tr_b16 v[190:191], v20 offset:19200
v_mfma_f32_16x16x32_bf16 a[156:159], v[80:83], v[156:159], a[156:159]
v_mfma_f32_16x16x32_bf16 a[160:163], v[128:131], v[84:87], a[160:163]
v_mfma_f32_16x16x32_bf16 a[164:167], v[132:135], v[84:87], a[164:167]
ds_read_b128 v[100:103], v16 offset:2048
v_mfma_f32_16x16x32_bf16 a[168:171], v[136:139], v[84:87], a[168:171]
v_mfma_f32_16x16x32_bf16 a[172:175], v[140:143], v[84:87], a[172:175]
ds_read_b128 v[104:107], v16 offset:4096
v_mfma_f32_16x16x32_bf16 a[176:179], v[84:87], v[144:147], a[176:179]
v_mfma_f32_16x16x32_bf16 a[180:183], v[84:87], v[148:151], a[180:183]
ds_read_b128 v[108:111], v16 offset:6144
v_mfma_f32_16x16x32_bf16 a[184:187], v[84:87], v[152:155], a[184:187]
v_mfma_f32_16x16x32_bf16 a[188:191], v[84:87], v[156:159], a[188:191]
v_mfma_f32_16x16x32_bf16 a[192:195], v[128:131], v[88:91], a[192:195]
ds_read_b128 v[112:115], v16 offset:8192
v_mfma_f32_16x16x32_bf16 a[196:199], v[132:135], v[88:91], a[196:199]
v_mfma_f32_16x16x32_bf16 a[200:203], v[136:139], v[88:91], a[200:203]
ds_read_b128 v[116:119], v16 offset:10240
v_mfma_f32_16x16x32_bf16 a[204:207], v[140:143], v[88:91], a[204:207]
v_mfma_f32_16x16x32_bf16 a[208:211], v[88:91], v[144:147], a[208:211]
v_mfma_f32_16x16x32_bf16 a[212:215], v[88:91], v[148:151], a[212:215]
ds_read_b128 v[120:123], v16 offset:12288
v_mfma_f32_16x16x32_bf16 a[216:219], v[88:91], v[152:155], a[216:219]
v_mfma_f32_16x16x32_bf16 a[220:223], v[88:91], v[156:159], a[220:223]
ds_read_b128 v[124:127], v16 offset:14336
v_mfma_f32_16x16x32_bf16 a[224:227], v[128:131], v[92:95], a[224:227]
v_mfma_f32_16x16x32_bf16 a[228:231], v[132:135], v[92:95], a[228:231]
v_mfma_f32_16x16x32_bf16 a[232:235], v[136:139], v[92:95], a[232:235]
v_mfma_f32_16x16x32_bf16 a[236:239], v[140:143], v[92:95], a[236:239]
v_mfma_f32_16x16x32_bf16 a[240:243], v[92:95], v[144:147], a[240:243]
v_mfma_f32_16x16x32_bf16 a[244:247], v[92:95], v[148:151], a[244:247]
v_mfma_f32_16x16x32_bf16 a[248:251], v[92:95], v[152:155], a[248:251]
v_mfma_f32_16x16x32_bf16 a[252:255], v[92:95], v[156:159], a[252:255]
s_waitcnt lgkmcnt(0)
s_barrier
s_mov_b32 m0, s96
v_mfma_f32_16x16x32_bf16 a[0:3], v[160:163], v[96:99], a[0:3]
buffer_load_dwordx4 v32, s[64:67], s89 offen sc1 lds
v_mfma_f32_16x16x32_bf16 a[4:7], v[164:167], v[96:99], a[4:7]
buffer_load_dwordx4 v32, s[64:67], s89 offen offset:1024 sc1 lds
v_mfma_f32_16x16x32_bf16 a[8:11], v[168:171], v[96:99], a[8:11]
buffer_load_dwordx4 v32, s[64:67], s89 offen offset:2048 sc1 lds
buffer_load_dwordx4 v32, s[64:67], s89 offen offset:3072 sc1 lds
v_mfma_f32_16x16x32_bf16 a[12:15], v[172:175], v[96:99], a[12:15]
s_mov_b32 m0, s97
v_mfma_f32_16x16x32_bf16 a[16:19], v[96:99], v[176:179], a[16:19]
buffer_load_dwordx4 v39, s[68:71], s90 offen lds
s_add_u32 m0, s97, 1024
v_mfma_f32_16x16x32_bf16 a[20:23], v[96:99], v[180:183], a[20:23]
buffer_load_dwordx4 v40, s[68:71], s90 offen lds
s_add_u32 m0, s97, 2048
v_mfma_f32_16x16x32_bf16 a[24:27], v[96:99], v[184:187], a[24:27]
buffer_load_dwordx4 v41, s[72:75], s91 offen lds
v_mfma_f32_16x16x32_bf16 a[28:31], v[96:99], v[188:191], a[28:31]
s_add_u32 s89, s89, 8192
v_mfma_f32_16x16x32_bf16 a[32:35], v[160:163], v[100:103], a[32:35]
s_add_u32 s90, s90, 262144
s_add_u32 s91, s91, 110592
v_mfma_f32_16x16x32_bf16 a[36:39], v[164:167], v[100:103], a[36:39]
s_add_u32 s92, s92, 28672
v_mfma_f32_16x16x32_bf16 a[40:43], v[168:171], v[100:103], a[40:43]
s_cmp_eq_u32 s92, 143360
s_cselect_b32 s92, 0, s92
v_mfma_f32_16x16x32_bf16 a[44:47], v[172:175], v[100:103], a[44:47]
s_add_u32 s96, s92, s94
v_mfma_f32_16x16x32_bf16 a[48:51], v[100:103], v[176:179], a[48:51]
s_add_u32 s97, s92, s95
s_mov_b32 m0, s96
v_mfma_f32_16x16x32_bf16 a[52:55], v[100:103], v[180:183], a[52:55]
buffer_load_dwordx4 v32, s[64:67], s89 offen sc1 lds
v_mfma_f32_16x16x32_bf16 a[56:59], v[100:103], v[184:187], a[56:59]
buffer_load_dwordx4 v32, s[64:67], s89 offen offset:1024 sc1 lds
buffer_load_dwordx4 v32, s[64:67], s89 offen offset:2048 sc1 lds
v_mfma_f32_16x16x32_bf16 a[60:63], v[100:103], v[188:191], a[60:63]
buffer_load_dwordx4 v32, s[64:67], s89 offen offset:3072 sc1 lds
s_mov_b32 m0, s97
v_mfma_f32_16x16x32_bf16 a[64:67], v[160:163], v[104:107], a[64:67]
buffer_load_dwordx4 v39, s[68:71], s90 offen lds
v_mfma_f32_16x16x32_bf16 a[68:71], v[164:167], v[104:107], a[68:71]
s_add_u32 m0, s97, 1024
v_mfma_f32_16x16x32_bf16 a[72:75], v[168:171], v[104:107], a[72:75]
buffer_load_dwordx4 v40, s[68:71], s90 offen lds
s_add_u32 m0, s97, 2048
v_mfma_f32_16x16x32_bf16 a[76:79], v[172:175], v[104:107], a[76:79]
buffer_load_dwordx4 v41, s[72:75], s91 offen lds
v_mfma_f32_16x16x32_bf16 a[80:83], v[104:107], v[176:179], a[80:83]
s_add_u32 s89, s89, 8192
s_add_u32 s90, s90, 262144
v_mfma_f32_16x16x32_bf16 a[84:87], v[104:107], v[180:183], a[84:87]
s_add_u32 s91, s91, 110592
v_mfma_f32_16x16x32_bf16 a[88:91], v[104:107], v[184:187], a[88:91]
s_add_u32 s92, s92, 28672
s_cmp_eq_u32 s92, 143360
v_mfma_f32_16x16x32_bf16 a[92:95], v[104:107], v[188:191], a[92:95]
s_cselect_b32 s92, 0, s92
v_mfma_f32_16x16x32_bf16 a[96:99], v[160:163], v[108:111], a[96:99]
s_add_u32 s96, s92, s94
s_add_u32 s97, s92, s95
v_mfma_f32_16x16x32_bf16 a[100:103], v[164:167], v[108:111], a[100:103]
s_mov_b32 m0, s96
v_mfma_f32_16x16x32_bf16 a[104:107], v[168:171], v[108:111], a[104:107]
buffer_load_dwordx4 v32, s[64:67], s89 offen sc1 lds
buffer_load_dwordx4 v32, s[64:67], s89 offen offset:1024 sc1 lds
v_mfma_f32_16x16x32_bf16 a[108:111], v[172:175], v[108:111], a[108:111]
buffer_load_dwordx4 v32, s[64:67], s89 offen offset:2048 sc1 lds
v_mfma_f32_16x16x32_bf16 a[112:115], v[108:111], v[176:179], a[112:115]
buffer_load_dwordx4 v32, s[64:67], s89 offen offset:3072 sc1 lds
s_mov_b32 m0, s97
v_mfma_f32_16x16x32_bf16 a[116:119], v[108:111], v[180:183], a[116:119]
buffer_load_dwordx4 v39, s[68:71], s90 offen lds
s_add_u32 m0, s97, 1024
v_mfma_f32_16x16x32_bf16 a[120:123], v[108:111], v[184:187], a[120:123]
buffer_load_dwordx4 v40, s[68:71], s90 offen lds
v_mfma_f32_16x16x32_bf16 a[124:127], v[108:111], v[188:191], a[124:127]
s_add_u32 m0, s97, 2048
v_mfma_f32_16x16x32_bf16 a[128:131], v[160:163], v[112:115], a[128:131]
buffer_load_dwordx4 v41, s[72:75], s91 offen lds
v_mfma_f32_16x16x32_bf16 a[132:135], v[164:167], v[112:115], a[132:135]
s_add_u32 s89, s89, 8192
s_add_u32 s90, s90, 262144
v_mfma_f32_16x16x32_bf16 a[136:139], v[168:171], v[112:115], a[136:139]
s_add_u32 s91, s91, 110592
v_mfma_f32_16x16x32_bf16 a[140:143], v[172:175], v[112:115], a[140:143]
s_add_u32 s92, s92, 28672
s_cmp_eq_u32 s92, 143360
v_mfma_f32_16x16x32_bf16 a[144:147], v[112:115], v[176:179], a[144:147]
s_cselect_b32 s92, 0, s92
v_mfma_f32_16x16x32_bf16 a[148:151], v[112:115], v[180:183], a[148:151]
s_add_u32 s96, s92, s94
s_add_u32 s97, s92, s95
v_mfma_f32_16x16x32_bf16 a[152:155], v[112:115], v[184:187], a[152:155]
s_mov_b32 m0, s96
v_mfma_f32_16x16x32_bf16 a[156:159], v[112:115], v[188:191], a[156:159]
buffer_load_dwordx4 v32, s[64:67], s89 offen sc1 lds
buffer_load_dwordx4 v32, s[64:67], s89 offen offset:1024 sc1 lds
v_mfma_f32_16x16x32_bf16 a[160:163], v[160:163], v[116:119], a[160:163]
buffer_load_dwordx4 v32, s[64:67], s89 offen offset:2048 sc1 lds
v_mfma_f32_16x16x32_bf16 a[164:167], v[164:167], v[116:119], a[164:167]
buffer_load_dwordx4 v32, s[64:67], s89 offen offset:3072 sc1 lds
s_mov_b32 m0, s97
v_mfma_f32_16x16x32_bf16 a[168:171], v[168:171], v[116:119], a[168:171]
buffer_load_dwordx4 v39, s[68:71], s90 offen lds
s_add_u32 m0, s97, 1024
v_mfma_f32_16x16x32_bf16 a[172:175], v[172:175], v[116:119], a[172:175]
buffer_load_dwordx4 v40, s[68:71], s90 offen lds
v_mfma_f32_16x16x32_bf16 a[176:179], v[116:119], v[176:179], a[176:179]
s_add_u32 m0, s97, 2048
v_mfma_f32_16x16x32_bf16 a[180:183], v[116:119], v[180:183], a[180:183]
buffer_load_dwordx4 v41, s[72:75], s91 offen lds
s_add_u32 s89, s89, 8192
v_mfma_f32_16x16x32_bf16 a[184:187], v[116:119], v[184:187], a[184:187]
s_add_u32 s90, s90, 262144
v_mfma_f32_16x16x32_bf16 a[188:191], v[116:119], v[188:191], a[188:191]
s_add_u32 s91, s91, 110592
s_add_u32 s92, s92, 28672
v_mfma_f32_16x16x32_bf16 a[192:195], v[160:163], v[120:123], a[192:195]
s_cmp_eq_u32 s92, 143360
v_mfma_f32_16x16x32_bf16 a[196:199], v[164:167], v[120:123], a[196:199]
s_cselect_b32 s92, 0, s92
s_add_u32 s96, s92, s94
v_mfma_f32_16x16x32_bf16 a[200:203], v[168:171], v[120:123], a[200:203]
s_add_u32 s97, s92, s95
s_mov_b32 m0, s96
v_mfma_f32_16x16x32_bf16 a[204:207], v[172:175], v[120:123], a[204:207]
buffer_load_dwordx4 v32, s[64:67], s89 offen sc1 lds
v_mfma_f32_16x16x32_bf16 a[208:211], v[120:123], v[176:179], a[208:211]
buffer_load_dwordx4 v32, s[64:67], s89 offen offset:1024 sc1 lds
v_mfma_f32_16x16x32_bf16 a[212:215], v[120:123], v[180:183], a[212:215]
buffer_load_dwordx4 v32, s[64:67], s89 offen offset:2048 sc1 lds
buffer_load_dwordx4 v32, s[64:67], s89 offen offset:3072 sc1 lds
v_mfma_f32_16x16x32_bf16 a[216:219], v[120:123], v[184:187], a[216:219]
s_mov_b32 m0, s97
v_mfma_f32_16x16x32_bf16 a[220:223], v[120:123], v[188:191], a[220:223]
buffer_load_dwordx4 v39, s[68:71], s90 offen lds
s_add_u32 m0, s97, 1024
v_mfma_f32_16x16x32_bf16 a[224:227], v[160:163], v[124:127], a[224:227]
buffer_load_dwordx4 v40, s[68:71], s90 offen lds
s_add_u32 m0, s97, 2048
v_mfma_f32_16x16x32_bf16 a[228:231], v[164:167], v[124:127], a[228:231]
buffer_load_dwordx4 v41, s[72:75], s91 offen lds
v_mfma_f32_16x16x32_bf16 a[232:235], v[168:171], v[124:127], a[232:235]
s_add_u32 s89, s89, 8192
v_mfma_f32_16x16x32_bf16 a[236:239], v[172:175], v[124:127], a[236:239]
s_add_u32 s90, s90, 262144
s_add_u32 s91, s91, 110592
v_mfma_f32_16x16x32_bf16 a[240:243], v[124:127], v[176:179], a[240:243]
s_add_u32 s92, s92, 28672
v_mfma_f32_16x16x32_bf16 a[244:247], v[124:127], v[180:183], a[244:247]
s_cmp_eq_u32 s92, 143360
s_cselect_b32 s92, 0, s92
v_mfma_f32_16x16x32_bf16 a[248:251], v[124:127], v[184:187], a[248:251]
s_add_u32 s96, s92, s94
v_mfma_f32_16x16x32_bf16 a[252:255], v[124:127], v[188:191], a[252:255]
s_add_u32 s97, s92, s95
v_and_b32 v9, 15, v8
v_lshrrev_b32 v10, 4, v8
v_lshrrev_b32 v11, 3, v9
v_lshlrev_b32 v11, 4, v11
v_and_b32 v26, 7, v9
v_add_u32 v26, v26, v11
v_and_b32 v11, 1, v10
v_lshl_add_u32 v26, v11, 5, v26
v_lshlrev_b32 v26, 4, v26
v_lshrrev_b32 v12, 1, v10
v_lshl_add_u32 v26, v12, 10, v26
v_lshl_add_u32 v27, v11, 5, v9
v_lshlrev_b32 v27, 4, v27
v_lshl_add_u32 v27, v12, 12, v27
s_lshl_b32 s44, s40, 19
s_lshl_b32 s45, s41, 16
s_add_u32 s44, s44, s45
s_lshr_b32 s45, s38, 1
s_lshl_b32 s45, s45, 15
s_add_u32 s44, s44, s45
s_and_b32 s45, s38, 1
s_lshl_b32 s46, s45, 12
s_add_u32 s84, s44, s46
s_add_u32 s84, s84, 2637824
s_lshl_b32 s46, s45, 11
s_add_u32 s85, s44, s46
s_add_u32 s85, s85, 19415040
v_accvgpr_read_b32 v64, a192
v_accvgpr_read_b32 v65, a193
v_accvgpr_read_b32 v66, a194
v_accvgpr_read_b32 v67, a195
v_accvgpr_read_b32 v68, a196
v_accvgpr_read_b32 v69, a197
v_accvgpr_read_b32 v70, a198
v_accvgpr_read_b32 v71, a199
v_cvt_pk_bf16_f32 v184, v64, v65
v_cvt_pk_bf16_f32 v185, v66, v67
v_cvt_pk_bf16_f32 v186, v68, v69
v_cvt_pk_bf16_f32 v187, v70, v71
v_accvgpr_read_b32 v80, a200
v_accvgpr_read_b32 v81, a201
v_accvgpr_read_b32 v82, a202
v_accvgpr_read_b32 v83, a203
v_accvgpr_read_b32 v84, a204
v_accvgpr_read_b32 v85, a205
v_accvgpr_read_b32 v86, a206
v_accvgpr_read_b32 v87, a207
v_cvt_pk_bf16_f32 v188, v80, v81
v_cvt_pk_bf16_f32 v189, v82, v83
v_cvt_pk_bf16_f32 v190, v84, v85
v_cvt_pk_bf16_f32 v191, v86, v87
v_accvgpr_read_b32 v96, a224
v_accvgpr_read_b32 v97, a225
v_accvgpr_read_b32 v98, a226
v_accvgpr_read_b32 v99, a227
v_accvgpr_read_b32 v100, a228
v_accvgpr_read_b32 v101, a229
v_accvgpr_read_b32 v102, a230
v_accvgpr_read_b32 v103, a231
v_cvt_pk_bf16_f32 v192, v96, v97
v_cvt_pk_bf16_f32 v193, v98, v99
v_cvt_pk_bf16_f32 v194, v100, v101
v_cvt_pk_bf16_f32 v195, v102, v103
v_accvgpr_read_b32 v112, a232
v_accvgpr_read_b32 v113, a233
v_accvgpr_read_b32 v114, a234
v_accvgpr_read_b32 v115, a235
v_accvgpr_read_b32 v116, a236
v_accvgpr_read_b32 v117, a237
v_accvgpr_read_b32 v118, a238
v_accvgpr_read_b32 v119, a239
v_cvt_pk_bf16_f32 v196, v112, v113
v_cvt_pk_bf16_f32 v197, v114, v115
v_cvt_pk_bf16_f32 v198, v116, v117
v_cvt_pk_bf16_f32 v199, v118, v119
v_accvgpr_read_b32 v128, a208
v_accvgpr_read_b32 v129, a209
v_accvgpr_read_b32 v130, a210
v_accvgpr_read_b32 v131, a211
v_accvgpr_read_b32 v132, a240
v_accvgpr_read_b32 v133, a241
v_accvgpr_read_b32 v134, a242
v_accvgpr_read_b32 v135, a243
v_cvt_pk_bf16_f32 v200, v128, v129
v_cvt_pk_bf16_f32 v201, v130, v131
v_cvt_pk_bf16_f32 v202, v132, v133
v_cvt_pk_bf16_f32 v203, v134, v135
v_accvgpr_read_b32 v144, a212
v_accvgpr_read_b32 v145, a213
v_accvgpr_read_b32 v146, a214
v_accvgpr_read_b32 v147, a215
v_accvgpr_read_b32 v148, a244
v_accvgpr_read_b32 v149, a245
v_accvgpr_read_b32 v150, a246
v_accvgpr_read_b32 v151, a247
v_cvt_pk_bf16_f32 v204, v144, v145
v_cvt_pk_bf16_f32 v205, v146, v147
v_cvt_pk_bf16_f32 v206, v148, v149
v_cvt_pk_bf16_f32 v207, v150, v151
v_accvgpr_read_b32 v64, a216
v_accvgpr_read_b32 v65, a217
v_accvgpr_read_b32 v66, a218
v_accvgpr_read_b32 v67, a219
v_accvgpr_read_b32 v68, a248
v_accvgpr_read_b32 v69, a249
v_accvgpr_read_b32 v70, a250
v_accvgpr_read_b32 v71, a251
v_cvt_pk_bf16_f32 v208, v64, v65
v_cvt_pk_bf16_f32 v209, v66, v67
v_cvt_pk_bf16_f32 v210, v68, v69
v_cvt_pk_bf16_f32 v211, v70, v71
v_accvgpr_read_b32 v80, a220
v_accvgpr_read_b32 v81, a221
v_accvgpr_read_b32 v82, a222
v_accvgpr_read_b32 v83, a223
v_accvgpr_read_b32 v84, a252
v_accvgpr_read_b32 v85, a253
v_accvgpr_read_b32 v86, a254
v_accvgpr_read_b32 v87, a255
v_cvt_pk_bf16_f32 v212, v80, v81
v_cvt_pk_bf16_f32 v213, v82, v83
v_cvt_pk_bf16_f32 v214, v84, v85
v_cvt_pk_bf16_f32 v215, v86, v87
v_accvgpr_read_b32 v96, a0
v_accvgpr_read_b32 v97, a1
v_accvgpr_read_b32 v98, a2
v_accvgpr_read_b32 v99, a3
v_accvgpr_read_b32 v100, a4
v_accvgpr_read_b32 v101, a5
v_accvgpr_read_b32 v102, a6
v_accvgpr_read_b32 v103, a7
v_cvt_pk_bf16_f32 v216, v96, v97
v_cvt_pk_bf16_f32 v217, v98, v99
v_cvt_pk_bf16_f32 v218, v100, v101
v_cvt_pk_bf16_f32 v219, v102, v103
v_accvgpr_read_b32 v112, a8
v_accvgpr_read_b32 v113, a9
v_accvgpr_read_b32 v114, a10
v_accvgpr_read_b32 v115, a11
v_accvgpr_read_b32 v116, a12
v_accvgpr_read_b32 v117, a13
v_accvgpr_read_b32 v118, a14
v_accvgpr_read_b32 v119, a15
v_cvt_pk_bf16_f32 v220, v112, v113
v_cvt_pk_bf16_f32 v221, v114, v115
v_cvt_pk_bf16_f32 v222, v116, v117
v_cvt_pk_bf16_f32 v223, v118, v119
v_accvgpr_read_b32 v128, a32
v_accvgpr_read_b32 v129, a33
v_accvgpr_read_b32 v130, a34
v_accvgpr_read_b32 v131, a35
v_accvgpr_read_b32 v132, a36
v_accvgpr_read_b32 v133, a37
v_accvgpr_read_b32 v134, a38
v_accvgpr_read_b32 v135, a39
v_cvt_pk_bf16_f32 v224, v128, v129
v_cvt_pk_bf16_f32 v225, v130, v131
v_cvt_pk_bf16_f32 v226, v132, v133
v_cvt_pk_bf16_f32 v227, v134, v135
v_accvgpr_read_b32 v144, a40
v_accvgpr_read_b32 v145, a41
v_accvgpr_read_b32 v146, a42
v_accvgpr_read_b32 v147, a43
v_accvgpr_read_b32 v148, a44
v_accvgpr_read_b32 v149, a45
v_accvgpr_read_b32 v150, a46
v_accvgpr_read_b32 v151, a47
v_cvt_pk_bf16_f32 v228, v144, v145
v_cvt_pk_bf16_f32 v229, v146, v147
v_cvt_pk_bf16_f32 v230, v148, v149
v_cvt_pk_bf16_f32 v231, v150, v151
v_accvgpr_read_b32 v64, a64
v_accvgpr_read_b32 v65, a65
v_accvgpr_read_b32 v66, a66
v_accvgpr_read_b32 v67, a67
v_accvgpr_read_b32 v68, a68
v_accvgpr_read_b32 v69, a69
v_accvgpr_read_b32 v70, a70
v_accvgpr_read_b32 v71, a71
v_cvt_pk_bf16_f32 v232, v64, v65
v_cvt_pk_bf16_f32 v233, v66, v67
v_cvt_pk_bf16_f32 v234, v68, v69
v_cvt_pk_bf16_f32 v235, v70, v71
v_accvgpr_read_b32 v80, a72
v_accvgpr_read_b32 v81, a73
v_accvgpr_read_b32 v82, a74
v_accvgpr_read_b32 v83, a75
v_accvgpr_read_b32 v84, a76
v_accvgpr_read_b32 v85, a77
v_accvgpr_read_b32 v86, a78
v_accvgpr_read_b32 v87, a79
v_cvt_pk_bf16_f32 v236, v80, v81
v_cvt_pk_bf16_f32 v237, v82, v83
v_cvt_pk_bf16_f32 v238, v84, v85
v_cvt_pk_bf16_f32 v239, v86, v87
v_accvgpr_read_b32 v96, a96
v_accvgpr_read_b32 v97, a97
v_accvgpr_read_b32 v98, a98
v_accvgpr_read_b32 v99, a99
v_accvgpr_read_b32 v100, a100
v_accvgpr_read_b32 v101, a101
v_accvgpr_read_b32 v102, a102
v_accvgpr_read_b32 v103, a103
v_cvt_pk_bf16_f32 v240, v96, v97
v_cvt_pk_bf16_f32 v241, v98, v99
v_cvt_pk_bf16_f32 v242, v100, v101
v_cvt_pk_bf16_f32 v243, v102, v103
v_accvgpr_read_b32 v112, a104
v_accvgpr_read_b32 v113, a105
v_accvgpr_read_b32 v114, a106
v_accvgpr_read_b32 v115, a107
v_accvgpr_read_b32 v116, a108
v_accvgpr_read_b32 v117, a109
v_accvgpr_read_b32 v118, a110
v_accvgpr_read_b32 v119, a111
v_cvt_pk_bf16_f32 v244, v112, v113
v_cvt_pk_bf16_f32 v245, v114, v115
v_cvt_pk_bf16_f32 v246, v116, v117
v_cvt_pk_bf16_f32 v247, v118, v119
v_accvgpr_read_b32 v128, a128
v_accvgpr_read_b32 v129, a129
v_accvgpr_read_b32 v130, a130
v_accvgpr_read_b32 v131, a131
v_accvgpr_read_b32 v132, a132
v_accvgpr_read_b32 v133, a133
v_accvgpr_read_b32 v134, a134
v_accvgpr_read_b32 v135, a135
v_cvt_pk_bf16_f32 v248, v128, v129
v_cvt_pk_bf16_f32 v249, v130, v131
v_cvt_pk_bf16_f32 v250, v132, v133
v_cvt_pk_bf16_f32 v251, v134, v135
v_accvgpr_read_b32 v144, a136
v_accvgpr_read_b32 v145, a137
v_accvgpr_read_b32 v146, a138
v_accvgpr_read_b32 v147, a139
v_accvgpr_read_b32 v148, a140
v_accvgpr_read_b32 v149, a141
v_accvgpr_read_b32 v150, a142
v_accvgpr_read_b32 v151, a143
v_cvt_pk_bf16_f32 v252, v144, v145
v_cvt_pk_bf16_f32 v253, v146, v147
v_cvt_pk_bf16_f32 v254, v148, v149
v_cvt_pk_bf16_f32 v255, v150, v151
v_accvgpr_read_b32 v64, a160
v_accvgpr_read_b32 v65, a161
v_accvgpr_read_b32 v66, a162
v_accvgpr_read_b32 v67, a163
v_accvgpr_read_b32 v68, a164
v_accvgpr_read_b32 v69, a165
v_accvgpr_read_b32 v70, a166
v_accvgpr_read_b32 v71, a167
v_cvt_pk_bf16_f32 v72, v64, v65
v_cvt_pk_bf16_f32 v73, v66, v67
v_cvt_pk_bf16_f32 v74, v68, v69
v_cvt_pk_bf16_f32 v75, v70, v71
v_accvgpr_write_b32 a200, v72
v_accvgpr_write_b32 a201, v73
v_accvgpr_write_b32 a202, v74
v_accvgpr_write_b32 a203, v75
v_accvgpr_read_b32 v80, a168
v_accvgpr_read_b32 v81, a169
v_accvgpr_read_b32 v82, a170
v_accvgpr_read_b32 v83, a171
v_accvgpr_read_b32 v84, a172
v_accvgpr_read_b32 v85, a173
v_accvgpr_read_b32 v86, a174
v_accvgpr_read_b32 v87, a175
v_cvt_pk_bf16_f32 v88, v80, v81
v_cvt_pk_bf16_f32 v89, v82, v83
v_cvt_pk_bf16_f32 v90, v84, v85
v_cvt_pk_bf16_f32 v91, v86, v87
v_accvgpr_write_b32 a204, v88
v_accvgpr_write_b32 a205, v89
v_accvgpr_write_b32 a206, v90
v_accvgpr_write_b32 a207, v91
v_accvgpr_read_b32 v96, a16
v_accvgpr_read_b32 v97, a17
v_accvgpr_read_b32 v98, a18
v_accvgpr_read_b32 v99, a19
v_accvgpr_read_b32 v100, a48
v_accvgpr_read_b32 v101, a49
v_accvgpr_read_b32 v102, a50
v_accvgpr_read_b32 v103, a51
v_cvt_pk_bf16_f32 v104, v96, v97
v_cvt_pk_bf16_f32 v105, v98, v99
v_cvt_pk_bf16_f32 v106, v100, v101
v_cvt_pk_bf16_f32 v107, v102, v103
v_accvgpr_write_b32 a208, v104
v_accvgpr_write_b32 a209, v105
v_accvgpr_write_b32 a210, v106
v_accvgpr_write_b32 a211, v107
v_accvgpr_read_b32 v112, a20
v_accvgpr_read_b32 v113, a21
v_accvgpr_read_b32 v114, a22
v_accvgpr_read_b32 v115, a23
v_accvgpr_read_b32 v116, a52
v_accvgpr_read_b32 v117, a53
v_accvgpr_read_b32 v118, a54
v_accvgpr_read_b32 v119, a55
v_cvt_pk_bf16_f32 v120, v112, v113
v_cvt_pk_bf16_f32 v121, v114, v115
v_cvt_pk_bf16_f32 v122, v116, v117
v_cvt_pk_bf16_f32 v123, v118, v119
v_accvgpr_write_b32 a212, v120
v_accvgpr_write_b32 a213, v121
v_accvgpr_write_b32 a214, v122
v_accvgpr_write_b32 a215, v123
v_accvgpr_read_b32 v128, a24
v_accvgpr_read_b32 v129, a25
v_accvgpr_read_b32 v130, a26
v_accvgpr_read_b32 v131, a27
v_accvgpr_read_b32 v132, a56
v_accvgpr_read_b32 v133, a57
v_accvgpr_read_b32 v134, a58
v_accvgpr_read_b32 v135, a59
v_cvt_pk_bf16_f32 v136, v128, v129
v_cvt_pk_bf16_f32 v137, v130, v131
v_cvt_pk_bf16_f32 v138, v132, v133
v_cvt_pk_bf16_f32 v139, v134, v135
v_accvgpr_write_b32 a216, v136
v_accvgpr_write_b32 a217, v137
v_accvgpr_write_b32 a218, v138
v_accvgpr_write_b32 a219, v139
v_accvgpr_read_b32 v144, a28
v_accvgpr_read_b32 v145, a29
v_accvgpr_read_b32 v146, a30
v_accvgpr_read_b32 v147, a31
v_accvgpr_read_b32 v148, a60
v_accvgpr_read_b32 v149, a61
v_accvgpr_read_b32 v150, a62
v_accvgpr_read_b32 v151, a63
v_cvt_pk_bf16_f32 v152, v144, v145
v_cvt_pk_bf16_f32 v153, v146, v147
v_cvt_pk_bf16_f32 v154, v148, v149
v_cvt_pk_bf16_f32 v155, v150, v151
v_accvgpr_write_b32 a220, v152
v_accvgpr_write_b32 a221, v153
v_accvgpr_write_b32 a222, v154
v_accvgpr_write_b32 a223, v155
v_accvgpr_read_b32 v64, a80
v_accvgpr_read_b32 v65, a81
v_accvgpr_read_b32 v66, a82
v_accvgpr_read_b32 v67, a83
v_accvgpr_read_b32 v68, a112
v_accvgpr_read_b32 v69, a113
v_accvgpr_read_b32 v70, a114
v_accvgpr_read_b32 v71, a115
v_cvt_pk_bf16_f32 v72, v64, v65
v_cvt_pk_bf16_f32 v73, v66, v67
v_cvt_pk_bf16_f32 v74, v68, v69
v_cvt_pk_bf16_f32 v75, v70, v71
v_accvgpr_write_b32 a224, v72
v_accvgpr_write_b32 a225, v73
v_accvgpr_write_b32 a226, v74
v_accvgpr_write_b32 a227, v75
v_accvgpr_read_b32 v80, a84
v_accvgpr_read_b32 v81, a85
v_accvgpr_read_b32 v82, a86
v_accvgpr_read_b32 v83, a87
v_accvgpr_read_b32 v84, a116
v_accvgpr_read_b32 v85, a117
v_accvgpr_read_b32 v86, a118
v_accvgpr_read_b32 v87, a119
v_cvt_pk_bf16_f32 v88, v80, v81
v_cvt_pk_bf16_f32 v89, v82, v83
v_cvt_pk_bf16_f32 v90, v84, v85
v_cvt_pk_bf16_f32 v91, v86, v87
v_accvgpr_write_b32 a228, v88
v_accvgpr_write_b32 a229, v89
v_accvgpr_write_b32 a230, v90
v_accvgpr_write_b32 a231, v91
v_accvgpr_read_b32 v96, a88
v_accvgpr_read_b32 v97, a89
v_accvgpr_read_b32 v98, a90
v_accvgpr_read_b32 v99, a91
v_accvgpr_read_b32 v100, a120
v_accvgpr_read_b32 v101, a121
v_accvgpr_read_b32 v102, a122
v_accvgpr_read_b32 v103, a123
v_cvt_pk_bf16_f32 v104, v96, v97
v_cvt_pk_bf16_f32 v105, v98, v99
v_cvt_pk_bf16_f32 v106, v100, v101
v_cvt_pk_bf16_f32 v107, v102, v103
v_accvgpr_write_b32 a232, v104
v_accvgpr_write_b32 a233, v105
v_accvgpr_write_b32 a234, v106
v_accvgpr_write_b32 a235, v107
v_accvgpr_read_b32 v112, a92
v_accvgpr_read_b32 v113, a93
v_accvgpr_read_b32 v114, a94
v_accvgpr_read_b32 v115, a95
v_accvgpr_read_b32 v116, a124
v_accvgpr_read_b32 v117, a125
v_accvgpr_read_b32 v118, a126
v_accvgpr_read_b32 v119, a127
v_cvt_pk_bf16_f32 v120, v112, v113
v_cvt_pk_bf16_f32 v121, v114, v115
v_cvt_pk_bf16_f32 v122, v116, v117
v_cvt_pk_bf16_f32 v123, v118, v119
v_accvgpr_write_b32 a236, v120
v_accvgpr_write_b32 a237, v121
v_accvgpr_write_b32 a238, v122
v_accvgpr_write_b32 a239, v123
v_accvgpr_read_b32 v128, a144
v_accvgpr_read_b32 v129, a145
v_accvgpr_read_b32 v130, a146
v_accvgpr_read_b32 v131, a147
v_accvgpr_read_b32 v132, a176
v_accvgpr_read_b32 v133, a177
v_accvgpr_read_b32 v134, a178
v_accvgpr_read_b32 v135, a179
v_cvt_pk_bf16_f32 v136, v128, v129
v_cvt_pk_bf16_f32 v137, v130, v131
v_cvt_pk_bf16_f32 v138, v132, v133
v_cvt_pk_bf16_f32 v139, v134, v135
v_accvgpr_write_b32 a240, v136
v_accvgpr_write_b32 a241, v137
v_accvgpr_write_b32 a242, v138
v_accvgpr_write_b32 a243, v139
v_accvgpr_read_b32 v144, a148
v_accvgpr_read_b32 v145, a149
v_accvgpr_read_b32 v146, a150
v_accvgpr_read_b32 v147, a151
v_accvgpr_read_b32 v148, a180
v_accvgpr_read_b32 v149, a181
v_accvgpr_read_b32 v150, a182
v_accvgpr_read_b32 v151, a183
v_cvt_pk_bf16_f32 v152, v144, v145
v_cvt_pk_bf16_f32 v153, v146, v147
v_cvt_pk_bf16_f32 v154, v148, v149
v_cvt_pk_bf16_f32 v155, v150, v151
v_accvgpr_write_b32 a244, v152
v_accvgpr_write_b32 a245, v153
v_accvgpr_write_b32 a246, v154
v_accvgpr_write_b32 a247, v155
v_accvgpr_read_b32 v64, a152
v_accvgpr_read_b32 v65, a153
v_accvgpr_read_b32 v66, a154
v_accvgpr_read_b32 v67, a155
v_accvgpr_read_b32 v68, a184
v_accvgpr_read_b32 v69, a185
v_accvgpr_read_b32 v70, a186
v_accvgpr_read_b32 v71, a187
v_cvt_pk_bf16_f32 v72, v64, v65
v_cvt_pk_bf16_f32 v73, v66, v67
v_cvt_pk_bf16_f32 v74, v68, v69
v_cvt_pk_bf16_f32 v75, v70, v71
v_accvgpr_write_b32 a248, v72
v_accvgpr_write_b32 a249, v73
v_accvgpr_write_b32 a250, v74
v_accvgpr_write_b32 a251, v75
v_accvgpr_read_b32 v80, a156
v_accvgpr_read_b32 v81, a157
v_accvgpr_read_b32 v82, a158
v_accvgpr_read_b32 v83, a159
v_accvgpr_read_b32 v84, a188
v_accvgpr_read_b32 v85, a189
v_accvgpr_read_b32 v86, a190
v_accvgpr_read_b32 v87, a191
v_cvt_pk_bf16_f32 v88, v80, v81
v_cvt_pk_bf16_f32 v89, v82, v83
v_cvt_pk_bf16_f32 v90, v84, v85
v_cvt_pk_bf16_f32 v91, v86, v87
v_accvgpr_write_b32 a252, v88
v_accvgpr_write_b32 a253, v89
v_accvgpr_write_b32 a254, v90
v_accvgpr_write_b32 a255, v91
s_waitcnt vmcnt(28)
s_barrier
v_add_u32 v34, 0, v33
v_add_u32 v37, 0, v35
v_add_u32 v38, 0, v36
ds_read_b128 v[64:67], v34 offset:0
ds_read_b64_tr_b16 v[128:129], v37 offset:0
ds_read_b64_tr_b16 v[130:131], v38 offset:0
ds_read_b64_tr_b16 v[132:133], v37 offset:256
ds_read_b64_tr_b16 v[134:135], v38 offset:256
ds_read_b64_tr_b16 v[136:137], v37 offset:1024
ds_read_b64_tr_b16 v[138:139], v38 offset:1024
ds_read_b64_tr_b16 v[140:141], v37 offset:1280
ds_read_b64_tr_b16 v[142:143], v38 offset:1280
ds_read_b64_tr_b16 v[144:145], v37 offset:2048
ds_read_b64_tr_b16 v[146:147], v38 offset:2048
ds_read_b64_tr_b16 v[148:149], v37 offset:2304
ds_read_b64_tr_b16 v[150:151], v38 offset:2304
ds_read_b128 v[68:71], v34 offset:1024
ds_read_b128 v[72:75], v34 offset:2048
ds_read_b128 v[76:79], v34 offset:3072
ds_read_b128 v[80:83], v34 offset:4096
ds_read_b128 v[84:87], v34 offset:5120
ds_read_b128 v[88:91], v34 offset:6144
ds_read_b128 v[92:95], v34 offset:7168
s_waitcnt vmcnt(21) lgkmcnt(0)
s_barrier
v_mfma_f32_16x16x32_bf16 a[0:3], v[128:131], v[64:67], 0
v_add_u32 v34, 28672, v33
v_mfma_f32_16x16x32_bf16 a[4:7], v[132:135], v[64:67], 0
v_add_u32 v37, 28672, v35
s_mov_b32 m0, s96
v_mfma_f32_16x16x32_bf16 a[8:11], v[136:139], v[64:67], 0
buffer_load_dwordx4 v32, s[64:67], s89 offen sc1 lds
v_add_u32 v38, 28672, v36
v_mfma_f32_16x16x32_bf16 a[12:15], v[140:143], v[64:67], 0
buffer_load_dwordx4 v32, s[64:67], s89 offen offset:1024 sc1 lds
ds_read_b128 v[96:99], v34 offset:0
v_mfma_f32_16x16x32_bf16 a[16:19], v[144:147], v[64:67], 0
v_mfma_f32_16x16x32_bf16 a[20:23], v[148:151], v[64:67], 0
buffer_load_dwordx4 v32, s[64:67], s89 offen offset:2048 sc1 lds
ds_read_b64_tr_b16 v[152:153], v37 offset:0
v_mfma_f32_16x16x32_bf16 a[192:195], v[144:147], v[44:47], 0
buffer_load_dwordx4 v32, s[64:67], s89 offen offset:3072 sc1 lds
v_mfma_f32_16x16x32_bf16 a[196:199], v[148:151], v[44:47], 0
ds_read_b64_tr_b16 v[154:155], v38 offset:0
v_mfma_f32_16x16x32_bf16 a[24:27], v[128:131], v[68:71], 0
s_mov_b32 m0, s97
v_mfma_f32_16x16x32_bf16 a[28:31], v[132:135], v[68:71], 0
buffer_load_dwordx4 v39, s[68:71], s90 offen lds
ds_read_b64_tr_b16 v[156:157], v37 offset:256
v_mfma_f32_16x16x32_bf16 a[32:35], v[136:139], v[68:71], 0
s_add_u32 m0, s97, 1024
v_mfma_f32_16x16x32_bf16 a[36:39], v[140:143], v[68:71], 0
ds_read_b64_tr_b16 v[158:159], v38 offset:256
v_mfma_f32_16x16x32_bf16 a[40:43], v[144:147], v[68:71], 0
buffer_load_dwordx4 v40, s[68:71], s90 offen lds
v_mfma_f32_16x16x32_bf16 a[44:47], v[148:151], v[68:71], 0
ds_read_b64_tr_b16 v[160:161], v37 offset:1024
s_add_u32 m0, s97, 2048
v_mfma_f32_16x16x32_bf16 a[48:51], v[128:131], v[72:75], 0
v_mfma_f32_16x16x32_bf16 a[52:55], v[132:135], v[72:75], 0
buffer_load_dwordx4 v41, s[72:75], s91 offen lds
ds_read_b64_tr_b16 v[162:163], v38 offset:1024
v_mfma_f32_16x16x32_bf16 a[56:59], v[136:139], v[72:75], 0
s_add_u32 s89, s89, 8192
v_mfma_f32_16x16x32_bf16 a[60:63], v[140:143], v[72:75], 0
ds_read_b64_tr_b16 v[164:165], v37 offset:1280
v_mfma_f32_16x16x32_bf16 a[64:67], v[144:147], v[72:75], 0
s_add_u32 s90, s90, 262144
v_mfma_f32_16x16x32_bf16 a[68:71], v[148:151], v[72:75], 0
ds_read_b64_tr_b16 v[166:167], v38 offset:1280
s_add_u32 s91, s91, 110592
v_mfma_f32_16x16x32_bf16 a[72:75], v[128:131], v[76:79], 0
s_add_u32 s92, s92, 28672
v_mfma_f32_16x16x32_bf16 a[76:79], v[132:135], v[76:79], 0
ds_read_b64_tr_b16 v[168:169], v37 offset:2048
v_mfma_f32_16x16x32_bf16 a[80:83], v[136:139], v[76:79], 0
s_cmp_eq_u32 s92, 143360
v_mfma_f32_16x16x32_bf16 a[84:87], v[140:143], v[76:79], 0
s_cselect_b32 s92, 0, s92
v_mfma_f32_16x16x32_bf16 a[88:91], v[144:147], v[76:79], 0
ds_read_b64_tr_b16 v[170:171], v38 offset:2048
v_mfma_f32_16x16x32_bf16 a[92:95], v[148:151], v[76:79], 0
s_add_u32 s96, s92, s94
v_mfma_f32_16x16x32_bf16 a[96:99], v[128:131], v[80:83], 0
ds_read_b64_tr_b16 v[172:173], v37 offset:2304
s_add_u32 s97, s92, s95
v_mfma_f32_16x16x32_bf16 a[100:103], v[132:135], v[80:83], 0
v_mfma_f32_16x16x32_bf16 a[104:107], v[136:139], v[80:83], 0
ds_read_b64_tr_b16 v[174:175], v38 offset:2304
v_mfma_f32_16x16x32_bf16 a[108:111], v[140:143], v[80:83], 0
v_mfma_f32_16x16x32_bf16 a[112:115], v[144:147], v[80:83], 0
ds_read_b128 v[100:103], v34 offset:1024
s_add_u32 s47, s84, 0
v_mfma_f32_16x16x32_bf16 a[116:119], v[148:151], v[80:83], 0
v_mfma_f32_16x16x32_bf16 a[120:123], v[128:131], v[84:87], 0
buffer_store_dwordx4 v[216:219], v26, s[48:51], s47 offen offset:0 sc1
ds_read_b128 v[104:107], v34 offset:2048
v_mfma_f32_16x16x32_bf16 a[124:127], v[132:135], v[84:87], 0
v_mfma_f32_16x16x32_bf16 a[128:131], v[136:139], v[84:87], 0
ds_read_b128 v[108:111], v34 offset:3072
s_add_u32 s47, s84, 0
v_mfma_f32_16x16x32_bf16 a[132:135], v[140:143], v[84:87], 0
v_mfma_f32_16x16x32_bf16 a[136:139], v[144:147], v[84:87], 0
ds_read_b128 v[112:115], v34 offset:4096
v_mfma_f32_16x16x32_bf16 a[140:143], v[148:151], v[84:87], 0
buffer_store_dwordx4 v[220:223], v26, s[48:51], s47 offen offset:2048 sc1
v_mfma_f32_16x16x32_bf16 a[144:147], v[128:131], v[88:91], 0
ds_read_b128 v[116:119], v34 offset:5120
v_mfma_f32_16x16x32_bf16 a[148:151], v[132:135], v[88:91], 0
s_add_u32 s47, s84, 0
v_mfma_f32_16x16x32_bf16 a[152:155], v[136:139], v[88:91], 0
ds_read_b128 v[120:123], v34 offset:6144
v_mfma_f32_16x16x32_bf16 a[156:159], v[140:143], v[88:91], 0
buffer_store_dwordx4 v[224:227], v26, s[48:51], s47 offen offset:128 sc1
v_mfma_f32_16x16x32_bf16 a[160:163], v[144:147], v[88:91], 0
ds_read_b128 v[124:127], v34 offset:7168
v_mfma_f32_16x16x32_bf16 a[164:167], v[148:151], v[88:91], 0
v_mfma_f32_16x16x32_bf16 a[168:171], v[128:131], v[92:95], 0
s_add_u32 s47, s84, 0
v_mfma_f32_16x16x32_bf16 a[172:175], v[132:135], v[92:95], 0
v_mfma_f32_16x16x32_bf16 a[176:179], v[136:139], v[92:95], 0
buffer_store_dwordx4 v[228:231], v26, s[48:51], s47 offen offset:2176 sc1
v_mfma_f32_16x16x32_bf16 a[180:183], v[140:143], v[92:95], 0
v_mfma_f32_16x16x32_bf16 a[184:187], v[144:147], v[92:95], 0
v_mfma_f32_16x16x32_bf16 a[188:191], v[148:151], v[92:95], 0
s_waitcnt vmcnt(25) lgkmcnt(0)
s_barrier
v_mfma_f32_16x16x32_bf16 a[0:3], v[152:155], v[96:99], a[0:3]
v_add_u32 v34, 57344, v33
v_mfma_f32_16x16x32_bf16 a[4:7], v[156:159], v[96:99], a[4:7]
v_add_u32 v37, 57344, v35
s_mov_b32 m0, s96
v_mfma_f32_16x16x32_bf16 a[8:11], v[160:163], v[96:99], a[8:11]
buffer_load_dwordx4 v32, s[64:67], s89 offen sc1 lds
v_add_u32 v38, 57344, v36
v_mfma_f32_16x16x32_bf16 a[12:15], v[164:167], v[96:99], a[12:15]
buffer_load_dwordx4 v32, s[64:67], s89 offen offset:1024 sc1 lds
ds_read_b128 v[64:67], v34 offset:0
v_mfma_f32_16x16x32_bf16 a[16:19], v[168:171], v[96:99], a[16:19]
v_mfma_f32_16x16x32_bf16 a[20:23], v[172:175], v[96:99], a[20:23]
buffer_load_dwordx4 v32, s[64:67], s89 offen offset:2048 sc1 lds
ds_read_b64_tr_b16 v[128:129], v37 offset:0
v_mfma_f32_16x16x32_bf16 a[192:195], v[168:171], v[44:47], a[192:195]
buffer_load_dwordx4 v32, s[64:67], s89 offen offset:3072 sc1 lds
v_mfma_f32_16x16x32_bf16 a[196:199], v[172:175], v[44:47], a[196:199]
ds_read_b64_tr_b16 v[130:131], v38 offset:0
v_mfma_f32_16x16x32_bf16 a[24:27], v[152:155], v[100:103], a[24:27]
s_mov_b32 m0, s97
v_mfma_f32_16x16x32_bf16 a[28:31], v[156:159], v[100:103], a[28:31]
buffer_load_dwordx4 v39, s[68:71], s90 offen lds
ds_read_b64_tr_b16 v[132:133], v37 offset:256
v_mfma_f32_16x16x32_bf16 a[32:35], v[160:163], v[100:103], a[32:35]
s_add_u32 m0, s97, 1024
v_mfma_f32_16x16x32_bf16 a[36:39], v[164:167], v[100:103], a[36:39]
ds_read_b64_tr_b16 v[134:135], v38 offset:256
v_mfma_f32_16x16x32_bf16 a[40:43], v[168:171], v[100:103], a[40:43]
buffer_load_dwordx4 v40, s[68:71], s90 offen lds
v_mfma_f32_16x16x32_bf16 a[44:47], v[172:175], v[100:103], a[44:47]
ds_read_b64_tr_b16 v[136:137], v37 offset:1024
s_add_u32 m0, s97, 2048
v_mfma_f32_16x16x32_bf16 a[48:51], v[152:155], v[104:107], a[48:51]
v_mfma_f32_16x16x32_bf16 a[52:55], v[156:159], v[104:107], a[52:55]
buffer_load_dwordx4 v41, s[72:75], s91 offen lds
ds_read_b64_tr_b16 v[138:139], v38 offset:1024
v_mfma_f32_16x16x32_bf16 a[56:59], v[160:163], v[104:107], a[56:59]
s_add_u32 s89, s89, 8192
v_mfma_f32_16x16x32_bf16 a[60:63], v[164:167], v[104:107], a[60:63]
ds_read_b64_tr_b16 v[140:141], v37 offset:1280
v_mfma_f32_16x16x32_bf16 a[64:67], v[168:171], v[104:107], a[64:67]
s_add_u32 s90, s90, 262144
v_mfma_f32_16x16x32_bf16 a[68:71], v[172:175], v[104:107], a[68:71]
ds_read_b64_tr_b16 v[142:143], v38 offset:1280
s_add_u32 s91, s91, 110592
v_mfma_f32_16x16x32_bf16 a[72:75], v[152:155], v[108:111], a[72:75]
s_add_u32 s92, s92, 28672
v_mfma_f32_16x16x32_bf16 a[76:79], v[156:159], v[108:111], a[76:79]
ds_read_b64_tr_b16 v[144:145], v37 offset:2048
v_mfma_f32_16x16x32_bf16 a[80:83], v[160:163], v[108:111], a[80:83]
s_cmp_eq_u32 s92, 143360
v_mfma_f32_16x16x32_bf16 a[84:87], v[164:167], v[108:111], a[84:87]
s_cselect_b32 s92, 0, s92
v_mfma_f32_16x16x32_bf16 a[88:91], v[168:171], v[108:111], a[88:91]
ds_read_b64_tr_b16 v[146:147], v38 offset:2048
v_mfma_f32_16x16x32_bf16 a[92:95], v[172:175], v[108:111], a[92:95]
s_add_u32 s96, s92, s94
v_mfma_f32_16x16x32_bf16 a[96:99], v[152:155], v[112:115], a[96:99]
ds_read_b64_tr_b16 v[148:149], v37 offset:2304
s_add_u32 s97, s92, s95
v_mfma_f32_16x16x32_bf16 a[100:103], v[156:159], v[112:115], a[100:103]
v_mfma_f32_16x16x32_bf16 a[104:107], v[160:163], v[112:115], a[104:107]
ds_read_b64_tr_b16 v[150:151], v38 offset:2304
v_mfma_f32_16x16x32_bf16 a[108:111], v[164:167], v[112:115], a[108:111]
v_mfma_f32_16x16x32_bf16 a[112:115], v[168:171], v[112:115], a[112:115]
ds_read_b128 v[68:71], v34 offset:1024
s_add_u32 s47, s84, 8192
v_mfma_f32_16x16x32_bf16 a[116:119], v[172:175], v[112:115], a[116:119]
v_mfma_f32_16x16x32_bf16 a[120:123], v[152:155], v[116:119], a[120:123]
ds_read_b128 v[72:75], v34 offset:2048
v_mfma_f32_16x16x32_bf16 a[124:127], v[156:159], v[116:119], a[124:127]
buffer_store_dwordx4 v[232:235], v26, s[48:51], s47 offen offset:0 sc1
v_mfma_f32_16x16x32_bf16 a[128:131], v[160:163], v[116:119], a[128:131]
ds_read_b128 v[76:79], v34 offset:3072
v_mfma_f32_16x16x32_bf16 a[132:135], v[164:167], v[116:119], a[132:135]
v_mfma_f32_16x16x32_bf16 a[136:139], v[168:171], v[116:119], a[136:139]
ds_read_b128 v[80:83], v34 offset:4096
s_add_u32 s47, s84, 8192
v_mfma_f32_16x16x32_bf16 a[140:143], v[172:175], v[116:119], a[140:143]
v_mfma_f32_16x16x32_bf16 a[144:147], v[152:155], v[120:123], a[144:147]
ds_read_b128 v[84:87], v34 offset:5120
v_mfma_f32_16x16x32_bf16 a[148:151], v[156:159], v[120:123], a[148:151]
buffer_store_dwordx4 v[236:239], v26, s[48:51], s47 offen offset:2048 sc1
v_mfma_f32_16x16x32_bf16 a[152:155], v[160:163], v[120:123], a[152:155]
ds_read_b128 v[88:91], v34 offset:6144
v_mfma_f32_16x16x32_bf16 a[156:159], v[164:167], v[120:123], a[156:159]
v_mfma_f32_16x16x32_bf16 a[160:163], v[168:171], v[120:123], a[160:163]
ds_read_b128 v[92:95], v34 offset:7168
s_add_u32 s47, s84, 8192
v_mfma_f32_16x16x32_bf16 a[164:167], v[172:175], v[120:123], a[164:167]
v_mfma_f32_16x16x32_bf16 a[168:171], v[152:155], v[124:127], a[168:171]
v_mfma_f32_16x16x32_bf16 a[172:175], v[156:159], v[124:127], a[172:175]
buffer_store_dwordx4 v[240:243], v26, s[48:51], s47 offen offset:128 sc1
v_mfma_f32_16x16x32_bf16 a[176:179], v[160:163], v[124:127], a[176:179]
v_mfma_f32_16x16x32_bf16 a[180:183], v[164:167], v[124:127], a[180:183]
v_mfma_f32_16x16x32_bf16 a[184:187], v[168:171], v[124:127], a[184:187]
v_mfma_f32_16x16x32_bf16 a[188:191], v[172:175], v[124:127], a[188:191]
s_waitcnt vmcnt(28) lgkmcnt(0)
s_barrier
v_mfma_f32_16x16x32_bf16 a[0:3], v[128:131], v[64:67], a[0:3]
v_add_u32 v34, 86016, v33
v_mfma_f32_16x16x32_bf16 a[4:7], v[132:135], v[64:67], a[4:7]
v_add_u32 v37, 86016, v35
s_mov_b32 m0, s96
v_mfma_f32_16x16x32_bf16 a[8:11], v[136:139], v[64:67], a[8:11]
buffer_load_dwordx4 v32, s[64:67], s89 offen sc1 lds
v_add_u32 v38, 86016, v36
v_mfma_f32_16x16x32_bf16 a[12:15], v[140:143], v[64:67], a[12:15]
buffer_load_dwordx4 v32, s[64:67], s89 offen offset:1024 sc1 lds
ds_read_b128 v[96:99], v34 offset:0
v_mfma_f32_16x16x32_bf16 a[16:19], v[144:147], v[64:67], a[16:19]
v_mfma_f32_16x16x32_bf16 a[20:23], v[148:151], v[64:67], a[20:23]
buffer_load_dwordx4 v32, s[64:67], s89 offen offset:2048 sc1 lds
ds_read_b64_tr_b16 v[152:153], v37 offset:0
v_mfma_f32_16x16x32_bf16 a[192:195], v[144:147], v[44:47], a[192:195]
buffer_load_dwordx4 v32, s[64:67], s89 offen offset:3072 sc1 lds
v_mfma_f32_16x16x32_bf16 a[196:199], v[148:151], v[44:47], a[196:199]
ds_read_b64_tr_b16 v[154:155], v38 offset:0
v_mfma_f32_16x16x32_bf16 a[24:27], v[128:131], v[68:71], a[24:27]
s_mov_b32 m0, s97
v_mfma_f32_16x16x32_bf16 a[28:31], v[132:135], v[68:71], a[28:31]
buffer_load_dwordx4 v39, s[68:71], s90 offen lds
ds_read_b64_tr_b16 v[156:157], v37 offset:256
v_mfma_f32_16x16x32_bf16 a[32:35], v[136:139], v[68:71], a[32:35]
s_add_u32 m0, s97, 1024
v_mfma_f32_16x16x32_bf16 a[36:39], v[140:143], v[68:71], a[36:39]
ds_read_b64_tr_b16 v[158:159], v38 offset:256
v_mfma_f32_16x16x32_bf16 a[40:43], v[144:147], v[68:71], a[40:43]
buffer_load_dwordx4 v40, s[68:71], s90 offen lds
v_mfma_f32_16x16x32_bf16 a[44:47], v[148:151], v[68:71], a[44:47]
ds_read_b64_tr_b16 v[160:161], v37 offset:1024
s_add_u32 m0, s97, 2048
v_mfma_f32_16x16x32_bf16 a[48:51], v[128:131], v[72:75], a[48:51]
v_mfma_f32_16x16x32_bf16 a[52:55], v[132:135], v[72:75], a[52:55]
buffer_load_dwordx4 v41, s[72:75], s91 offen lds
ds_read_b64_tr_b16 v[162:163], v38 offset:1024
v_mfma_f32_16x16x32_bf16 a[56:59], v[136:139], v[72:75], a[56:59]
s_add_u32 s89, s89, 8192
v_mfma_f32_16x16x32_bf16 a[60:63], v[140:143], v[72:75], a[60:63]
ds_read_b64_tr_b16 v[164:165], v37 offset:1280
v_mfma_f32_16x16x32_bf16 a[64:67], v[144:147], v[72:75], a[64:67]
s_add_u32 s90, s90, 262144
v_mfma_f32_16x16x32_bf16 a[68:71], v[148:151], v[72:75], a[68:71]
ds_read_b64_tr_b16 v[166:167], v38 offset:1280
s_add_u32 s91, s91, 110592
v_mfma_f32_16x16x32_bf16 a[72:75], v[128:131], v[76:79], a[72:75]
s_add_u32 s92, s92, 28672
v_mfma_f32_16x16x32_bf16 a[76:79], v[132:135], v[76:79], a[76:79]
ds_read_b64_tr_b16 v[168:169], v37 offset:2048
v_mfma_f32_16x16x32_bf16 a[80:83], v[136:139], v[76:79], a[80:83]
s_cmp_eq_u32 s92, 143360
v_mfma_f32_16x16x32_bf16 a[84:87], v[140:143], v[76:79], a[84:87]
s_cselect_b32 s92, 0, s92
v_mfma_f32_16x16x32_bf16 a[88:91], v[144:147], v[76:79], a[88:91]
ds_read_b64_tr_b16 v[170:171], v38 offset:2048
v_mfma_f32_16x16x32_bf16 a[92:95], v[148:151], v[76:79], a[92:95]
s_add_u32 s96, s92, s94
v_mfma_f32_16x16x32_bf16 a[96:99], v[128:131], v[80:83], a[96:99]
ds_read_b64_tr_b16 v[172:173], v37 offset:2304
s_add_u32 s97, s92, s95
v_mfma_f32_16x16x32_bf16 a[100:103], v[132:135], v[80:83], a[100:103]
v_mfma_f32_16x16x32_bf16 a[104:107], v[136:139], v[80:83], a[104:107]
ds_read_b64_tr_b16 v[174:175], v38 offset:2304
v_mfma_f32_16x16x32_bf16 a[108:111], v[140:143], v[80:83], a[108:111]
v_mfma_f32_16x16x32_bf16 a[112:115], v[144:147], v[80:83], a[112:115]
ds_read_b128 v[100:103], v34 offset:1024
s_add_u32 s47, s84, 8192
v_mfma_f32_16x16x32_bf16 a[116:119], v[148:151], v[80:83], a[116:119]
v_mfma_f32_16x16x32_bf16 a[120:123], v[128:131], v[84:87], a[120:123]
ds_read_b128 v[104:107], v34 offset:2048
v_mfma_f32_16x16x32_bf16 a[124:127], v[132:135], v[84:87], a[124:127]
buffer_store_dwordx4 v[244:247], v26, s[48:51], s47 offen offset:2176 sc1
v_mfma_f32_16x16x32_bf16 a[128:131], v[136:139], v[84:87], a[128:131]
ds_read_b128 v[108:111], v34 offset:3072
v_mfma_f32_16x16x32_bf16 a[132:135], v[140:143], v[84:87], a[132:135]
v_mfma_f32_16x16x32_bf16 a[136:139], v[144:147], v[84:87], a[136:139]
ds_read_b128 v[112:115], v34 offset:4096
s_add_u32 s47, s84, 16384
v_mfma_f32_16x16x32_bf16 a[140:143], v[148:151], v[84:87], a[140:143]
v_mfma_f32_16x16x32_bf16 a[144:147], v[128:131], v[88:91], a[144:147]
ds_read_b128 v[116:119], v34 offset:5120
v_mfma_f32_16x16x32_bf16 a[148:151], v[132:135], v[88:91], a[148:151]
buffer_store_dwordx4 v[248:251], v26, s[48:51], s47 offen offset:0 sc1
v_mfma_f32_16x16x32_bf16 a[152:155], v[136:139], v[88:91], a[152:155]
ds_read_b128 v[120:123], v34 offset:6144
v_mfma_f32_16x16x32_bf16 a[156:159], v[140:143], v[88:91], a[156:159]
v_mfma_f32_16x16x32_bf16 a[160:163], v[144:147], v[88:91], a[160:163]
ds_read_b128 v[124:127], v34 offset:7168
s_add_u32 s47, s84, 16384
v_mfma_f32_16x16x32_bf16 a[164:167], v[148:151], v[88:91], a[164:167]
v_mfma_f32_16x16x32_bf16 a[168:171], v[128:131], v[92:95], a[168:171]
v_mfma_f32_16x16x32_bf16 a[172:175], v[132:135], v[92:95], a[172:175]
buffer_store_dwordx4 v[252:255], v26, s[48:51], s47 offen offset:2048 sc1
v_mfma_f32_16x16x32_bf16 a[176:179], v[136:139], v[92:95], a[176:179]
v_mfma_f32_16x16x32_bf16 a[180:183], v[140:143], v[92:95], a[180:183]
v_mfma_f32_16x16x32_bf16 a[184:187], v[144:147], v[92:95], a[184:187]
v_mfma_f32_16x16x32_bf16 a[188:191], v[148:151], v[92:95], a[188:191]
s_waitcnt vmcnt(31) lgkmcnt(0)
s_barrier
v_mfma_f32_16x16x32_bf16 a[0:3], v[152:155], v[96:99], a[0:3]
v_add_u32 v34, 114688, v33
v_mfma_f32_16x16x32_bf16 a[4:7], v[156:159], v[96:99], a[4:7]
v_add_u32 v37, 114688, v35
s_mov_b32 m0, s96
v_mfma_f32_16x16x32_bf16 a[8:11], v[160:163], v[96:99], a[8:11]
buffer_load_dwordx4 v32, s[64:67], s89 offen sc1 lds
v_add_u32 v38, 114688, v36
v_mfma_f32_16x16x32_bf16 a[12:15], v[164:167], v[96:99], a[12:15]
buffer_load_dwordx4 v32, s[64:67], s89 offen offset:1024 sc1 lds
ds_read_b128 v[64:67], v34 offset:0
v_mfma_f32_16x16x32_bf16 a[16:19], v[168:171], v[96:99], a[16:19]
v_mfma_f32_16x16x32_bf16 a[20:23], v[172:175], v[96:99], a[20:23]
buffer_load_dwordx4 v32, s[64:67], s89 offen offset:2048 sc1 lds
ds_read_b64_tr_b16 v[128:129], v37 offset:0
v_mfma_f32_16x16x32_bf16 a[192:195], v[168:171], v[44:47], a[192:195]
buffer_load_dwordx4 v32, s[64:67], s89 offen offset:3072 sc1 lds
v_mfma_f32_16x16x32_bf16 a[196:199], v[172:175], v[44:47], a[196:199]
ds_read_b64_tr_b16 v[130:131], v38 offset:0
v_mfma_f32_16x16x32_bf16 a[24:27], v[152:155], v[100:103], a[24:27]
s_mov_b32 m0, s97
v_mfma_f32_16x16x32_bf16 a[28:31], v[156:159], v[100:103], a[28:31]
buffer_load_dwordx4 v39, s[68:71], s90 offen lds
ds_read_b64_tr_b16 v[132:133], v37 offset:256
v_mfma_f32_16x16x32_bf16 a[32:35], v[160:163], v[100:103], a[32:35]
s_add_u32 m0, s97, 1024
v_mfma_f32_16x16x32_bf16 a[36:39], v[164:167], v[100:103], a[36:39]
ds_read_b64_tr_b16 v[134:135], v38 offset:256
v_mfma_f32_16x16x32_bf16 a[40:43], v[168:171], v[100:103], a[40:43]
buffer_load_dwordx4 v40, s[68:71], s90 offen lds
v_mfma_f32_16x16x32_bf16 a[44:47], v[172:175], v[100:103], a[44:47]
ds_read_b64_tr_b16 v[136:137], v37 offset:1024
s_add_u32 m0, s97, 2048
v_mfma_f32_16x16x32_bf16 a[48:51], v[152:155], v[104:107], a[48:51]
v_mfma_f32_16x16x32_bf16 a[52:55], v[156:159], v[104:107], a[52:55]
buffer_load_dwordx4 v41, s[72:75], s91 offen lds
ds_read_b64_tr_b16 v[138:139], v38 offset:1024
v_mfma_f32_16x16x32_bf16 a[56:59], v[160:163], v[104:107], a[56:59]
s_add_u32 s89, s89, 8192
v_mfma_f32_16x16x32_bf16 a[60:63], v[164:167], v[104:107], a[60:63]
ds_read_b64_tr_b16 v[140:141], v37 offset:1280
v_mfma_f32_16x16x32_bf16 a[64:67], v[168:171], v[104:107], a[64:67]
s_add_u32 s90, s90, 262144
v_mfma_f32_16x16x32_bf16 a[68:71], v[172:175], v[104:107], a[68:71]
ds_read_b64_tr_b16 v[142:143], v38 offset:1280
s_add_u32 s91, s91, 110592
v_mfma_f32_16x16x32_bf16 a[72:75], v[152:155], v[108:111], a[72:75]
s_add_u32 s92, s92, 28672
v_mfma_f32_16x16x32_bf16 a[76:79], v[156:159], v[108:111], a[76:79]
ds_read_b64_tr_b16 v[144:145], v37 offset:2048
v_mfma_f32_16x16x32_bf16 a[80:83], v[160:163], v[108:111], a[80:83]
s_cmp_eq_u32 s92, 143360
v_mfma_f32_16x16x32_bf16 a[84:87], v[164:167], v[108:111], a[84:87]
s_cselect_b32 s92, 0, s92
v_mfma_f32_16x16x32_bf16 a[88:91], v[168:171], v[108:111], a[88:91]
ds_read_b64_tr_b16 v[146:147], v38 offset:2048
v_mfma_f32_16x16x32_bf16 a[92:95], v[172:175], v[108:111], a[92:95]
s_add_u32 s96, s92, s94
v_mfma_f32_16x16x32_bf16 a[96:99], v[152:155], v[112:115], a[96:99]
ds_read_b64_tr_b16 v[148:149], v37 offset:2304
s_add_u32 s97, s92, s95
v_mfma_f32_16x16x32_bf16 a[100:103], v[156:159], v[112:115], a[100:103]
v_mfma_f32_16x16x32_bf16 a[104:107], v[160:163], v[112:115], a[104:107]
ds_read_b64_tr_b16 v[150:151], v38 offset:2304
v_mfma_f32_16x16x32_bf16 a[108:111], v[164:167], v[112:115], a[108:111]
v_mfma_f32_16x16x32_bf16 a[112:115], v[168:171], v[112:115], a[112:115]
ds_read_b128 v[68:71], v34 offset:1024
s_add_u32 s47, s84, 16384
v_mfma_f32_16x16x32_bf16 a[116:119], v[172:175], v[112:115], a[116:119]
v_mfma_f32_16x16x32_bf16 a[120:123], v[152:155], v[116:119], a[120:123]
ds_read_b128 v[72:75], v34 offset:2048
v_mfma_f32_16x16x32_bf16 a[124:127], v[156:159], v[116:119], a[124:127]
buffer_store_dwordx4 a[200:203], v26, s[48:51], s47 offen offset:128 sc1
v_mfma_f32_16x16x32_bf16 a[128:131], v[160:163], v[116:119], a[128:131]
ds_read_b128 v[76:79], v34 offset:3072
v_mfma_f32_16x16x32_bf16 a[132:135], v[164:167], v[116:119], a[132:135]
v_mfma_f32_16x16x32_bf16 a[136:139], v[168:171], v[116:119], a[136:139]
ds_read_b128 v[80:83], v34 offset:4096
s_add_u32 s47, s84, 16384
v_mfma_f32_16x16x32_bf16 a[140:143], v[172:175], v[116:119], a[140:143]
v_mfma_f32_16x16x32_bf16 a[144:147], v[152:155], v[120:123], a[144:147]
ds_read_b128 v[84:87], v34 offset:5120
v_mfma_f32_16x16x32_bf16 a[148:151], v[156:159], v[120:123], a[148:151]
buffer_store_dwordx4 a[204:207], v26, s[48:51], s47 offen offset:2176 sc1
v_mfma_f32_16x16x32_bf16 a[152:155], v[160:163], v[120:123], a[152:155]
ds_read_b128 v[88:91], v34 offset:6144
v_mfma_f32_16x16x32_bf16 a[156:159], v[164:167], v[120:123], a[156:159]
v_mfma_f32_16x16x32_bf16 a[160:163], v[168:171], v[120:123], a[160:163]
ds_read_b128 v[92:95], v34 offset:7168
s_add_u32 s47, s84, 24576
v_mfma_f32_16x16x32_bf16 a[164:167], v[172:175], v[120:123], a[164:167]
v_mfma_f32_16x16x32_bf16 a[168:171], v[152:155], v[124:127], a[168:171]
v_mfma_f32_16x16x32_bf16 a[172:175], v[156:159], v[124:127], a[172:175]
buffer_store_dwordx4 v[184:187], v26, s[48:51], s47 offen offset:0 sc1
v_mfma_f32_16x16x32_bf16 a[176:179], v[160:163], v[124:127], a[176:179]
v_mfma_f32_16x16x32_bf16 a[180:183], v[164:167], v[124:127], a[180:183]
v_mfma_f32_16x16x32_bf16 a[184:187], v[168:171], v[124:127], a[184:187]
v_mfma_f32_16x16x32_bf16 a[188:191], v[172:175], v[124:127], a[188:191]
s_waitcnt vmcnt(34) lgkmcnt(0)
s_barrier
v_mfma_f32_16x16x32_bf16 a[0:3], v[128:131], v[64:67], a[0:3]
v_add_u32 v34, 0, v33
v_mfma_f32_16x16x32_bf16 a[4:7], v[132:135], v[64:67], a[4:7]
v_add_u32 v37, 0, v35
s_mov_b32 m0, s96
v_mfma_f32_16x16x32_bf16 a[8:11], v[136:139], v[64:67], a[8:11]
buffer_load_dwordx4 v32, s[64:67], s89 offen sc1 lds
v_add_u32 v38, 0, v36
v_mfma_f32_16x16x32_bf16 a[12:15], v[140:143], v[64:67], a[12:15]
buffer_load_dwordx4 v32, s[64:67], s89 offen offset:1024 sc1 lds
ds_read_b128 v[96:99], v34 offset:0
v_mfma_f32_16x16x32_bf16 a[16:19], v[144:147], v[64:67], a[16:19]
v_mfma_f32_16x16x32_bf16 a[20:23], v[148:151], v[64:67], a[20:23]
buffer_load_dwordx4 v32, s[64:67], s89 offen offset:2048 sc1 lds
ds_read_b64_tr_b16 v[152:153], v37 offset:0
v_mfma_f32_16x16x32_bf16 a[192:195], v[144:147], v[44:47], a[192:195]
buffer_load_dwordx4 v32, s[64:67], s89 offen offset:3072 sc1 lds
v_mfma_f32_16x16x32_bf16 a[196:199], v[148:151], v[44:47], a[196:199]
ds_read_b64_tr_b16 v[154:155], v38 offset:0
v_mfma_f32_16x16x32_bf16 a[24:27], v[128:131], v[68:71], a[24:27]
s_mov_b32 m0, s97
v_mfma_f32_16x16x32_bf16 a[28:31], v[132:135], v[68:71], a[28:31]
buffer_load_dwordx4 v39, s[68:71], s90 offen lds
ds_read_b64_tr_b16 v[156:157], v37 offset:256
v_mfma_f32_16x16x32_bf16 a[32:35], v[136:139], v[68:71], a[32:35]
s_add_u32 m0, s97, 1024
v_mfma_f32_16x16x32_bf16 a[36:39], v[140:143], v[68:71], a[36:39]
ds_read_b64_tr_b16 v[158:159], v38 offset:256
v_mfma_f32_16x16x32_bf16 a[40:43], v[144:147], v[68:71], a[40:43]
buffer_load_dwordx4 v40, s[68:71], s90 offen lds
v_mfma_f32_16x16x32_bf16 a[44:47], v[148:151], v[68:71], a[44:47]
ds_read_b64_tr_b16 v[160:161], v37 offset:1024
s_add_u32 m0, s97, 2048
v_mfma_f32_16x16x32_bf16 a[48:51], v[128:131], v[72:75], a[48:51]
v_mfma_f32_16x16x32_bf16 a[52:55], v[132:135], v[72:75], a[52:55]
buffer_load_dwordx4 v41, s[72:75], s91 offen lds
ds_read_b64_tr_b16 v[162:163], v38 offset:1024
v_mfma_f32_16x16x32_bf16 a[56:59], v[136:139], v[72:75], a[56:59]
s_add_u32 s89, s89, 8192
v_mfma_f32_16x16x32_bf16 a[60:63], v[140:143], v[72:75], a[60:63]
ds_read_b64_tr_b16 v[164:165], v37 offset:1280
v_mfma_f32_16x16x32_bf16 a[64:67], v[144:147], v[72:75], a[64:67]
s_add_u32 s90, s90, 262144
v_mfma_f32_16x16x32_bf16 a[68:71], v[148:151], v[72:75], a[68:71]
ds_read_b64_tr_b16 v[166:167], v38 offset:1280
s_add_u32 s91, s91, 110592
v_mfma_f32_16x16x32_bf16 a[72:75], v[128:131], v[76:79], a[72:75]
s_add_u32 s92, s92, 28672
v_mfma_f32_16x16x32_bf16 a[76:79], v[132:135], v[76:79], a[76:79]
ds_read_b64_tr_b16 v[168:169], v37 offset:2048
v_mfma_f32_16x16x32_bf16 a[80:83], v[136:139], v[76:79], a[80:83]
s_cmp_eq_u32 s92, 143360
v_mfma_f32_16x16x32_bf16 a[84:87], v[140:143], v[76:79], a[84:87]
s_cselect_b32 s92, 0, s92
v_mfma_f32_16x16x32_bf16 a[88:91], v[144:147], v[76:79], a[88:91]
ds_read_b64_tr_b16 v[170:171], v38 offset:2048
v_mfma_f32_16x16x32_bf16 a[92:95], v[148:151], v[76:79], a[92:95]
s_add_u32 s96, s92, s94
v_mfma_f32_16x16x32_bf16 a[96:99], v[128:131], v[80:83], a[96:99]
ds_read_b64_tr_b16 v[172:173], v37 offset:2304
s_add_u32 s97, s92, s95
v_mfma_f32_16x16x32_bf16 a[100:103], v[132:135], v[80:83], a[100:103]
v_mfma_f32_16x16x32_bf16 a[104:107], v[136:139], v[80:83], a[104:107]
ds_read_b64_tr_b16 v[174:175], v38 offset:2304
v_mfma_f32_16x16x32_bf16 a[108:111], v[140:143], v[80:83], a[108:111]
v_mfma_f32_16x16x32_bf16 a[112:115], v[144:147], v[80:83], a[112:115]
ds_read_b128 v[100:103], v34 offset:1024
s_add_u32 s47, s84, 24576
v_mfma_f32_16x16x32_bf16 a[116:119], v[148:151], v[80:83], a[116:119]
v_mfma_f32_16x16x32_bf16 a[120:123], v[128:131], v[84:87], a[120:123]
ds_read_b128 v[104:107], v34 offset:2048
v_mfma_f32_16x16x32_bf16 a[124:127], v[132:135], v[84:87], a[124:127]
buffer_store_dwordx4 v[188:191], v26, s[48:51], s47 offen offset:2048 sc1
v_mfma_f32_16x16x32_bf16 a[128:131], v[136:139], v[84:87], a[128:131]
ds_read_b128 v[108:111], v34 offset:3072
v_mfma_f32_16x16x32_bf16 a[132:135], v[140:143], v[84:87], a[132:135]
v_mfma_f32_16x16x32_bf16 a[136:139], v[144:147], v[84:87], a[136:139]
ds_read_b128 v[112:115], v34 offset:4096
s_add_u32 s47, s84, 24576
v_mfma_f32_16x16x32_bf16 a[140:143], v[148:151], v[84:87], a[140:143]
v_mfma_f32_16x16x32_bf16 a[144:147], v[128:131], v[88:91], a[144:147]
ds_read_b128 v[116:119], v34 offset:5120
v_mfma_f32_16x16x32_bf16 a[148:151], v[132:135], v[88:91], a[148:151]
buffer_store_dwordx4 v[192:195], v26, s[48:51], s47 offen offset:128 sc1
v_mfma_f32_16x16x32_bf16 a[152:155], v[136:139], v[88:91], a[152:155]
ds_read_b128 v[120:123], v34 offset:6144
v_mfma_f32_16x16x32_bf16 a[156:159], v[140:143], v[88:91], a[156:159]
v_mfma_f32_16x16x32_bf16 a[160:163], v[144:147], v[88:91], a[160:163]
ds_read_b128 v[124:127], v34 offset:7168
s_add_u32 s47, s84, 24576
v_mfma_f32_16x16x32_bf16 a[164:167], v[148:151], v[88:91], a[164:167]
v_mfma_f32_16x16x32_bf16 a[168:171], v[128:131], v[92:95], a[168:171]
v_mfma_f32_16x16x32_bf16 a[172:175], v[132:135], v[92:95], a[172:175]
buffer_store_dwordx4 v[196:199], v26, s[48:51], s47 offen offset:2176 sc1
v_mfma_f32_16x16x32_bf16 a[176:179], v[136:139], v[92:95], a[176:179]
v_mfma_f32_16x16x32_bf16 a[180:183], v[140:143], v[92:95], a[180:183]
v_mfma_f32_16x16x32_bf16 a[184:187], v[144:147], v[92:95], a[184:187]
v_mfma_f32_16x16x32_bf16 a[188:191], v[148:151], v[92:95], a[188:191]
s_waitcnt vmcnt(33) lgkmcnt(0)
s_barrier
v_mfma_f32_16x16x32_bf16 a[0:3], v[152:155], v[96:99], a[0:3]
v_add_u32 v34, 28672, v33
v_mfma_f32_16x16x32_bf16 a[4:7], v[156:159], v[96:99], a[4:7]
v_add_u32 v37, 28672, v35
s_mov_b32 m0, s96
v_mfma_f32_16x16x32_bf16 a[8:11], v[160:163], v[96:99], a[8:11]
buffer_load_dwordx4 v32, s[64:67], s89 offen sc1 lds
v_add_u32 v38, 28672, v36
v_mfma_f32_16x16x32_bf16 a[12:15], v[164:167], v[96:99], a[12:15]
buffer_load_dwordx4 v32, s[64:67], s89 offen offset:1024 sc1 lds
ds_read_b128 v[64:67], v34 offset:0
v_mfma_f32_16x16x32_bf16 a[16:19], v[168:171], v[96:99], a[16:19]
v_mfma_f32_16x16x32_bf16 a[20:23], v[172:175], v[96:99], a[20:23]
buffer_load_dwordx4 v32, s[64:67], s89 offen offset:2048 sc1 lds
ds_read_b64_tr_b16 v[128:129], v37 offset:0
v_mfma_f32_16x16x32_bf16 a[192:195], v[168:171], v[44:47], a[192:195]
buffer_load_dwordx4 v32, s[64:67], s89 offen offset:3072 sc1 lds
v_mfma_f32_16x16x32_bf16 a[196:199], v[172:175], v[44:47], a[196:199]
ds_read_b64_tr_b16 v[130:131], v38 offset:0
v_mfma_f32_16x16x32_bf16 a[24:27], v[152:155], v[100:103], a[24:27]
s_mov_b32 m0, s97
v_mfma_f32_16x16x32_bf16 a[28:31], v[156:159], v[100:103], a[28:31]
buffer_load_dwordx4 v39, s[68:71], s90 offen lds
ds_read_b64_tr_b16 v[132:133], v37 offset:256
v_mfma_f32_16x16x32_bf16 a[32:35], v[160:163], v[100:103], a[32:35]
s_add_u32 m0, s97, 1024
v_mfma_f32_16x16x32_bf16 a[36:39], v[164:167], v[100:103], a[36:39]
ds_read_b64_tr_b16 v[134:135], v38 offset:256
v_mfma_f32_16x16x32_bf16 a[40:43], v[168:171], v[100:103], a[40:43]
buffer_load_dwordx4 v40, s[68:71], s90 offen lds
v_mfma_f32_16x16x32_bf16 a[44:47], v[172:175], v[100:103], a[44:47]
ds_read_b64_tr_b16 v[136:137], v37 offset:1024
s_add_u32 m0, s97, 2048
v_mfma_f32_16x16x32_bf16 a[48:51], v[152:155], v[104:107], a[48:51]
v_mfma_f32_16x16x32_bf16 a[52:55], v[156:159], v[104:107], a[52:55]
buffer_load_dwordx4 v41, s[72:75], s91 offen lds
ds_read_b64_tr_b16 v[138:139], v38 offset:1024
v_mfma_f32_16x16x32_bf16 a[56:59], v[160:163], v[104:107], a[56:59]
s_add_u32 s89, s89, 8192
v_mfma_f32_16x16x32_bf16 a[60:63], v[164:167], v[104:107], a[60:63]
ds_read_b64_tr_b16 v[140:141], v37 offset:1280
v_mfma_f32_16x16x32_bf16 a[64:67], v[168:171], v[104:107], a[64:67]
s_add_u32 s90, s90, 262144
v_mfma_f32_16x16x32_bf16 a[68:71], v[172:175], v[104:107], a[68:71]
ds_read_b64_tr_b16 v[142:143], v38 offset:1280
s_add_u32 s91, s91, 110592
v_mfma_f32_16x16x32_bf16 a[72:75], v[152:155], v[108:111], a[72:75]
s_add_u32 s92, s92, 28672
v_mfma_f32_16x16x32_bf16 a[76:79], v[156:159], v[108:111], a[76:79]
ds_read_b64_tr_b16 v[144:145], v37 offset:2048
v_mfma_f32_16x16x32_bf16 a[80:83], v[160:163], v[108:111], a[80:83]
s_cmp_eq_u32 s92, 143360
v_mfma_f32_16x16x32_bf16 a[84:87], v[164:167], v[108:111], a[84:87]
s_cselect_b32 s92, 0, s92
v_mfma_f32_16x16x32_bf16 a[88:91], v[168:171], v[108:111], a[88:91]
ds_read_b64_tr_b16 v[146:147], v38 offset:2048
v_mfma_f32_16x16x32_bf16 a[92:95], v[172:175], v[108:111], a[92:95]
s_add_u32 s96, s92, s94
v_mfma_f32_16x16x32_bf16 a[96:99], v[152:155], v[112:115], a[96:99]
ds_read_b64_tr_b16 v[148:149], v37 offset:2304
s_add_u32 s97, s92, s95
v_mfma_f32_16x16x32_bf16 a[100:103], v[156:159], v[112:115], a[100:103]
v_mfma_f32_16x16x32_bf16 a[104:107], v[160:163], v[112:115], a[104:107]
ds_read_b64_tr_b16 v[150:151], v38 offset:2304
v_mfma_f32_16x16x32_bf16 a[108:111], v[164:167], v[112:115], a[108:111]
v_mfma_f32_16x16x32_bf16 a[112:115], v[168:171], v[112:115], a[112:115]
ds_read_b128 v[68:71], v34 offset:1024
s_add_u32 s47, s85, 0
v_mfma_f32_16x16x32_bf16 a[116:119], v[172:175], v[112:115], a[116:119]
v_mfma_f32_16x16x32_bf16 a[120:123], v[152:155], v[116:119], a[120:123]
buffer_store_dwordx4 a[208:211], v27, s[48:51], s47 offen offset:0 sc1
ds_read_b128 v[72:75], v34 offset:2048
v_mfma_f32_16x16x32_bf16 a[124:127], v[156:159], v[116:119], a[124:127]
v_mfma_f32_16x16x32_bf16 a[128:131], v[160:163], v[116:119], a[128:131]
ds_read_b128 v[76:79], v34 offset:3072
s_add_u32 s47, s85, 0
v_mfma_f32_16x16x32_bf16 a[132:135], v[164:167], v[116:119], a[132:135]
v_mfma_f32_16x16x32_bf16 a[136:139], v[168:171], v[116:119], a[136:139]
ds_read_b128 v[80:83], v34 offset:4096
v_mfma_f32_16x16x32_bf16 a[140:143], v[172:175], v[116:119], a[140:143]
buffer_store_dwordx4 a[212:215], v27, s[48:51], s47 offen offset:256 sc1
v_mfma_f32_16x16x32_bf16 a[144:147], v[152:155], v[120:123], a[144:147]
ds_read_b128 v[84:87], v34 offset:5120
v_mfma_f32_16x16x32_bf16 a[148:151], v[156:159], v[120:123], a[148:151]
s_add_u32 s47, s85, 0
v_mfma_f32_16x16x32_bf16 a[152:155], v[160:163], v[120:123], a[152:155]
ds_read_b128 v[88:91], v34 offset:6144
v_mfma_f32_16x16x32_bf16 a[156:159], v[164:167], v[120:123], a[156:159]
buffer_store_dwordx4 a[216:219], v27, s[48:51], s47 offen offset:1024 sc1
v_mfma_f32_16x16x32_bf16 a[160:163], v[168:171], v[120:123], a[160:163]
ds_read_b128 v[92:95], v34 offset:7168
v_mfma_f32_16x16x32_bf16 a[164:167], v[172:175], v[120:123], a[164:167]
v_mfma_f32_16x16x32_bf16 a[168:171], v[152:155], v[124:127], a[168:171]
s_add_u32 s47, s85, 0
v_mfma_f32_16x16x32_bf16 a[172:175], v[156:159], v[124:127], a[172:175]
v_mfma_f32_16x16x32_bf16 a[176:179], v[160:163], v[124:127], a[176:179]
buffer_store_dwordx4 a[220:223], v27, s[48:51], s47 offen offset:1280 sc1
v_mfma_f32_16x16x32_bf16 a[180:183], v[164:167], v[124:127], a[180:183]
v_mfma_f32_16x16x32_bf16 a[184:187], v[168:171], v[124:127], a[184:187]
v_mfma_f32_16x16x32_bf16 a[188:191], v[172:175], v[124:127], a[188:191]
s_waitcnt vmcnt(34) lgkmcnt(0)
s_barrier
v_mfma_f32_16x16x32_bf16 a[0:3], v[128:131], v[64:67], a[0:3]
v_add_u32 v34, 57344, v33
v_mfma_f32_16x16x32_bf16 a[4:7], v[132:135], v[64:67], a[4:7]
v_add_u32 v37, 57344, v35
s_mov_b32 m0, s96
v_mfma_f32_16x16x32_bf16 a[8:11], v[136:139], v[64:67], a[8:11]
buffer_load_dwordx4 v32, s[64:67], s89 offen sc1 lds
v_add_u32 v38, 57344, v36
v_mfma_f32_16x16x32_bf16 a[12:15], v[140:143], v[64:67], a[12:15]
buffer_load_dwordx4 v32, s[64:67], s89 offen offset:1024 sc1 lds
ds_read_b128 v[96:99], v34 offset:0
v_mfma_f32_16x16x32_bf16 a[16:19], v[144:147], v[64:67], a[16:19]
v_mfma_f32_16x16x32_bf16 a[20:23], v[148:151], v[64:67], a[20:23]
buffer_load_dwordx4 v32, s[64:67], s89 offen offset:2048 sc1 lds
ds_read_b64_tr_b16 v[152:153], v37 offset:0
v_mfma_f32_16x16x32_bf16 a[192:195], v[144:147], v[44:47], a[192:195]
buffer_load_dwordx4 v32, s[64:67], s89 offen offset:3072 sc1 lds
v_mfma_f32_16x16x32_bf16 a[196:199], v[148:151], v[44:47], a[196:199]
ds_read_b64_tr_b16 v[154:155], v38 offset:0
v_mfma_f32_16x16x32_bf16 a[24:27], v[128:131], v[68:71], a[24:27]
s_mov_b32 m0, s97
v_mfma_f32_16x16x32_bf16 a[28:31], v[132:135], v[68:71], a[28:31]
buffer_load_dwordx4 v39, s[68:71], s90 offen lds
ds_read_b64_tr_b16 v[156:157], v37 offset:256
v_mfma_f32_16x16x32_bf16 a[32:35], v[136:139], v[68:71], a[32:35]
s_add_u32 m0, s97, 1024
v_mfma_f32_16x16x32_bf16 a[36:39], v[140:143], v[68:71], a[36:39]
ds_read_b64_tr_b16 v[158:159], v38 offset:256
v_mfma_f32_16x16x32_bf16 a[40:43], v[144:147], v[68:71], a[40:43]
buffer_load_dwordx4 v40, s[68:71], s90 offen lds
v_mfma_f32_16x16x32_bf16 a[44:47], v[148:151], v[68:71], a[44:47]
ds_read_b64_tr_b16 v[160:161], v37 offset:1024
s_add_u32 m0, s97, 2048
v_mfma_f32_16x16x32_bf16 a[48:51], v[128:131], v[72:75], a[48:51]
v_mfma_f32_16x16x32_bf16 a[52:55], v[132:135], v[72:75], a[52:55]
buffer_load_dwordx4 v41, s[72:75], s91 offen lds
ds_read_b64_tr_b16 v[162:163], v38 offset:1024
v_mfma_f32_16x16x32_bf16 a[56:59], v[136:139], v[72:75], a[56:59]
s_add_u32 s89, s89, 8192
v_mfma_f32_16x16x32_bf16 a[60:63], v[140:143], v[72:75], a[60:63]
ds_read_b64_tr_b16 v[164:165], v37 offset:1280
v_mfma_f32_16x16x32_bf16 a[64:67], v[144:147], v[72:75], a[64:67]
s_add_u32 s90, s90, 262144
v_mfma_f32_16x16x32_bf16 a[68:71], v[148:151], v[72:75], a[68:71]
ds_read_b64_tr_b16 v[166:167], v38 offset:1280
s_add_u32 s91, s91, 110592
v_mfma_f32_16x16x32_bf16 a[72:75], v[128:131], v[76:79], a[72:75]
s_add_u32 s92, s92, 28672
v_mfma_f32_16x16x32_bf16 a[76:79], v[132:135], v[76:79], a[76:79]
ds_read_b64_tr_b16 v[168:169], v37 offset:2048
v_mfma_f32_16x16x32_bf16 a[80:83], v[136:139], v[76:79], a[80:83]
s_cmp_eq_u32 s92, 143360
v_mfma_f32_16x16x32_bf16 a[84:87], v[140:143], v[76:79], a[84:87]
s_cselect_b32 s92, 0, s92
v_mfma_f32_16x16x32_bf16 a[88:91], v[144:147], v[76:79], a[88:91]
ds_read_b64_tr_b16 v[170:171], v38 offset:2048
v_mfma_f32_16x16x32_bf16 a[92:95], v[148:151], v[76:79], a[92:95]
s_add_u32 s96, s92, s94
v_mfma_f32_16x16x32_bf16 a[96:99], v[128:131], v[80:83], a[96:99]
ds_read_b64_tr_b16 v[172:173], v37 offset:2304
s_add_u32 s97, s92, s95
v_mfma_f32_16x16x32_bf16 a[100:103], v[132:135], v[80:83], a[100:103]
v_mfma_f32_16x16x32_bf16 a[104:107], v[136:139], v[80:83], a[104:107]
ds_read_b64_tr_b16 v[174:175], v38 offset:2304
v_mfma_f32_16x16x32_bf16 a[108:111], v[140:143], v[80:83], a[108:111]
v_mfma_f32_16x16x32_bf16 a[112:115], v[144:147], v[80:83], a[112:115]
ds_read_b128 v[100:103], v34 offset:1024
s_add_u32 s47, s85, 8192
v_mfma_f32_16x16x32_bf16 a[116:119], v[148:151], v[80:83], a[116:119]
v_mfma_f32_16x16x32_bf16 a[120:123], v[128:131], v[84:87], a[120:123]
ds_read_b128 v[104:107], v34 offset:2048
v_mfma_f32_16x16x32_bf16 a[124:127], v[132:135], v[84:87], a[124:127]
buffer_store_dwordx4 a[224:227], v27, s[48:51], s47 offen offset:0 sc1
v_mfma_f32_16x16x32_bf16 a[128:131], v[136:139], v[84:87], a[128:131]
ds_read_b128 v[108:111], v34 offset:3072
v_mfma_f32_16x16x32_bf16 a[132:135], v[140:143], v[84:87], a[132:135]
v_mfma_f32_16x16x32_bf16 a[136:139], v[144:147], v[84:87], a[136:139]
ds_read_b128 v[112:115], v34 offset:4096
s_add_u32 s47, s85, 8192
v_mfma_f32_16x16x32_bf16 a[140:143], v[148:151], v[84:87], a[140:143]
v_mfma_f32_16x16x32_bf16 a[144:147], v[128:131], v[88:91], a[144:147]
ds_read_b128 v[116:119], v34 offset:5120
v_mfma_f32_16x16x32_bf16 a[148:151], v[132:135], v[88:91], a[148:151]
buffer_store_dwordx4 a[228:231], v27, s[48:51], s47 offen offset:256 sc1
v_mfma_f32_16x16x32_bf16 a[152:155], v[136:139], v[88:91], a[152:155]
ds_read_b128 v[120:123], v34 offset:6144
v_mfma_f32_16x16x32_bf16 a[156:159], v[140:143], v[88:91], a[156:159]
v_mfma_f32_16x16x32_bf16 a[160:163], v[144:147], v[88:91], a[160:163]
ds_read_b128 v[124:127], v34 offset:7168
s_add_u32 s47, s85, 8192
v_mfma_f32_16x16x32_bf16 a[164:167], v[148:151], v[88:91], a[164:167]
v_mfma_f32_16x16x32_bf16 a[168:171], v[128:131], v[92:95], a[168:171]
v_mfma_f32_16x16x32_bf16 a[172:175], v[132:135], v[92:95], a[172:175]
buffer_store_dwordx4 a[232:235], v27, s[48:51], s47 offen offset:1024 sc1
v_mfma_f32_16x16x32_bf16 a[176:179], v[136:139], v[92:95], a[176:179]
v_mfma_f32_16x16x32_bf16 a[180:183], v[140:143], v[92:95], a[180:183]
v_mfma_f32_16x16x32_bf16 a[184:187], v[144:147], v[92:95], a[184:187]
v_mfma_f32_16x16x32_bf16 a[188:191], v[148:151], v[92:95], a[188:191]
s_waitcnt vmcnt(34) lgkmcnt(0)
s_barrier
v_mfma_f32_16x16x32_bf16 a[0:3], v[152:155], v[96:99], a[0:3]
v_add_u32 v34, 86016, v33
v_mfma_f32_16x16x32_bf16 a[4:7], v[156:159], v[96:99], a[4:7]
v_add_u32 v37, 86016, v35
s_mov_b32 m0, s96
v_mfma_f32_16x16x32_bf16 a[8:11], v[160:163], v[96:99], a[8:11]
buffer_load_dwordx4 v32, s[64:67], s89 offen sc1 lds
v_add_u32 v38, 86016, v36
v_mfma_f32_16x16x32_bf16 a[12:15], v[164:167], v[96:99], a[12:15]
buffer_load_dwordx4 v32, s[64:67], s89 offen offset:1024 sc1 lds
ds_read_b128 v[64:67], v34 offset:0
v_mfma_f32_16x16x32_bf16 a[16:19], v[168:171], v[96:99], a[16:19]
v_mfma_f32_16x16x32_bf16 a[20:23], v[172:175], v[96:99], a[20:23]
buffer_load_dwordx4 v32, s[64:67], s89 offen offset:2048 sc1 lds
ds_read_b64_tr_b16 v[128:129], v37 offset:0
v_mfma_f32_16x16x32_bf16 a[192:195], v[168:171], v[44:47], a[192:195]
buffer_load_dwordx4 v32, s[64:67], s89 offen offset:3072 sc1 lds
v_mfma_f32_16x16x32_bf16 a[196:199], v[172:175], v[44:47], a[196:199]
ds_read_b64_tr_b16 v[130:131], v38 offset:0
v_mfma_f32_16x16x32_bf16 a[24:27], v[152:155], v[100:103], a[24:27]
s_mov_b32 m0, s97
v_mfma_f32_16x16x32_bf16 a[28:31], v[156:159], v[100:103], a[28:31]
buffer_load_dwordx4 v39, s[68:71], s90 offen lds
ds_read_b64_tr_b16 v[132:133], v37 offset:256
v_mfma_f32_16x16x32_bf16 a[32:35], v[160:163], v[100:103], a[32:35]
s_add_u32 m0, s97, 1024
v_mfma_f32_16x16x32_bf16 a[36:39], v[164:167], v[100:103], a[36:39]
ds_read_b64_tr_b16 v[134:135], v38 offset:256
v_mfma_f32_16x16x32_bf16 a[40:43], v[168:171], v[100:103], a[40:43]
buffer_load_dwordx4 v40, s[68:71], s90 offen lds
v_mfma_f32_16x16x32_bf16 a[44:47], v[172:175], v[100:103], a[44:47]
ds_read_b64_tr_b16 v[136:137], v37 offset:1024
s_add_u32 m0, s97, 2048
v_mfma_f32_16x16x32_bf16 a[48:51], v[152:155], v[104:107], a[48:51]
v_mfma_f32_16x16x32_bf16 a[52:55], v[156:159], v[104:107], a[52:55]
buffer_load_dwordx4 v41, s[72:75], s91 offen lds
ds_read_b64_tr_b16 v[138:139], v38 offset:1024
v_mfma_f32_16x16x32_bf16 a[56:59], v[160:163], v[104:107], a[56:59]
s_add_u32 s89, s89, 8192
v_mfma_f32_16x16x32_bf16 a[60:63], v[164:167], v[104:107], a[60:63]
ds_read_b64_tr_b16 v[140:141], v37 offset:1280
v_mfma_f32_16x16x32_bf16 a[64:67], v[168:171], v[104:107], a[64:67]
s_add_u32 s90, s90, 262144
v_mfma_f32_16x16x32_bf16 a[68:71], v[172:175], v[104:107], a[68:71]
ds_read_b64_tr_b16 v[142:143], v38 offset:1280
s_add_u32 s91, s91, 110592
v_mfma_f32_16x16x32_bf16 a[72:75], v[152:155], v[108:111], a[72:75]
s_add_u32 s92, s92, 28672
v_mfma_f32_16x16x32_bf16 a[76:79], v[156:159], v[108:111], a[76:79]
ds_read_b64_tr_b16 v[144:145], v37 offset:2048
v_mfma_f32_16x16x32_bf16 a[80:83], v[160:163], v[108:111], a[80:83]
s_cmp_eq_u32 s92, 143360
v_mfma_f32_16x16x32_bf16 a[84:87], v[164:167], v[108:111], a[84:87]
s_cselect_b32 s92, 0, s92
v_mfma_f32_16x16x32_bf16 a[88:91], v[168:171], v[108:111], a[88:91]
ds_read_b64_tr_b16 v[146:147], v38 offset:2048
v_mfma_f32_16x16x32_bf16 a[92:95], v[172:175], v[108:111], a[92:95]
s_add_u32 s96, s92, s94
v_mfma_f32_16x16x32_bf16 a[96:99], v[152:155], v[112:115], a[96:99]
ds_read_b64_tr_b16 v[148:149], v37 offset:2304
s_add_u32 s97, s92, s95
v_mfma_f32_16x16x32_bf16 a[100:103], v[156:159], v[112:115], a[100:103]
v_mfma_f32_16x16x32_bf16 a[104:107], v[160:163], v[112:115], a[104:107]
ds_read_b64_tr_b16 v[150:151], v38 offset:2304
v_mfma_f32_16x16x32_bf16 a[108:111], v[164:167], v[112:115], a[108:111]
v_mfma_f32_16x16x32_bf16 a[112:115], v[168:171], v[112:115], a[112:115]
ds_read_b128 v[68:71], v34 offset:1024
s_add_u32 s47, s85, 8192
v_mfma_f32_16x16x32_bf16 a[116:119], v[172:175], v[112:115], a[116:119]
v_mfma_f32_16x16x32_bf16 a[120:123], v[152:155], v[116:119], a[120:123]
ds_read_b128 v[72:75], v34 offset:2048
v_mfma_f32_16x16x32_bf16 a[124:127], v[156:159], v[116:119], a[124:127]
buffer_store_dwordx4 a[236:239], v27, s[48:51], s47 offen offset:1280 sc1
v_mfma_f32_16x16x32_bf16 a[128:131], v[160:163], v[116:119], a[128:131]
ds_read_b128 v[76:79], v34 offset:3072
v_mfma_f32_16x16x32_bf16 a[132:135], v[164:167], v[116:119], a[132:135]
v_mfma_f32_16x16x32_bf16 a[136:139], v[168:171], v[116:119], a[136:139]
ds_read_b128 v[80:83], v34 offset:4096
s_add_u32 s47, s85, 16384
v_mfma_f32_16x16x32_bf16 a[140:143], v[172:175], v[116:119], a[140:143]
v_mfma_f32_16x16x32_bf16 a[144:147], v[152:155], v[120:123], a[144:147]
ds_read_b128 v[84:87], v34 offset:5120
v_mfma_f32_16x16x32_bf16 a[148:151], v[156:159], v[120:123], a[148:151]
buffer_store_dwordx4 a[240:243], v27, s[48:51], s47 offen offset:0 sc1
v_mfma_f32_16x16x32_bf16 a[152:155], v[160:163], v[120:123], a[152:155]
ds_read_b128 v[88:91], v34 offset:6144
v_mfma_f32_16x16x32_bf16 a[156:159], v[164:167], v[120:123], a[156:159]
v_mfma_f32_16x16x32_bf16 a[160:163], v[168:171], v[120:123], a[160:163]
ds_read_b128 v[92:95], v34 offset:7168
s_add_u32 s47, s85, 16384
v_mfma_f32_16x16x32_bf16 a[164:167], v[172:175], v[120:123], a[164:167]
v_mfma_f32_16x16x32_bf16 a[168:171], v[152:155], v[124:127], a[168:171]
v_mfma_f32_16x16x32_bf16 a[172:175], v[156:159], v[124:127], a[172:175]
buffer_store_dwordx4 a[244:247], v27, s[48:51], s47 offen offset:256 sc1
v_mfma_f32_16x16x32_bf16 a[176:179], v[160:163], v[124:127], a[176:179]
v_mfma_f32_16x16x32_bf16 a[180:183], v[164:167], v[124:127], a[180:183]
v_mfma_f32_16x16x32_bf16 a[184:187], v[168:171], v[124:127], a[184:187]
v_mfma_f32_16x16x32_bf16 a[188:191], v[172:175], v[124:127], a[188:191]
s_waitcnt vmcnt(34) lgkmcnt(0)
s_barrier
v_mfma_f32_16x16x32_bf16 a[0:3], v[128:131], v[64:67], a[0:3]
v_add_u32 v34, 114688, v33
v_mfma_f32_16x16x32_bf16 a[4:7], v[132:135], v[64:67], a[4:7]
v_add_u32 v37, 114688, v35
s_mov_b32 m0, s96
v_mfma_f32_16x16x32_bf16 a[8:11], v[136:139], v[64:67], a[8:11]
buffer_load_dwordx4 v32, s[64:67], s89 offen sc1 lds
v_add_u32 v38, 114688, v36
v_mfma_f32_16x16x32_bf16 a[12:15], v[140:143], v[64:67], a[12:15]
buffer_load_dwordx4 v32, s[64:67], s89 offen offset:1024 sc1 lds
ds_read_b128 v[96:99], v34 offset:0
v_mfma_f32_16x16x32_bf16 a[16:19], v[144:147], v[64:67], a[16:19]
v_mfma_f32_16x16x32_bf16 a[20:23], v[148:151], v[64:67], a[20:23]
buffer_load_dwordx4 v32, s[64:67], s89 offen offset:2048 sc1 lds
ds_read_b64_tr_b16 v[152:153], v37 offset:0
v_mfma_f32_16x16x32_bf16 a[192:195], v[144:147], v[44:47], a[192:195]
buffer_load_dwordx4 v32, s[64:67], s89 offen offset:3072 sc1 lds
v_mfma_f32_16x16x32_bf16 a[196:199], v[148:151], v[44:47], a[196:199]
ds_read_b64_tr_b16 v[154:155], v38 offset:0
v_mfma_f32_16x16x32_bf16 a[24:27], v[128:131], v[68:71], a[24:27]
s_mov_b32 m0, s97
v_mfma_f32_16x16x32_bf16 a[28:31], v[132:135], v[68:71], a[28:31]
buffer_load_dwordx4 v39, s[68:71], s90 offen lds
ds_read_b64_tr_b16 v[156:157], v37 offset:256
v_mfma_f32_16x16x32_bf16 a[32:35], v[136:139], v[68:71], a[32:35]
s_add_u32 m0, s97, 1024
v_mfma_f32_16x16x32_bf16 a[36:39], v[140:143], v[68:71], a[36:39]
ds_read_b64_tr_b16 v[158:159], v38 offset:256
v_mfma_f32_16x16x32_bf16 a[40:43], v[144:147], v[68:71], a[40:43]
buffer_load_dwordx4 v40, s[68:71], s90 offen lds
v_mfma_f32_16x16x32_bf16 a[44:47], v[148:151], v[68:71], a[44:47]
ds_read_b64_tr_b16 v[160:161], v37 offset:1024
s_add_u32 m0, s97, 2048
v_mfma_f32_16x16x32_bf16 a[48:51], v[128:131], v[72:75], a[48:51]
v_mfma_f32_16x16x32_bf16 a[52:55], v[132:135], v[72:75], a[52:55]
buffer_load_dwordx4 v41, s[72:75], s91 offen lds
ds_read_b64_tr_b16 v[162:163], v38 offset:1024
v_mfma_f32_16x16x32_bf16 a[56:59], v[136:139], v[72:75], a[56:59]
s_add_u32 s89, s89, 8192
v_mfma_f32_16x16x32_bf16 a[60:63], v[140:143], v[72:75], a[60:63]
ds_read_b64_tr_b16 v[164:165], v37 offset:1280
v_mfma_f32_16x16x32_bf16 a[64:67], v[144:147], v[72:75], a[64:67]
s_add_u32 s90, s90, 262144
v_mfma_f32_16x16x32_bf16 a[68:71], v[148:151], v[72:75], a[68:71]
ds_read_b64_tr_b16 v[166:167], v38 offset:1280
s_add_u32 s91, s91, 110592
v_mfma_f32_16x16x32_bf16 a[72:75], v[128:131], v[76:79], a[72:75]
s_add_u32 s92, s92, 28672
v_mfma_f32_16x16x32_bf16 a[76:79], v[132:135], v[76:79], a[76:79]
ds_read_b64_tr_b16 v[168:169], v37 offset:2048
v_mfma_f32_16x16x32_bf16 a[80:83], v[136:139], v[76:79], a[80:83]
s_cmp_eq_u32 s92, 143360
v_mfma_f32_16x16x32_bf16 a[84:87], v[140:143], v[76:79], a[84:87]
s_cselect_b32 s92, 0, s92
v_mfma_f32_16x16x32_bf16 a[88:91], v[144:147], v[76:79], a[88:91]
ds_read_b64_tr_b16 v[170:171], v38 offset:2048
v_mfma_f32_16x16x32_bf16 a[92:95], v[148:151], v[76:79], a[92:95]
s_add_u32 s96, s92, s94
v_mfma_f32_16x16x32_bf16 a[96:99], v[128:131], v[80:83], a[96:99]
ds_read_b64_tr_b16 v[172:173], v37 offset:2304
s_add_u32 s97, s92, s95
v_mfma_f32_16x16x32_bf16 a[100:103], v[132:135], v[80:83], a[100:103]
v_mfma_f32_16x16x32_bf16 a[104:107], v[136:139], v[80:83], a[104:107]
ds_read_b64_tr_b16 v[174:175], v38 offset:2304
v_mfma_f32_16x16x32_bf16 a[108:111], v[140:143], v[80:83], a[108:111]
v_mfma_f32_16x16x32_bf16 a[112:115], v[144:147], v[80:83], a[112:115]
ds_read_b128 v[100:103], v34 offset:1024
s_add_u32 s47, s85, 16384
v_mfma_f32_16x16x32_bf16 a[116:119], v[148:151], v[80:83], a[116:119]
v_mfma_f32_16x16x32_bf16 a[120:123], v[128:131], v[84:87], a[120:123]
ds_read_b128 v[104:107], v34 offset:2048
v_mfma_f32_16x16x32_bf16 a[124:127], v[132:135], v[84:87], a[124:127]
buffer_store_dwordx4 a[248:251], v27, s[48:51], s47 offen offset:1024 sc1
v_mfma_f32_16x16x32_bf16 a[128:131], v[136:139], v[84:87], a[128:131]
ds_read_b128 v[108:111], v34 offset:3072
v_mfma_f32_16x16x32_bf16 a[132:135], v[140:143], v[84:87], a[132:135]
v_mfma_f32_16x16x32_bf16 a[136:139], v[144:147], v[84:87], a[136:139]
ds_read_b128 v[112:115], v34 offset:4096
s_add_u32 s47, s85, 16384
v_mfma_f32_16x16x32_bf16 a[140:143], v[148:151], v[84:87], a[140:143]
v_mfma_f32_16x16x32_bf16 a[144:147], v[128:131], v[88:91], a[144:147]
ds_read_b128 v[116:119], v34 offset:5120
v_mfma_f32_16x16x32_bf16 a[148:151], v[132:135], v[88:91], a[148:151]
buffer_store_dwordx4 a[252:255], v27, s[48:51], s47 offen offset:1280 sc1
v_mfma_f32_16x16x32_bf16 a[152:155], v[136:139], v[88:91], a[152:155]
ds_read_b128 v[120:123], v34 offset:6144
v_mfma_f32_16x16x32_bf16 a[156:159], v[140:143], v[88:91], a[156:159]
v_mfma_f32_16x16x32_bf16 a[160:163], v[144:147], v[88:91], a[160:163]
ds_read_b128 v[124:127], v34 offset:7168
s_add_u32 s47, s85, 24576
v_mfma_f32_16x16x32_bf16 a[164:167], v[148:151], v[88:91], a[164:167]
v_mfma_f32_16x16x32_bf16 a[168:171], v[128:131], v[92:95], a[168:171]
v_mfma_f32_16x16x32_bf16 a[172:175], v[132:135], v[92:95], a[172:175]
buffer_store_dwordx4 v[200:203], v27, s[48:51], s47 offen offset:0 sc1
v_mfma_f32_16x16x32_bf16 a[176:179], v[136:139], v[92:95], a[176:179]
v_mfma_f32_16x16x32_bf16 a[180:183], v[140:143], v[92:95], a[180:183]
v_mfma_f32_16x16x32_bf16 a[184:187], v[144:147], v[92:95], a[184:187]
v_mfma_f32_16x16x32_bf16 a[188:191], v[148:151], v[92:95], a[188:191]
s_waitcnt vmcnt(34) lgkmcnt(0)
s_barrier
v_mfma_f32_16x16x32_bf16 a[0:3], v[152:155], v[96:99], a[0:3]
v_add_u32 v34, 0, v33
v_mfma_f32_16x16x32_bf16 a[4:7], v[156:159], v[96:99], a[4:7]
v_add_u32 v37, 0, v35
s_mov_b32 m0, s96
v_mfma_f32_16x16x32_bf16 a[8:11], v[160:163], v[96:99], a[8:11]
buffer_load_dwordx4 v32, s[64:67], s89 offen sc1 lds
v_add_u32 v38, 0, v36
v_mfma_f32_16x16x32_bf16 a[12:15], v[164:167], v[96:99], a[12:15]
buffer_load_dwordx4 v32, s[64:67], s89 offen offset:1024 sc1 lds
ds_read_b128 v[64:67], v34 offset:0
v_mfma_f32_16x16x32_bf16 a[16:19], v[168:171], v[96:99], a[16:19]
v_mfma_f32_16x16x32_bf16 a[20:23], v[172:175], v[96:99], a[20:23]
buffer_load_dwordx4 v32, s[64:67], s89 offen offset:2048 sc1 lds
ds_read_b64_tr_b16 v[128:129], v37 offset:0
v_mfma_f32_16x16x32_bf16 a[192:195], v[168:171], v[44:47], a[192:195]
buffer_load_dwordx4 v32, s[64:67], s89 offen offset:3072 sc1 lds
v_mfma_f32_16x16x32_bf16 a[196:199], v[172:175], v[44:47], a[196:199]
ds_read_b64_tr_b16 v[130:131], v38 offset:0
v_mfma_f32_16x16x32_bf16 a[24:27], v[152:155], v[100:103], a[24:27]
s_mov_b32 m0, s97
v_mfma_f32_16x16x32_bf16 a[28:31], v[156:159], v[100:103], a[28:31]
buffer_load_dwordx4 v39, s[68:71], s90 offen lds
ds_read_b64_tr_b16 v[132:133], v37 offset:256
v_mfma_f32_16x16x32_bf16 a[32:35], v[160:163], v[100:103], a[32:35]
s_add_u32 m0, s97, 1024
v_mfma_f32_16x16x32_bf16 a[36:39], v[164:167], v[100:103], a[36:39]
ds_read_b64_tr_b16 v[134:135], v38 offset:256
v_mfma_f32_16x16x32_bf16 a[40:43], v[168:171], v[100:103], a[40:43]
buffer_load_dwordx4 v40, s[68:71], s90 offen lds
v_mfma_f32_16x16x32_bf16 a[44:47], v[172:175], v[100:103], a[44:47]
ds_read_b64_tr_b16 v[136:137], v37 offset:1024
s_add_u32 m0, s97, 2048
v_mfma_f32_16x16x32_bf16 a[48:51], v[152:155], v[104:107], a[48:51]
v_mfma_f32_16x16x32_bf16 a[52:55], v[156:159], v[104:107], a[52:55]
buffer_load_dwordx4 v41, s[72:75], s91 offen lds
ds_read_b64_tr_b16 v[138:139], v38 offset:1024
v_mfma_f32_16x16x32_bf16 a[56:59], v[160:163], v[104:107], a[56:59]
s_add_u32 s89, s89, 8192
v_mfma_f32_16x16x32_bf16 a[60:63], v[164:167], v[104:107], a[60:63]
ds_read_b64_tr_b16 v[140:141], v37 offset:1280
v_mfma_f32_16x16x32_bf16 a[64:67], v[168:171], v[104:107], a[64:67]
s_add_u32 s90, s90, 262144
v_mfma_f32_16x16x32_bf16 a[68:71], v[172:175], v[104:107], a[68:71]
ds_read_b64_tr_b16 v[142:143], v38 offset:1280
s_add_u32 s91, s91, 110592
v_mfma_f32_16x16x32_bf16 a[72:75], v[152:155], v[108:111], a[72:75]
s_add_u32 s92, s92, 28672
v_mfma_f32_16x16x32_bf16 a[76:79], v[156:159], v[108:111], a[76:79]
ds_read_b64_tr_b16 v[144:145], v37 offset:2048
v_mfma_f32_16x16x32_bf16 a[80:83], v[160:163], v[108:111], a[80:83]
s_cmp_eq_u32 s92, 143360
v_mfma_f32_16x16x32_bf16 a[84:87], v[164:167], v[108:111], a[84:87]
s_cselect_b32 s92, 0, s92
v_mfma_f32_16x16x32_bf16 a[88:91], v[168:171], v[108:111], a[88:91]
ds_read_b64_tr_b16 v[146:147], v38 offset:2048
v_mfma_f32_16x16x32_bf16 a[92:95], v[172:175], v[108:111], a[92:95]
s_add_u32 s96, s92, s94
v_mfma_f32_16x16x32_bf16 a[96:99], v[152:155], v[112:115], a[96:99]
ds_read_b64_tr_b16 v[148:149], v37 offset:2304
s_add_u32 s97, s92, s95
v_mfma_f32_16x16x32_bf16 a[100:103], v[156:159], v[112:115], a[100:103]
v_mfma_f32_16x16x32_bf16 a[104:107], v[160:163], v[112:115], a[104:107]
ds_read_b64_tr_b16 v[150:151], v38 offset:2304
v_mfma_f32_16x16x32_bf16 a[108:111], v[164:167], v[112:115], a[108:111]
v_mfma_f32_16x16x32_bf16 a[112:115], v[168:171], v[112:115], a[112:115]
ds_read_b128 v[68:71], v34 offset:1024
s_add_u32 s47, s85, 24576
v_mfma_f32_16x16x32_bf16 a[116:119], v[172:175], v[112:115], a[116:119]
v_mfma_f32_16x16x32_bf16 a[120:123], v[152:155], v[116:119], a[120:123]
ds_read_b128 v[72:75], v34 offset:2048
v_mfma_f32_16x16x32_bf16 a[124:127], v[156:159], v[116:119], a[124:127]
buffer_store_dwordx4 v[204:207], v27, s[48:51], s47 offen offset:256 sc1
v_mfma_f32_16x16x32_bf16 a[128:131], v[160:163], v[116:119], a[128:131]
ds_read_b128 v[76:79], v34 offset:3072
v_mfma_f32_16x16x32_bf16 a[132:135], v[164:167], v[116:119], a[132:135]
v_mfma_f32_16x16x32_bf16 a[136:139], v[168:171], v[116:119], a[136:139]
ds_read_b128 v[80:83], v34 offset:4096
s_add_u32 s47, s85, 24576
v_mfma_f32_16x16x32_bf16 a[140:143], v[172:175], v[116:119], a[140:143]
v_mfma_f32_16x16x32_bf16 a[144:147], v[152:155], v[120:123], a[144:147]
ds_read_b128 v[84:87], v34 offset:5120
v_mfma_f32_16x16x32_bf16 a[148:151], v[156:159], v[120:123], a[148:151]
buffer_store_dwordx4 v[208:211], v27, s[48:51], s47 offen offset:1024 sc1
v_mfma_f32_16x16x32_bf16 a[152:155], v[160:163], v[120:123], a[152:155]
ds_read_b128 v[88:91], v34 offset:6144
v_mfma_f32_16x16x32_bf16 a[156:159], v[164:167], v[120:123], a[156:159]
v_mfma_f32_16x16x32_bf16 a[160:163], v[168:171], v[120:123], a[160:163]
ds_read_b128 v[92:95], v34 offset:7168
s_add_u32 s47, s85, 24576
v_mfma_f32_16x16x32_bf16 a[164:167], v[172:175], v[120:123], a[164:167]
v_mfma_f32_16x16x32_bf16 a[168:171], v[152:155], v[124:127], a[168:171]
v_mfma_f32_16x16x32_bf16 a[172:175], v[156:159], v[124:127], a[172:175]
buffer_store_dwordx4 v[212:215], v27, s[48:51], s47 offen offset:1280 sc1
v_mfma_f32_16x16x32_bf16 a[176:179], v[160:163], v[124:127], a[176:179]
v_mfma_f32_16x16x32_bf16 a[180:183], v[164:167], v[124:127], a[180:183]
v_mfma_f32_16x16x32_bf16 a[184:187], v[168:171], v[124:127], a[184:187]
v_mfma_f32_16x16x32_bf16 a[188:191], v[172:175], v[124:127], a[188:191]
s_waitcnt vmcnt(33) lgkmcnt(0)
s_barrier
v_mfma_f32_16x16x32_bf16 a[0:3], v[128:131], v[64:67], a[0:3]
v_add_u32 v34, 28672, v33
v_mfma_f32_16x16x32_bf16 a[4:7], v[132:135], v[64:67], a[4:7]
v_add_u32 v37, 28672, v35
s_mov_b32 m0, s96
v_mfma_f32_16x16x32_bf16 a[8:11], v[136:139], v[64:67], a[8:11]
buffer_load_dwordx4 v32, s[64:67], s89 offen sc1 lds
v_add_u32 v38, 28672, v36
v_mfma_f32_16x16x32_bf16 a[12:15], v[140:143], v[64:67], a[12:15]
buffer_load_dwordx4 v32, s[64:67], s89 offen offset:1024 sc1 lds
ds_read_b128 v[96:99], v34 offset:0
v_mfma_f32_16x16x32_bf16 a[16:19], v[144:147], v[64:67], a[16:19]
v_mfma_f32_16x16x32_bf16 a[20:23], v[148:151], v[64:67], a[20:23]
buffer_load_dwordx4 v32, s[64:67], s89 offen offset:2048 sc1 lds
ds_read_b64_tr_b16 v[152:153], v37 offset:0
v_mfma_f32_16x16x32_bf16 a[192:195], v[144:147], v[44:47], a[192:195]
buffer_load_dwordx4 v32, s[64:67], s89 offen offset:3072 sc1 lds
v_mfma_f32_16x16x32_bf16 a[196:199], v[148:151], v[44:47], a[196:199]
ds_read_b64_tr_b16 v[154:155], v38 offset:0
v_mfma_f32_16x16x32_bf16 a[24:27], v[128:131], v[68:71], a[24:27]
s_mov_b32 m0, s97
v_mfma_f32_16x16x32_bf16 a[28:31], v[132:135], v[68:71], a[28:31]
buffer_load_dwordx4 v39, s[68:71], s90 offen lds
ds_read_b64_tr_b16 v[156:157], v37 offset:256
v_mfma_f32_16x16x32_bf16 a[32:35], v[136:139], v[68:71], a[32:35]
s_add_u32 m0, s97, 1024
v_mfma_f32_16x16x32_bf16 a[36:39], v[140:143], v[68:71], a[36:39]
ds_read_b64_tr_b16 v[158:159], v38 offset:256
v_mfma_f32_16x16x32_bf16 a[40:43], v[144:147], v[68:71], a[40:43]
buffer_load_dwordx4 v40, s[68:71], s90 offen lds
v_mfma_f32_16x16x32_bf16 a[44:47], v[148:151], v[68:71], a[44:47]
ds_read_b64_tr_b16 v[160:161], v37 offset:1024
s_add_u32 m0, s97, 2048
v_mfma_f32_16x16x32_bf16 a[48:51], v[128:131], v[72:75], a[48:51]
v_mfma_f32_16x16x32_bf16 a[52:55], v[132:135], v[72:75], a[52:55]
buffer_load_dwordx4 v41, s[72:75], s91 offen lds
ds_read_b64_tr_b16 v[162:163], v38 offset:1024
v_mfma_f32_16x16x32_bf16 a[56:59], v[136:139], v[72:75], a[56:59]
s_add_u32 s89, s89, 8192
v_mfma_f32_16x16x32_bf16 a[60:63], v[140:143], v[72:75], a[60:63]
ds_read_b64_tr_b16 v[164:165], v37 offset:1280
v_mfma_f32_16x16x32_bf16 a[64:67], v[144:147], v[72:75], a[64:67]
s_add_u32 s90, s90, 262144
v_mfma_f32_16x16x32_bf16 a[68:71], v[148:151], v[72:75], a[68:71]
ds_read_b64_tr_b16 v[166:167], v38 offset:1280
s_add_u32 s91, s91, 110592
v_mfma_f32_16x16x32_bf16 a[72:75], v[128:131], v[76:79], a[72:75]
s_add_u32 s92, s92, 28672
v_mfma_f32_16x16x32_bf16 a[76:79], v[132:135], v[76:79], a[76:79]
ds_read_b64_tr_b16 v[168:169], v37 offset:2048
v_mfma_f32_16x16x32_bf16 a[80:83], v[136:139], v[76:79], a[80:83]
s_cmp_eq_u32 s92, 143360
v_mfma_f32_16x16x32_bf16 a[84:87], v[140:143], v[76:79], a[84:87]
s_cselect_b32 s92, 0, s92
v_mfma_f32_16x16x32_bf16 a[88:91], v[144:147], v[76:79], a[88:91]
ds_read_b64_tr_b16 v[170:171], v38 offset:2048
v_mfma_f32_16x16x32_bf16 a[92:95], v[148:151], v[76:79], a[92:95]
s_add_u32 s96, s92, s94
v_mfma_f32_16x16x32_bf16 a[96:99], v[128:131], v[80:83], a[96:99]
ds_read_b64_tr_b16 v[172:173], v37 offset:2304
s_add_u32 s97, s92, s95
v_mfma_f32_16x16x32_bf16 a[100:103], v[132:135], v[80:83], a[100:103]
v_mfma_f32_16x16x32_bf16 a[104:107], v[136:139], v[80:83], a[104:107]
ds_read_b64_tr_b16 v[174:175], v38 offset:2304
v_mfma_f32_16x16x32_bf16 a[108:111], v[140:143], v[80:83], a[108:111]
v_mfma_f32_16x16x32_bf16 a[112:115], v[144:147], v[80:83], a[112:115]
ds_read_b128 v[100:103], v34 offset:1024
v_mfma_f32_16x16x32_bf16 a[116:119], v[148:151], v[80:83], a[116:119]
v_mfma_f32_16x16x32_bf16 a[120:123], v[128:131], v[84:87], a[120:123]
ds_read_b128 v[104:107], v34 offset:2048
v_mfma_f32_16x16x32_bf16 a[124:127], v[132:135], v[84:87], a[124:127]
v_mfma_f32_16x16x32_bf16 a[128:131], v[136:139], v[84:87], a[128:131]
ds_read_b128 v[108:111], v34 offset:3072
v_mfma_f32_16x16x32_bf16 a[132:135], v[140:143], v[84:87], a[132:135]
v_mfma_f32_16x16x32_bf16 a[136:139], v[144:147], v[84:87], a[136:139]
ds_read_b128 v[112:115], v34 offset:4096
v_mfma_f32_16x16x32_bf16 a[140:143], v[148:151], v[84:87], a[140:143]
v_mfma_f32_16x16x32_bf16 a[144:147], v[128:131], v[88:91], a[144:147]
ds_read_b128 v[116:119], v34 offset:5120
v_mfma_f32_16x16x32_bf16 a[148:151], v[132:135], v[88:91], a[148:151]
v_mfma_f32_16x16x32_bf16 a[152:155], v[136:139], v[88:91], a[152:155]
ds_read_b128 v[120:123], v34 offset:6144
v_mfma_f32_16x16x32_bf16 a[156:159], v[140:143], v[88:91], a[156:159]
v_mfma_f32_16x16x32_bf16 a[160:163], v[144:147], v[88:91], a[160:163]
ds_read_b128 v[124:127], v34 offset:7168
v_mfma_f32_16x16x32_bf16 a[164:167], v[148:151], v[88:91], a[164:167]
v_mfma_f32_16x16x32_bf16 a[168:171], v[128:131], v[92:95], a[168:171]
v_mfma_f32_16x16x32_bf16 a[172:175], v[132:135], v[92:95], a[172:175]
v_mfma_f32_16x16x32_bf16 a[176:179], v[136:139], v[92:95], a[176:179]
v_mfma_f32_16x16x32_bf16 a[180:183], v[140:143], v[92:95], a[180:183]
v_mfma_f32_16x16x32_bf16 a[184:187], v[144:147], v[92:95], a[184:187]
v_mfma_f32_16x16x32_bf16 a[188:191], v[148:151], v[92:95], a[188:191]
s_waitcnt vmcnt(30) lgkmcnt(0)
s_barrier
v_mfma_f32_16x16x32_bf16 a[0:3], v[152:155], v[96:99], a[0:3]
v_add_u32 v34, 57344, v33
v_mfma_f32_16x16x32_bf16 a[4:7], v[156:159], v[96:99], a[4:7]
v_add_u32 v37, 57344, v35
s_mov_b32 m0, s96
v_mfma_f32_16x16x32_bf16 a[8:11], v[160:163], v[96:99], a[8:11]
buffer_load_dwordx4 v32, s[64:67], s89 offen sc1 lds
v_add_u32 v38, 57344, v36
v_mfma_f32_16x16x32_bf16 a[12:15], v[164:167], v[96:99], a[12:15]
buffer_load_dwordx4 v32, s[64:67], s89 offen offset:1024 sc1 lds
ds_read_b128 v[64:67], v34 offset:0
v_mfma_f32_16x16x32_bf16 a[16:19], v[168:171], v[96:99], a[16:19]
v_mfma_f32_16x16x32_bf16 a[20:23], v[172:175], v[96:99], a[20:23]
buffer_load_dwordx4 v32, s[64:67], s89 offen offset:2048 sc1 lds
ds_read_b64_tr_b16 v[128:129], v37 offset:0
v_mfma_f32_16x16x32_bf16 a[192:195], v[168:171], v[44:47], a[192:195]
buffer_load_dwordx4 v32, s[64:67], s89 offen offset:3072 sc1 lds
v_mfma_f32_16x16x32_bf16 a[196:199], v[172:175], v[44:47], a[196:199]
ds_read_b64_tr_b16 v[130:131], v38 offset:0
v_mfma_f32_16x16x32_bf16 a[24:27], v[152:155], v[100:103], a[24:27]
s_mov_b32 m0, s97
v_mfma_f32_16x16x32_bf16 a[28:31], v[156:159], v[100:103], a[28:31]
buffer_load_dwordx4 v39, s[68:71], s90 offen lds
ds_read_b64_tr_b16 v[132:133], v37 offset:256
v_mfma_f32_16x16x32_bf16 a[32:35], v[160:163], v[100:103], a[32:35]
s_add_u32 m0, s97, 1024
v_mfma_f32_16x16x32_bf16 a[36:39], v[164:167], v[100:103], a[36:39]
ds_read_b64_tr_b16 v[134:135], v38 offset:256
v_mfma_f32_16x16x32_bf16 a[40:43], v[168:171], v[100:103], a[40:43]
buffer_load_dwordx4 v40, s[68:71], s90 offen lds
v_mfma_f32_16x16x32_bf16 a[44:47], v[172:175], v[100:103], a[44:47]
ds_read_b64_tr_b16 v[136:137], v37 offset:1024
s_add_u32 m0, s97, 2048
v_mfma_f32_16x16x32_bf16 a[48:51], v[152:155], v[104:107], a[48:51]
v_mfma_f32_16x16x32_bf16 a[52:55], v[156:159], v[104:107], a[52:55]
buffer_load_dwordx4 v41, s[72:75], s91 offen lds
ds_read_b64_tr_b16 v[138:139], v38 offset:1024
v_mfma_f32_16x16x32_bf16 a[56:59], v[160:163], v[104:107], a[56:59]
s_add_u32 s89, s89, 8192
v_mfma_f32_16x16x32_bf16 a[60:63], v[164:167], v[104:107], a[60:63]
ds_read_b64_tr_b16 v[140:141], v37 offset:1280
v_mfma_f32_16x16x32_bf16 a[64:67], v[168:171], v[104:107], a[64:67]
s_add_u32 s90, s90, 262144
v_mfma_f32_16x16x32_bf16 a[68:71], v[172:175], v[104:107], a[68:71]
ds_read_b64_tr_b16 v[142:143], v38 offset:1280
s_add_u32 s91, s91, 110592
v_mfma_f32_16x16x32_bf16 a[72:75], v[152:155], v[108:111], a[72:75]
s_add_u32 s92, s92, 28672
v_mfma_f32_16x16x32_bf16 a[76:79], v[156:159], v[108:111], a[76:79]
ds_read_b64_tr_b16 v[144:145], v37 offset:2048
v_mfma_f32_16x16x32_bf16 a[80:83], v[160:163], v[108:111], a[80:83]
s_cmp_eq_u32 s92, 143360
v_mfma_f32_16x16x32_bf16 a[84:87], v[164:167], v[108:111], a[84:87]
s_cselect_b32 s92, 0, s92
v_mfma_f32_16x16x32_bf16 a[88:91], v[168:171], v[108:111], a[88:91]
ds_read_b64_tr_b16 v[146:147], v38 offset:2048
v_mfma_f32_16x16x32_bf16 a[92:95], v[172:175], v[108:111], a[92:95]
s_add_u32 s96, s92, s94
v_mfma_f32_16x16x32_bf16 a[96:99], v[152:155], v[112:115], a[96:99]
ds_read_b64_tr_b16 v[148:149], v37 offset:2304
s_add_u32 s97, s92, s95
v_mfma_f32_16x16x32_bf16 a[100:103], v[156:159], v[112:115], a[100:103]
v_mfma_f32_16x16x32_bf16 a[104:107], v[160:163], v[112:115], a[104:107]
ds_read_b64_tr_b16 v[150:151], v38 offset:2304
v_mfma_f32_16x16x32_bf16 a[108:111], v[164:167], v[112:115], a[108:111]
v_mfma_f32_16x16x32_bf16 a[112:115], v[168:171], v[112:115], a[112:115]
ds_read_b128 v[68:71], v34 offset:1024
v_mfma_f32_16x16x32_bf16 a[116:119], v[172:175], v[112:115], a[116:119]
v_mfma_f32_16x16x32_bf16 a[120:123], v[152:155], v[116:119], a[120:123]
ds_read_b128 v[72:75], v34 offset:2048
v_mfma_f32_16x16x32_bf16 a[124:127], v[156:159], v[116:119], a[124:127]
v_mfma_f32_16x16x32_bf16 a[128:131], v[160:163], v[116:119], a[128:131]
ds_read_b128 v[76:79], v34 offset:3072
v_mfma_f32_16x16x32_bf16 a[132:135], v[164:167], v[116:119], a[132:135]
v_mfma_f32_16x16x32_bf16 a[136:139], v[168:171], v[116:119], a[136:139]
ds_read_b128 v[80:83], v34 offset:4096
v_mfma_f32_16x16x32_bf16 a[140:143], v[172:175], v[116:119], a[140:143]
v_mfma_f32_16x16x32_bf16 a[144:147], v[152:155], v[120:123], a[144:147]
ds_read_b128 v[84:87], v34 offset:5120
v_mfma_f32_16x16x32_bf16 a[148:151], v[156:159], v[120:123], a[148:151]
v_mfma_f32_16x16x32_bf16 a[152:155], v[160:163], v[120:123], a[152:155]
ds_read_b128 v[88:91], v34 offset:6144
v_mfma_f32_16x16x32_bf16 a[156:159], v[164:167], v[120:123], a[156:159]
v_mfma_f32_16x16x32_bf16 a[160:163], v[168:171], v[120:123], a[160:163]
ds_read_b128 v[92:95], v34 offset:7168
v_mfma_f32_16x16x32_bf16 a[164:167], v[172:175], v[120:123], a[164:167]
v_mfma_f32_16x16x32_bf16 a[168:171], v[152:155], v[124:127], a[168:171]
v_mfma_f32_16x16x32_bf16 a[172:175], v[156:159], v[124:127], a[172:175]
v_mfma_f32_16x16x32_bf16 a[176:179], v[160:163], v[124:127], a[176:179]
v_mfma_f32_16x16x32_bf16 a[180:183], v[164:167], v[124:127], a[180:183]
v_mfma_f32_16x16x32_bf16 a[184:187], v[168:171], v[124:127], a[184:187]
v_mfma_f32_16x16x32_bf16 a[188:191], v[172:175], v[124:127], a[188:191]
s_waitcnt vmcnt(27) lgkmcnt(0)
s_barrier
v_mfma_f32_16x16x32_bf16 a[0:3], v[128:131], v[64:67], a[0:3]
v_add_u32 v34, 86016, v33
v_mfma_f32_16x16x32_bf16 a[4:7], v[132:135], v[64:67], a[4:7]
v_add_u32 v37, 86016, v35
s_mov_b32 m0, s96
v_mfma_f32_16x16x32_bf16 a[8:11], v[136:139], v[64:67], a[8:11]
buffer_load_dwordx4 v32, s[64:67], s89 offen sc1 lds
v_add_u32 v38, 86016, v36
v_mfma_f32_16x16x32_bf16 a[12:15], v[140:143], v[64:67], a[12:15]
buffer_load_dwordx4 v32, s[64:67], s89 offen offset:1024 sc1 lds
ds_read_b128 v[96:99], v34 offset:0
v_mfma_f32_16x16x32_bf16 a[16:19], v[144:147], v[64:67], a[16:19]
v_mfma_f32_16x16x32_bf16 a[20:23], v[148:151], v[64:67], a[20:23]
buffer_load_dwordx4 v32, s[64:67], s89 offen offset:2048 sc1 lds
ds_read_b64_tr_b16 v[152:153], v37 offset:0
v_mfma_f32_16x16x32_bf16 a[192:195], v[144:147], v[44:47], a[192:195]
buffer_load_dwordx4 v32, s[64:67], s89 offen offset:3072 sc1 lds
v_mfma_f32_16x16x32_bf16 a[196:199], v[148:151], v[44:47], a[196:199]
ds_read_b64_tr_b16 v[154:155], v38 offset:0
v_mfma_f32_16x16x32_bf16 a[24:27], v[128:131], v[68:71], a[24:27]
s_mov_b32 m0, s97
v_mfma_f32_16x16x32_bf16 a[28:31], v[132:135], v[68:71], a[28:31]
buffer_load_dwordx4 v39, s[68:71], s90 offen lds
ds_read_b64_tr_b16 v[156:157], v37 offset:256
v_mfma_f32_16x16x32_bf16 a[32:35], v[136:139], v[68:71], a[32:35]
s_add_u32 m0, s97, 1024
v_mfma_f32_16x16x32_bf16 a[36:39], v[140:143], v[68:71], a[36:39]
ds_read_b64_tr_b16 v[158:159], v38 offset:256
v_mfma_f32_16x16x32_bf16 a[40:43], v[144:147], v[68:71], a[40:43]
buffer_load_dwordx4 v40, s[68:71], s90 offen lds
v_mfma_f32_16x16x32_bf16 a[44:47], v[148:151], v[68:71], a[44:47]
ds_read_b64_tr_b16 v[160:161], v37 offset:1024
s_add_u32 m0, s97, 2048
v_mfma_f32_16x16x32_bf16 a[48:51], v[128:131], v[72:75], a[48:51]
v_mfma_f32_16x16x32_bf16 a[52:55], v[132:135], v[72:75], a[52:55]
buffer_load_dwordx4 v41, s[72:75], s91 offen lds
ds_read_b64_tr_b16 v[162:163], v38 offset:1024
v_mfma_f32_16x16x32_bf16 a[56:59], v[136:139], v[72:75], a[56:59]
s_add_u32 s89, s89, 8192
v_mfma_f32_16x16x32_bf16 a[60:63], v[140:143], v[72:75], a[60:63]
ds_read_b64_tr_b16 v[164:165], v37 offset:1280
v_mfma_f32_16x16x32_bf16 a[64:67], v[144:147], v[72:75], a[64:67]
s_add_u32 s90, s90, 262144
v_mfma_f32_16x16x32_bf16 a[68:71], v[148:151], v[72:75], a[68:71]
ds_read_b64_tr_b16 v[166:167], v38 offset:1280
s_add_u32 s91, s91, 110592
v_mfma_f32_16x16x32_bf16 a[72:75], v[128:131], v[76:79], a[72:75]
s_add_u32 s92, s92, 28672
v_mfma_f32_16x16x32_bf16 a[76:79], v[132:135], v[76:79], a[76:79]
ds_read_b64_tr_b16 v[168:169], v37 offset:2048
v_mfma_f32_16x16x32_bf16 a[80:83], v[136:139], v[76:79], a[80:83]
s_cmp_eq_u32 s92, 143360
v_mfma_f32_16x16x32_bf16 a[84:87], v[140:143], v[76:79], a[84:87]
s_cselect_b32 s92, 0, s92
v_mfma_f32_16x16x32_bf16 a[88:91], v[144:147], v[76:79], a[88:91]
ds_read_b64_tr_b16 v[170:171], v38 offset:2048
v_mfma_f32_16x16x32_bf16 a[92:95], v[148:151], v[76:79], a[92:95]
s_add_u32 s96, s92, s94
v_mfma_f32_16x16x32_bf16 a[96:99], v[128:131], v[80:83], a[96:99]
ds_read_b64_tr_b16 v[172:173], v37 offset:2304
s_add_u32 s97, s92, s95
v_mfma_f32_16x16x32_bf16 a[100:103], v[132:135], v[80:83], a[100:103]
v_mfma_f32_16x16x32_bf16 a[104:107], v[136:139], v[80:83], a[104:107]
ds_read_b64_tr_b16 v[174:175], v38 offset:2304
v_mfma_f32_16x16x32_bf16 a[108:111], v[140:143], v[80:83], a[108:111]
v_mfma_f32_16x16x32_bf16 a[112:115], v[144:147], v[80:83], a[112:115]
ds_read_b128 v[100:103], v34 offset:1024
v_mfma_f32_16x16x32_bf16 a[116:119], v[148:151], v[80:83], a[116:119]
v_mfma_f32_16x16x32_bf16 a[120:123], v[128:131], v[84:87], a[120:123]
ds_read_b128 v[104:107], v34 offset:2048
v_mfma_f32_16x16x32_bf16 a[124:127], v[132:135], v[84:87], a[124:127]
v_mfma_f32_16x16x32_bf16 a[128:131], v[136:139], v[84:87], a[128:131]
ds_read_b128 v[108:111], v34 offset:3072
v_mfma_f32_16x16x32_bf16 a[132:135], v[140:143], v[84:87], a[132:135]
v_mfma_f32_16x16x32_bf16 a[136:139], v[144:147], v[84:87], a[136:139]
ds_read_b128 v[112:115], v34 offset:4096
v_mfma_f32_16x16x32_bf16 a[140:143], v[148:151], v[84:87], a[140:143]
v_mfma_f32_16x16x32_bf16 a[144:147], v[128:131], v[88:91], a[144:147]
ds_read_b128 v[116:119], v34 offset:5120
v_mfma_f32_16x16x32_bf16 a[148:151], v[132:135], v[88:91], a[148:151]
v_mfma_f32_16x16x32_bf16 a[152:155], v[136:139], v[88:91], a[152:155]
ds_read_b128 v[120:123], v34 offset:6144
v_mfma_f32_16x16x32_bf16 a[156:159], v[140:143], v[88:91], a[156:159]
v_mfma_f32_16x16x32_bf16 a[160:163], v[144:147], v[88:91], a[160:163]
ds_read_b128 v[124:127], v34 offset:7168
v_mfma_f32_16x16x32_bf16 a[164:167], v[148:151], v[88:91], a[164:167]
v_mfma_f32_16x16x32_bf16 a[168:171], v[128:131], v[92:95], a[168:171]
v_mfma_f32_16x16x32_bf16 a[172:175], v[132:135], v[92:95], a[172:175]
v_mfma_f32_16x16x32_bf16 a[176:179], v[136:139], v[92:95], a[176:179]
v_mfma_f32_16x16x32_bf16 a[180:183], v[140:143], v[92:95], a[180:183]
v_mfma_f32_16x16x32_bf16 a[184:187], v[144:147], v[92:95], a[184:187]
v_mfma_f32_16x16x32_bf16 a[188:191], v[148:151], v[92:95], a[188:191]
s_waitcnt vmcnt(24) lgkmcnt(0)
s_barrier
v_mfma_f32_16x16x32_bf16 a[0:3], v[152:155], v[96:99], a[0:3]
v_add_u32 v34, 114688, v33
v_mfma_f32_16x16x32_bf16 a[4:7], v[156:159], v[96:99], a[4:7]
v_add_u32 v37, 114688, v35
s_mov_b32 m0, s96
v_mfma_f32_16x16x32_bf16 a[8:11], v[160:163], v[96:99], a[8:11]
buffer_load_dwordx4 v32, s[64:67], s89 offen sc1 lds
v_add_u32 v38, 114688, v36
v_mfma_f32_16x16x32_bf16 a[12:15], v[164:167], v[96:99], a[12:15]
buffer_load_dwordx4 v32, s[64:67], s89 offen offset:1024 sc1 lds
ds_read_b128 v[64:67], v34 offset:0
v_mfma_f32_16x16x32_bf16 a[16:19], v[168:171], v[96:99], a[16:19]
v_mfma_f32_16x16x32_bf16 a[20:23], v[172:175], v[96:99], a[20:23]
buffer_load_dwordx4 v32, s[64:67], s89 offen offset:2048 sc1 lds
ds_read_b64_tr_b16 v[128:129], v37 offset:0
v_mfma_f32_16x16x32_bf16 a[192:195], v[168:171], v[44:47], a[192:195]
buffer_load_dwordx4 v32, s[64:67], s89 offen offset:3072 sc1 lds
v_mfma_f32_16x16x32_bf16 a[196:199], v[172:175], v[44:47], a[196:199]
ds_read_b64_tr_b16 v[130:131], v38 offset:0
v_mfma_f32_16x16x32_bf16 a[24:27], v[152:155], v[100:103], a[24:27]
s_mov_b32 m0, s97
v_mfma_f32_16x16x32_bf16 a[28:31], v[156:159], v[100:103], a[28:31]
buffer_load_dwordx4 v39, s[68:71], s90 offen lds
ds_read_b64_tr_b16 v[132:133], v37 offset:256
v_mfma_f32_16x16x32_bf16 a[32:35], v[160:163], v[100:103], a[32:35]
s_add_u32 m0, s97, 1024
v_mfma_f32_16x16x32_bf16 a[36:39], v[164:167], v[100:103], a[36:39]
ds_read_b64_tr_b16 v[134:135], v38 offset:256
v_mfma_f32_16x16x32_bf16 a[40:43], v[168:171], v[100:103], a[40:43]
buffer_load_dwordx4 v40, s[68:71], s90 offen lds
v_mfma_f32_16x16x32_bf16 a[44:47], v[172:175], v[100:103], a[44:47]
ds_read_b64_tr_b16 v[136:137], v37 offset:1024
s_add_u32 m0, s97, 2048
v_mfma_f32_16x16x32_bf16 a[48:51], v[152:155], v[104:107], a[48:51]
v_mfma_f32_16x16x32_bf16 a[52:55], v[156:159], v[104:107], a[52:55]
buffer_load_dwordx4 v41, s[72:75], s91 offen lds
ds_read_b64_tr_b16 v[138:139], v38 offset:1024
v_mfma_f32_16x16x32_bf16 a[56:59], v[160:163], v[104:107], a[56:59]
s_add_u32 s89, s89, 8192
v_mfma_f32_16x16x32_bf16 a[60:63], v[164:167], v[104:107], a[60:63]
ds_read_b64_tr_b16 v[140:141], v37 offset:1280
v_mfma_f32_16x16x32_bf16 a[64:67], v[168:171], v[104:107], a[64:67]
s_add_u32 s90, s90, 262144
v_mfma_f32_16x16x32_bf16 a[68:71], v[172:175], v[104:107], a[68:71]
ds_read_b64_tr_b16 v[142:143], v38 offset:1280
s_add_u32 s91, s91, 110592
v_mfma_f32_16x16x32_bf16 a[72:75], v[152:155], v[108:111], a[72:75]
s_add_u32 s92, s92, 28672
v_mfma_f32_16x16x32_bf16 a[76:79], v[156:159], v[108:111], a[76:79]
ds_read_b64_tr_b16 v[144:145], v37 offset:2048
v_mfma_f32_16x16x32_bf16 a[80:83], v[160:163], v[108:111], a[80:83]
s_cmp_eq_u32 s92, 143360
v_mfma_f32_16x16x32_bf16 a[84:87], v[164:167], v[108:111], a[84:87]
s_cselect_b32 s92, 0, s92
v_mfma_f32_16x16x32_bf16 a[88:91], v[168:171], v[108:111], a[88:91]
ds_read_b64_tr_b16 v[146:147], v38 offset:2048
v_mfma_f32_16x16x32_bf16 a[92:95], v[172:175], v[108:111], a[92:95]
s_add_u32 s96, s92, s94
v_mfma_f32_16x16x32_bf16 a[96:99], v[152:155], v[112:115], a[96:99]
ds_read_b64_tr_b16 v[148:149], v37 offset:2304
s_add_u32 s97, s92, s95
v_mfma_f32_16x16x32_bf16 a[100:103], v[156:159], v[112:115], a[100:103]
v_mfma_f32_16x16x32_bf16 a[104:107], v[160:163], v[112:115], a[104:107]
ds_read_b64_tr_b16 v[150:151], v38 offset:2304
v_mfma_f32_16x16x32_bf16 a[108:111], v[164:167], v[112:115], a[108:111]
v_mfma_f32_16x16x32_bf16 a[112:115], v[168:171], v[112:115], a[112:115]
ds_read_b128 v[68:71], v34 offset:1024
v_mfma_f32_16x16x32_bf16 a[116:119], v[172:175], v[112:115], a[116:119]
v_mfma_f32_16x16x32_bf16 a[120:123], v[152:155], v[116:119], a[120:123]
ds_read_b128 v[72:75], v34 offset:2048
v_mfma_f32_16x16x32_bf16 a[124:127], v[156:159], v[116:119], a[124:127]
v_mfma_f32_16x16x32_bf16 a[128:131], v[160:163], v[116:119], a[128:131]
ds_read_b128 v[76:79], v34 offset:3072
v_mfma_f32_16x16x32_bf16 a[132:135], v[164:167], v[116:119], a[132:135]
v_mfma_f32_16x16x32_bf16 a[136:139], v[168:171], v[116:119], a[136:139]
ds_read_b128 v[80:83], v34 offset:4096
v_mfma_f32_16x16x32_bf16 a[140:143], v[172:175], v[116:119], a[140:143]
v_mfma_f32_16x16x32_bf16 a[144:147], v[152:155], v[120:123], a[144:147]
ds_read_b128 v[84:87], v34 offset:5120
v_mfma_f32_16x16x32_bf16 a[148:151], v[156:159], v[120:123], a[148:151]
v_mfma_f32_16x16x32_bf16 a[152:155], v[160:163], v[120:123], a[152:155]
ds_read_b128 v[88:91], v34 offset:6144
v_mfma_f32_16x16x32_bf16 a[156:159], v[164:167], v[120:123], a[156:159]
v_mfma_f32_16x16x32_bf16 a[160:163], v[168:171], v[120:123], a[160:163]
ds_read_b128 v[92:95], v34 offset:7168
v_mfma_f32_16x16x32_bf16 a[164:167], v[172:175], v[120:123], a[164:167]
v_mfma_f32_16x16x32_bf16 a[168:171], v[152:155], v[124:127], a[168:171]
v_mfma_f32_16x16x32_bf16 a[172:175], v[156:159], v[124:127], a[172:175]
v_mfma_f32_16x16x32_bf16 a[176:179], v[160:163], v[124:127], a[176:179]
v_mfma_f32_16x16x32_bf16 a[180:183], v[164:167], v[124:127], a[180:183]
v_mfma_f32_16x16x32_bf16 a[184:187], v[168:171], v[124:127], a[184:187]
v_mfma_f32_16x16x32_bf16 a[188:191], v[172:175], v[124:127], a[188:191]
s_waitcnt vmcnt(21) lgkmcnt(0)
s_barrier
s_lshl_b32 s44, s40, 3
s_add_u32 s44, s44, s41
s_lshl_b32 s44, s44, 2
s_add_u32 s44, s44, 67912704
s_cmp_lg_u32 s38, 0
s_cbranch_scc1 L_pb3%=
v_mov_b32 v9, s42
v_mov_b32 v10, 0
buffer_store_dword v9, v10, s[48:51], s44 offen sc1
L_pb3%=:
v_mfma_f32_16x16x32_bf16 a[0:3], v[128:131], v[64:67], a[0:3]
v_add_u32 v34, 0, v33
v_mfma_f32_16x16x32_bf16 a[4:7], v[132:135], v[64:67], a[4:7]
v_add_u32 v37, 0, v35
s_mov_b32 m0, s96
v_mfma_f32_16x16x32_bf16 a[8:11], v[136:139], v[64:67], a[8:11]
buffer_load_dwordx4 v32, s[64:67], s89 offen sc1 lds
v_add_u32 v38, 0, v36
v_mfma_f32_16x16x32_bf16 a[12:15], v[140:143], v[64:67], a[12:15]
buffer_load_dwordx4 v32, s[64:67], s89 offen offset:1024 sc1 lds
ds_read_b128 v[96:99], v34 offset:0
v_mfma_f32_16x16x32_bf16 a[16:19], v[144:147], v[64:67], a[16:19]
v_mfma_f32_16x16x32_bf16 a[20:23], v[148:151], v[64:67], a[20:23]
buffer_load_dwordx4 v32, s[64:67], s89 offen offset:2048 sc1 lds
ds_read_b64_tr_b16 v[152:153], v37 offset:0
v_mfma_f32_16x16x32_bf16 a[192:195], v[144:147], v[44:47], a[192:195]
buffer_load_dwordx4 v32, s[64:67], s89 offen offset:3072 sc1 lds
v_mfma_f32_16x16x32_bf16 a[196:199], v[148:151], v[44:47], a[196:199]
ds_read_b64_tr_b16 v[154:155], v38 offset:0
v_mfma_f32_16x16x32_bf16 a[24:27], v[128:131], v[68:71], a[24:27]
s_mov_b32 m0, s97
v_mfma_f32_16x16x32_bf16 a[28:31], v[132:135], v[68:71], a[28:31]
buffer_load_dwordx4 v39, s[68:71], s90 offen lds
ds_read_b64_tr_b16 v[156:157], v37 offset:256
v_mfma_f32_16x16x32_bf16 a[32:35], v[136:139], v[68:71], a[32:35]
s_add_u32 m0, s97, 1024
v_mfma_f32_16x16x32_bf16 a[36:39], v[140:143], v[68:71], a[36:39]
ds_read_b64_tr_b16 v[158:159], v38 offset:256
v_mfma_f32_16x16x32_bf16 a[40:43], v[144:147], v[68:71], a[40:43]
buffer_load_dwordx4 v40, s[68:71], s90 offen lds
v_mfma_f32_16x16x32_bf16 a[44:47], v[148:151], v[68:71], a[44:47]
ds_read_b64_tr_b16 v[160:161], v37 offset:1024
s_add_u32 m0, s97, 2048
v_mfma_f32_16x16x32_bf16 a[48:51], v[128:131], v[72:75], a[48:51]
v_mfma_f32_16x16x32_bf16 a[52:55], v[132:135], v[72:75], a[52:55]
buffer_load_dwordx4 v41, s[72:75], s91 offen lds
ds_read_b64_tr_b16 v[162:163], v38 offset:1024
v_mfma_f32_16x16x32_bf16 a[56:59], v[136:139], v[72:75], a[56:59]
s_add_u32 s89, s89, 8192
v_mfma_f32_16x16x32_bf16 a[60:63], v[140:143], v[72:75], a[60:63]
ds_read_b64_tr_b16 v[164:165], v37 offset:1280
v_mfma_f32_16x16x32_bf16 a[64:67], v[144:147], v[72:75], a[64:67]
s_add_u32 s90, s90, 262144
v_mfma_f32_16x16x32_bf16 a[68:71], v[148:151], v[72:75], a[68:71]
ds_read_b64_tr_b16 v[166:167], v38 offset:1280
s_add_u32 s91, s91, 110592
v_mfma_f32_16x16x32_bf16 a[72:75], v[128:131], v[76:79], a[72:75]
s_add_u32 s92, s92, 28672
v_mfma_f32_16x16x32_bf16 a[76:79], v[132:135], v[76:79], a[76:79]
ds_read_b64_tr_b16 v[168:169], v37 offset:2048
v_mfma_f32_16x16x32_bf16 a[80:83], v[136:139], v[76:79], a[80:83]
s_cmp_eq_u32 s92, 143360
v_mfma_f32_16x16x32_bf16 a[84:87], v[140:143], v[76:79], a[84:87]
s_cselect_b32 s92, 0, s92
v_mfma_f32_16x16x32_bf16 a[88:91], v[144:147], v[76:79], a[88:91]
ds_read_b64_tr_b16 v[170:171], v38 offset:2048
v_mfma_f32_16x16x32_bf16 a[92:95], v[148:151], v[76:79], a[92:95]
s_add_u32 s96, s92, s94
v_mfma_f32_16x16x32_bf16 a[96:99], v[128:131], v[80:83], a[96:99]
ds_read_b64_tr_b16 v[172:173], v37 offset:2304
s_add_u32 s97, s92, s95
v_mfma_f32_16x16x32_bf16 a[100:103], v[132:135], v[80:83], a[100:103]
v_mfma_f32_16x16x32_bf16 a[104:107], v[136:139], v[80:83], a[104:107]
ds_read_b64_tr_b16 v[174:175], v38 offset:2304
v_mfma_f32_16x16x32_bf16 a[108:111], v[140:143], v[80:83], a[108:111]
v_mfma_f32_16x16x32_bf16 a[112:115], v[144:147], v[80:83], a[112:115]
ds_read_b128 v[100:103], v34 offset:1024
v_mfma_f32_16x16x32_bf16 a[116:119], v[148:151], v[80:83], a[116:119]
v_mfma_f32_16x16x32_bf16 a[120:123], v[128:131], v[84:87], a[120:123]
ds_read_b128 v[104:107], v34 offset:2048
v_mfma_f32_16x16x32_bf16 a[124:127], v[132:135], v[84:87], a[124:127]
v_mfma_f32_16x16x32_bf16 a[128:131], v[136:139], v[84:87], a[128:131]
ds_read_b128 v[108:111], v34 offset:3072
v_mfma_f32_16x16x32_bf16 a[132:135], v[140:143], v[84:87], a[132:135]
v_mfma_f32_16x16x32_bf16 a[136:139], v[144:147], v[84:87], a[136:139]
ds_read_b128 v[112:115], v34 offset:4096
v_mfma_f32_16x16x32_bf16 a[140:143], v[148:151], v[84:87], a[140:143]
v_mfma_f32_16x16x32_bf16 a[144:147], v[128:131], v[88:91], a[144:147]
ds_read_b128 v[116:119], v34 offset:5120
v_mfma_f32_16x16x32_bf16 a[148:151], v[132:135], v[88:91], a[148:151]
v_mfma_f32_16x16x32_bf16 a[152:155], v[136:139], v[88:91], a[152:155]
ds_read_b128 v[120:123], v34 offset:6144
v_mfma_f32_16x16x32_bf16 a[156:159], v[140:143], v[88:91], a[156:159]
v_mfma_f32_16x16x32_bf16 a[160:163], v[144:147], v[88:91], a[160:163]
ds_read_b128 v[124:127], v34 offset:7168
v_mfma_f32_16x16x32_bf16 a[164:167], v[148:151], v[88:91], a[164:167]
v_mfma_f32_16x16x32_bf16 a[168:171], v[128:131], v[92:95], a[168:171]
v_mfma_f32_16x16x32_bf16 a[172:175], v[132:135], v[92:95], a[172:175]
v_mfma_f32_16x16x32_bf16 a[176:179], v[136:139], v[92:95], a[176:179]
v_mfma_f32_16x16x32_bf16 a[180:183], v[140:143], v[92:95], a[180:183]
v_mfma_f32_16x16x32_bf16 a[184:187], v[144:147], v[92:95], a[184:187]
v_mfma_f32_16x16x32_bf16 a[188:191], v[148:151], v[92:95], a[188:191]
s_waitcnt vmcnt(21) lgkmcnt(0)
s_barrier
v_mfma_f32_16x16x32_bf16 a[0:3], v[152:155], v[96:99], a[0:3]
v_add_u32 v34, 28672, v33
v_mfma_f32_16x16x32_bf16 a[4:7], v[156:159], v[96:99], a[4:7]
v_add_u32 v37, 28672, v35
v_mfma_f32_16x16x32_bf16 a[8:11], v[160:163], v[96:99], a[8:11]
v_add_u32 v38, 28672, v36
s_lshr_b32 s46, s38, 1
v_mfma_f32_16x16x32_bf16 a[12:15], v[164:167], v[96:99], a[12:15]
ds_read_b128 v[64:67], v34 offset:0
v_mfma_f32_16x16x32_bf16 a[16:19], v[168:171], v[96:99], a[16:19]
v_mfma_f32_16x16x32_bf16 a[20:23], v[172:175], v[96:99], a[20:23]
ds_read_b64_tr_b16 v[128:129], v37 offset:0
v_mfma_f32_16x16x32_bf16 a[192:195], v[168:171], v[44:47], a[192:195]
v_mfma_f32_16x16x32_bf16 a[196:199], v[172:175], v[44:47], a[196:199]
ds_read_b64_tr_b16 v[130:131], v38 offset:0
s_mov_b32 s44, 2637824
v_mfma_f32_16x16x32_bf16 a[24:27], v[152:155], v[100:103], a[24:27]
v_mfma_f32_16x16x32_bf16 a[28:31], v[156:159], v[100:103], a[28:31]
ds_read_b64_tr_b16 v[132:133], v37 offset:256
v_mfma_f32_16x16x32_bf16 a[32:35], v[160:163], v[100:103], a[32:35]
v_mfma_f32_16x16x32_bf16 a[36:39], v[164:167], v[100:103], a[36:39]
ds_read_b64_tr_b16 v[134:135], v38 offset:256
v_mfma_f32_16x16x32_bf16 a[40:43], v[168:171], v[100:103], a[40:43]
s_mov_b32 s45, 19415040
v_mfma_f32_16x16x32_bf16 a[44:47], v[172:175], v[100:103], a[44:47]
ds_read_b64_tr_b16 v[136:137], v37 offset:1024
v_mfma_f32_16x16x32_bf16 a[48:51], v[152:155], v[104:107], a[48:51]
v_mfma_f32_16x16x32_bf16 a[52:55], v[156:159], v[104:107], a[52:55]
ds_read_b64_tr_b16 v[138:139], v38 offset:1024
v_mfma_f32_16x16x32_bf16 a[56:59], v[160:163], v[104:107], a[56:59]
v_mfma_f32_16x16x32_bf16 a[60:63], v[164:167], v[104:107], a[60:63]
ds_read_b64_tr_b16 v[140:141], v37 offset:1280
s_cmp_eq_u32 s46, 0
v_mfma_f32_16x16x32_bf16 a[64:67], v[168:171], v[104:107], a[64:67]
v_mfma_f32_16x16x32_bf16 a[68:71], v[172:175], v[104:107], a[68:71]
ds_read_b64_tr_b16 v[142:143], v38 offset:1280
v_mfma_f32_16x16x32_bf16 a[72:75], v[152:155], v[108:111], a[72:75]
s_load_dwordx2 s[60:61], s[36:37], 40
v_mfma_f32_16x16x32_bf16 a[76:79], v[156:159], v[108:111], a[76:79]
ds_read_b64_tr_b16 v[144:145], v37 offset:2048
s_load_dwordx2 s[64:65], s[36:37], 64
v_mfma_f32_16x16x32_bf16 a[80:83], v[160:163], v[108:111], a[80:83]
s_cselect_b32 s44, s44, s45
v_mfma_f32_16x16x32_bf16 a[84:87], v[164:167], v[108:111], a[84:87]
v_mfma_f32_16x16x32_bf16 a[88:91], v[168:171], v[108:111], a[88:91]
ds_read_b64_tr_b16 v[146:147], v38 offset:2048
v_mfma_f32_16x16x32_bf16 a[92:95], v[172:175], v[108:111], a[92:95]
v_mfma_f32_16x16x32_bf16 a[96:99], v[152:155], v[112:115], a[96:99]
ds_read_b64_tr_b16 v[148:149], v37 offset:2304
v_mfma_f32_16x16x32_bf16 a[100:103], v[156:159], v[112:115], a[100:103]
s_cselect_b32 s77, 8192, 0
v_mfma_f32_16x16x32_bf16 a[104:107], v[160:163], v[112:115], a[104:107]
ds_read_b64_tr_b16 v[150:151], v38 offset:2304
v_mfma_f32_16x16x32_bf16 a[108:111], v[164:167], v[112:115], a[108:111]
v_mfma_f32_16x16x32_bf16 a[112:115], v[168:171], v[112:115], a[112:115]
ds_read_b128 v[68:71], v34 offset:1024
v_mfma_f32_16x16x32_bf16 a[116:119], v[172:175], v[112:115], a[116:119]
v_mfma_f32_16x16x32_bf16 a[120:123], v[152:155], v[116:119], a[120:123]
ds_read_b128 v[72:75], v34 offset:2048
s_lshl_b32 s45, s40, 19
v_mfma_f32_16x16x32_bf16 a[124:127], v[156:159], v[116:119], a[124:127]
v_mfma_f32_16x16x32_bf16 a[128:131], v[160:163], v[116:119], a[128:131]
ds_read_b128 v[76:79], v34 offset:3072
v_mfma_f32_16x16x32_bf16 a[132:135], v[164:167], v[116:119], a[132:135]
v_mfma_f32_16x16x32_bf16 a[136:139], v[168:171], v[116:119], a[136:139]
ds_read_b128 v[80:83], v34 offset:4096
v_mfma_f32_16x16x32_bf16 a[140:143], v[172:175], v[116:119], a[140:143]
s_add_u32 s44, s44, s45
v_mfma_f32_16x16x32_bf16 a[144:147], v[152:155], v[120:123], a[144:147]
ds_read_b128 v[84:87], v34 offset:5120
v_mfma_f32_16x16x32_bf16 a[148:151], v[156:159], v[120:123], a[148:151]
v_mfma_f32_16x16x32_bf16 a[152:155], v[160:163], v[120:123], a[152:155]
ds_read_b128 v[88:91], v34 offset:6144
v_mfma_f32_16x16x32_bf16 a[156:159], v[164:167], v[120:123], a[156:159]
v_mfma_f32_16x16x32_bf16 a[160:163], v[168:171], v[120:123], a[160:163]
ds_read_b128 v[92:95], v34 offset:7168
s_add_u32 s52, s48, s44
v_mfma_f32_16x16x32_bf16 a[164:167], v[172:175], v[120:123], a[164:167]
v_mfma_f32_16x16x32_bf16 a[168:171], v[152:155], v[124:127], a[168:171]
v_mfma_f32_16x16x32_bf16 a[172:175], v[156:159], v[124:127], a[172:175]
v_mfma_f32_16x16x32_bf16 a[176:179], v[160:163], v[124:127], a[176:179]
v_mfma_f32_16x16x32_bf16 a[180:183], v[164:167], v[124:127], a[180:183]
v_mfma_f32_16x16x32_bf16 a[184:187], v[168:171], v[124:127], a[184:187]
v_mfma_f32_16x16x32_bf16 a[188:191], v[172:175], v[124:127], a[188:191]
s_waitcnt vmcnt(14) lgkmcnt(0)
s_barrier
v_mfma_f32_16x16x32_bf16 a[0:3], v[128:131], v[64:67], a[0:3]
v_add_u32 v34, 57344, v33
v_mfma_f32_16x16x32_bf16 a[4:7], v[132:135], v[64:67], a[4:7]
v_add_u32 v37, 57344, v35
v_mfma_f32_16x16x32_bf16 a[8:11], v[136:139], v[64:67], a[8:11]
v_add_u32 v38, 57344, v36
s_addc_u32 s53, s49, 0
v_mfma_f32_16x16x32_bf16 a[12:15], v[140:143], v[64:67], a[12:15]
ds_read_b128 v[96:99], v34 offset:0
v_mfma_f32_16x16x32_bf16 a[16:19], v[144:147], v[64:67], a[16:19]
v_mfma_f32_16x16x32_bf16 a[20:23], v[148:151], v[64:67], a[20:23]
ds_read_b64_tr_b16 v[152:153], v37 offset:0
v_mfma_f32_16x16x32_bf16 a[192:195], v[144:147], v[44:47], a[192:195]
s_mov_b32 s55, 0x20000
v_mfma_f32_16x16x32_bf16 a[196:199], v[148:151], v[44:47], a[196:199]
ds_read_b64_tr_b16 v[154:155], v38 offset:0
v_mfma_f32_16x16x32_bf16 a[24:27], v[128:131], v[68:71], a[24:27]
v_mfma_f32_16x16x32_bf16 a[28:31], v[132:135], v[68:71], a[28:31]
ds_read_b64_tr_b16 v[156:157], v37 offset:256
v_mfma_f32_16x16x32_bf16 a[32:35], v[136:139], v[68:71], a[32:35]
v_mfma_f32_16x16x32_bf16 a[36:39], v[140:143], v[68:71], a[36:39]
ds_read_b64_tr_b16 v[158:159], v38 offset:256
s_sub_u32 s88, 16, s41
v_mfma_f32_16x16x32_bf16 a[40:43], v[144:147], v[68:71], a[40:43]
v_mfma_f32_16x16x32_bf16 a[44:47], v[148:151], v[68:71], a[44:47]
ds_read_b64_tr_b16 v[160:161], v37 offset:1024
v_mfma_f32_16x16x32_bf16 a[48:51], v[128:131], v[72:75], a[48:51]
v_mfma_f32_16x16x32_bf16 a[52:55], v[132:135], v[72:75], a[52:55]
ds_read_b64_tr_b16 v[162:163], v38 offset:1024
s_add_u32 s87, s41, 1
v_mfma_f32_16x16x32_bf16 a[56:59], v[136:139], v[72:75], a[56:59]
v_mfma_f32_16x16x32_bf16 a[60:63], v[140:143], v[72:75], a[60:63]
ds_read_b64_tr_b16 v[164:165], v37 offset:1280
v_mfma_f32_16x16x32_bf16 a[64:67], v[144:147], v[72:75], a[64:67]
v_mfma_f32_16x16x32_bf16 a[68:71], v[148:151], v[72:75], a[68:71]
ds_read_b64_tr_b16 v[166:167], v38 offset:1280
v_mfma_f32_16x16x32_bf16 a[72:75], v[128:131], v[76:79], a[72:75]
s_lshl_b32 s54, s88, 15
v_mfma_f32_16x16x32_bf16 a[76:79], v[132:135], v[76:79], a[76:79]
ds_read_b64_tr_b16 v[168:169], v37 offset:2048
v_mfma_f32_16x16x32_bf16 a[80:83], v[136:139], v[76:79], a[80:83]
v_mfma_f32_16x16x32_bf16 a[84:87], v[140:143], v[76:79], a[84:87]
v_mfma_f32_16x16x32_bf16 a[88:91], v[144:147], v[76:79], a[88:91]
ds_read_b64_tr_b16 v[170:171], v38 offset:2048
s_and_b32 s44, s38, 1
v_mfma_f32_16x16x32_bf16 a[92:95], v[148:151], v[76:79], a[92:95]
v_mfma_f32_16x16x32_bf16 a[96:99], v[128:131], v[80:83], a[96:99]
ds_read_b64_tr_b16 v[172:173], v37 offset:2304
v_mfma_f32_16x16x32_bf16 a[100:103], v[132:135], v[80:83], a[100:103]
v_mfma_f32_16x16x32_bf16 a[104:107], v[136:139], v[80:83], a[104:107]
ds_read_b64_tr_b16 v[174:175], v38 offset:2304
v_mfma_f32_16x16x32_bf16 a[108:111], v[140:143], v[80:83], a[108:111]
s_lshl_b32 s76, s44, 12
v_mfma_f32_16x16x32_bf16 a[112:115], v[144:147], v[80:83], a[112:115]
ds_read_b128 v[100:103], v34 offset:1024
v_mfma_f32_16x16x32_bf16 a[116:119], v[148:151], v[80:83], a[116:119]
v_mfma_f32_16x16x32_bf16 a[120:123], v[128:131], v[84:87], a[120:123]
ds_read_b128 v[104:107], v34 offset:2048
v_mfma_f32_16x16x32_bf16 a[124:127], v[132:135], v[84:87], a[124:127]
s_lshl_b32 s45, s46, 13
v_mfma_f32_16x16x32_bf16 a[128:131], v[136:139], v[84:87], a[128:131]
ds_read_b128 v[108:111], v34 offset:3072
v_mfma_f32_16x16x32_bf16 a[132:135], v[140:143], v[84:87], a[132:135]
v_mfma_f32_16x16x32_bf16 a[136:139], v[144:147], v[84:87], a[136:139]
ds_read_b128 v[112:115], v34 offset:4096
v_mfma_f32_16x16x32_bf16 a[140:143], v[148:151], v[84:87], a[140:143]
v_mfma_f32_16x16x32_bf16 a[144:147], v[128:131], v[88:91], a[144:147]
ds_read_b128 v[116:119], v34 offset:5120
s_add_u32 s81, s45, s76
v_mfma_f32_16x16x32_bf16 a[148:151], v[132:135], v[88:91], a[148:151]
v_mfma_f32_16x16x32_bf16 a[152:155], v[136:139], v[88:91], a[152:155]
ds_read_b128 v[120:123], v34 offset:6144
v_mfma_f32_16x16x32_bf16 a[156:159], v[140:143], v[88:91], a[156:159]
v_mfma_f32_16x16x32_bf16 a[160:163], v[144:147], v[88:91], a[160:163]
ds_read_b128 v[124:127], v34 offset:7168
s_mov_b32 s82, 0
v_mfma_f32_16x16x32_bf16 a[164:167], v[148:151], v[88:91], a[164:167]
v_mfma_f32_16x16x32_bf16 a[168:171], v[128:131], v[92:95], a[168:171]
v_mfma_f32_16x16x32_bf16 a[172:175], v[132:135], v[92:95], a[172:175]
v_mfma_f32_16x16x32_bf16 a[176:179], v[136:139], v[92:95], a[176:179]
v_mfma_f32_16x16x32_bf16 a[180:183], v[140:143], v[92:95], a[180:183]
v_mfma_f32_16x16x32_bf16 a[184:187], v[144:147], v[92:95], a[184:187]
v_mfma_f32_16x16x32_bf16 a[188:191], v[148:151], v[92:95], a[188:191]
s_waitcnt vmcnt(7) lgkmcnt(0)
s_barrier
v_mfma_f32_16x16x32_bf16 a[0:3], v[152:155], v[96:99], a[0:3]
v_add_u32 v34, 86016, v33
v_mfma_f32_16x16x32_bf16 a[4:7], v[156:159], v[96:99], a[4:7]
v_add_u32 v37, 86016, v35
s_lshl_b32 s47, s40, 5
v_mfma_f32_16x16x32_bf16 a[8:11], v[160:163], v[96:99], a[8:11]
v_add_u32 v38, 86016, v36
s_add_u32 s47, s47, 67912704
v_mfma_f32_16x16x32_bf16 a[12:15], v[164:167], v[96:99], a[12:15]
ds_read_b128 v[64:67], v34 offset:0
v_and_b32 v177, 7, v8
v_mfma_f32_16x16x32_bf16 a[16:19], v[168:171], v[96:99], a[16:19]
v_lshlrev_b32 v177, 2, v177
v_mfma_f32_16x16x32_bf16 a[20:23], v[172:175], v[96:99], a[20:23]
ds_read_b64_tr_b16 v[128:129], v37 offset:0
v_add_u32 v177, s47, v177
v_mfma_f32_16x16x32_bf16 a[192:195], v[168:171], v[44:47], a[192:195]
buffer_load_dword v176, v177, s[48:51], 0 offen sc1
v_mfma_f32_16x16x32_bf16 a[196:199], v[172:175], v[44:47], a[196:199]
ds_read_b64_tr_b16 v[130:131], v38 offset:0
v_mfma_f32_16x16x32_bf16 a[24:27], v[152:155], v[100:103], a[24:27]
s_load_dwordx2 s[56:57], s[36:37], 56
v_mfma_f32_16x16x32_bf16 a[28:31], v[156:159], v[100:103], a[28:31]
ds_read_b64_tr_b16 v[132:133], v37 offset:256
v_and_b32 v42, 15, v8
v_mfma_f32_16x16x32_bf16 a[32:35], v[160:163], v[100:103], a[32:35]
v_mfma_f32_16x16x32_bf16 a[36:39], v[164:167], v[100:103], a[36:39]
ds_read_b64_tr_b16 v[134:135], v38 offset:256
v_lshlrev_b32 v42, 3, v42
v_mfma_f32_16x16x32_bf16 a[40:43], v[168:171], v[100:103], a[40:43]
s_lshl_b32 s44, s98, 10
v_mfma_f32_16x16x32_bf16 a[44:47], v[172:175], v[100:103], a[44:47]
ds_read_b64_tr_b16 v[136:137], v37 offset:1024
v_mfma_f32_16x16x32_bf16 a[48:51], v[152:155], v[104:107], a[48:51]
s_add_u32 s44, s44, 2621440
v_mfma_f32_16x16x32_bf16 a[52:55], v[156:159], v[104:107], a[52:55]
ds_read_b64_tr_b16 v[138:139], v38 offset:1024
v_mfma_f32_16x16x32_bf16 a[56:59], v[160:163], v[104:107], a[56:59]
buffer_load_dwordx2 v[192:193], v42, s[48:51], s44 offen offset:0 sc1
v_mfma_f32_16x16x32_bf16 a[60:63], v[164:167], v[104:107], a[60:63]
buffer_load_dwordx2 v[194:195], v42, s[48:51], s44 offen offset:128 sc1
ds_read_b64_tr_b16 v[140:141], v37 offset:1280
v_mfma_f32_16x16x32_bf16 a[64:67], v[168:171], v[104:107], a[64:67]
v_mfma_f32_16x16x32_bf16 a[68:71], v[172:175], v[104:107], a[68:71]
buffer_load_dwordx2 v[196:197], v42, s[48:51], s44 offen offset:256 sc1
ds_read_b64_tr_b16 v[142:143], v38 offset:1280
v_mfma_f32_16x16x32_bf16 a[72:75], v[152:155], v[108:111], a[72:75]
v_mfma_f32_16x16x32_bf16 a[76:79], v[156:159], v[108:111], a[76:79]
buffer_load_dwordx2 v[198:199], v42, s[48:51], s44 offen offset:384 sc1
ds_read_b64_tr_b16 v[144:145], v37 offset:2048
v_mfma_f32_16x16x32_bf16 a[80:83], v[160:163], v[108:111], a[80:83]
buffer_load_dwordx2 v[200:201], v42, s[48:51], s44 offen offset:512 sc1
v_mfma_f32_16x16x32_bf16 a[84:87], v[164:167], v[108:111], a[84:87]
v_mfma_f32_16x16x32_bf16 a[88:91], v[168:171], v[108:111], a[88:91]
buffer_load_dwordx2 v[202:203], v42, s[48:51], s44 offen offset:640 sc1
ds_read_b64_tr_b16 v[146:147], v38 offset:2048
v_mfma_f32_16x16x32_bf16 a[92:95], v[172:175], v[108:111], a[92:95]
buffer_load_dwordx2 v[204:205], v42, s[48:51], s44 offen offset:768 sc1
v_mfma_f32_16x16x32_bf16 a[96:99], v[152:155], v[112:115], a[96:99]
ds_read_b64_tr_b16 v[148:149], v37 offset:2304
v_mfma_f32_16x16x32_bf16 a[100:103], v[156:159], v[112:115], a[100:103]
buffer_load_dwordx2 v[206:207], v42, s[48:51], s44 offen offset:896 sc1
v_mfma_f32_16x16x32_bf16 a[104:107], v[160:163], v[112:115], a[104:107]
ds_read_b64_tr_b16 v[150:151], v38 offset:2304
v_mfma_f32_16x16x32_bf16 a[108:111], v[164:167], v[112:115], a[108:111]
v_lshrrev_b32 v43, 4, v8
v_mfma_f32_16x16x32_bf16 a[112:115], v[168:171], v[112:115], a[112:115]
ds_read_b128 v[68:71], v34 offset:1024
v_lshlrev_b32 v43, 3, v43
v_mfma_f32_16x16x32_bf16 a[116:119], v[172:175], v[112:115], a[116:119]
v_mfma_f32_16x16x32_bf16 a[120:123], v[152:155], v[116:119], a[120:123]
ds_read_b128 v[72:75], v34 offset:2048
s_lshl_b32 s45, s99, 7
v_mfma_f32_16x16x32_bf16 a[124:127], v[156:159], v[116:119], a[124:127]
v_mfma_f32_16x16x32_bf16 a[128:131], v[160:163], v[116:119], a[128:131]
ds_read_b128 v[76:79], v34 offset:3072
s_lshl_b32 s46, s101, 6
v_mfma_f32_16x16x32_bf16 a[132:135], v[164:167], v[116:119], a[132:135]
s_add_u32 s45, s45, s46
v_mfma_f32_16x16x32_bf16 a[136:139], v[168:171], v[116:119], a[136:139]
ds_read_b128 v[80:83], v34 offset:4096
v_mfma_f32_16x16x32_bf16 a[140:143], v[172:175], v[116:119], a[140:143]
s_waitcnt lgkmcnt(0)
v_mfma_f32_16x16x32_bf16 a[144:147], v[152:155], v[120:123], a[144:147]
ds_read_b128 v[84:87], v34 offset:5120
s_and_b32 s57, s57, 0xffff
v_mfma_f32_16x16x32_bf16 a[148:151], v[156:159], v[120:123], a[148:151]
v_mfma_f32_16x16x32_bf16 a[152:155], v[160:163], v[120:123], a[152:155]
ds_read_b128 v[88:91], v34 offset:6144
s_mov_b32 s58, 3456
v_mfma_f32_16x16x32_bf16 a[156:159], v[164:167], v[120:123], a[156:159]
v_mfma_f32_16x16x32_bf16 a[160:163], v[168:171], v[120:123], a[160:163]
ds_read_b128 v[92:95], v34 offset:7168
s_mov_b32 s59, 0x20000
v_mfma_f32_16x16x32_bf16 a[164:167], v[172:175], v[120:123], a[164:167]
buffer_load_dwordx2 v[216:217], v43, s[56:59], s45 offen offset:0
v_mfma_f32_16x16x32_bf16 a[168:171], v[152:155], v[124:127], a[168:171]
v_mfma_f32_16x16x32_bf16 a[172:175], v[156:159], v[124:127], a[172:175]
buffer_load_dwordx2 v[218:219], v43, s[56:59], s45 offen offset:32
v_mfma_f32_16x16x32_bf16 a[176:179], v[160:163], v[124:127], a[176:179]
v_mfma_f32_16x16x32_bf16 a[180:183], v[164:167], v[124:127], a[180:183]
v_mfma_f32_16x16x32_bf16 a[184:187], v[168:171], v[124:127], a[184:187]
v_mfma_f32_16x16x32_bf16 a[188:191], v[172:175], v[124:127], a[188:191]
s_waitcnt vmcnt(11) lgkmcnt(0)
s_barrier
v_mfma_f32_16x16x32_bf16 a[0:3], v[128:131], v[64:67], a[0:3]
v_add_u32 v34, 114688, v33
v_mfma_f32_16x16x32_bf16 a[4:7], v[132:135], v[64:67], a[4:7]
v_add_u32 v37, 114688, v35
v_mfma_f32_16x16x32_bf16 a[8:11], v[136:139], v[64:67], a[8:11]
v_add_u32 v38, 114688, v36
v_mfma_f32_16x16x32_bf16 a[12:15], v[140:143], v[64:67], a[12:15]
ds_read_b128 v[96:99], v34 offset:0
v_mfma_f32_16x16x32_bf16 a[16:19], v[144:147], v[64:67], a[16:19]
s_lshl_b32 s46, s43, 2
v_mfma_f32_16x16x32_bf16 a[20:23], v[148:151], v[64:67], a[20:23]
ds_read_b64_tr_b16 v[152:153], v37 offset:0
v_mfma_f32_16x16x32_bf16 a[192:195], v[144:147], v[44:47], a[192:195]
s_add_u32 s46, s46, s38
v_mfma_f32_16x16x32_bf16 a[196:199], v[148:151], v[44:47], a[196:199]
ds_read_b64_tr_b16 v[154:155], v38 offset:0
v_mfma_f32_16x16x32_bf16 a[24:27], v[128:131], v[68:71], a[24:27]
v_mfma_f32_16x16x32_bf16 a[28:31], v[132:135], v[68:71], a[28:31]
ds_read_b64_tr_b16 v[156:157], v37 offset:256
v_lshlrev_b32 v43, 7, v8
v_mfma_f32_16x16x32_bf16 a[32:35], v[136:139], v[68:71], a[32:35]
v_mfma_f32_16x16x32_bf16 a[36:39], v[140:143], v[68:71], a[36:39]
ds_read_b64_tr_b16 v[158:159], v38 offset:256
v_mfma_f32_16x16x32_bf16 a[40:43], v[144:147], v[68:71], a[40:43]
s_mul_i32 s44, s46, 5120
v_mfma_f32_16x16x32_bf16 a[44:47], v[148:151], v[68:71], a[44:47]
ds_read_b64_tr_b16 v[160:161], v37 offset:1024
v_mfma_f32_16x16x32_bf16 a[48:51], v[128:131], v[72:75], a[48:51]
s_and_b32 s61, s61, 0xffff
v_mfma_f32_16x16x32_bf16 a[52:55], v[132:135], v[72:75], a[52:55]
ds_read_b64_tr_b16 v[162:163], v38 offset:1024
v_mfma_f32_16x16x32_bf16 a[56:59], v[136:139], v[72:75], a[56:59]
v_mfma_f32_16x16x32_bf16 a[60:63], v[140:143], v[72:75], a[60:63]
ds_read_b64_tr_b16 v[164:165], v37 offset:1280
s_mov_b32 s63, 0x20000
v_mfma_f32_16x16x32_bf16 a[64:67], v[144:147], v[72:75], a[64:67]
v_mfma_f32_16x16x32_bf16 a[68:71], v[148:151], v[72:75], a[68:71]
ds_read_b64_tr_b16 v[166:167], v38 offset:1280
v_mfma_f32_16x16x32_bf16 a[72:75], v[128:131], v[76:79], a[72:75]
s_add_u32 s62, s44, 5120
v_mfma_f32_16x16x32_bf16 a[76:79], v[132:135], v[76:79], a[76:79]
ds_read_b64_tr_b16 v[168:169], v37 offset:2048
v_mfma_f32_16x16x32_bf16 a[80:83], v[136:139], v[76:79], a[80:83]
v_mfma_f32_16x16x32_bf16 a[84:87], v[140:143], v[76:79], a[84:87]
s_min_u32 s62, s62, 5242880
v_mfma_f32_16x16x32_bf16 a[88:91], v[144:147], v[76:79], a[88:91]
ds_read_b64_tr_b16 v[170:171], v38 offset:2048
v_mfma_f32_16x16x32_bf16 a[92:95], v[148:151], v[76:79], a[92:95]
buffer_load_dword v182, v43, s[60:63], s44 offen
v_mfma_f32_16x16x32_bf16 a[96:99], v[128:131], v[80:83], a[96:99]
ds_read_b64_tr_b16 v[172:173], v37 offset:2304
v_mfma_f32_16x16x32_bf16 a[100:103], v[132:135], v[80:83], a[100:103]
v_mfma_f32_16x16x32_bf16 a[104:107], v[136:139], v[80:83], a[104:107]
ds_read_b64_tr_b16 v[174:175], v38 offset:2304
s_mul_i32 s44, s46, 2176
v_mfma_f32_16x16x32_bf16 a[108:111], v[140:143], v[80:83], a[108:111]
v_mfma_f32_16x16x32_bf16 a[112:115], v[144:147], v[80:83], a[112:115]
ds_read_b128 v[100:103], v34 offset:1024
v_mfma_f32_16x16x32_bf16 a[116:119], v[148:151], v[80:83], a[116:119]
s_and_b32 s65, s65, 0xffff
v_mfma_f32_16x16x32_bf16 a[120:123], v[128:131], v[84:87], a[120:123]
ds_read_b128 v[104:107], v34 offset:2048
v_mfma_f32_16x16x32_bf16 a[124:127], v[132:135], v[84:87], a[124:127]
v_mfma_f32_16x16x32_bf16 a[128:131], v[136:139], v[84:87], a[128:131]
ds_read_b128 v[108:111], v34 offset:3072
s_mov_b32 s67, 0x20000
v_mfma_f32_16x16x32_bf16 a[132:135], v[140:143], v[84:87], a[132:135]
v_mfma_f32_16x16x32_bf16 a[136:139], v[144:147], v[84:87], a[136:139]
ds_read_b128 v[112:115], v34 offset:4096
s_add_u32 s66, s44, 2176
v_mfma_f32_16x16x32_bf16 a[140:143], v[148:151], v[84:87], a[140:143]
v_mfma_f32_16x16x32_bf16 a[144:147], v[128:131], v[88:91], a[144:147]
ds_read_b128 v[116:119], v34 offset:5120
v_mfma_f32_16x16x32_bf16 a[148:151], v[132:135], v[88:91], a[148:151]
s_min_u32 s66, s66, 2211840
v_mfma_f32_16x16x32_bf16 a[152:155], v[136:139], v[88:91], a[152:155]
ds_read_b128 v[120:123], v34 offset:6144
v_mfma_f32_16x16x32_bf16 a[156:159], v[140:143], v[88:91], a[156:159]
v_mfma_f32_16x16x32_bf16 a[160:163], v[144:147], v[88:91], a[160:163]
buffer_load_dword v183, v43, s[64:67], s44 offen
ds_read_b128 v[124:127], v34 offset:7168
v_mfma_f32_16x16x32_bf16 a[164:167], v[148:151], v[88:91], a[164:167]
v_mfma_f32_16x16x32_bf16 a[168:171], v[128:131], v[92:95], a[168:171]
v_mfma_f32_16x16x32_bf16 a[172:175], v[132:135], v[92:95], a[172:175]
v_mfma_f32_16x16x32_bf16 a[176:179], v[136:139], v[92:95], a[176:179]
v_mfma_f32_16x16x32_bf16 a[180:183], v[140:143], v[92:95], a[180:183]
v_mfma_f32_16x16x32_bf16 a[184:187], v[144:147], v[92:95], a[184:187]
v_mfma_f32_16x16x32_bf16 a[188:191], v[148:151], v[92:95], a[188:191]
s_waitcnt vmcnt(12) lgkmcnt(0)
v_cmp_ne_u32 vcc, s42, v176
s_nop 3
s_cmp_eq_u64 vcc, 0
s_cbranch_scc1 L_kd4%=
s_mov_b32 s45, 3000000
L_kl4%=:
buffer_load_dword v176, v177, s[48:51], 0 offen sc1
s_waitcnt vmcnt(0)
v_cmp_ne_u32 vcc, s42, v176
s_nop 3
s_cmp_eq_u64 vcc, 0
s_cbranch_scc1 L_kd4%=
s_sleep 1
s_sub_u32 s45, s45, 1
s_cmp_lg_u32 s45, 0
s_cbranch_scc1 L_kl4%=
L_kd4%=:
s_barrier
v_mfma_f32_16x16x32_bf16 a[0:3], v[152:155], v[96:99], a[0:3]
s_add_u32 m0, s81, s82
v_mfma_f32_16x16x32_bf16 a[4:7], v[156:159], v[96:99], a[4:7]
v_mfma_f32_16x16x32_bf16 a[8:11], v[160:163], v[96:99], a[8:11]
v_mfma_f32_16x16x32_bf16 a[12:15], v[164:167], v[96:99], a[12:15]
v_mfma_f32_16x16x32_bf16 a[16:19], v[168:171], v[96:99], a[16:19]
v_mfma_f32_16x16x32_bf16 a[20:23], v[172:175], v[96:99], a[20:23]
buffer_load_dwordx4 v32, s[52:55], s76 offen sc1 lds
v_mfma_f32_16x16x32_bf16 a[192:195], v[168:171], v[44:47], a[192:195]
v_mfma_f32_16x16x32_bf16 a[196:199], v[172:175], v[44:47], a[196:199]
v_mfma_f32_16x16x32_bf16 a[24:27], v[152:155], v[100:103], a[24:27]
v_mfma_f32_16x16x32_bf16 a[28:31], v[156:159], v[100:103], a[28:31]
v_mfma_f32_16x16x32_bf16 a[32:35], v[160:163], v[100:103], a[32:35]
buffer_load_dwordx4 v32, s[52:55], s76 offen offset:1024 sc1 lds
v_mfma_f32_16x16x32_bf16 a[36:39], v[164:167], v[100:103], a[36:39]
v_mfma_f32_16x16x32_bf16 a[40:43], v[168:171], v[100:103], a[40:43]
v_mfma_f32_16x16x32_bf16 a[44:47], v[172:175], v[100:103], a[44:47]
v_mfma_f32_16x16x32_bf16 a[48:51], v[152:155], v[104:107], a[48:51]
v_mfma_f32_16x16x32_bf16 a[52:55], v[156:159], v[104:107], a[52:55]
v_mfma_f32_16x16x32_bf16 a[56:59], v[160:163], v[104:107], a[56:59]
buffer_load_dwordx4 v32, s[52:55], s76 offen offset:2048 sc1 lds
v_mfma_f32_16x16x32_bf16 a[60:63], v[164:167], v[104:107], a[60:63]
v_mfma_f32_16x16x32_bf16 a[64:67], v[168:171], v[104:107], a[64:67]
v_mfma_f32_16x16x32_bf16 a[68:71], v[172:175], v[104:107], a[68:71]
v_mfma_f32_16x16x32_bf16 a[72:75], v[152:155], v[108:111], a[72:75]
v_mfma_f32_16x16x32_bf16 a[76:79], v[156:159], v[108:111], a[76:79]
buffer_load_dwordx4 v32, s[52:55], s76 offen offset:3072 sc1 lds
v_mfma_f32_16x16x32_bf16 a[80:83], v[160:163], v[108:111], a[80:83]
v_mfma_f32_16x16x32_bf16 a[84:87], v[164:167], v[108:111], a[84:87]
v_mfma_f32_16x16x32_bf16 a[88:91], v[168:171], v[108:111], a[88:91]
v_mfma_f32_16x16x32_bf16 a[92:95], v[172:175], v[108:111], a[92:95]
v_mfma_f32_16x16x32_bf16 a[96:99], v[152:155], v[112:115], a[96:99]
v_mfma_f32_16x16x32_bf16 a[100:103], v[156:159], v[112:115], a[100:103]
s_add_u32 s76, s76, s77
v_mfma_f32_16x16x32_bf16 a[104:107], v[160:163], v[112:115], a[104:107]
v_mfma_f32_16x16x32_bf16 a[108:111], v[164:167], v[112:115], a[108:111]
v_mfma_f32_16x16x32_bf16 a[112:115], v[168:171], v[112:115], a[112:115]
v_mfma_f32_16x16x32_bf16 a[116:119], v[172:175], v[112:115], a[116:119]
v_mfma_f32_16x16x32_bf16 a[120:123], v[152:155], v[116:119], a[120:123]
s_add_u32 s82, s82, 16384
v_mfma_f32_16x16x32_bf16 a[124:127], v[156:159], v[116:119], a[124:127]
v_mfma_f32_16x16x32_bf16 a[128:131], v[160:163], v[116:119], a[128:131]
v_mfma_f32_16x16x32_bf16 a[132:135], v[164:167], v[116:119], a[132:135]
v_mfma_f32_16x16x32_bf16 a[136:139], v[168:171], v[116:119], a[136:139]
v_mfma_f32_16x16x32_bf16 a[140:143], v[172:175], v[116:119], a[140:143]
v_mfma_f32_16x16x32_bf16 a[144:147], v[152:155], v[120:123], a[144:147]
s_cmp_eq_u32 s82, 163840
s_cselect_b32 s82, 0, s82
v_mfma_f32_16x16x32_bf16 a[148:151], v[156:159], v[120:123], a[148:151]
v_mfma_f32_16x16x32_bf16 a[152:155], v[160:163], v[120:123], a[152:155]
v_mfma_f32_16x16x32_bf16 a[156:159], v[164:167], v[120:123], a[156:159]
v_mfma_f32_16x16x32_bf16 a[160:163], v[168:171], v[120:123], a[160:163]
v_mfma_f32_16x16x32_bf16 a[164:167], v[172:175], v[120:123], a[164:167]
v_mfma_f32_16x16x32_bf16 a[168:171], v[152:155], v[124:127], a[168:171]
v_mfma_f32_16x16x32_bf16 a[172:175], v[156:159], v[124:127], a[172:175]
v_mfma_f32_16x16x32_bf16 a[176:179], v[160:163], v[124:127], a[176:179]
v_mfma_f32_16x16x32_bf16 a[180:183], v[164:167], v[124:127], a[180:183]
v_mfma_f32_16x16x32_bf16 a[184:187], v[168:171], v[124:127], a[184:187]
v_mfma_f32_16x16x32_bf16 a[188:191], v[172:175], v[124:127], a[188:191]
v_lshrrev_b32 v10, 4, v8
v_and_b32 v9, 15, v8
v_and_b32 v11, 1, v10
v_lshl_add_u32 v9, v11, 5, v9
v_lshlrev_b32 v9, 4, v9
v_lshrrev_b32 v11, 1, v10
v_lshl_add_u32 v28, v11, 10, v9
s_lshl_b32 s45, s101, 11
s_mov_b32 s44, 131072
s_mov_b32 s46, 81920
s_cmp_eq_u32 s100, 0
s_cselect_b32 s44, s44, s46
s_add_u32 s44, s44, s45
v_add_u32 v28, s44, v28
v_add_u32 v29, 16384, v28
s_mov_b32 s47, 0x3e0293ee
s_mov_b32 s46, 0x3e0293ee
v_accvgpr_read_b32 v64, a0
v_accvgpr_read_b32 v65, a1
v_accvgpr_read_b32 v66, a2
v_accvgpr_read_b32 v67, a3
v_accvgpr_read_b32 v68, a4
v_accvgpr_read_b32 v69, a5
v_accvgpr_read_b32 v70, a6
v_accvgpr_read_b32 v71, a7
v_pk_mul_f32 v[64:65], v[64:65], s[46:47]
v_pk_mul_f32 v[66:67], v[66:67], s[46:47]
v_pk_mul_f32 v[68:69], v[68:69], s[46:47]
v_pk_mul_f32 v[70:71], v[70:71], s[46:47]
v_cvt_pk_bf16_f32 v72, v64, v65
v_cvt_pk_bf16_f32 v73, v66, v67
v_cvt_pk_bf16_f32 v74, v68, v69
v_cvt_pk_bf16_f32 v75, v70, v71
ds_write_b128 v28, v[72:75] offset:0
s_add_u32 m0, s81, s82
s_nop 0
buffer_load_dwordx4 v32, s[52:55], s76 offen sc1 lds
v_accvgpr_read_b32 v80, a8
v_accvgpr_read_b32 v81, a9
v_accvgpr_read_b32 v82, a10
v_accvgpr_read_b32 v83, a11
v_accvgpr_read_b32 v84, a12
v_accvgpr_read_b32 v85, a13
v_accvgpr_read_b32 v86, a14
v_accvgpr_read_b32 v87, a15
v_pk_mul_f32 v[80:81], v[80:81], s[46:47]
v_pk_mul_f32 v[82:83], v[82:83], s[46:47]
v_pk_mul_f32 v[84:85], v[84:85], s[46:47]
v_pk_mul_f32 v[86:87], v[86:87], s[46:47]
v_cvt_pk_bf16_f32 v88, v80, v81
v_cvt_pk_bf16_f32 v89, v82, v83
v_cvt_pk_bf16_f32 v90, v84, v85
v_cvt_pk_bf16_f32 v91, v86, v87
ds_write_b128 v28, v[88:91] offset:4096
buffer_load_dwordx4 v32, s[52:55], s76 offen offset:1024 sc1 lds
v_accvgpr_read_b32 v96, a24
v_accvgpr_read_b32 v97, a25
v_accvgpr_read_b32 v98, a26
v_accvgpr_read_b32 v99, a27
v_accvgpr_read_b32 v100, a28
v_accvgpr_read_b32 v101, a29
v_accvgpr_read_b32 v102, a30
v_accvgpr_read_b32 v103, a31
v_pk_mul_f32 v[96:97], v[96:97], s[46:47]
v_pk_mul_f32 v[98:99], v[98:99], s[46:47]
v_pk_mul_f32 v[100:101], v[100:101], s[46:47]
v_pk_mul_f32 v[102:103], v[102:103], s[46:47]
v_cvt_pk_bf16_f32 v104, v96, v97
v_cvt_pk_bf16_f32 v105, v98, v99
v_cvt_pk_bf16_f32 v106, v100, v101
v_cvt_pk_bf16_f32 v107, v102, v103
ds_write_b128 v28, v[104:107] offset:256
buffer_load_dwordx4 v32, s[52:55], s76 offen offset:2048 sc1 lds
v_accvgpr_read_b32 v112, a32
v_accvgpr_read_b32 v113, a33
v_accvgpr_read_b32 v114, a34
v_accvgpr_read_b32 v115, a35
v_accvgpr_read_b32 v116, a36
v_accvgpr_read_b32 v117, a37
v_accvgpr_read_b32 v118, a38
v_accvgpr_read_b32 v119, a39
v_pk_mul_f32 v[112:113], v[112:113], s[46:47]
v_pk_mul_f32 v[114:115], v[114:115], s[46:47]
v_pk_mul_f32 v[116:117], v[116:117], s[46:47]
v_pk_mul_f32 v[118:119], v[118:119], s[46:47]
v_cvt_pk_bf16_f32 v120, v112, v113
v_cvt_pk_bf16_f32 v121, v114, v115
v_cvt_pk_bf16_f32 v122, v116, v117
v_cvt_pk_bf16_f32 v123, v118, v119
ds_write_b128 v28, v[120:123] offset:4352
buffer_load_dwordx4 v32, s[52:55], s76 offen offset:3072 sc1 lds
v_accvgpr_read_b32 v128, a48
v_accvgpr_read_b32 v129, a49
v_accvgpr_read_b32 v130, a50
v_accvgpr_read_b32 v131, a51
v_accvgpr_read_b32 v132, a52
v_accvgpr_read_b32 v133, a53
v_accvgpr_read_b32 v134, a54
v_accvgpr_read_b32 v135, a55
v_pk_mul_f32 v[128:129], v[128:129], s[46:47]
v_pk_mul_f32 v[130:131], v[130:131], s[46:47]
v_pk_mul_f32 v[132:133], v[132:133], s[46:47]
v_pk_mul_f32 v[134:135], v[134:135], s[46:47]
v_cvt_pk_bf16_f32 v136, v128, v129
v_cvt_pk_bf16_f32 v137, v130, v131
v_cvt_pk_bf16_f32 v138, v132, v133
v_cvt_pk_bf16_f32 v139, v134, v135
ds_write_b128 v28, v[136:139] offset:8192
s_add_u32 s76, s76, 8192
s_add_u32 s82, s82, 16384
s_cmp_eq_u32 s82, 163840
s_cselect_b32 s82, 0, s82
s_add_u32 m0, s81, s82
s_nop 0
buffer_load_dwordx4 v32, s[52:55], s76 offen sc1 lds
v_accvgpr_read_b32 v144, a56
v_accvgpr_read_b32 v145, a57
v_accvgpr_read_b32 v146, a58
v_accvgpr_read_b32 v147, a59
v_accvgpr_read_b32 v148, a60
v_accvgpr_read_b32 v149, a61
v_accvgpr_read_b32 v150, a62
v_accvgpr_read_b32 v151, a63
v_pk_mul_f32 v[144:145], v[144:145], s[46:47]
v_pk_mul_f32 v[146:147], v[146:147], s[46:47]
v_pk_mul_f32 v[148:149], v[148:149], s[46:47]
v_pk_mul_f32 v[150:151], v[150:151], s[46:47]
v_cvt_pk_bf16_f32 v152, v144, v145
v_cvt_pk_bf16_f32 v153, v146, v147
v_cvt_pk_bf16_f32 v154, v148, v149
v_cvt_pk_bf16_f32 v155, v150, v151
ds_write_b128 v28, v[152:155] offset:12288
buffer_load_dwordx4 v32, s[52:55], s76 offen offset:1024 sc1 lds
v_accvgpr_read_b32 v64, a72
v_accvgpr_read_b32 v65, a73
v_accvgpr_read_b32 v66, a74
v_accvgpr_read_b32 v67, a75
v_accvgpr_read_b32 v68, a76
v_accvgpr_read_b32 v69, a77
v_accvgpr_read_b32 v70, a78
v_accvgpr_read_b32 v71, a79
v_pk_mul_f32 v[64:65], v[64:65], s[46:47]
v_pk_mul_f32 v[66:67], v[66:67], s[46:47]
v_pk_mul_f32 v[68:69], v[68:69], s[46:47]
v_pk_mul_f32 v[70:71], v[70:71], s[46:47]
v_cvt_pk_bf16_f32 v72, v64, v65
v_cvt_pk_bf16_f32 v73, v66, v67
v_cvt_pk_bf16_f32 v74, v68, v69
v_cvt_pk_bf16_f32 v75, v70, v71
ds_write_b128 v28, v[72:75] offset:8448
buffer_load_dwordx4 v32, s[52:55], s76 offen offset:2048 sc1 lds
v_accvgpr_read_b32 v80, a80
v_accvgpr_read_b32 v81, a81
v_accvgpr_read_b32 v82, a82
v_accvgpr_read_b32 v83, a83
v_accvgpr_read_b32 v84, a84
v_accvgpr_read_b32 v85, a85
v_accvgpr_read_b32 v86, a86
v_accvgpr_read_b32 v87, a87
v_pk_mul_f32 v[80:81], v[80:81], s[46:47]
v_pk_mul_f32 v[82:83], v[82:83], s[46:47]
v_pk_mul_f32 v[84:85], v[84:85], s[46:47]
v_pk_mul_f32 v[86:87], v[86:87], s[46:47]
v_cvt_pk_bf16_f32 v88, v80, v81
v_cvt_pk_bf16_f32 v89, v82, v83
v_cvt_pk_bf16_f32 v90, v84, v85
v_cvt_pk_bf16_f32 v91, v86, v87
ds_write_b128 v28, v[88:91] offset:12544
buffer_load_dwordx4 v32, s[52:55], s76 offen offset:3072 sc1 lds
v_accvgpr_read_b32 v96, a96
v_accvgpr_read_b32 v97, a97
v_accvgpr_read_b32 v98, a98
v_accvgpr_read_b32 v99, a99
v_accvgpr_read_b32 v100, a100
v_accvgpr_read_b32 v101, a101
v_accvgpr_read_b32 v102, a102
v_accvgpr_read_b32 v103, a103
v_pk_mul_f32 v[96:97], v[96:97], s[46:47]
v_pk_mul_f32 v[98:99], v[98:99], s[46:47]
v_pk_mul_f32 v[100:101], v[100:101], s[46:47]
v_pk_mul_f32 v[102:103], v[102:103], s[46:47]
v_cvt_pk_bf16_f32 v104, v96, v97
v_cvt_pk_bf16_f32 v105, v98, v99
v_cvt_pk_bf16_f32 v106, v100, v101
v_cvt_pk_bf16_f32 v107, v102, v103
ds_write_b128 v29, v[104:107] offset:0
s_add_u32 s76, s76, 8192
s_add_u32 s82, s82, 16384
s_cmp_eq_u32 s82, 163840
s_cselect_b32 s82, 0, s82
s_add_u32 m0, s81, s82
s_nop 0
buffer_load_dwordx4 v32, s[52:55], s76 offen sc1 lds
v_accvgpr_read_b32 v112, a104
v_accvgpr_read_b32 v113, a105
v_accvgpr_read_b32 v114, a106
v_accvgpr_read_b32 v115, a107
v_accvgpr_read_b32 v116, a108
v_accvgpr_read_b32 v117, a109
v_accvgpr_read_b32 v118, a110
v_accvgpr_read_b32 v119, a111
v_pk_mul_f32 v[112:113], v[112:113], s[46:47]
v_pk_mul_f32 v[114:115], v[114:115], s[46:47]
v_pk_mul_f32 v[116:117], v[116:117], s[46:47]
v_pk_mul_f32 v[118:119], v[118:119], s[46:47]
v_cvt_pk_bf16_f32 v120, v112, v113
v_cvt_pk_bf16_f32 v121, v114, v115
v_cvt_pk_bf16_f32 v122, v116, v117
v_cvt_pk_bf16_f32 v123, v118, v119
ds_write_b128 v29, v[120:123] offset:4096
buffer_load_dwordx4 v32, s[52:55], s76 offen offset:1024 sc1 lds
v_accvgpr_read_b32 v128, a120
v_accvgpr_read_b32 v129, a121
v_accvgpr_read_b32 v130, a122
v_accvgpr_read_b32 v131, a123
v_accvgpr_read_b32 v132, a124
v_accvgpr_read_b32 v133, a125
v_accvgpr_read_b32 v134, a126
v_accvgpr_read_b32 v135, a127
v_pk_mul_f32 v[128:129], v[128:129], s[46:47]
v_pk_mul_f32 v[130:131], v[130:131], s[46:47]
v_pk_mul_f32 v[132:133], v[132:133], s[46:47]
v_pk_mul_f32 v[134:135], v[134:135], s[46:47]
v_cvt_pk_bf16_f32 v136, v128, v129
v_cvt_pk_bf16_f32 v137, v130, v131
v_cvt_pk_bf16_f32 v138, v132, v133
v_cvt_pk_bf16_f32 v139, v134, v135
ds_write_b128 v29, v[136:139] offset:256
buffer_load_dwordx4 v32, s[52:55], s76 offen offset:2048 sc1 lds
v_accvgpr_read_b32 v144, a128
v_accvgpr_read_b32 v145, a129
v_accvgpr_read_b32 v146, a130
v_accvgpr_read_b32 v147, a131
v_accvgpr_read_b32 v148, a132
v_accvgpr_read_b32 v149, a133
v_accvgpr_read_b32 v150, a134
v_accvgpr_read_b32 v151, a135
v_pk_mul_f32 v[144:145], v[144:145], s[46:47]
v_pk_mul_f32 v[146:147], v[146:147], s[46:47]
v_pk_mul_f32 v[148:149], v[148:149], s[46:47]
v_pk_mul_f32 v[150:151], v[150:151], s[46:47]
v_cvt_pk_bf16_f32 v152, v144, v145
v_cvt_pk_bf16_f32 v153, v146, v147
v_cvt_pk_bf16_f32 v154, v148, v149
v_cvt_pk_bf16_f32 v155, v150, v151
ds_write_b128 v29, v[152:155] offset:4352
buffer_load_dwordx4 v32, s[52:55], s76 offen offset:3072 sc1 lds
v_accvgpr_read_b32 v64, a144
v_accvgpr_read_b32 v65, a145
v_accvgpr_read_b32 v66, a146
v_accvgpr_read_b32 v67, a147
v_accvgpr_read_b32 v68, a148
v_accvgpr_read_b32 v69, a149
v_accvgpr_read_b32 v70, a150
v_accvgpr_read_b32 v71, a151
v_pk_mul_f32 v[64:65], v[64:65], s[46:47]
v_pk_mul_f32 v[66:67], v[66:67], s[46:47]
v_pk_mul_f32 v[68:69], v[68:69], s[46:47]
v_pk_mul_f32 v[70:71], v[70:71], s[46:47]
v_cvt_pk_bf16_f32 v72, v64, v65
v_cvt_pk_bf16_f32 v73, v66, v67
v_cvt_pk_bf16_f32 v74, v68, v69
v_cvt_pk_bf16_f32 v75, v70, v71
ds_write_b128 v29, v[72:75] offset:8192
s_add_u32 s76, s76, 8192
s_add_u32 s82, s82, 16384
s_cmp_eq_u32 s82, 163840
s_cselect_b32 s82, 0, s82
s_add_u32 m0, s81, s82
s_nop 0
buffer_load_dwordx4 v32, s[52:55], s76 offen sc1 lds
v_accvgpr_read_b32 v80, a152
v_accvgpr_read_b32 v81, a153
v_accvgpr_read_b32 v82, a154
v_accvgpr_read_b32 v83, a155
v_accvgpr_read_b32 v84, a156
v_accvgpr_read_b32 v85, a157
v_accvgpr_read_b32 v86, a158
v_accvgpr_read_b32 v87, a159
v_pk_mul_f32 v[80:81], v[80:81], s[46:47]
v_pk_mul_f32 v[82:83], v[82:83], s[46:47]
v_pk_mul_f32 v[84:85], v[84:85], s[46:47]
v_pk_mul_f32 v[86:87], v[86:87], s[46:47]
v_cvt_pk_bf16_f32 v88, v80, v81
v_cvt_pk_bf16_f32 v89, v82, v83
v_cvt_pk_bf16_f32 v90, v84, v85
v_cvt_pk_bf16_f32 v91, v86, v87
ds_write_b128 v29, v[88:91] offset:12288
buffer_load_dwordx4 v32, s[52:55], s76 offen offset:1024 sc1 lds
v_accvgpr_read_b32 v96, a168
v_accvgpr_read_b32 v97, a169
v_accvgpr_read_b32 v98, a170
v_accvgpr_read_b32 v99, a171
v_accvgpr_read_b32 v100, a172
v_accvgpr_read_b32 v101, a173
v_accvgpr_read_b32 v102, a174
v_accvgpr_read_b32 v103, a175
v_pk_mul_f32 v[96:97], v[96:97], s[46:47]
v_pk_mul_f32 v[98:99], v[98:99], s[46:47]
v_pk_mul_f32 v[100:101], v[100:101], s[46:47]
v_pk_mul_f32 v[102:103], v[102:103], s[46:47]
v_cvt_pk_bf16_f32 v104, v96, v97
v_cvt_pk_bf16_f32 v105, v98, v99
v_cvt_pk_bf16_f32 v106, v100, v101
v_cvt_pk_bf16_f32 v107, v102, v103
ds_write_b128 v29, v[104:107] offset:8448
buffer_load_dwordx4 v32, s[52:55], s76 offen offset:2048 sc1 lds
v_accvgpr_read_b32 v112, a176
v_accvgpr_read_b32 v113, a177
v_accvgpr_read_b32 v114, a178
v_accvgpr_read_b32 v115, a179
v_accvgpr_read_b32 v116, a180
v_accvgpr_read_b32 v117, a181
v_accvgpr_read_b32 v118, a182
v_accvgpr_read_b32 v119, a183
v_pk_mul_f32 v[112:113], v[112:113], s[46:47]
v_pk_mul_f32 v[114:115], v[114:115], s[46:47]
v_pk_mul_f32 v[116:117], v[116:117], s[46:47]
v_pk_mul_f32 v[118:119], v[118:119], s[46:47]
v_cvt_pk_bf16_f32 v120, v112, v113
v_cvt_pk_bf16_f32 v121, v114, v115
v_cvt_pk_bf16_f32 v122, v116, v117
v_cvt_pk_bf16_f32 v123, v118, v119
ds_write_b128 v29, v[120:123] offset:12544
buffer_load_dwordx4 v32, s[52:55], s76 offen offset:3072 sc1 lds
s_add_u32 s76, s76, 8192
s_add_u32 s82, s82, 16384
s_cmp_eq_u32 s82, 163840
s_cselect_b32 s82, 0, s82
s_waitcnt vmcnt(22)
v_lshlrev_b32 v208, 16, v216
v_and_b32 v209, 0xffff0000, v216
v_lshlrev_b32 v210, 16, v217
v_and_b32 v211, 0xffff0000, v217
v_lshlrev_b32 v212, 16, v218
v_and_b32 v213, 0xffff0000, v218
v_lshlrev_b32 v214, 16, v219
v_and_b32 v215, 0xffff0000, v219
v_accvgpr_read_b32 v216, a192
v_accvgpr_read_b32 v217, a193
v_accvgpr_read_b32 v218, a194
v_accvgpr_read_b32 v219, a195
v_accvgpr_read_b32 v220, a196
v_accvgpr_read_b32 v221, a197
v_accvgpr_read_b32 v222, a198
v_accvgpr_read_b32 v223, a199
v_mov_b32 v30, 0xbdd2d3e8
v_mov_b32 v24, 0xbdd2d3e8
v_mov_b32 v25, 0xbdd2d3e8
v_mov_b32 v26, 0xc0135761
v_mov_b32 v27, 0xc0135761
s_mul_i32 s44, s98, 182
s_lshl_b32 s45, s99, 1
s_add_u32 s44, s44, s45
s_add_u32 s44, s44, s101
s_add_u32 s44, s44, 128
s_lshl_b32 s44, s44, 13
s_add_u32 s44, s44, 36192256
s_cmp_lt_u32 s40, 27
s_cselect_b32 s44, s44, 0x7f000000
v_accvgpr_read_b32 v64, a16
v_accvgpr_read_b32 v65, a17
v_accvgpr_read_b32 v66, a18
v_accvgpr_read_b32 v67, a19
v_accvgpr_read_b32 v68, a20
v_accvgpr_read_b32 v69, a21
v_accvgpr_read_b32 v70, a22
v_accvgpr_read_b32 v71, a23
v_pk_fma_f32 v[64:65], v[192:193], v[216:217], v[64:65] op_sel_hi:[0,1,1] neg_lo:[1,0,0] neg_hi:[1,0,0]
v_pk_fma_f32 v[66:67], v[192:193], v[218:219], v[66:67] op_sel_hi:[0,1,1] neg_lo:[1,0,0] neg_hi:[1,0,0]
v_pk_fma_f32 v[68:69], v[192:193], v[220:221], v[68:69] op_sel_hi:[0,1,1] neg_lo:[1,0,0] neg_hi:[1,0,0]
v_pk_fma_f32 v[70:71], v[192:193], v[222:223], v[70:71] op_sel_hi:[0,1,1] neg_lo:[1,0,0] neg_hi:[1,0,0]
v_pk_fma_f32 v[64:65], v[64:65], v[192:193], v[208:209] op_sel:[0,1,0] op_sel_hi:[1,1,1]
v_pk_fma_f32 v[66:67], v[66:67], v[192:193], v[210:211] op_sel:[0,1,0] op_sel_hi:[1,1,1]
v_pk_fma_f32 v[68:69], v[68:69], v[192:193], v[212:213] op_sel:[0,1,0] op_sel_hi:[1,1,1]
v_pk_fma_f32 v[70:71], v[70:71], v[192:193], v[214:215] op_sel:[0,1,0] op_sel_hi:[1,1,1]
v_pk_mul_f32 v[128:129], v[64:65], v[64:65]
v_pk_mul_f32 v[130:131], v[66:67], v[66:67]
v_pk_mul_f32 v[132:133], v[68:69], v[68:69]
v_pk_mul_f32 v[134:135], v[70:71], v[70:71]
v_pk_fma_f32 v[128:129], v[128:129], v[24:25], v[26:27]
v_pk_fma_f32 v[130:131], v[130:131], v[24:25], v[26:27]
v_pk_fma_f32 v[132:133], v[132:133], v[24:25], v[26:27]
v_pk_fma_f32 v[134:135], v[134:135], v[24:25], v[26:27]
v_pk_mul_f32 v[128:129], v[128:129], v[64:65]
v_pk_mul_f32 v[130:131], v[130:131], v[66:67]
v_pk_mul_f32 v[132:133], v[132:133], v[68:69]
v_pk_mul_f32 v[134:135], v[134:135], v[70:71]
v_exp_f32 v128, v128
v_exp_f32 v129, v129
v_exp_f32 v130, v130
v_exp_f32 v131, v131
v_exp_f32 v132, v132
v_exp_f32 v133, v133
v_exp_f32 v134, v134
v_exp_f32 v135, v135
v_pk_add_f32 v[128:129], v[128:129], 1.0 op_sel_hi:[1,0]
v_pk_add_f32 v[130:131], v[130:131], 1.0 op_sel_hi:[1,0]
v_pk_add_f32 v[132:133], v[132:133], 1.0 op_sel_hi:[1,0]
v_pk_add_f32 v[134:135], v[134:135], 1.0 op_sel_hi:[1,0]
v_rcp_f32 v128, v128
v_rcp_f32 v129, v129
v_rcp_f32 v130, v130
v_rcp_f32 v131, v131
v_rcp_f32 v132, v132
v_rcp_f32 v133, v133
v_rcp_f32 v134, v134
v_rcp_f32 v135, v135
v_pk_mul_f32 v[64:65], v[64:65], v[128:129]
v_pk_mul_f32 v[66:67], v[66:67], v[130:131]
v_pk_mul_f32 v[68:69], v[68:69], v[132:133]
v_pk_mul_f32 v[70:71], v[70:71], v[134:135]
v_cvt_pk_bf16_f32 v72, v64, v65
v_cvt_pk_bf16_f32 v73, v66, v67
v_cvt_pk_bf16_f32 v74, v68, v69
v_cvt_pk_bf16_f32 v75, v70, v71
buffer_store_dwordx4 v[72:75], v32, s[48:51], s44 offen offset:0 sc1
v_accvgpr_read_b32 v80, a40
v_accvgpr_read_b32 v81, a41
v_accvgpr_read_b32 v82, a42
v_accvgpr_read_b32 v83, a43
v_accvgpr_read_b32 v84, a44
v_accvgpr_read_b32 v85, a45
v_accvgpr_read_b32 v86, a46
v_accvgpr_read_b32 v87, a47
v_pk_fma_f32 v[80:81], v[194:195], v[216:217], v[80:81] op_sel_hi:[0,1,1] neg_lo:[1,0,0] neg_hi:[1,0,0]
v_pk_fma_f32 v[82:83], v[194:195], v[218:219], v[82:83] op_sel_hi:[0,1,1] neg_lo:[1,0,0] neg_hi:[1,0,0]
v_pk_fma_f32 v[84:85], v[194:195], v[220:221], v[84:85] op_sel_hi:[0,1,1] neg_lo:[1,0,0] neg_hi:[1,0,0]
v_pk_fma_f32 v[86:87], v[194:195], v[222:223], v[86:87] op_sel_hi:[0,1,1] neg_lo:[1,0,0] neg_hi:[1,0,0]
v_pk_fma_f32 v[80:81], v[80:81], v[194:195], v[208:209] op_sel:[0,1,0] op_sel_hi:[1,1,1]
v_pk_fma_f32 v[82:83], v[82:83], v[194:195], v[210:211] op_sel:[0,1,0] op_sel_hi:[1,1,1]
v_pk_fma_f32 v[84:85], v[84:85], v[194:195], v[212:213] op_sel:[0,1,0] op_sel_hi:[1,1,1]
v_pk_fma_f32 v[86:87], v[86:87], v[194:195], v[214:215] op_sel:[0,1,0] op_sel_hi:[1,1,1]
v_pk_mul_f32 v[136:137], v[80:81], v[80:81]
v_pk_mul_f32 v[138:139], v[82:83], v[82:83]
v_pk_mul_f32 v[140:141], v[84:85], v[84:85]
v_pk_mul_f32 v[142:143], v[86:87], v[86:87]
v_pk_fma_f32 v[136:137], v[136:137], v[24:25], v[26:27]
v_pk_fma_f32 v[138:139], v[138:139], v[24:25], v[26:27]
v_pk_fma_f32 v[140:141], v[140:141], v[24:25], v[26:27]
v_pk_fma_f32 v[142:143], v[142:143], v[24:25], v[26:27]
v_pk_mul_f32 v[136:137], v[136:137], v[80:81]
v_pk_mul_f32 v[138:139], v[138:139], v[82:83]
v_pk_mul_f32 v[140:141], v[140:141], v[84:85]
v_pk_mul_f32 v[142:143], v[142:143], v[86:87]
v_exp_f32 v136, v136
v_exp_f32 v137, v137
v_exp_f32 v138, v138
v_exp_f32 v139, v139
v_exp_f32 v140, v140
v_exp_f32 v141, v141
v_exp_f32 v142, v142
v_exp_f32 v143, v143
v_pk_add_f32 v[136:137], v[136:137], 1.0 op_sel_hi:[1,0]
v_pk_add_f32 v[138:139], v[138:139], 1.0 op_sel_hi:[1,0]
v_pk_add_f32 v[140:141], v[140:141], 1.0 op_sel_hi:[1,0]
v_pk_add_f32 v[142:143], v[142:143], 1.0 op_sel_hi:[1,0]
v_rcp_f32 v136, v136
v_rcp_f32 v137, v137
v_rcp_f32 v138, v138
v_rcp_f32 v139, v139
v_rcp_f32 v140, v140
v_rcp_f32 v141, v141
v_rcp_f32 v142, v142
v_rcp_f32 v143, v143
v_pk_mul_f32 v[80:81], v[80:81], v[136:137]
v_pk_mul_f32 v[82:83], v[82:83], v[138:139]
v_pk_mul_f32 v[84:85], v[84:85], v[140:141]
v_pk_mul_f32 v[86:87], v[86:87], v[142:143]
v_cvt_pk_bf16_f32 v88, v80, v81
v_cvt_pk_bf16_f32 v89, v82, v83
v_cvt_pk_bf16_f32 v90, v84, v85
v_cvt_pk_bf16_f32 v91, v86, v87
buffer_store_dwordx4 v[88:91], v32, s[48:51], s44 offen offset:1024 sc1
v_accvgpr_read_b32 v96, a64
v_accvgpr_read_b32 v97, a65
v_accvgpr_read_b32 v98, a66
v_accvgpr_read_b32 v99, a67
v_accvgpr_read_b32 v100, a68
v_accvgpr_read_b32 v101, a69
v_accvgpr_read_b32 v102, a70
v_accvgpr_read_b32 v103, a71
v_pk_fma_f32 v[96:97], v[196:197], v[216:217], v[96:97] op_sel_hi:[0,1,1] neg_lo:[1,0,0] neg_hi:[1,0,0]
v_pk_fma_f32 v[98:99], v[196:197], v[218:219], v[98:99] op_sel_hi:[0,1,1] neg_lo:[1,0,0] neg_hi:[1,0,0]
v_pk_fma_f32 v[100:101], v[196:197], v[220:221], v[100:101] op_sel_hi:[0,1,1] neg_lo:[1,0,0] neg_hi:[1,0,0]
v_pk_fma_f32 v[102:103], v[196:197], v[222:223], v[102:103] op_sel_hi:[0,1,1] neg_lo:[1,0,0] neg_hi:[1,0,0]
v_pk_fma_f32 v[96:97], v[96:97], v[196:197], v[208:209] op_sel:[0,1,0] op_sel_hi:[1,1,1]
v_pk_fma_f32 v[98:99], v[98:99], v[196:197], v[210:211] op_sel:[0,1,0] op_sel_hi:[1,1,1]
v_pk_fma_f32 v[100:101], v[100:101], v[196:197], v[212:213] op_sel:[0,1,0] op_sel_hi:[1,1,1]
v_pk_fma_f32 v[102:103], v[102:103], v[196:197], v[214:215] op_sel:[0,1,0] op_sel_hi:[1,1,1]
v_pk_mul_f32 v[128:129], v[96:97], v[96:97]
v_pk_mul_f32 v[130:131], v[98:99], v[98:99]
v_pk_mul_f32 v[132:133], v[100:101], v[100:101]
v_pk_mul_f32 v[134:135], v[102:103], v[102:103]
v_pk_fma_f32 v[128:129], v[128:129], v[24:25], v[26:27]
v_pk_fma_f32 v[130:131], v[130:131], v[24:25], v[26:27]
v_pk_fma_f32 v[132:133], v[132:133], v[24:25], v[26:27]
v_pk_fma_f32 v[134:135], v[134:135], v[24:25], v[26:27]
v_pk_mul_f32 v[128:129], v[128:129], v[96:97]
v_pk_mul_f32 v[130:131], v[130:131], v[98:99]
v_pk_mul_f32 v[132:133], v[132:133], v[100:101]
v_pk_mul_f32 v[134:135], v[134:135], v[102:103]
v_exp_f32 v128, v128
v_exp_f32 v129, v129
v_exp_f32 v130, v130
v_exp_f32 v131, v131
v_exp_f32 v132, v132
v_exp_f32 v133, v133
v_exp_f32 v134, v134
v_exp_f32 v135, v135
v_pk_add_f32 v[128:129], v[128:129], 1.0 op_sel_hi:[1,0]
v_pk_add_f32 v[130:131], v[130:131], 1.0 op_sel_hi:[1,0]
v_pk_add_f32 v[132:133], v[132:133], 1.0 op_sel_hi:[1,0]
v_pk_add_f32 v[134:135], v[134:135], 1.0 op_sel_hi:[1,0]
v_rcp_f32 v128, v128
v_rcp_f32 v129, v129
v_rcp_f32 v130, v130
v_rcp_f32 v131, v131
v_rcp_f32 v132, v132
v_rcp_f32 v133, v133
v_rcp_f32 v134, v134
v_rcp_f32 v135, v135
v_pk_mul_f32 v[96:97], v[96:97], v[128:129]
v_pk_mul_f32 v[98:99], v[98:99], v[130:131]
v_pk_mul_f32 v[100:101], v[100:101], v[132:133]
v_pk_mul_f32 v[102:103], v[102:103], v[134:135]
v_cvt_pk_bf16_f32 v104, v96, v97
v_cvt_pk_bf16_f32 v105, v98, v99
v_cvt_pk_bf16_f32 v106, v100, v101
v_cvt_pk_bf16_f32 v107, v102, v103
buffer_store_dwordx4 v[104:107], v32, s[48:51], s44 offen offset:2048 sc1
v_accvgpr_read_b32 v112, a88
v_accvgpr_read_b32 v113, a89
v_accvgpr_read_b32 v114, a90
v_accvgpr_read_b32 v115, a91
v_accvgpr_read_b32 v116, a92
v_accvgpr_read_b32 v117, a93
v_accvgpr_read_b32 v118, a94
v_accvgpr_read_b32 v119, a95
v_pk_fma_f32 v[112:113], v[198:199], v[216:217], v[112:113] op_sel_hi:[0,1,1] neg_lo:[1,0,0] neg_hi:[1,0,0]
v_pk_fma_f32 v[114:115], v[198:199], v[218:219], v[114:115] op_sel_hi:[0,1,1] neg_lo:[1,0,0] neg_hi:[1,0,0]
v_pk_fma_f32 v[116:117], v[198:199], v[220:221], v[116:117] op_sel_hi:[0,1,1] neg_lo:[1,0,0] neg_hi:[1,0,0]
v_pk_fma_f32 v[118:119], v[198:199], v[222:223], v[118:119] op_sel_hi:[0,1,1] neg_lo:[1,0,0] neg_hi:[1,0,0]
v_pk_fma_f32 v[112:113], v[112:113], v[198:199], v[208:209] op_sel:[0,1,0] op_sel_hi:[1,1,1]
v_pk_fma_f32 v[114:115], v[114:115], v[198:199], v[210:211] op_sel:[0,1,0] op_sel_hi:[1,1,1]
v_pk_fma_f32 v[116:117], v[116:117], v[198:199], v[212:213] op_sel:[0,1,0] op_sel_hi:[1,1,1]
v_pk_fma_f32 v[118:119], v[118:119], v[198:199], v[214:215] op_sel:[0,1,0] op_sel_hi:[1,1,1]
v_pk_mul_f32 v[136:137], v[112:113], v[112:113]
v_pk_mul_f32 v[138:139], v[114:115], v[114:115]
v_pk_mul_f32 v[140:141], v[116:117], v[116:117]
v_pk_mul_f32 v[142:143], v[118:119], v[118:119]
v_pk_fma_f32 v[136:137], v[136:137], v[24:25], v[26:27]
v_pk_fma_f32 v[138:139], v[138:139], v[24:25], v[26:27]
v_pk_fma_f32 v[140:141], v[140:141], v[24:25], v[26:27]
v_pk_fma_f32 v[142:143], v[142:143], v[24:25], v[26:27]
v_pk_mul_f32 v[136:137], v[136:137], v[112:113]
v_pk_mul_f32 v[138:139], v[138:139], v[114:115]
v_pk_mul_f32 v[140:141], v[140:141], v[116:117]
v_pk_mul_f32 v[142:143], v[142:143], v[118:119]
v_exp_f32 v136, v136
v_exp_f32 v137, v137
v_exp_f32 v138, v138
v_exp_f32 v139, v139
v_exp_f32 v140, v140
v_exp_f32 v141, v141
v_exp_f32 v142, v142
v_exp_f32 v143, v143
v_pk_add_f32 v[136:137], v[136:137], 1.0 op_sel_hi:[1,0]
v_pk_add_f32 v[138:139], v[138:139], 1.0 op_sel_hi:[1,0]
v_pk_add_f32 v[140:141], v[140:141], 1.0 op_sel_hi:[1,0]
v_pk_add_f32 v[142:143], v[142:143], 1.0 op_sel_hi:[1,0]
v_rcp_f32 v136, v136
v_rcp_f32 v137, v137
v_rcp_f32 v138, v138
v_rcp_f32 v139, v139
v_rcp_f32 v140, v140
v_rcp_f32 v141, v141
v_rcp_f32 v142, v142
v_rcp_f32 v143, v143
v_pk_mul_f32 v[112:113], v[112:113], v[136:137]
v_pk_mul_f32 v[114:115], v[114:115], v[138:139]
v_pk_mul_f32 v[116:117], v[116:117], v[140:141]
v_pk_mul_f32 v[118:119], v[118:119], v[142:143]
v_cvt_pk_bf16_f32 v120, v112, v113
v_cvt_pk_bf16_f32 v121, v114, v115
v_cvt_pk_bf16_f32 v122, v116, v117
v_cvt_pk_bf16_f32 v123, v118, v119
buffer_store_dwordx4 v[120:123], v32, s[48:51], s44 offen offset:3072 sc1
v_accvgpr_read_b32 v64, a112
v_accvgpr_read_b32 v65, a113
v_accvgpr_read_b32 v66, a114
v_accvgpr_read_b32 v67, a115
v_accvgpr_read_b32 v68, a116
v_accvgpr_read_b32 v69, a117
v_accvgpr_read_b32 v70, a118
v_accvgpr_read_b32 v71, a119
v_pk_fma_f32 v[64:65], v[200:201], v[216:217], v[64:65] op_sel_hi:[0,1,1] neg_lo:[1,0,0] neg_hi:[1,0,0]
v_pk_fma_f32 v[66:67], v[200:201], v[218:219], v[66:67] op_sel_hi:[0,1,1] neg_lo:[1,0,0] neg_hi:[1,0,0]
v_pk_fma_f32 v[68:69], v[200:201], v[220:221], v[68:69] op_sel_hi:[0,1,1] neg_lo:[1,0,0] neg_hi:[1,0,0]
v_pk_fma_f32 v[70:71], v[200:201], v[222:223], v[70:71] op_sel_hi:[0,1,1] neg_lo:[1,0,0] neg_hi:[1,0,0]
v_pk_fma_f32 v[64:65], v[64:65], v[200:201], v[208:209] op_sel:[0,1,0] op_sel_hi:[1,1,1]
v_pk_fma_f32 v[66:67], v[66:67], v[200:201], v[210:211] op_sel:[0,1,0] op_sel_hi:[1,1,1]
v_pk_fma_f32 v[68:69], v[68:69], v[200:201], v[212:213] op_sel:[0,1,0] op_sel_hi:[1,1,1]
v_pk_fma_f32 v[70:71], v[70:71], v[200:201], v[214:215] op_sel:[0,1,0] op_sel_hi:[1,1,1]
v_pk_mul_f32 v[128:129], v[64:65], v[64:65]
v_pk_mul_f32 v[130:131], v[66:67], v[66:67]
v_pk_mul_f32 v[132:133], v[68:69], v[68:69]
v_pk_mul_f32 v[134:135], v[70:71], v[70:71]
v_pk_fma_f32 v[128:129], v[128:129], v[24:25], v[26:27]
v_pk_fma_f32 v[130:131], v[130:131], v[24:25], v[26:27]
v_pk_fma_f32 v[132:133], v[132:133], v[24:25], v[26:27]
v_pk_fma_f32 v[134:135], v[134:135], v[24:25], v[26:27]
v_pk_mul_f32 v[128:129], v[128:129], v[64:65]
v_pk_mul_f32 v[130:131], v[130:131], v[66:67]
v_pk_mul_f32 v[132:133], v[132:133], v[68:69]
v_pk_mul_f32 v[134:135], v[134:135], v[70:71]
v_exp_f32 v128, v128
v_exp_f32 v129, v129
v_exp_f32 v130, v130
v_exp_f32 v131, v131
v_exp_f32 v132, v132
v_exp_f32 v133, v133
v_exp_f32 v134, v134
v_exp_f32 v135, v135
v_pk_add_f32 v[128:129], v[128:129], 1.0 op_sel_hi:[1,0]
v_pk_add_f32 v[130:131], v[130:131], 1.0 op_sel_hi:[1,0]
v_pk_add_f32 v[132:133], v[132:133], 1.0 op_sel_hi:[1,0]
v_pk_add_f32 v[134:135], v[134:135], 1.0 op_sel_hi:[1,0]
v_rcp_f32 v128, v128
v_rcp_f32 v129, v129
v_rcp_f32 v130, v130
v_rcp_f32 v131, v131
v_rcp_f32 v132, v132
v_rcp_f32 v133, v133
v_rcp_f32 v134, v134
v_rcp_f32 v135, v135
v_pk_mul_f32 v[64:65], v[64:65], v[128:129]
v_pk_mul_f32 v[66:67], v[66:67], v[130:131]
v_pk_mul_f32 v[68:69], v[68:69], v[132:133]
v_pk_mul_f32 v[70:71], v[70:71], v[134:135]
v_cvt_pk_bf16_f32 v72, v64, v65
v_cvt_pk_bf16_f32 v73, v66, v67
v_cvt_pk_bf16_f32 v74, v68, v69
v_cvt_pk_bf16_f32 v75, v70, v71
s_add_u32 s44, s44, 4096
buffer_store_dwordx4 v[72:75], v32, s[48:51], s44 offen offset:0 sc1
v_accvgpr_read_b32 v80, a136
v_accvgpr_read_b32 v81, a137
v_accvgpr_read_b32 v82, a138
v_accvgpr_read_b32 v83, a139
v_accvgpr_read_b32 v84, a140
v_accvgpr_read_b32 v85, a141
v_accvgpr_read_b32 v86, a142
v_accvgpr_read_b32 v87, a143
v_pk_fma_f32 v[80:81], v[202:203], v[216:217], v[80:81] op_sel_hi:[0,1,1] neg_lo:[1,0,0] neg_hi:[1,0,0]
v_pk_fma_f32 v[82:83], v[202:203], v[218:219], v[82:83] op_sel_hi:[0,1,1] neg_lo:[1,0,0] neg_hi:[1,0,0]
v_pk_fma_f32 v[84:85], v[202:203], v[220:221], v[84:85] op_sel_hi:[0,1,1] neg_lo:[1,0,0] neg_hi:[1,0,0]
v_pk_fma_f32 v[86:87], v[202:203], v[222:223], v[86:87] op_sel_hi:[0,1,1] neg_lo:[1,0,0] neg_hi:[1,0,0]
v_pk_fma_f32 v[80:81], v[80:81], v[202:203], v[208:209] op_sel:[0,1,0] op_sel_hi:[1,1,1]
v_pk_fma_f32 v[82:83], v[82:83], v[202:203], v[210:211] op_sel:[0,1,0] op_sel_hi:[1,1,1]
v_pk_fma_f32 v[84:85], v[84:85], v[202:203], v[212:213] op_sel:[0,1,0] op_sel_hi:[1,1,1]
v_pk_fma_f32 v[86:87], v[86:87], v[202:203], v[214:215] op_sel:[0,1,0] op_sel_hi:[1,1,1]
v_pk_mul_f32 v[136:137], v[80:81], v[80:81]
v_pk_mul_f32 v[138:139], v[82:83], v[82:83]
v_pk_mul_f32 v[140:141], v[84:85], v[84:85]
v_pk_mul_f32 v[142:143], v[86:87], v[86:87]
v_pk_fma_f32 v[136:137], v[136:137], v[24:25], v[26:27]
v_pk_fma_f32 v[138:139], v[138:139], v[24:25], v[26:27]
v_pk_fma_f32 v[140:141], v[140:141], v[24:25], v[26:27]
v_pk_fma_f32 v[142:143], v[142:143], v[24:25], v[26:27]
v_pk_mul_f32 v[136:137], v[136:137], v[80:81]
v_pk_mul_f32 v[138:139], v[138:139], v[82:83]
v_pk_mul_f32 v[140:141], v[140:141], v[84:85]
v_pk_mul_f32 v[142:143], v[142:143], v[86:87]
v_exp_f32 v136, v136
v_exp_f32 v137, v137
v_exp_f32 v138, v138
v_exp_f32 v139, v139
v_exp_f32 v140, v140
v_exp_f32 v141, v141
v_exp_f32 v142, v142
v_exp_f32 v143, v143
v_pk_add_f32 v[136:137], v[136:137], 1.0 op_sel_hi:[1,0]
v_pk_add_f32 v[138:139], v[138:139], 1.0 op_sel_hi:[1,0]
v_pk_add_f32 v[140:141], v[140:141], 1.0 op_sel_hi:[1,0]
v_pk_add_f32 v[142:143], v[142:143], 1.0 op_sel_hi:[1,0]
v_rcp_f32 v136, v136
v_rcp_f32 v137, v137
v_rcp_f32 v138, v138
v_rcp_f32 v139, v139
v_rcp_f32 v140, v140
v_rcp_f32 v141, v141
v_rcp_f32 v142, v142
v_rcp_f32 v143, v143
v_pk_mul_f32 v[80:81], v[80:81], v[136:137]
v_pk_mul_f32 v[82:83], v[82:83], v[138:139]
v_pk_mul_f32 v[84:85], v[84:85], v[140:141]
v_pk_mul_f32 v[86:87], v[86:87], v[142:143]
v_cvt_pk_bf16_f32 v88, v80, v81
v_cvt_pk_bf16_f32 v89, v82, v83
v_cvt_pk_bf16_f32 v90, v84, v85
v_cvt_pk_bf16_f32 v91, v86, v87
buffer_store_dwordx4 v[88:91], v32, s[48:51], s44 offen offset:1024 sc1
v_accvgpr_read_b32 v96, a160
v_accvgpr_read_b32 v97, a161
v_accvgpr_read_b32 v98, a162
v_accvgpr_read_b32 v99, a163
v_accvgpr_read_b32 v100, a164
v_accvgpr_read_b32 v101, a165
v_accvgpr_read_b32 v102, a166
v_accvgpr_read_b32 v103, a167
v_pk_fma_f32 v[96:97], v[204:205], v[216:217], v[96:97] op_sel_hi:[0,1,1] neg_lo:[1,0,0] neg_hi:[1,0,0]
v_pk_fma_f32 v[98:99], v[204:205], v[218:219], v[98:99] op_sel_hi:[0,1,1] neg_lo:[1,0,0] neg_hi:[1,0,0]
v_pk_fma_f32 v[100:101], v[204:205], v[220:221], v[100:101] op_sel_hi:[0,1,1] neg_lo:[1,0,0] neg_hi:[1,0,0]
v_pk_fma_f32 v[102:103], v[204:205], v[222:223], v[102:103] op_sel_hi:[0,1,1] neg_lo:[1,0,0] neg_hi:[1,0,0]
v_pk_fma_f32 v[96:97], v[96:97], v[204:205], v[208:209] op_sel:[0,1,0] op_sel_hi:[1,1,1]
v_pk_fma_f32 v[98:99], v[98:99], v[204:205], v[210:211] op_sel:[0,1,0] op_sel_hi:[1,1,1]
v_pk_fma_f32 v[100:101], v[100:101], v[204:205], v[212:213] op_sel:[0,1,0] op_sel_hi:[1,1,1]
v_pk_fma_f32 v[102:103], v[102:103], v[204:205], v[214:215] op_sel:[0,1,0] op_sel_hi:[1,1,1]
v_pk_mul_f32 v[128:129], v[96:97], v[96:97]
v_pk_mul_f32 v[130:131], v[98:99], v[98:99]
v_pk_mul_f32 v[132:133], v[100:101], v[100:101]
v_pk_mul_f32 v[134:135], v[102:103], v[102:103]
v_pk_fma_f32 v[128:129], v[128:129], v[24:25], v[26:27]
v_pk_fma_f32 v[130:131], v[130:131], v[24:25], v[26:27]
v_pk_fma_f32 v[132:133], v[132:133], v[24:25], v[26:27]
v_pk_fma_f32 v[134:135], v[134:135], v[24:25], v[26:27]
v_pk_mul_f32 v[128:129], v[128:129], v[96:97]
v_pk_mul_f32 v[130:131], v[130:131], v[98:99]
v_pk_mul_f32 v[132:133], v[132:133], v[100:101]
v_pk_mul_f32 v[134:135], v[134:135], v[102:103]
v_exp_f32 v128, v128
v_exp_f32 v129, v129
v_exp_f32 v130, v130
v_exp_f32 v131, v131
v_exp_f32 v132, v132
v_exp_f32 v133, v133
v_exp_f32 v134, v134
v_exp_f32 v135, v135
v_pk_add_f32 v[128:129], v[128:129], 1.0 op_sel_hi:[1,0]
v_pk_add_f32 v[130:131], v[130:131], 1.0 op_sel_hi:[1,0]
v_pk_add_f32 v[132:133], v[132:133], 1.0 op_sel_hi:[1,0]
v_pk_add_f32 v[134:135], v[134:135], 1.0 op_sel_hi:[1,0]
v_rcp_f32 v128, v128
v_rcp_f32 v129, v129
v_rcp_f32 v130, v130
v_rcp_f32 v131, v131
v_rcp_f32 v132, v132
v_rcp_f32 v133, v133
v_rcp_f32 v134, v134
v_rcp_f32 v135, v135
v_pk_mul_f32 v[96:97], v[96:97], v[128:129]
v_pk_mul_f32 v[98:99], v[98:99], v[130:131]
v_pk_mul_f32 v[100:101], v[100:101], v[132:133]
v_pk_mul_f32 v[102:103], v[102:103], v[134:135]
v_cvt_pk_bf16_f32 v104, v96, v97
v_cvt_pk_bf16_f32 v105, v98, v99
v_cvt_pk_bf16_f32 v106, v100, v101
v_cvt_pk_bf16_f32 v107, v102, v103
buffer_store_dwordx4 v[104:107], v32, s[48:51], s44 offen offset:2048 sc1
v_accvgpr_read_b32 v112, a184
v_accvgpr_read_b32 v113, a185
v_accvgpr_read_b32 v114, a186
v_accvgpr_read_b32 v115, a187
v_accvgpr_read_b32 v116, a188
v_accvgpr_read_b32 v117, a189
v_accvgpr_read_b32 v118, a190
v_accvgpr_read_b32 v119, a191
v_pk_fma_f32 v[112:113], v[206:207], v[216:217], v[112:113] op_sel_hi:[0,1,1] neg_lo:[1,0,0] neg_hi:[1,0,0]
v_pk_fma_f32 v[114:115], v[206:207], v[218:219], v[114:115] op_sel_hi:[0,1,1] neg_lo:[1,0,0] neg_hi:[1,0,0]
v_pk_fma_f32 v[116:117], v[206:207], v[220:221], v[116:117] op_sel_hi:[0,1,1] neg_lo:[1,0,0] neg_hi:[1,0,0]
v_pk_fma_f32 v[118:119], v[206:207], v[222:223], v[118:119] op_sel_hi:[0,1,1] neg_lo:[1,0,0] neg_hi:[1,0,0]
v_pk_fma_f32 v[112:113], v[112:113], v[206:207], v[208:209] op_sel:[0,1,0] op_sel_hi:[1,1,1]
v_pk_fma_f32 v[114:115], v[114:115], v[206:207], v[210:211] op_sel:[0,1,0] op_sel_hi:[1,1,1]
v_pk_fma_f32 v[116:117], v[116:117], v[206:207], v[212:213] op_sel:[0,1,0] op_sel_hi:[1,1,1]
v_pk_fma_f32 v[118:119], v[118:119], v[206:207], v[214:215] op_sel:[0,1,0] op_sel_hi:[1,1,1]
v_pk_mul_f32 v[136:137], v[112:113], v[112:113]
v_pk_mul_f32 v[138:139], v[114:115], v[114:115]
v_pk_mul_f32 v[140:141], v[116:117], v[116:117]
v_pk_mul_f32 v[142:143], v[118:119], v[118:119]
v_pk_fma_f32 v[136:137], v[136:137], v[24:25], v[26:27]
v_pk_fma_f32 v[138:139], v[138:139], v[24:25], v[26:27]
v_pk_fma_f32 v[140:141], v[140:141], v[24:25], v[26:27]
v_pk_fma_f32 v[142:143], v[142:143], v[24:25], v[26:27]
v_pk_mul_f32 v[136:137], v[136:137], v[112:113]
v_pk_mul_f32 v[138:139], v[138:139], v[114:115]
v_pk_mul_f32 v[140:141], v[140:141], v[116:117]
v_pk_mul_f32 v[142:143], v[142:143], v[118:119]
v_exp_f32 v136, v136
v_exp_f32 v137, v137
v_exp_f32 v138, v138
v_exp_f32 v139, v139
v_exp_f32 v140, v140
v_exp_f32 v141, v141
v_exp_f32 v142, v142
v_exp_f32 v143, v143
v_pk_add_f32 v[136:137], v[136:137], 1.0 op_sel_hi:[1,0]
v_pk_add_f32 v[138:139], v[138:139], 1.0 op_sel_hi:[1,0]
v_pk_add_f32 v[140:141], v[140:141], 1.0 op_sel_hi:[1,0]
v_pk_add_f32 v[142:143], v[142:143], 1.0 op_sel_hi:[1,0]
v_rcp_f32 v136, v136
v_rcp_f32 v137, v137
v_rcp_f32 v138, v138
v_rcp_f32 v139, v139
v_rcp_f32 v140, v140
v_rcp_f32 v141, v141
v_rcp_f32 v142, v142
v_rcp_f32 v143, v143
v_pk_mul_f32 v[112:113], v[112:113], v[136:137]
v_pk_mul_f32 v[114:115], v[114:115], v[138:139]
v_pk_mul_f32 v[116:117], v[116:117], v[140:141]
v_pk_mul_f32 v[118:119], v[118:119], v[142:143]
v_cvt_pk_bf16_f32 v120, v112, v113
v_cvt_pk_bf16_f32 v121, v114, v115
v_cvt_pk_bf16_f32 v122, v116, v117
v_cvt_pk_bf16_f32 v123, v118, v119
buffer_store_dwordx4 v[120:123], v32, s[48:51], s44 offen offset:3072 sc1
s_waitcnt lgkmcnt(0)
s_barrier
s_lshl_b32 s44, s38, 13
v_add_u32 v9, s44, v32
v_add_u32 v10, 131072, v9
v_add_u32 v11, 81920, v9
ds_read_b128 a[128:131], v10 offset:0
ds_read_b128 a[132:135], v10 offset:1024
ds_read_b128 a[136:139], v10 offset:2048
ds_read_b128 a[140:143], v10 offset:3072
ds_read_b128 a[144:147], v10 offset:4096
ds_read_b128 a[148:151], v10 offset:5120
ds_read_b128 a[152:155], v10 offset:6144
ds_read_b128 a[156:159], v10 offset:7168
ds_read_b128 a[160:163], v11 offset:0
ds_read_b128 a[164:167], v11 offset:1024
ds_read_b128 a[168:171], v11 offset:2048
ds_read_b128 a[172:175], v11 offset:3072
ds_read_b128 a[176:179], v11 offset:4096
ds_read_b128 a[180:183], v11 offset:5120
ds_read_b128 a[184:187], v11 offset:6144
ds_read_b128 a[188:191], v11 offset:7168
v_mov_b32 v57, 0xff800000
v_mov_b32 v48, v32
v_and_b32 v9, 31, v8
v_lshrrev_b32 v10, 5, v8
v_lshlrev_b32 v10, 2, v10
v_sub_u32 v9, v9, v10
s_lshl_b32 s44, s38, 5
v_add_u32 v58, s44, v9
v_subrev_u32 v59, 32, v58
v_subrev_u32 v60, 64, v58
v_subrev_u32 v61, 96, v58
s_mov_b32 s91, 0
s_mov_b32 s78, 0
v_and_b32 v9, 15, v8
v_lshrrev_b32 v10, 5, v8
v_lshl_add_u32 v9, v10, 4, v9
v_lshlrev_b32 v9, 4, v9
v_bfe_u32 v10, v8, 4, 1
v_lshl_add_u32 v62, v10, 10, v9
s_load_dwordx2 s[64:65], s[36:37], 64
s_load_dwordx2 s[68:69], s[36:37], 40
s_waitcnt lgkmcnt(0)
s_waitcnt vmcnt(12) lgkmcnt(0)
s_barrier
s_mov_b32 s83, 0
v_mov_b32 v49, v48
ds_read_b128 a[224:227], v49 offset:0
ds_read_b128 a[228:231], v49 offset:1024
ds_read_b128 a[232:235], v49 offset:2048
ds_read_b128 a[236:239], v49 offset:3072
ds_read_b128 a[240:243], v49 offset:4096
ds_read_b128 a[244:247], v49 offset:5120
ds_read_b128 a[248:251], v49 offset:6144
ds_read_b128 a[252:255], v49 offset:7168
v_mov_b32 v52, 0
v_mov_b32 v53, 0
s_waitcnt lgkmcnt(0)
v_mfma_f32_32x32x16_bf16 v[64:79], a[224:227], a[128:131], 0
s_add_u32 m0, s81, s82
v_accvgpr_write_b32 a0, 0
v_accvgpr_write_b32 a1, 0
v_accvgpr_write_b32 a2, 0
v_accvgpr_write_b32 a3, 0
v_accvgpr_write_b32 a4, 0
v_accvgpr_write_b32 a5, 0
v_accvgpr_write_b32 a6, 0
v_accvgpr_write_b32 a7, 0
v_mfma_f32_32x32x16_bf16 v[80:95], a[224:227], a[160:163], 0
buffer_load_dwordx4 v32, s[52:55], s76 offen sc1 lds
v_accvgpr_write_b32 a8, 0
v_accvgpr_write_b32 a9, 0
v_accvgpr_write_b32 a10, 0
v_accvgpr_write_b32 a11, 0
v_accvgpr_write_b32 a12, 0
v_accvgpr_write_b32 a13, 0
v_accvgpr_write_b32 a14, 0
v_accvgpr_write_b32 a15, 0
v_mfma_f32_32x32x16_bf16 v[64:79], a[228:231], a[132:135], v[64:79]
v_accvgpr_write_b32 a16, 0
v_accvgpr_write_b32 a17, 0
v_accvgpr_write_b32 a18, 0
v_accvgpr_write_b32 a19, 0
v_accvgpr_write_b32 a20, 0
v_accvgpr_write_b32 a21, 0
v_accvgpr_write_b32 a22, 0
v_accvgpr_write_b32 a23, 0
v_mfma_f32_32x32x16_bf16 v[80:95], a[228:231], a[164:167], v[80:95]
buffer_load_dwordx4 v32, s[52:55], s76 offen offset:1024 sc1 lds
v_accvgpr_write_b32 a24, 0
v_accvgpr_write_b32 a25, 0
v_accvgpr_write_b32 a26, 0
v_accvgpr_write_b32 a27, 0
v_accvgpr_write_b32 a28, 0
v_accvgpr_write_b32 a29, 0
v_accvgpr_write_b32 a30, 0
v_accvgpr_write_b32 a31, 0
v_mfma_f32_32x32x16_bf16 v[64:79], a[232:235], a[136:139], v[64:79]
v_accvgpr_write_b32 a32, 0
v_accvgpr_write_b32 a33, 0
v_accvgpr_write_b32 a34, 0
v_accvgpr_write_b32 a35, 0
v_accvgpr_write_b32 a36, 0
v_accvgpr_write_b32 a37, 0
v_accvgpr_write_b32 a38, 0
v_accvgpr_write_b32 a39, 0
v_mfma_f32_32x32x16_bf16 v[80:95], a[232:235], a[168:171], v[80:95]
buffer_load_dwordx4 v32, s[52:55], s76 offen offset:2048 sc1 lds
v_accvgpr_write_b32 a40, 0
v_accvgpr_write_b32 a41, 0
v_accvgpr_write_b32 a42, 0
v_accvgpr_write_b32 a43, 0
v_accvgpr_write_b32 a44, 0
v_accvgpr_write_b32 a45, 0
v_accvgpr_write_b32 a46, 0
v_accvgpr_write_b32 a47, 0
v_mfma_f32_32x32x16_bf16 v[64:79], a[236:239], a[140:143], v[64:79]
v_accvgpr_write_b32 a48, 0
v_accvgpr_write_b32 a49, 0
v_accvgpr_write_b32 a50, 0
v_accvgpr_write_b32 a51, 0
v_accvgpr_write_b32 a52, 0
v_accvgpr_write_b32 a53, 0
v_accvgpr_write_b32 a54, 0
v_accvgpr_write_b32 a55, 0
v_mfma_f32_32x32x16_bf16 v[80:95], a[236:239], a[172:175], v[80:95]
buffer_load_dwordx4 v32, s[52:55], s76 offen offset:3072 sc1 lds
v_accvgpr_write_b32 a56, 0
v_accvgpr_write_b32 a57, 0
v_accvgpr_write_b32 a58, 0
v_accvgpr_write_b32 a59, 0
v_accvgpr_write_b32 a60, 0
v_accvgpr_write_b32 a61, 0
v_accvgpr_write_b32 a62, 0
v_accvgpr_write_b32 a63, 0
v_mfma_f32_32x32x16_bf16 v[64:79], a[240:243], a[144:147], v[64:79]
s_add_u32 s76, s76, 8192
s_add_u32 s82, s82, 16384
s_cmp_eq_u32 s82, 163840
s_cselect_b32 s82, 0, s82
s_add_u32 m0, s81, s82
v_accvgpr_write_b32 a64, 0
v_accvgpr_write_b32 a65, 0
v_accvgpr_write_b32 a66, 0
v_accvgpr_write_b32 a67, 0
v_accvgpr_write_b32 a68, 0
v_accvgpr_write_b32 a69, 0
v_accvgpr_write_b32 a70, 0
v_accvgpr_write_b32 a71, 0
v_mfma_f32_32x32x16_bf16 v[80:95], a[240:243], a[176:179], v[80:95]
buffer_load_dwordx4 v32, s[52:55], s76 offen sc1 lds
v_accvgpr_write_b32 a72, 0
v_accvgpr_write_b32 a73, 0
v_accvgpr_write_b32 a74, 0
v_accvgpr_write_b32 a75, 0
v_accvgpr_write_b32 a76, 0
v_accvgpr_write_b32 a77, 0
v_accvgpr_write_b32 a78, 0
v_accvgpr_write_b32 a79, 0
v_mfma_f32_32x32x16_bf16 v[64:79], a[244:247], a[148:151], v[64:79]
v_accvgpr_write_b32 a80, 0
v_accvgpr_write_b32 a81, 0
v_accvgpr_write_b32 a82, 0
v_accvgpr_write_b32 a83, 0
v_accvgpr_write_b32 a84, 0
v_accvgpr_write_b32 a85, 0
v_accvgpr_write_b32 a86, 0
v_accvgpr_write_b32 a87, 0
v_mfma_f32_32x32x16_bf16 v[80:95], a[244:247], a[180:183], v[80:95]
buffer_load_dwordx4 v32, s[52:55], s76 offen offset:1024 sc1 lds
v_accvgpr_write_b32 a88, 0
v_accvgpr_write_b32 a89, 0
v_accvgpr_write_b32 a90, 0
v_accvgpr_write_b32 a91, 0
v_accvgpr_write_b32 a92, 0
v_accvgpr_write_b32 a93, 0
v_accvgpr_write_b32 a94, 0
v_accvgpr_write_b32 a95, 0
v_mfma_f32_32x32x16_bf16 v[64:79], a[248:251], a[152:155], v[64:79]
v_accvgpr_write_b32 a96, 0
v_accvgpr_write_b32 a97, 0
v_accvgpr_write_b32 a98, 0
v_accvgpr_write_b32 a99, 0
v_accvgpr_write_b32 a100, 0
v_accvgpr_write_b32 a101, 0
v_accvgpr_write_b32 a102, 0
v_accvgpr_write_b32 a103, 0
v_mfma_f32_32x32x16_bf16 v[80:95], a[248:251], a[184:187], v[80:95]
buffer_load_dwordx4 v32, s[52:55], s76 offen offset:2048 sc1 lds
v_accvgpr_write_b32 a104, 0
v_accvgpr_write_b32 a105, 0
v_accvgpr_write_b32 a106, 0
v_accvgpr_write_b32 a107, 0
v_accvgpr_write_b32 a108, 0
v_accvgpr_write_b32 a109, 0
v_accvgpr_write_b32 a110, 0
v_accvgpr_write_b32 a111, 0
v_mfma_f32_32x32x16_bf16 v[64:79], a[252:255], a[156:159], v[64:79]
buffer_load_dwordx4 v32, s[52:55], s76 offen offset:3072 sc1 lds
v_accvgpr_write_b32 a112, 0
v_accvgpr_write_b32 a113, 0
v_accvgpr_write_b32 a114, 0
v_accvgpr_write_b32 a115, 0
v_accvgpr_write_b32 a116, 0
v_accvgpr_write_b32 a117, 0
v_accvgpr_write_b32 a118, 0
v_accvgpr_write_b32 a119, 0
v_mfma_f32_32x32x16_bf16 v[80:95], a[252:255], a[188:191], v[80:95]
s_add_u32 s76, s76, 8192
s_add_u32 s82, s82, 16384
s_cmp_eq_u32 s82, 163840
s_cselect_b32 s82, 0, s82
s_add_u32 s83, s83, 16384
s_cmp_eq_u32 s83, 163840
s_cselect_b32 s83, 0, s83
v_add_u32 v49, s83, v48
v_accvgpr_write_b32 a120, 0
v_accvgpr_write_b32 a121, 0
v_accvgpr_write_b32 a122, 0
v_accvgpr_write_b32 a123, 0
v_accvgpr_write_b32 a124, 0
v_accvgpr_write_b32 a125, 0
v_accvgpr_write_b32 a126, 0
v_accvgpr_write_b32 a127, 0
ds_read_b128 a[224:227], v49 offset:0
ds_read_b128 a[228:231], v49 offset:1024
ds_read_b128 a[232:235], v49 offset:2048
ds_read_b128 a[236:239], v49 offset:3072
ds_read_b128 a[240:243], v49 offset:4096
ds_read_b128 a[244:247], v49 offset:5120
ds_read_b128 a[248:251], v49 offset:6144
ds_read_b128 a[252:255], v49 offset:7168
s_nop 7
s_cmp_lg_u32 s87, 1
s_cbranch_scc1 L_nomask0%=
v_cmp_gt_i32_e64 s[92:93], 0, v58
v_cmp_gt_i32_e64 s[94:95], 1, v58
v_cmp_gt_i32_e64 s[96:97], 2, v58
v_cmp_gt_i32_e64 s[98:99], 3, v58
s_nop 1
v_cndmask_b32_e64 v80, v80, v57, s[92:93]
v_cndmask_b32_e64 v81, v81, v57, s[94:95]
v_cndmask_b32_e64 v82, v82, v57, s[96:97]
v_cndmask_b32_e64 v83, v83, v57, s[98:99]
v_cmp_gt_i32_e64 s[92:93], 16, v58
v_cmp_gt_i32_e64 s[94:95], 17, v58
v_cmp_gt_i32_e64 s[96:97], 18, v58
v_cmp_gt_i32_e64 s[98:99], 19, v58
s_nop 1
v_cndmask_b32_e64 v84, v84, v57, s[92:93]
v_cndmask_b32_e64 v85, v85, v57, s[94:95]
v_cndmask_b32_e64 v86, v86, v57, s[96:97]
v_cndmask_b32_e64 v87, v87, v57, s[98:99]
v_cmp_gt_i32_e64 s[92:93], 8, v58
v_cmp_gt_i32_e64 s[94:95], 9, v58
v_cmp_gt_i32_e64 s[96:97], 10, v58
v_cmp_gt_i32_e64 s[98:99], 11, v58
s_nop 1
v_cndmask_b32_e64 v88, v88, v57, s[92:93]
v_cndmask_b32_e64 v89, v89, v57, s[94:95]
v_cndmask_b32_e64 v90, v90, v57, s[96:97]
v_cndmask_b32_e64 v91, v91, v57, s[98:99]
v_cmp_gt_i32_e64 s[92:93], 24, v58
v_cmp_gt_i32_e64 s[94:95], 25, v58
v_cmp_gt_i32_e64 s[96:97], 26, v58
v_cmp_gt_i32_e64 s[98:99], 27, v58
s_nop 1
v_cndmask_b32_e64 v92, v92, v57, s[92:93]
v_cndmask_b32_e64 v93, v93, v57, s[94:95]
v_cndmask_b32_e64 v94, v94, v57, s[96:97]
v_cndmask_b32_e64 v95, v95, v57, s[98:99]
v_cmp_gt_i32_e64 s[96:97], 18, v58
v_cmp_gt_i32_e64 s[98:99], 19, v58
L_nomask0%=:
v_max3_f32 v9, v64, v65, v66
v_max3_f32 v11, v80, v81, v82
v_max3_f32 v9, v9, v67, v68
v_max3_f32 v11, v11, v83, v84
v_max3_f32 v9, v9, v69, v70
v_max3_f32 v11, v11, v85, v86
v_max3_f32 v9, v9, v71, v72
v_max3_f32 v11, v11, v87, v88
v_max3_f32 v9, v9, v73, v74
v_max3_f32 v11, v11, v89, v90
v_max3_f32 v9, v9, v75, v76
v_max3_f32 v11, v11, v91, v92
v_max3_f32 v9, v9, v77, v78
v_max3_f32 v11, v11, v93, v94
v_max_f32 v9, v9, v79
v_max_f32 v11, v11, v95
v_mov_b32 v10, v9
v_mov_b32 v12, v11
s_nop 0
v_permlane32_swap_b32 v9, v10
v_permlane32_swap_b32 v11, v12
v_max_f32 v9, v9, v10
v_max_f32 v11, v11, v12
v_max_f32 v54, 0xf149f2ca, v9
v_max_f32 v55, 0xf149f2ca, v11
v_sub_f32 v44, 0, v54
v_sub_f32 v45, 0, v54
v_sub_f32 v46, 0, v55
v_sub_f32 v47, 0, v55
v_pk_add_f32 v[64:65], v[64:65], v[44:45]
v_pk_add_f32 v[66:67], v[66:67], v[44:45]
v_pk_add_f32 v[68:69], v[68:69], v[44:45]
v_pk_add_f32 v[70:71], v[70:71], v[44:45]
v_pk_add_f32 v[72:73], v[72:73], v[44:45]
v_pk_add_f32 v[74:75], v[74:75], v[44:45]
v_pk_add_f32 v[76:77], v[76:77], v[44:45]
v_pk_add_f32 v[78:79], v[78:79], v[44:45]
v_pk_add_f32 v[80:81], v[80:81], v[46:47]
v_pk_add_f32 v[82:83], v[82:83], v[46:47]
v_pk_add_f32 v[84:85], v[84:85], v[46:47]
v_pk_add_f32 v[86:87], v[86:87], v[46:47]
v_pk_add_f32 v[88:89], v[88:89], v[46:47]
v_pk_add_f32 v[90:91], v[90:91], v[46:47]
v_pk_add_f32 v[92:93], v[92:93], v[46:47]
v_pk_add_f32 v[94:95], v[94:95], v[46:47]
v_pk_mov_b32 v[160:161], v[44:45], v[44:45] op_sel:[0,1]
v_pk_mov_b32 v[162:163], v[44:45], v[44:45] op_sel:[0,1]
v_pk_mov_b32 v[164:165], v[44:45], v[44:45] op_sel:[0,1]
v_pk_mov_b32 v[166:167], v[44:45], v[44:45] op_sel:[0,1]
v_pk_mov_b32 v[168:169], v[44:45], v[44:45] op_sel:[0,1]
v_pk_mov_b32 v[170:171], v[44:45], v[44:45] op_sel:[0,1]
v_pk_mov_b32 v[172:173], v[44:45], v[44:45] op_sel:[0,1]
v_pk_mov_b32 v[174:175], v[44:45], v[44:45] op_sel:[0,1]
v_pk_mov_b32 v[176:177], v[46:47], v[46:47] op_sel:[0,1]
v_pk_mov_b32 v[178:179], v[46:47], v[46:47] op_sel:[0,1]
v_pk_mov_b32 v[180:181], v[46:47], v[46:47] op_sel:[0,1]
v_pk_mov_b32 v[182:183], v[46:47], v[46:47] op_sel:[0,1]
v_pk_mov_b32 v[184:185], v[46:47], v[46:47] op_sel:[0,1]
v_pk_mov_b32 v[186:187], v[46:47], v[46:47] op_sel:[0,1]
v_pk_mov_b32 v[188:189], v[46:47], v[46:47] op_sel:[0,1]
v_pk_mov_b32 v[190:191], v[46:47], v[46:47] op_sel:[0,1]
s_waitcnt lgkmcnt(0)
s_add_u32 m0, s81, s82
s_nop 0
buffer_load_dwordx4 v32, s[52:55], s76 offen sc1 lds
buffer_load_dwordx4 v32, s[52:55], s76 offen offset:1024 sc1 lds
buffer_load_dwordx4 v32, s[52:55], s76 offen offset:2048 sc1 lds
buffer_load_dwordx4 v32, s[52:55], s76 offen offset:3072 sc1 lds
s_add_u32 s76, s76, 8192
s_add_u32 s82, s82, 16384
s_cmp_eq_u32 s82, 163840
s_cselect_b32 s82, 0, s82
s_barrier
v_exp_f32 v64, v64
s_nop 0
v_add_f32 v52, v52, v64
v_exp_f32 v65, v65
s_nop 0
v_add_f32 v52, v52, v65
v_exp_f32 v66, v66
v_cvt_pk_bf16_f32 v128, v64, v65
v_add_f32 v52, v52, v66
v_exp_f32 v67, v67
s_nop 0
v_add_f32 v52, v52, v67
v_exp_f32 v68, v68
v_cvt_pk_bf16_f32 v129, v66, v67
v_add_f32 v52, v52, v68
v_exp_f32 v69, v69
s_nop 0
v_add_f32 v52, v52, v69
v_exp_f32 v70, v70
v_cvt_pk_bf16_f32 v130, v68, v69
v_add_f32 v52, v52, v70
v_exp_f32 v71, v71
s_nop 0
v_add_f32 v52, v52, v71
v_exp_f32 v80, v80
v_cvt_pk_bf16_f32 v131, v70, v71
v_add_f32 v53, v53, v80
v_exp_f32 v81, v81
s_nop 0
v_add_f32 v53, v53, v81
v_exp_f32 v82, v82
v_cvt_pk_bf16_f32 v136, v80, v81
v_add_f32 v53, v53, v82
v_exp_f32 v83, v83
s_nop 0
v_add_f32 v53, v53, v83
v_exp_f32 v84, v84
v_cvt_pk_bf16_f32 v137, v82, v83
s_sub_u32 s90, s88, s87
s_lshl_b32 s90, s90, 1
s_sub_i32 s90, s90, 3
s_lshl_b32 s89, s87, 1
s_sub_u32 s89, s89, 2
s_cmp_eq_u32 s89, 0
s_cbranch_scc1 L_fm0Q1%=
s_branch L_fQ1%=
L_f%=:
s_waitcnt vmcnt(16) lgkmcnt(0)
s_barrier
v_mfma_f32_32x32x16_bf16 a[0:15], a[192:195], v[144:147], a[0:15]
ds_read_b128 a[224:227], v49 offset:0
v_add_f32 v53, v53, v124
v_exp_f32 v125, v125
v_mfma_f32_32x32x16_bf16 a[16:31], a[192:195], v[152:155], a[16:31]
ds_read_b128 a[228:231], v49 offset:1024
v_add_f32 v53, v53, v125
v_exp_f32 v126, v126
v_cvt_pk_bf16_f32 v158, v124, v125
s_cmp_eq_u32 s78, 0
s_cbranch_scc1 L_lpa%=
L_lpba%=:
v_mfma_f32_32x32x16_bf16 a[32:47], a[196:199], v[144:147], a[32:47]
ds_read_b128 a[232:235], v49 offset:2048
v_add_f32 v53, v53, v126
v_exp_f32 v127, v127
v_mfma_f32_32x32x16_bf16 a[48:63], a[196:199], v[152:155], a[48:63]
ds_read_b128 a[236:239], v49 offset:3072
v_add_f32 v53, v53, v127
v_exp_f32 v64, v64
v_cvt_pk_bf16_f32 v159, v126, v127
v_mfma_f32_32x32x16_bf16 a[64:79], a[200:203], v[144:147], a[64:79]
ds_read_b128 a[240:243], v49 offset:4096
v_add_f32 v52, v52, v64
v_exp_f32 v65, v65
v_mfma_f32_32x32x16_bf16 a[80:95], a[200:203], v[152:155], a[80:95]
ds_read_b128 a[244:247], v49 offset:5120
v_add_f32 v52, v52, v65
v_exp_f32 v66, v66
v_cvt_pk_bf16_f32 v128, v64, v65
v_mfma_f32_32x32x16_bf16 a[96:111], a[204:207], v[144:147], a[96:111]
ds_read_b128 a[248:251], v49 offset:6144
v_add_f32 v52, v52, v66
v_exp_f32 v67, v67
v_mfma_f32_32x32x16_bf16 a[112:127], a[204:207], v[152:155], a[112:127]
ds_read_b128 a[252:255], v49 offset:7168
v_add_f32 v52, v52, v67
v_exp_f32 v68, v68
v_cvt_pk_bf16_f32 v129, v66, v67
s_add_u32 m0, s81, s82
v_mfma_f32_32x32x16_bf16 a[0:15], a[208:211], v[148:151], a[0:15]
buffer_load_dwordx4 v32, s[52:55], s76 offen sc1 lds
v_add_f32 v52, v52, v68
v_exp_f32 v69, v69
v_mfma_f32_32x32x16_bf16 a[16:31], a[208:211], v[156:159], a[16:31]
v_add_f32 v52, v52, v69
v_exp_f32 v70, v70
v_cvt_pk_bf16_f32 v130, v68, v69
v_mfma_f32_32x32x16_bf16 a[32:47], a[212:215], v[148:151], a[32:47]
buffer_load_dwordx4 v32, s[52:55], s76 offen offset:1024 sc1 lds
v_add_f32 v52, v52, v70
v_exp_f32 v71, v71
v_mfma_f32_32x32x16_bf16 a[48:63], a[212:215], v[156:159], a[48:63]
v_add_f32 v52, v52, v71
v_exp_f32 v80, v80
v_cvt_pk_bf16_f32 v131, v70, v71
v_mfma_f32_32x32x16_bf16 a[64:79], a[216:219], v[148:151], a[64:79]
buffer_load_dwordx4 v32, s[52:55], s76 offen offset:2048 sc1 lds
v_add_f32 v53, v53, v80
v_exp_f32 v81, v81
v_mfma_f32_32x32x16_bf16 a[80:95], a[216:219], v[156:159], a[80:95]
v_add_f32 v53, v53, v81
v_exp_f32 v82, v82
v_cvt_pk_bf16_f32 v136, v80, v81
v_mfma_f32_32x32x16_bf16 a[96:111], a[220:223], v[148:151], a[96:111]
buffer_load_dwordx4 v32, s[52:55], s76 offen offset:3072 sc1 lds
v_add_f32 v53, v53, v82
v_exp_f32 v83, v83
v_mfma_f32_32x32x16_bf16 a[112:127], a[220:223], v[156:159], a[112:127]
v_add_f32 v53, v53, v83
v_exp_f32 v84, v84
v_cvt_pk_bf16_f32 v137, v82, v83
s_add_u32 s76, s76, 8192
s_add_u32 s82, s82, 16384
L_fQ1%=:
s_waitcnt lgkmcnt(0)
v_mfma_f32_32x32x16_bf16 v[96:111], a[224:227], a[128:131], v[160:175]
ds_read_b128 a[192:195], v49 offset:8192
v_add_f32 v53, v53, v84
v_exp_f32 v85, v85
s_cmp_eq_u32 s82, 163840
s_cselect_b32 s82, 0, s82
v_mfma_f32_32x32x16_bf16 v[112:127], a[224:227], a[160:163], v[176:191]
ds_read_b128 a[196:199], v49 offset:9216
v_add_f32 v53, v53, v85
v_exp_f32 v86, v86
v_cvt_pk_bf16_f32 v138, v84, v85
v_mfma_f32_32x32x16_bf16 v[96:111], a[228:231], a[132:135], v[96:111]
ds_read_b128 a[200:203], v49 offset:10240
v_add_f32 v53, v53, v86
v_exp_f32 v87, v87
v_mfma_f32_32x32x16_bf16 v[112:127], a[228:231], a[164:167], v[112:127]
ds_read_b128 a[204:207], v49 offset:11264
v_add_f32 v53, v53, v87
v_exp_f32 v72, v72
v_cvt_pk_bf16_f32 v139, v86, v87
v_mfma_f32_32x32x16_bf16 v[96:111], a[232:235], a[136:139], v[96:111]
ds_read_b128 a[208:211], v49 offset:12288
v_add_f32 v52, v52, v72
v_exp_f32 v73, v73
s_add_u32 s83, s83, 16384
v_mfma_f32_32x32x16_bf16 v[112:127], a[232:235], a[168:171], v[112:127]
ds_read_b128 a[212:215], v49 offset:13312
v_add_f32 v52, v52, v73
v_exp_f32 v74, v74
v_cvt_pk_bf16_f32 v132, v72, v73
v_mfma_f32_32x32x16_bf16 v[96:111], a[236:239], a[140:143], v[96:111]
ds_read_b128 a[216:219], v49 offset:14336
v_add_f32 v52, v52, v74
v_exp_f32 v75, v75
s_cmp_eq_u32 s83, 163840
s_cselect_b32 s83, 0, s83
v_mfma_f32_32x32x16_bf16 v[112:127], a[236:239], a[172:175], v[112:127]
ds_read_b128 a[220:223], v49 offset:15360
v_add_f32 v52, v52, v75
v_exp_f32 v76, v76
v_cvt_pk_bf16_f32 v133, v74, v75
v_add_u32 v49, s83, v48
v_mfma_f32_32x32x16_bf16 v[96:111], a[240:243], a[144:147], v[96:111]
v_add_f32 v52, v52, v76
v_exp_f32 v77, v77
v_mfma_f32_32x32x16_bf16 v[112:127], a[240:243], a[176:179], v[112:127]
v_add_f32 v52, v52, v77
v_exp_f32 v78, v78
v_cvt_pk_bf16_f32 v134, v76, v77
v_mfma_f32_32x32x16_bf16 v[96:111], a[244:247], a[148:151], v[96:111]
v_add_f32 v52, v52, v78
v_exp_f32 v79, v79
v_mfma_f32_32x32x16_bf16 v[112:127], a[244:247], a[180:183], v[112:127]
v_add_f32 v52, v52, v79
v_exp_f32 v88, v88
v_cvt_pk_bf16_f32 v135, v78, v79
v_mfma_f32_32x32x16_bf16 v[96:111], a[248:251], a[152:155], v[96:111]
v_add_f32 v53, v53, v88
v_exp_f32 v89, v89
v_mfma_f32_32x32x16_bf16 v[112:127], a[248:251], a[184:187], v[112:127]
v_add_f32 v53, v53, v89
v_exp_f32 v90, v90
v_cvt_pk_bf16_f32 v140, v88, v89
v_mfma_f32_32x32x16_bf16 v[96:111], a[252:255], a[156:159], v[96:111]
v_add_f32 v53, v53, v90
v_exp_f32 v91, v91
v_mfma_f32_32x32x16_bf16 v[112:127], a[252:255], a[188:191], v[112:127]
v_add_f32 v53, v53, v91
v_exp_f32 v92, v92
v_cvt_pk_bf16_f32 v141, v90, v91
s_waitcnt lgkmcnt(0)
v_mfma_f32_32x32x16_bf16 a[0:15], a[192:195], v[128:131], a[0:15]
ds_read_b128 a[224:227], v49 offset:0
v_add_f32 v53, v53, v92
v_exp_f32 v93, v93
v_mfma_f32_32x32x16_bf16 a[16:31], a[192:195], v[136:139], a[16:31]
ds_read_b128 a[228:231], v49 offset:1024
v_add_f32 v53, v53, v93
v_exp_f32 v94, v94
v_cvt_pk_bf16_f32 v142, v92, v93
v_mfma_f32_32x32x16_bf16 a[32:47], a[196:199], v[128:131], a[32:47]
ds_read_b128 a[232:235], v49 offset:2048
v_add_f32 v53, v53, v94
v_exp_f32 v95, v95
v_mfma_f32_32x32x16_bf16 a[48:63], a[196:199], v[136:139], a[48:63]
ds_read_b128 a[236:239], v49 offset:3072
v_add_f32 v53, v53, v95
v_exp_f32 v96, v96
v_cvt_pk_bf16_f32 v143, v94, v95
v_mfma_f32_32x32x16_bf16 a[64:79], a[200:203], v[128:131], a[64:79]
ds_read_b128 a[240:243], v49 offset:4096
v_add_f32 v52, v52, v96
v_exp_f32 v97, v97
v_mfma_f32_32x32x16_bf16 a[80:95], a[200:203], v[136:139], a[80:95]
ds_read_b128 a[244:247], v49 offset:5120
v_add_f32 v52, v52, v97
v_exp_f32 v98, v98
v_cvt_pk_bf16_f32 v144, v96, v97
v_mfma_f32_32x32x16_bf16 a[96:111], a[204:207], v[128:131], a[96:111]
ds_read_b128 a[248:251], v49 offset:6144
v_add_f32 v52, v52, v98
v_exp_f32 v99, v99
v_mfma_f32_32x32x16_bf16 a[112:127], a[204:207], v[136:139], a[112:127]
ds_read_b128 a[252:255], v49 offset:7168
v_add_f32 v52, v52, v99
v_exp_f32 v100, v100
v_cvt_pk_bf16_f32 v145, v98, v99
s_add_u32 m0, s81, s82
v_mfma_f32_32x32x16_bf16 a[0:15], a[208:211], v[132:135], a[0:15]
buffer_load_dwordx4 v32, s[52:55], s76 offen sc1 lds
v_add_f32 v52, v52, v100
v_exp_f32 v101, v101
v_mfma_f32_32x32x16_bf16 a[16:31], a[208:211], v[140:143], a[16:31]
v_add_f32 v52, v52, v101
v_exp_f32 v102, v102
v_cvt_pk_bf16_f32 v146, v100, v101
v_mfma_f32_32x32x16_bf16 a[32:47], a[212:215], v[132:135], a[32:47]
buffer_load_dwordx4 v32, s[52:55], s76 offen offset:1024 sc1 lds
v_add_f32 v52, v52, v102
v_exp_f32 v103, v103
v_mfma_f32_32x32x16_bf16 a[48:63], a[212:215], v[140:143], a[48:63]
v_add_f32 v52, v52, v103
v_exp_f32 v112, v112
v_cvt_pk_bf16_f32 v147, v102, v103
v_mfma_f32_32x32x16_bf16 a[64:79], a[216:219], v[132:135], a[64:79]
buffer_load_dwordx4 v32, s[52:55], s76 offen offset:2048 sc1 lds
v_add_f32 v53, v53, v112
v_exp_f32 v113, v113
v_mfma_f32_32x32x16_bf16 a[80:95], a[216:219], v[140:143], a[80:95]
v_add_f32 v53, v53, v113
v_exp_f32 v114, v114
v_cvt_pk_bf16_f32 v152, v112, v113
v_mfma_f32_32x32x16_bf16 a[96:111], a[220:223], v[132:135], a[96:111]
buffer_load_dwordx4 v32, s[52:55], s76 offen offset:3072 sc1 lds
v_add_f32 v53, v53, v114
v_exp_f32 v115, v115
v_mfma_f32_32x32x16_bf16 a[112:127], a[220:223], v[140:143], a[112:127]
v_add_f32 v53, v53, v115
v_exp_f32 v116, v116
v_cvt_pk_bf16_f32 v153, v114, v115
s_add_u32 s76, s76, 8192
s_add_u32 s82, s82, 16384
s_waitcnt lgkmcnt(0)
v_mfma_f32_32x32x16_bf16 v[64:79], a[224:227], a[128:131], v[160:175]
ds_read_b128 a[192:195], v49 offset:8192
v_add_f32 v53, v53, v116
v_exp_f32 v117, v117
s_cmp_eq_u32 s82, 163840
s_cselect_b32 s82, 0, s82
v_mfma_f32_32x32x16_bf16 v[80:95], a[224:227], a[160:163], v[176:191]
ds_read_b128 a[196:199], v49 offset:9216
v_add_f32 v53, v53, v117
v_exp_f32 v118, v118
v_cvt_pk_bf16_f32 v154, v116, v117
v_mfma_f32_32x32x16_bf16 v[64:79], a[228:231], a[132:135], v[64:79]
ds_read_b128 a[200:203], v49 offset:10240
v_add_f32 v53, v53, v118
v_exp_f32 v119, v119
v_mfma_f32_32x32x16_bf16 v[80:95], a[228:231], a[164:167], v[80:95]
ds_read_b128 a[204:207], v49 offset:11264
v_add_f32 v53, v53, v119
v_exp_f32 v104, v104
v_cvt_pk_bf16_f32 v155, v118, v119
v_mfma_f32_32x32x16_bf16 v[64:79], a[232:235], a[136:139], v[64:79]
ds_read_b128 a[208:211], v49 offset:12288
v_add_f32 v52, v52, v104
v_exp_f32 v105, v105
s_sub_u32 s89, s89, 1
v_mfma_f32_32x32x16_bf16 v[80:95], a[232:235], a[168:171], v[80:95]
ds_read_b128 a[212:215], v49 offset:13312
v_add_f32 v52, v52, v105
v_exp_f32 v106, v106
v_cvt_pk_bf16_f32 v148, v104, v105
s_add_u32 s83, s83, 16384
v_mfma_f32_32x32x16_bf16 v[64:79], a[236:239], a[140:143], v[64:79]
ds_read_b128 a[216:219], v49 offset:14336
v_add_f32 v52, v52, v106
v_exp_f32 v107, v107
s_cmp_eq_u32 s83, 163840
s_cselect_b32 s83, 0, s83
v_mfma_f32_32x32x16_bf16 v[80:95], a[236:239], a[172:175], v[80:95]
ds_read_b128 a[220:223], v49 offset:15360
v_add_f32 v52, v52, v107
v_exp_f32 v108, v108
v_cvt_pk_bf16_f32 v149, v106, v107
v_add_u32 v49, s83, v48
v_mfma_f32_32x32x16_bf16 v[64:79], a[240:243], a[144:147], v[64:79]
v_add_f32 v52, v52, v108
v_exp_f32 v109, v109
v_mfma_f32_32x32x16_bf16 v[80:95], a[240:243], a[176:179], v[80:95]
v_add_f32 v52, v52, v109
v_exp_f32 v110, v110
v_cvt_pk_bf16_f32 v150, v108, v109
v_mfma_f32_32x32x16_bf16 v[64:79], a[244:247], a[148:151], v[64:79]
v_add_f32 v52, v52, v110
v_exp_f32 v111, v111
v_mfma_f32_32x32x16_bf16 v[80:95], a[244:247], a[180:183], v[80:95]
v_add_f32 v52, v52, v111
v_exp_f32 v120, v120
v_cvt_pk_bf16_f32 v151, v110, v111
v_mfma_f32_32x32x16_bf16 v[64:79], a[248:251], a[152:155], v[64:79]
v_add_f32 v53, v53, v120
v_exp_f32 v121, v121
v_mfma_f32_32x32x16_bf16 v[80:95], a[248:251], a[184:187], v[80:95]
v_add_f32 v53, v53, v121
v_exp_f32 v122, v122
v_cvt_pk_bf16_f32 v156, v120, v121
v_mfma_f32_32x32x16_bf16 v[64:79], a[252:255], a[156:159], v[64:79]
v_add_f32 v53, v53, v122
v_exp_f32 v123, v123
v_mfma_f32_32x32x16_bf16 v[80:95], a[252:255], a[188:191], v[80:95]
v_add_f32 v53, v53, v123
v_exp_f32 v124, v124
v_cvt_pk_bf16_f32 v157, v122, v123
s_cmp_lg_u32 s89, 0
s_cbranch_scc1 L_f%=
s_waitcnt vmcnt(16) lgkmcnt(0)
s_barrier
v_mfma_f32_32x32x16_bf16 a[0:15], a[192:195], v[144:147], a[0:15]
ds_read_b128 a[224:227], v49 offset:0
v_add_f32 v53, v53, v124
v_exp_f32 v125, v125
v_mfma_f32_32x32x16_bf16 a[16:31], a[192:195], v[152:155], a[16:31]
ds_read_b128 a[228:231], v49 offset:1024
v_add_f32 v53, v53, v125
v_exp_f32 v126, v126
v_cvt_pk_bf16_f32 v158, v124, v125
v_mfma_f32_32x32x16_bf16 a[32:47], a[196:199], v[144:147], a[32:47]
ds_read_b128 a[232:235], v49 offset:2048
v_add_f32 v53, v53, v126
v_exp_f32 v127, v127
v_mfma_f32_32x32x16_bf16 a[48:63], a[196:199], v[152:155], a[48:63]
ds_read_b128 a[236:239], v49 offset:3072
v_add_f32 v53, v53, v127
v_exp_f32 v64, v64
v_cvt_pk_bf16_f32 v159, v126, v127
v_mfma_f32_32x32x16_bf16 a[64:79], a[200:203], v[144:147], a[64:79]
ds_read_b128 a[240:243], v49 offset:4096
v_add_f32 v52, v52, v64
v_exp_f32 v65, v65
v_mfma_f32_32x32x16_bf16 a[80:95], a[200:203], v[152:155], a[80:95]
ds_read_b128 a[244:247], v49 offset:5120
v_add_f32 v52, v52, v65
v_exp_f32 v66, v66
v_cvt_pk_bf16_f32 v128, v64, v65
v_mfma_f32_32x32x16_bf16 a[96:111], a[204:207], v[144:147], a[96:111]
ds_read_b128 a[248:251], v49 offset:6144
v_add_f32 v52, v52, v66
v_exp_f32 v67, v67
v_mfma_f32_32x32x16_bf16 a[112:127], a[204:207], v[152:155], a[112:127]
ds_read_b128 a[252:255], v49 offset:7168
v_add_f32 v52, v52, v67
v_exp_f32 v68, v68
v_cvt_pk_bf16_f32 v129, v66, v67
s_add_u32 m0, s81, s82
v_mfma_f32_32x32x16_bf16 a[0:15], a[208:211], v[148:151], a[0:15]
buffer_load_dwordx4 v32, s[52:55], s76 offen sc1 lds
v_cmp_gt_i32_e64 s[92:93], 0, v58
v_add_f32 v52, v52, v68
v_exp_f32 v69, v69
v_mfma_f32_32x32x16_bf16 a[16:31], a[208:211], v[156:159], a[16:31]
v_cmp_gt_i32_e64 s[94:95], 1, v58
v_add_f32 v52, v52, v69
v_exp_f32 v70, v70
v_cvt_pk_bf16_f32 v130, v68, v69
v_mfma_f32_32x32x16_bf16 a[32:47], a[212:215], v[148:151], a[32:47]
buffer_load_dwordx4 v32, s[52:55], s76 offen offset:1024 sc1 lds
v_cndmask_b32_e64 v80, v80, v57, s[92:93]
v_cmp_gt_i32_e64 s[96:97], 2, v58
v_add_f32 v52, v52, v70
v_exp_f32 v71, v71
v_mfma_f32_32x32x16_bf16 a[48:63], a[212:215], v[156:159], a[48:63]
v_cndmask_b32_e64 v81, v81, v57, s[94:95]
v_cmp_gt_i32_e64 s[98:99], 3, v58
v_add_f32 v52, v52, v71
v_exp_f32 v80, v80
v_cvt_pk_bf16_f32 v131, v70, v71
v_mfma_f32_32x32x16_bf16 a[64:79], a[216:219], v[148:151], a[64:79]
buffer_load_dwordx4 v32, s[52:55], s76 offen offset:2048 sc1 lds
v_cndmask_b32_e64 v82, v82, v57, s[96:97]
v_cmp_gt_i32_e64 s[92:93], 16, v58
v_add_f32 v53, v53, v80
v_exp_f32 v81, v81
v_mfma_f32_32x32x16_bf16 a[80:95], a[216:219], v[156:159], a[80:95]
v_cndmask_b32_e64 v83, v83, v57, s[98:99]
v_cmp_gt_i32_e64 s[94:95], 17, v58
v_add_f32 v53, v53, v81
v_exp_f32 v82, v82
v_cvt_pk_bf16_f32 v136, v80, v81
v_mfma_f32_32x32x16_bf16 a[96:111], a[220:223], v[148:151], a[96:111]
buffer_load_dwordx4 v32, s[52:55], s76 offen offset:3072 sc1 lds
v_cndmask_b32_e64 v84, v84, v57, s[92:93]
v_cmp_gt_i32_e64 s[96:97], 18, v58
v_add_f32 v53, v53, v82
v_exp_f32 v83, v83
v_mfma_f32_32x32x16_bf16 a[112:127], a[220:223], v[156:159], a[112:127]
v_cndmask_b32_e64 v85, v85, v57, s[94:95]
v_cmp_gt_i32_e64 s[98:99], 19, v58
v_add_f32 v53, v53, v83
v_exp_f32 v84, v84
v_cvt_pk_bf16_f32 v137, v82, v83
s_add_u32 s76, s76, 8192
s_add_u32 s82, s82, 16384
L_fm0Q1%=:
s_waitcnt lgkmcnt(0)
v_mfma_f32_32x32x16_bf16 v[96:111], a[224:227], a[128:131], v[160:175]
ds_read_b128 a[192:195], v49 offset:8192
v_cndmask_b32_e64 v86, v86, v57, s[96:97]
v_add_f32 v53, v53, v84
v_exp_f32 v85, v85
s_cmp_eq_u32 s82, 163840
s_cselect_b32 s82, 0, s82
v_mfma_f32_32x32x16_bf16 v[112:127], a[224:227], a[160:163], v[176:191]
ds_read_b128 a[196:199], v49 offset:9216
v_cndmask_b32_e64 v87, v87, v57, s[98:99]
v_add_f32 v53, v53, v85
v_exp_f32 v86, v86
v_cvt_pk_bf16_f32 v138, v84, v85
v_mfma_f32_32x32x16_bf16 v[96:111], a[228:231], a[132:135], v[96:111]
ds_read_b128 a[200:203], v49 offset:10240
v_add_f32 v53, v53, v86
v_exp_f32 v87, v87
v_mfma_f32_32x32x16_bf16 v[112:127], a[228:231], a[164:167], v[112:127]
ds_read_b128 a[204:207], v49 offset:11264
v_add_f32 v53, v53, v87
v_exp_f32 v72, v72
v_cvt_pk_bf16_f32 v139, v86, v87
v_mfma_f32_32x32x16_bf16 v[96:111], a[232:235], a[136:139], v[96:111]
ds_read_b128 a[208:211], v49 offset:12288
v_add_f32 v52, v52, v72
v_exp_f32 v73, v73
s_add_u32 s83, s83, 16384
v_mfma_f32_32x32x16_bf16 v[112:127], a[232:235], a[168:171], v[112:127]
ds_read_b128 a[212:215], v49 offset:13312
v_add_f32 v52, v52, v73
v_exp_f32 v74, v74
v_cvt_pk_bf16_f32 v132, v72, v73
v_mfma_f32_32x32x16_bf16 v[96:111], a[236:239], a[140:143], v[96:111]
ds_read_b128 a[216:219], v49 offset:14336
v_add_f32 v52, v52, v74
v_exp_f32 v75, v75
s_cmp_eq_u32 s83, 163840
s_cselect_b32 s83, 0, s83
v_mfma_f32_32x32x16_bf16 v[112:127], a[236:239], a[172:175], v[112:127]
ds_read_b128 a[220:223], v49 offset:15360
v_add_f32 v52, v52, v75
v_exp_f32 v76, v76
v_cvt_pk_bf16_f32 v133, v74, v75
v_add_u32 v49, s83, v48
v_mfma_f32_32x32x16_bf16 v[96:111], a[240:243], a[144:147], v[96:111]
v_cmp_gt_i32_e64 s[92:93], 8, v58
v_add_f32 v52, v52, v76
v_exp_f32 v77, v77
v_mfma_f32_32x32x16_bf16 v[112:127], a[240:243], a[176:179], v[112:127]
v_cmp_gt_i32_e64 s[94:95], 9, v58
v_add_f32 v52, v52, v77
v_exp_f32 v78, v78
v_cvt_pk_bf16_f32 v134, v76, v77
v_mfma_f32_32x32x16_bf16 v[96:111], a[244:247], a[148:151], v[96:111]
v_cndmask_b32_e64 v88, v88, v57, s[92:93]
v_cmp_gt_i32_e64 s[96:97], 10, v58
v_add_f32 v52, v52, v78
v_exp_f32 v79, v79
v_mfma_f32_32x32x16_bf16 v[112:127], a[244:247], a[180:183], v[112:127]
v_cndmask_b32_e64 v89, v89, v57, s[94:95]
v_cmp_gt_i32_e64 s[98:99], 11, v58
v_add_f32 v52, v52, v79
v_exp_f32 v88, v88
v_cvt_pk_bf16_f32 v135, v78, v79
v_mfma_f32_32x32x16_bf16 v[96:111], a[248:251], a[152:155], v[96:111]
v_cndmask_b32_e64 v90, v90, v57, s[96:97]
v_cmp_gt_i32_e64 s[92:93], 24, v58
v_add_f32 v53, v53, v88
v_exp_f32 v89, v89
v_mfma_f32_32x32x16_bf16 v[112:127], a[248:251], a[184:187], v[112:127]
v_cndmask_b32_e64 v91, v91, v57, s[98:99]
v_cmp_gt_i32_e64 s[94:95], 25, v58
v_add_f32 v53, v53, v89
v_exp_f32 v90, v90
v_cvt_pk_bf16_f32 v140, v88, v89
v_mfma_f32_32x32x16_bf16 v[96:111], a[252:255], a[156:159], v[96:111]
v_cndmask_b32_e64 v92, v92, v57, s[92:93]
v_cmp_gt_i32_e64 s[96:97], 26, v58
v_add_f32 v53, v53, v90
v_exp_f32 v91, v91
v_mfma_f32_32x32x16_bf16 v[112:127], a[252:255], a[188:191], v[112:127]
v_cndmask_b32_e64 v93, v93, v57, s[94:95]
v_cmp_gt_i32_e64 s[98:99], 27, v58
v_add_f32 v53, v53, v91
v_exp_f32 v92, v92
v_cvt_pk_bf16_f32 v141, v90, v91
s_waitcnt lgkmcnt(0)
v_mfma_f32_32x32x16_bf16 a[0:15], a[192:195], v[128:131], a[0:15]
ds_read_b128 a[224:227], v49 offset:0
v_cndmask_b32_e64 v94, v94, v57, s[96:97]
v_add_f32 v53, v53, v92
v_exp_f32 v93, v93
v_mfma_f32_32x32x16_bf16 a[16:31], a[192:195], v[136:139], a[16:31]
ds_read_b128 a[228:231], v49 offset:1024
v_cndmask_b32_e64 v95, v95, v57, s[98:99]
v_add_f32 v53, v53, v93
v_exp_f32 v94, v94
v_cvt_pk_bf16_f32 v142, v92, v93
v_mfma_f32_32x32x16_bf16 a[32:47], a[196:199], v[128:131], a[32:47]
ds_read_b128 a[232:235], v49 offset:2048
v_add_f32 v53, v53, v94
v_exp_f32 v95, v95
v_mfma_f32_32x32x16_bf16 a[48:63], a[196:199], v[136:139], a[48:63]
ds_read_b128 a[236:239], v49 offset:3072
v_add_f32 v53, v53, v95
v_exp_f32 v96, v96
v_cvt_pk_bf16_f32 v143, v94, v95
v_mfma_f32_32x32x16_bf16 a[64:79], a[200:203], v[128:131], a[64:79]
ds_read_b128 a[240:243], v49 offset:4096
v_add_f32 v52, v52, v96
v_exp_f32 v97, v97
v_mfma_f32_32x32x16_bf16 a[80:95], a[200:203], v[136:139], a[80:95]
ds_read_b128 a[244:247], v49 offset:5120
v_add_f32 v52, v52, v97
v_exp_f32 v98, v98
v_cvt_pk_bf16_f32 v144, v96, v97
v_mfma_f32_32x32x16_bf16 a[96:111], a[204:207], v[128:131], a[96:111]
ds_read_b128 a[248:251], v49 offset:6144
v_add_f32 v52, v52, v98
v_exp_f32 v99, v99
v_mfma_f32_32x32x16_bf16 a[112:127], a[204:207], v[136:139], a[112:127]
ds_read_b128 a[252:255], v49 offset:7168
v_add_f32 v52, v52, v99
v_exp_f32 v100, v100
v_cvt_pk_bf16_f32 v145, v98, v99
s_add_u32 m0, s81, s82
v_mfma_f32_32x32x16_bf16 a[0:15], a[208:211], v[132:135], a[0:15]
buffer_load_dwordx4 v32, s[52:55], s76 offen sc1 lds
v_cmp_gt_i32_e64 s[92:93], 0, v59
v_add_f32 v52, v52, v100
v_exp_f32 v101, v101
v_mfma_f32_32x32x16_bf16 a[16:31], a[208:211], v[140:143], a[16:31]
v_cmp_gt_i32_e64 s[94:95], 1, v59
v_add_f32 v52, v52, v101
v_exp_f32 v102, v102
v_cvt_pk_bf16_f32 v146, v100, v101
v_mfma_f32_32x32x16_bf16 a[32:47], a[212:215], v[132:135], a[32:47]
buffer_load_dwordx4 v32, s[52:55], s76 offen offset:1024 sc1 lds
v_cndmask_b32_e64 v112, v112, v57, s[92:93]
v_cmp_gt_i32_e64 s[96:97], 2, v59
v_add_f32 v52, v52, v102
v_exp_f32 v103, v103
v_mfma_f32_32x32x16_bf16 a[48:63], a[212:215], v[140:143], a[48:63]
v_cndmask_b32_e64 v113, v113, v57, s[94:95]
v_cmp_gt_i32_e64 s[98:99], 3, v59
v_add_f32 v52, v52, v103
v_exp_f32 v112, v112
v_cvt_pk_bf16_f32 v147, v102, v103
v_mfma_f32_32x32x16_bf16 a[64:79], a[216:219], v[132:135], a[64:79]
buffer_load_dwordx4 v32, s[52:55], s76 offen offset:2048 sc1 lds
v_cndmask_b32_e64 v114, v114, v57, s[96:97]
v_cmp_gt_i32_e64 s[92:93], 16, v59
v_add_f32 v53, v53, v112
v_exp_f32 v113, v113
v_mfma_f32_32x32x16_bf16 a[80:95], a[216:219], v[140:143], a[80:95]
v_cndmask_b32_e64 v115, v115, v57, s[98:99]
v_cmp_gt_i32_e64 s[94:95], 17, v59
v_add_f32 v53, v53, v113
v_exp_f32 v114, v114
v_cvt_pk_bf16_f32 v152, v112, v113
v_mfma_f32_32x32x16_bf16 a[96:111], a[220:223], v[132:135], a[96:111]
buffer_load_dwordx4 v32, s[52:55], s76 offen offset:3072 sc1 lds
v_cndmask_b32_e64 v116, v116, v57, s[92:93]
v_cmp_gt_i32_e64 s[96:97], 18, v59
v_add_f32 v53, v53, v114
v_exp_f32 v115, v115
v_mfma_f32_32x32x16_bf16 a[112:127], a[220:223], v[140:143], a[112:127]
v_cndmask_b32_e64 v117, v117, v57, s[94:95]
v_cmp_gt_i32_e64 s[98:99], 19, v59
v_add_f32 v53, v53, v115
v_exp_f32 v116, v116
v_cvt_pk_bf16_f32 v153, v114, v115
s_add_u32 s76, s76, 8192
s_add_u32 s82, s82, 16384
s_waitcnt lgkmcnt(0)
v_mfma_f32_32x32x16_bf16 v[64:79], a[224:227], a[128:131], v[160:175]
ds_read_b128 a[192:195], v49 offset:8192
v_cndmask_b32_e64 v118, v118, v57, s[96:97]
v_add_f32 v53, v53, v116
v_exp_f32 v117, v117
s_cmp_eq_u32 s82, 163840
s_cselect_b32 s82, 0, s82
v_mfma_f32_32x32x16_bf16 v[80:95], a[224:227], a[160:163], v[176:191]
ds_read_b128 a[196:199], v49 offset:9216
v_cndmask_b32_e64 v119, v119, v57, s[98:99]
v_add_f32 v53, v53, v117
v_exp_f32 v118, v118
v_cvt_pk_bf16_f32 v154, v116, v117
v_mfma_f32_32x32x16_bf16 v[64:79], a[228:231], a[132:135], v[64:79]
ds_read_b128 a[200:203], v49 offset:10240
v_add_f32 v53, v53, v118
v_exp_f32 v119, v119
v_mfma_f32_32x32x16_bf16 v[80:95], a[228:231], a[164:167], v[80:95]
ds_read_b128 a[204:207], v49 offset:11264
v_add_f32 v53, v53, v119
v_exp_f32 v104, v104
v_cvt_pk_bf16_f32 v155, v118, v119
v_mfma_f32_32x32x16_bf16 v[64:79], a[232:235], a[136:139], v[64:79]
ds_read_b128 a[208:211], v49 offset:12288
v_add_f32 v52, v52, v104
v_exp_f32 v105, v105
v_mfma_f32_32x32x16_bf16 v[80:95], a[232:235], a[168:171], v[80:95]
ds_read_b128 a[212:215], v49 offset:13312
v_add_f32 v52, v52, v105
v_exp_f32 v106, v106
v_cvt_pk_bf16_f32 v148, v104, v105
s_add_u32 s83, s83, 16384
v_mfma_f32_32x32x16_bf16 v[64:79], a[236:239], a[140:143], v[64:79]
ds_read_b128 a[216:219], v49 offset:14336
v_add_f32 v52, v52, v106
v_exp_f32 v107, v107
s_cmp_eq_u32 s83, 163840
s_cselect_b32 s83, 0, s83
v_mfma_f32_32x32x16_bf16 v[80:95], a[236:239], a[172:175], v[80:95]
ds_read_b128 a[220:223], v49 offset:15360
v_add_f32 v52, v52, v107
v_exp_f32 v108, v108
v_cvt_pk_bf16_f32 v149, v106, v107
v_add_u32 v49, s83, v48
v_mfma_f32_32x32x16_bf16 v[64:79], a[240:243], a[144:147], v[64:79]
v_cmp_gt_i32_e64 s[92:93], 8, v59
v_add_f32 v52, v52, v108
v_exp_f32 v109, v109
v_mfma_f32_32x32x16_bf16 v[80:95], a[240:243], a[176:179], v[80:95]
v_cmp_gt_i32_e64 s[94:95], 9, v59
v_add_f32 v52, v52, v109
v_exp_f32 v110, v110
v_cvt_pk_bf16_f32 v150, v108, v109
v_mfma_f32_32x32x16_bf16 v[64:79], a[244:247], a[148:151], v[64:79]
v_cndmask_b32_e64 v120, v120, v57, s[92:93]
v_cmp_gt_i32_e64 s[96:97], 10, v59
v_add_f32 v52, v52, v110
v_exp_f32 v111, v111
v_mfma_f32_32x32x16_bf16 v[80:95], a[244:247], a[180:183], v[80:95]
v_cndmask_b32_e64 v121, v121, v57, s[94:95]
v_cmp_gt_i32_e64 s[98:99], 11, v59
v_add_f32 v52, v52, v111
v_exp_f32 v120, v120
v_cvt_pk_bf16_f32 v151, v110, v111
v_mfma_f32_32x32x16_bf16 v[64:79], a[248:251], a[152:155], v[64:79]
v_cndmask_b32_e64 v122, v122, v57, s[96:97]
v_cmp_gt_i32_e64 s[92:93], 24, v59
v_add_f32 v53, v53, v120
v_exp_f32 v121, v121
v_mfma_f32_32x32x16_bf16 v[80:95], a[248:251], a[184:187], v[80:95]
v_cndmask_b32_e64 v123, v123, v57, s[98:99]
v_cmp_gt_i32_e64 s[94:95], 25, v59
v_add_f32 v53, v53, v121
v_exp_f32 v122, v122
v_cvt_pk_bf16_f32 v156, v120, v121
v_mfma_f32_32x32x16_bf16 v[64:79], a[252:255], a[156:159], v[64:79]
v_cndmask_b32_e64 v124, v124, v57, s[92:93]
v_cmp_gt_i32_e64 s[96:97], 26, v59
v_add_f32 v53, v53, v122
v_exp_f32 v123, v123
v_mfma_f32_32x32x16_bf16 v[80:95], a[252:255], a[188:191], v[80:95]
v_cndmask_b32_e64 v125, v125, v57, s[94:95]
v_cmp_gt_i32_e64 s[98:99], 27, v59
v_add_f32 v53, v53, v123
v_exp_f32 v124, v124
v_cvt_pk_bf16_f32 v157, v122, v123
s_waitcnt vmcnt(16) lgkmcnt(0)
s_barrier
v_mfma_f32_32x32x16_bf16 a[0:15], a[192:195], v[144:147], a[0:15]
ds_read_b128 a[224:227], v49 offset:0
v_cndmask_b32_e64 v126, v126, v57, s[96:97]
v_add_f32 v53, v53, v124
v_exp_f32 v125, v125
v_mfma_f32_32x32x16_bf16 a[16:31], a[192:195], v[152:155], a[16:31]
ds_read_b128 a[228:231], v49 offset:1024
v_cndmask_b32_e64 v127, v127, v57, s[98:99]
v_add_f32 v53, v53, v125
v_exp_f32 v126, v126
v_cvt_pk_bf16_f32 v158, v124, v125
s_cmp_eq_u32 s78, 0
s_cbranch_scc1 L_lpb%=
L_lpbb%=:
v_mfma_f32_32x32x16_bf16 a[32:47], a[196:199], v[144:147], a[32:47]
ds_read_b128 a[232:235], v49 offset:2048
v_add_f32 v53, v53, v126
v_exp_f32 v127, v127
v_mfma_f32_32x32x16_bf16 a[48:63], a[196:199], v[152:155], a[48:63]
ds_read_b128 a[236:239], v49 offset:3072
v_add_f32 v53, v53, v127
v_exp_f32 v64, v64
v_cvt_pk_bf16_f32 v159, v126, v127
v_mfma_f32_32x32x16_bf16 a[64:79], a[200:203], v[144:147], a[64:79]
ds_read_b128 a[240:243], v49 offset:4096
v_add_f32 v52, v52, v64
v_exp_f32 v65, v65
v_mfma_f32_32x32x16_bf16 a[80:95], a[200:203], v[152:155], a[80:95]
ds_read_b128 a[244:247], v49 offset:5120
v_add_f32 v52, v52, v65
v_exp_f32 v66, v66
v_cvt_pk_bf16_f32 v128, v64, v65
v_mfma_f32_32x32x16_bf16 a[96:111], a[204:207], v[144:147], a[96:111]
ds_read_b128 a[248:251], v49 offset:6144
v_add_f32 v52, v52, v66
v_exp_f32 v67, v67
v_mfma_f32_32x32x16_bf16 a[112:127], a[204:207], v[152:155], a[112:127]
ds_read_b128 a[252:255], v49 offset:7168
v_add_f32 v52, v52, v67
v_exp_f32 v68, v68
v_cvt_pk_bf16_f32 v129, v66, v67
s_add_u32 m0, s81, s82
v_mfma_f32_32x32x16_bf16 a[0:15], a[208:211], v[148:151], a[0:15]
buffer_load_dwordx4 v32, s[52:55], s76 offen sc1 lds
v_cmp_gt_i32_e64 s[92:93], 0, v60
v_add_f32 v52, v52, v68
v_exp_f32 v69, v69
v_mfma_f32_32x32x16_bf16 a[16:31], a[208:211], v[156:159], a[16:31]
v_cmp_gt_i32_e64 s[94:95], 1, v60
v_add_f32 v52, v52, v69
v_exp_f32 v70, v70
v_cvt_pk_bf16_f32 v130, v68, v69
v_mfma_f32_32x32x16_bf16 a[32:47], a[212:215], v[148:151], a[32:47]
buffer_load_dwordx4 v32, s[52:55], s76 offen offset:1024 sc1 lds
v_cndmask_b32_e64 v80, v80, v57, s[92:93]
v_cmp_gt_i32_e64 s[96:97], 2, v60
v_add_f32 v52, v52, v70
v_exp_f32 v71, v71
v_mfma_f32_32x32x16_bf16 a[48:63], a[212:215], v[156:159], a[48:63]
v_cndmask_b32_e64 v81, v81, v57, s[94:95]
v_cmp_gt_i32_e64 s[98:99], 3, v60
v_add_f32 v52, v52, v71
v_exp_f32 v80, v80
v_cvt_pk_bf16_f32 v131, v70, v71
v_mfma_f32_32x32x16_bf16 a[64:79], a[216:219], v[148:151], a[64:79]
buffer_load_dwordx4 v32, s[52:55], s76 offen offset:2048 sc1 lds
v_cndmask_b32_e64 v82, v82, v57, s[96:97]
v_cmp_gt_i32_e64 s[92:93], 16, v60
v_add_f32 v53, v53, v80
v_exp_f32 v81, v81
v_mfma_f32_32x32x16_bf16 a[80:95], a[216:219], v[156:159], a[80:95]
v_cndmask_b32_e64 v83, v83, v57, s[98:99]
v_cmp_gt_i32_e64 s[94:95], 17, v60
v_add_f32 v53, v53, v81
v_exp_f32 v82, v82
v_cvt_pk_bf16_f32 v136, v80, v81
v_mfma_f32_32x32x16_bf16 a[96:111], a[220:223], v[148:151], a[96:111]
buffer_load_dwordx4 v32, s[52:55], s76 offen offset:3072 sc1 lds
v_cndmask_b32_e64 v84, v84, v57, s[92:93]
v_cmp_gt_i32_e64 s[96:97], 18, v60
v_add_f32 v53, v53, v82
v_exp_f32 v83, v83
v_mfma_f32_32x32x16_bf16 a[112:127], a[220:223], v[156:159], a[112:127]
v_cndmask_b32_e64 v85, v85, v57, s[94:95]
v_cmp_gt_i32_e64 s[98:99], 19, v60
v_add_f32 v53, v53, v83
v_exp_f32 v84, v84
v_cvt_pk_bf16_f32 v137, v82, v83
s_add_u32 s76, s76, 8192
s_add_u32 s82, s82, 16384
s_waitcnt lgkmcnt(0)
v_mfma_f32_32x32x16_bf16 v[96:111], a[224:227], a[128:131], v[160:175]
ds_read_b128 a[192:195], v49 offset:8192
v_cndmask_b32_e64 v86, v86, v57, s[96:97]
v_add_f32 v53, v53, v84
v_exp_f32 v85, v85
s_cmp_eq_u32 s82, 163840
s_cselect_b32 s82, 0, s82
v_mfma_f32_32x32x16_bf16 v[112:127], a[224:227], a[160:163], v[176:191]
ds_read_b128 a[196:199], v49 offset:9216
v_cndmask_b32_e64 v87, v87, v57, s[98:99]
v_add_f32 v53, v53, v85
v_exp_f32 v86, v86
v_cvt_pk_bf16_f32 v138, v84, v85
v_mfma_f32_32x32x16_bf16 v[96:111], a[228:231], a[132:135], v[96:111]
ds_read_b128 a[200:203], v49 offset:10240
v_add_f32 v53, v53, v86
v_exp_f32 v87, v87
v_mfma_f32_32x32x16_bf16 v[112:127], a[228:231], a[164:167], v[112:127]
ds_read_b128 a[204:207], v49 offset:11264
v_add_f32 v53, v53, v87
v_exp_f32 v72, v72
v_cvt_pk_bf16_f32 v139, v86, v87
v_mfma_f32_32x32x16_bf16 v[96:111], a[232:235], a[136:139], v[96:111]
ds_read_b128 a[208:211], v49 offset:12288
v_add_f32 v52, v52, v72
v_exp_f32 v73, v73
s_add_u32 s83, s83, 16384
v_mfma_f32_32x32x16_bf16 v[112:127], a[232:235], a[168:171], v[112:127]
ds_read_b128 a[212:215], v49 offset:13312
v_add_f32 v52, v52, v73
v_exp_f32 v74, v74
v_cvt_pk_bf16_f32 v132, v72, v73
v_mfma_f32_32x32x16_bf16 v[96:111], a[236:239], a[140:143], v[96:111]
ds_read_b128 a[216:219], v49 offset:14336
v_add_f32 v52, v52, v74
v_exp_f32 v75, v75
s_cmp_eq_u32 s83, 163840
s_cselect_b32 s83, 0, s83
v_mfma_f32_32x32x16_bf16 v[112:127], a[236:239], a[172:175], v[112:127]
ds_read_b128 a[220:223], v49 offset:15360
v_add_f32 v52, v52, v75
v_exp_f32 v76, v76
v_cvt_pk_bf16_f32 v133, v74, v75
v_add_u32 v49, s83, v48
v_mfma_f32_32x32x16_bf16 v[96:111], a[240:243], a[144:147], v[96:111]
v_cmp_gt_i32_e64 s[92:93], 8, v60
v_add_f32 v52, v52, v76
v_exp_f32 v77, v77
v_mfma_f32_32x32x16_bf16 v[112:127], a[240:243], a[176:179], v[112:127]
v_cmp_gt_i32_e64 s[94:95], 9, v60
v_add_f32 v52, v52, v77
v_exp_f32 v78, v78
v_cvt_pk_bf16_f32 v134, v76, v77
v_mfma_f32_32x32x16_bf16 v[96:111], a[244:247], a[148:151], v[96:111]
v_cndmask_b32_e64 v88, v88, v57, s[92:93]
v_cmp_gt_i32_e64 s[96:97], 10, v60
v_add_f32 v52, v52, v78
v_exp_f32 v79, v79
v_mfma_f32_32x32x16_bf16 v[112:127], a[244:247], a[180:183], v[112:127]
v_cndmask_b32_e64 v89, v89, v57, s[94:95]
v_cmp_gt_i32_e64 s[98:99], 11, v60
v_add_f32 v52, v52, v79
v_exp_f32 v88, v88
v_cvt_pk_bf16_f32 v135, v78, v79
v_mfma_f32_32x32x16_bf16 v[96:111], a[248:251], a[152:155], v[96:111]
v_cndmask_b32_e64 v90, v90, v57, s[96:97]
v_cmp_gt_i32_e64 s[92:93], 24, v60
v_add_f32 v53, v53, v88
v_exp_f32 v89, v89
v_mfma_f32_32x32x16_bf16 v[112:127], a[248:251], a[184:187], v[112:127]
v_cndmask_b32_e64 v91, v91, v57, s[98:99]
v_cmp_gt_i32_e64 s[94:95], 25, v60
v_add_f32 v53, v53, v89
v_exp_f32 v90, v90
v_cvt_pk_bf16_f32 v140, v88, v89
v_mfma_f32_32x32x16_bf16 v[96:111], a[252:255], a[156:159], v[96:111]
v_cndmask_b32_e64 v92, v92, v57, s[92:93]
v_cmp_gt_i32_e64 s[96:97], 26, v60
v_add_f32 v53, v53, v90
v_exp_f32 v91, v91
v_mfma_f32_32x32x16_bf16 v[112:127], a[252:255], a[188:191], v[112:127]
v_cndmask_b32_e64 v93, v93, v57, s[94:95]
v_cmp_gt_i32_e64 s[98:99], 27, v60
v_add_f32 v53, v53, v91
v_exp_f32 v92, v92
v_cvt_pk_bf16_f32 v141, v90, v91
s_waitcnt lgkmcnt(0)
v_mfma_f32_32x32x16_bf16 a[0:15], a[192:195], v[128:131], a[0:15]
ds_read_b128 a[224:227], v49 offset:0
v_cndmask_b32_e64 v94, v94, v57, s[96:97]
v_add_f32 v53, v53, v92
v_exp_f32 v93, v93
v_mfma_f32_32x32x16_bf16 a[16:31], a[192:195], v[136:139], a[16:31]
ds_read_b128 a[228:231], v49 offset:1024
v_cndmask_b32_e64 v95, v95, v57, s[98:99]
v_add_f32 v53, v53, v93
v_exp_f32 v94, v94
v_cvt_pk_bf16_f32 v142, v92, v93
v_mfma_f32_32x32x16_bf16 a[32:47], a[196:199], v[128:131], a[32:47]
ds_read_b128 a[232:235], v49 offset:2048
v_add_f32 v53, v53, v94
v_exp_f32 v95, v95
v_mfma_f32_32x32x16_bf16 a[48:63], a[196:199], v[136:139], a[48:63]
ds_read_b128 a[236:239], v49 offset:3072
v_add_f32 v53, v53, v95
v_exp_f32 v96, v96
v_cvt_pk_bf16_f32 v143, v94, v95
v_mfma_f32_32x32x16_bf16 a[64:79], a[200:203], v[128:131], a[64:79]
ds_read_b128 a[240:243], v49 offset:4096
v_add_f32 v52, v52, v96
v_exp_f32 v97, v97
v_mfma_f32_32x32x16_bf16 a[80:95], a[200:203], v[136:139], a[80:95]
ds_read_b128 a[244:247], v49 offset:5120
v_add_f32 v52, v52, v97
v_exp_f32 v98, v98
v_cvt_pk_bf16_f32 v144, v96, v97
v_mfma_f32_32x32x16_bf16 a[96:111], a[204:207], v[128:131], a[96:111]
ds_read_b128 a[248:251], v49 offset:6144
v_add_f32 v52, v52, v98
v_exp_f32 v99, v99
v_mfma_f32_32x32x16_bf16 a[112:127], a[204:207], v[136:139], a[112:127]
ds_read_b128 a[252:255], v49 offset:7168
v_add_f32 v52, v52, v99
v_exp_f32 v100, v100
v_cvt_pk_bf16_f32 v145, v98, v99
s_add_u32 m0, s81, s82
v_mfma_f32_32x32x16_bf16 a[0:15], a[208:211], v[132:135], a[0:15]
buffer_load_dwordx4 v32, s[52:55], s76 offen sc1 lds
v_cmp_gt_i32_e64 s[92:93], 0, v61
v_add_f32 v52, v52, v100
v_exp_f32 v101, v101
v_mfma_f32_32x32x16_bf16 a[16:31], a[208:211], v[140:143], a[16:31]
v_cmp_gt_i32_e64 s[94:95], 1, v61
v_add_f32 v52, v52, v101
v_exp_f32 v102, v102
v_cvt_pk_bf16_f32 v146, v100, v101
v_mfma_f32_32x32x16_bf16 a[32:47], a[212:215], v[132:135], a[32:47]
buffer_load_dwordx4 v32, s[52:55], s76 offen offset:1024 sc1 lds
v_cndmask_b32_e64 v112, v112, v57, s[92:93]
v_cmp_gt_i32_e64 s[96:97], 2, v61
v_add_f32 v52, v52, v102
v_exp_f32 v103, v103
v_mfma_f32_32x32x16_bf16 a[48:63], a[212:215], v[140:143], a[48:63]
v_cndmask_b32_e64 v113, v113, v57, s[94:95]
v_cmp_gt_i32_e64 s[98:99], 3, v61
v_add_f32 v52, v52, v103
v_exp_f32 v112, v112
v_cvt_pk_bf16_f32 v147, v102, v103
v_mfma_f32_32x32x16_bf16 a[64:79], a[216:219], v[132:135], a[64:79]
buffer_load_dwordx4 v32, s[52:55], s76 offen offset:2048 sc1 lds
v_cndmask_b32_e64 v114, v114, v57, s[96:97]
v_cmp_gt_i32_e64 s[92:93], 16, v61
v_add_f32 v53, v53, v112
v_exp_f32 v113, v113
v_mfma_f32_32x32x16_bf16 a[80:95], a[216:219], v[140:143], a[80:95]
v_cndmask_b32_e64 v115, v115, v57, s[98:99]
v_cmp_gt_i32_e64 s[94:95], 17, v61
v_add_f32 v53, v53, v113
v_exp_f32 v114, v114
v_cvt_pk_bf16_f32 v152, v112, v113
v_mfma_f32_32x32x16_bf16 a[96:111], a[220:223], v[132:135], a[96:111]
buffer_load_dwordx4 v32, s[52:55], s76 offen offset:3072 sc1 lds
v_cndmask_b32_e64 v116, v116, v57, s[92:93]
v_cmp_gt_i32_e64 s[96:97], 18, v61
v_add_f32 v53, v53, v114
v_exp_f32 v115, v115
v_mfma_f32_32x32x16_bf16 a[112:127], a[220:223], v[140:143], a[112:127]
v_cndmask_b32_e64 v117, v117, v57, s[94:95]
v_cmp_gt_i32_e64 s[98:99], 19, v61
v_add_f32 v53, v53, v115
v_exp_f32 v116, v116
v_cvt_pk_bf16_f32 v153, v114, v115
s_add_u32 s76, s76, 8192
s_add_u32 s82, s82, 16384
s_waitcnt lgkmcnt(0)
v_mfma_f32_32x32x16_bf16 v[64:79], a[224:227], a[128:131], v[160:175]
ds_read_b128 a[192:195], v49 offset:8192
v_cndmask_b32_e64 v118, v118, v57, s[96:97]
v_add_f32 v53, v53, v116
v_exp_f32 v117, v117
s_cmp_eq_u32 s82, 163840
s_cselect_b32 s82, 0, s82
v_mfma_f32_32x32x16_bf16 v[80:95], a[224:227], a[160:163], v[176:191]
ds_read_b128 a[196:199], v49 offset:9216
v_cndmask_b32_e64 v119, v119, v57, s[98:99]
v_add_f32 v53, v53, v117
v_exp_f32 v118, v118
v_cvt_pk_bf16_f32 v154, v116, v117
v_mfma_f32_32x32x16_bf16 v[64:79], a[228:231], a[132:135], v[64:79]
ds_read_b128 a[200:203], v49 offset:10240
v_add_f32 v53, v53, v118
v_exp_f32 v119, v119
v_mfma_f32_32x32x16_bf16 v[80:95], a[228:231], a[164:167], v[80:95]
ds_read_b128 a[204:207], v49 offset:11264
v_add_f32 v53, v53, v119
v_exp_f32 v104, v104
v_cvt_pk_bf16_f32 v155, v118, v119
v_mfma_f32_32x32x16_bf16 v[64:79], a[232:235], a[136:139], v[64:79]
ds_read_b128 a[208:211], v49 offset:12288
v_add_f32 v52, v52, v104
v_exp_f32 v105, v105
v_mfma_f32_32x32x16_bf16 v[80:95], a[232:235], a[168:171], v[80:95]
ds_read_b128 a[212:215], v49 offset:13312
v_add_f32 v52, v52, v105
v_exp_f32 v106, v106
v_cvt_pk_bf16_f32 v148, v104, v105
s_add_u32 s83, s83, 16384
v_mfma_f32_32x32x16_bf16 v[64:79], a[236:239], a[140:143], v[64:79]
ds_read_b128 a[216:219], v49 offset:14336
v_add_f32 v52, v52, v106
v_exp_f32 v107, v107
s_cmp_eq_u32 s83, 163840
s_cselect_b32 s83, 0, s83
v_mfma_f32_32x32x16_bf16 v[80:95], a[236:239], a[172:175], v[80:95]
ds_read_b128 a[220:223], v49 offset:15360
v_add_f32 v52, v52, v107
v_exp_f32 v108, v108
v_cvt_pk_bf16_f32 v149, v106, v107
v_add_u32 v49, s83, v48
v_mfma_f32_32x32x16_bf16 v[64:79], a[240:243], a[144:147], v[64:79]
v_cmp_gt_i32_e64 s[92:93], 8, v61
v_add_f32 v52, v52, v108
v_exp_f32 v109, v109
v_mfma_f32_32x32x16_bf16 v[80:95], a[240:243], a[176:179], v[80:95]
v_cmp_gt_i32_e64 s[94:95], 9, v61
v_add_f32 v52, v52, v109
v_exp_f32 v110, v110
v_cvt_pk_bf16_f32 v150, v108, v109
v_mfma_f32_32x32x16_bf16 v[64:79], a[244:247], a[148:151], v[64:79]
v_cndmask_b32_e64 v120, v120, v57, s[92:93]
v_cmp_gt_i32_e64 s[96:97], 10, v61
v_add_f32 v52, v52, v110
v_exp_f32 v111, v111
v_mfma_f32_32x32x16_bf16 v[80:95], a[244:247], a[180:183], v[80:95]
v_cndmask_b32_e64 v121, v121, v57, s[94:95]
v_cmp_gt_i32_e64 s[98:99], 11, v61
v_add_f32 v52, v52, v111
v_exp_f32 v120, v120
v_cvt_pk_bf16_f32 v151, v110, v111
v_mfma_f32_32x32x16_bf16 v[64:79], a[248:251], a[152:155], v[64:79]
v_cndmask_b32_e64 v122, v122, v57, s[96:97]
v_cmp_gt_i32_e64 s[92:93], 24, v61
v_add_f32 v53, v53, v120
v_exp_f32 v121, v121
v_mfma_f32_32x32x16_bf16 v[80:95], a[248:251], a[184:187], v[80:95]
v_cndmask_b32_e64 v123, v123, v57, s[98:99]
v_cmp_gt_i32_e64 s[94:95], 25, v61
v_add_f32 v53, v53, v121
v_exp_f32 v122, v122
v_cvt_pk_bf16_f32 v156, v120, v121
v_mfma_f32_32x32x16_bf16 v[64:79], a[252:255], a[156:159], v[64:79]
v_cndmask_b32_e64 v124, v124, v57, s[92:93]
v_cmp_gt_i32_e64 s[96:97], 26, v61
v_add_f32 v53, v53, v122
v_exp_f32 v123, v123
v_mfma_f32_32x32x16_bf16 v[80:95], a[252:255], a[188:191], v[80:95]
v_cndmask_b32_e64 v125, v125, v57, s[94:95]
v_cmp_gt_i32_e64 s[98:99], 27, v61
v_add_f32 v53, v53, v123
v_exp_f32 v124, v124
v_cvt_pk_bf16_f32 v157, v122, v123
s_cmp_lt_i32 s90, 0
s_cbranch_scc1 L_tm0%=
s_waitcnt vmcnt(16) lgkmcnt(0)
s_barrier
v_mfma_f32_32x32x16_bf16 a[0:15], a[192:195], v[144:147], a[0:15]
ds_read_b128 a[224:227], v49 offset:0
v_cndmask_b32_e64 v126, v126, v57, s[96:97]
v_add_f32 v53, v53, v124
v_exp_f32 v125, v125
v_mfma_f32_32x32x16_bf16 a[16:31], a[192:195], v[152:155], a[16:31]
ds_read_b128 a[228:231], v49 offset:1024
v_cndmask_b32_e64 v127, v127, v57, s[98:99]
v_add_f32 v53, v53, v125
v_exp_f32 v126, v126
v_cvt_pk_bf16_f32 v158, v124, v125
v_mfma_f32_32x32x16_bf16 a[32:47], a[196:199], v[144:147], a[32:47]
ds_read_b128 a[232:235], v49 offset:2048
v_exp_f32 v64, v64
v_add_f32 v53, v53, v126
v_exp_f32 v127, v127
v_mfma_f32_32x32x16_bf16 a[48:63], a[196:199], v[152:155], a[48:63]
ds_read_b128 a[236:239], v49 offset:3072
v_exp_f32 v65, v65
v_add_f32 v53, v53, v127
v_add_f32 v52, v52, v64
v_cvt_pk_bf16_f32 v159, v126, v127
v_mfma_f32_32x32x16_bf16 a[64:79], a[200:203], v[144:147], a[64:79]
ds_read_b128 a[240:243], v49 offset:4096
v_exp_f32 v66, v66
v_add_f32 v52, v52, v65
v_cvt_pk_bf16_f32 v128, v64, v65
v_mfma_f32_32x32x16_bf16 a[80:95], a[200:203], v[152:155], a[80:95]
ds_read_b128 a[244:247], v49 offset:5120
v_exp_f32 v67, v67
v_add_f32 v52, v52, v66
v_mfma_f32_32x32x16_bf16 a[96:111], a[204:207], v[144:147], a[96:111]
ds_read_b128 a[248:251], v49 offset:6144
v_exp_f32 v68, v68
v_add_f32 v52, v52, v67
v_cvt_pk_bf16_f32 v129, v66, v67
v_mfma_f32_32x32x16_bf16 a[112:127], a[204:207], v[152:155], a[112:127]
ds_read_b128 a[252:255], v49 offset:7168
v_exp_f32 v69, v69
v_add_f32 v52, v52, v68
s_add_u32 m0, s81, s82
v_mfma_f32_32x32x16_bf16 a[0:15], a[208:211], v[148:151], a[0:15]
buffer_load_dwordx4 v32, s[52:55], s76 offen sc1 lds
v_exp_f32 v70, v70
v_add_f32 v52, v52, v69
v_cvt_pk_bf16_f32 v130, v68, v69
v_mfma_f32_32x32x16_bf16 a[16:31], a[208:211], v[156:159], a[16:31]
v_exp_f32 v71, v71
v_add_f32 v52, v52, v70
v_mfma_f32_32x32x16_bf16 a[32:47], a[212:215], v[148:151], a[32:47]
buffer_load_dwordx4 v32, s[52:55], s76 offen offset:1024 sc1 lds
v_exp_f32 v72, v72
v_add_f32 v52, v52, v71
v_cvt_pk_bf16_f32 v131, v70, v71
v_mfma_f32_32x32x16_bf16 a[48:63], a[212:215], v[156:159], a[48:63]
v_exp_f32 v73, v73
v_add_f32 v52, v52, v72
v_mfma_f32_32x32x16_bf16 a[64:79], a[216:219], v[148:151], a[64:79]
buffer_load_dwordx4 v32, s[52:55], s76 offen offset:2048 sc1 lds
v_exp_f32 v74, v74
v_add_f32 v52, v52, v73
v_cvt_pk_bf16_f32 v132, v72, v73
v_mfma_f32_32x32x16_bf16 a[80:95], a[216:219], v[156:159], a[80:95]
v_exp_f32 v75, v75
v_add_f32 v52, v52, v74
v_mfma_f32_32x32x16_bf16 a[96:111], a[220:223], v[148:151], a[96:111]
buffer_load_dwordx4 v32, s[52:55], s76 offen offset:3072 sc1 lds
v_exp_f32 v76, v76
v_add_f32 v52, v52, v75
v_cvt_pk_bf16_f32 v133, v74, v75
v_mfma_f32_32x32x16_bf16 a[112:127], a[220:223], v[156:159], a[112:127]
v_exp_f32 v77, v77
v_add_f32 v52, v52, v76
s_add_u32 s76, s76, 8192
s_add_u32 s82, s82, 16384
s_nop 7
s_nop 3
v_mov_b32 v9, v53
s_nop 1
v_permlane32_swap_b32 v53, v9
v_add_f32 v53, v53, v9
v_cmp_nge_f32 vcc, 0x6c800000, v53
v_rcp_f32 v222, v53
s_mov_b32 s44, s41
s_mul_i32 s44, s44, 1490944
s_lshl_b32 s45, s40, 15
s_add_u32 s44, s44, s45
s_lshl_b32 s45, s38, 11
s_add_u32 s44, s44, s45
s_add_u32 s44, s44, 36192256
s_or_b32 s91, s91, vcc_lo
s_or_b32 s91, s91, vcc_hi
v_mov_b32 v223, v222
v_accvgpr_read_b32 v192, a16
v_accvgpr_read_b32 v193, a17
v_accvgpr_read_b32 v194, a18
v_accvgpr_read_b32 v195, a19
v_accvgpr_read_b32 v196, a24
v_accvgpr_read_b32 v197, a25
v_accvgpr_read_b32 v198, a26
v_accvgpr_read_b32 v199, a27
v_pk_mul_f32 v[192:193], v[192:193], v[222:223]
v_pk_mul_f32 v[194:195], v[194:195], v[222:223]
v_pk_mul_f32 v[196:197], v[196:197], v[222:223]
v_pk_mul_f32 v[198:199], v[198:199], v[222:223]
v_cvt_pk_bf16_f32 v200, v192, v193
v_cvt_pk_bf16_f32 v201, v194, v195
v_cvt_pk_bf16_f32 v202, v196, v197
v_cvt_pk_bf16_f32 v203, v198, v199
buffer_store_dwordx4 v[200:203], v62, s[48:51], s44 offen offset:0 sc1
v_accvgpr_read_b32 v206, a20
v_accvgpr_read_b32 v207, a21
v_accvgpr_read_b32 v208, a22
v_accvgpr_read_b32 v209, a23
v_accvgpr_read_b32 v210, a28
v_accvgpr_read_b32 v211, a29
v_accvgpr_read_b32 v212, a30
v_accvgpr_read_b32 v213, a31
v_pk_mul_f32 v[206:207], v[206:207], v[222:223]
v_pk_mul_f32 v[208:209], v[208:209], v[222:223]
v_pk_mul_f32 v[210:211], v[210:211], v[222:223]
v_pk_mul_f32 v[212:213], v[212:213], v[222:223]
v_cvt_pk_bf16_f32 v214, v206, v207
v_cvt_pk_bf16_f32 v215, v208, v209
v_cvt_pk_bf16_f32 v216, v210, v211
v_cvt_pk_bf16_f32 v217, v212, v213
buffer_store_dwordx4 v[214:217], v62, s[48:51], s44 offen offset:512 sc1
s_add_u32 s44, s44, 8192
v_accvgpr_read_b32 v192, a48
v_accvgpr_read_b32 v193, a49
v_accvgpr_read_b32 v194, a50
v_accvgpr_read_b32 v195, a51
v_accvgpr_read_b32 v196, a56
v_accvgpr_read_b32 v197, a57
v_accvgpr_read_b32 v198, a58
v_accvgpr_read_b32 v199, a59
v_pk_mul_f32 v[192:193], v[192:193], v[222:223]
v_pk_mul_f32 v[194:195], v[194:195], v[222:223]
v_pk_mul_f32 v[196:197], v[196:197], v[222:223]
v_pk_mul_f32 v[198:199], v[198:199], v[222:223]
v_cvt_pk_bf16_f32 v200, v192, v193
v_cvt_pk_bf16_f32 v201, v194, v195
v_cvt_pk_bf16_f32 v202, v196, v197
v_cvt_pk_bf16_f32 v203, v198, v199
buffer_store_dwordx4 v[200:203], v62, s[48:51], s44 offen offset:0 sc1
v_accvgpr_read_b32 v206, a52
v_accvgpr_read_b32 v207, a53
v_accvgpr_read_b32 v208, a54
v_accvgpr_read_b32 v209, a55
v_accvgpr_read_b32 v210, a60
v_accvgpr_read_b32 v211, a61
v_accvgpr_read_b32 v212, a62
v_accvgpr_read_b32 v213, a63
v_pk_mul_f32 v[206:207], v[206:207], v[222:223]
v_pk_mul_f32 v[208:209], v[208:209], v[222:223]
v_pk_mul_f32 v[210:211], v[210:211], v[222:223]
v_pk_mul_f32 v[212:213], v[212:213], v[222:223]
v_cvt_pk_bf16_f32 v214, v206, v207
v_cvt_pk_bf16_f32 v215, v208, v209
v_cvt_pk_bf16_f32 v216, v210, v211
v_cvt_pk_bf16_f32 v217, v212, v213
buffer_store_dwordx4 v[214:217], v62, s[48:51], s44 offen offset:512 sc1
s_add_u32 s44, s44, 8192
v_accvgpr_read_b32 v192, a80
v_accvgpr_read_b32 v193, a81
v_accvgpr_read_b32 v194, a82
v_accvgpr_read_b32 v195, a83
v_accvgpr_read_b32 v196, a88
v_accvgpr_read_b32 v197, a89
v_accvgpr_read_b32 v198, a90
v_accvgpr_read_b32 v199, a91
v_pk_mul_f32 v[192:193], v[192:193], v[222:223]
v_pk_mul_f32 v[194:195], v[194:195], v[222:223]
v_pk_mul_f32 v[196:197], v[196:197], v[222:223]
v_pk_mul_f32 v[198:199], v[198:199], v[222:223]
v_cvt_pk_bf16_f32 v200, v192, v193
v_cvt_pk_bf16_f32 v201, v194, v195
v_cvt_pk_bf16_f32 v202, v196, v197
v_cvt_pk_bf16_f32 v203, v198, v199
buffer_store_dwordx4 v[200:203], v62, s[48:51], s44 offen offset:0 sc1
v_accvgpr_read_b32 v206, a84
v_accvgpr_read_b32 v207, a85
v_accvgpr_read_b32 v208, a86
v_accvgpr_read_b32 v209, a87
v_accvgpr_read_b32 v210, a92
v_accvgpr_read_b32 v211, a93
v_accvgpr_read_b32 v212, a94
v_accvgpr_read_b32 v213, a95
v_pk_mul_f32 v[206:207], v[206:207], v[222:223]
v_pk_mul_f32 v[208:209], v[208:209], v[222:223]
v_pk_mul_f32 v[210:211], v[210:211], v[222:223]
v_pk_mul_f32 v[212:213], v[212:213], v[222:223]
v_cvt_pk_bf16_f32 v214, v206, v207
v_cvt_pk_bf16_f32 v215, v208, v209
v_cvt_pk_bf16_f32 v216, v210, v211
v_cvt_pk_bf16_f32 v217, v212, v213
buffer_store_dwordx4 v[214:217], v62, s[48:51], s44 offen offset:512 sc1
s_add_u32 s44, s44, 8192
v_accvgpr_read_b32 v192, a112
v_accvgpr_read_b32 v193, a113
v_accvgpr_read_b32 v194, a114
v_accvgpr_read_b32 v195, a115
v_accvgpr_read_b32 v196, a120
v_accvgpr_read_b32 v197, a121
v_accvgpr_read_b32 v198, a122
v_accvgpr_read_b32 v199, a123
v_pk_mul_f32 v[192:193], v[192:193], v[222:223]
v_pk_mul_f32 v[194:195], v[194:195], v[222:223]
v_pk_mul_f32 v[196:197], v[196:197], v[222:223]
v_pk_mul_f32 v[198:199], v[198:199], v[222:223]
v_cvt_pk_bf16_f32 v200, v192, v193
v_cvt_pk_bf16_f32 v201, v194, v195
v_cvt_pk_bf16_f32 v202, v196, v197
v_cvt_pk_bf16_f32 v203, v198, v199
buffer_store_dwordx4 v[200:203], v62, s[48:51], s44 offen offset:0 sc1
v_accvgpr_read_b32 v206, a116
v_accvgpr_read_b32 v207, a117
v_accvgpr_read_b32 v208, a118
v_accvgpr_read_b32 v209, a119
v_accvgpr_read_b32 v210, a124
v_accvgpr_read_b32 v211, a125
v_accvgpr_read_b32 v212, a126
v_accvgpr_read_b32 v213, a127
v_pk_mul_f32 v[206:207], v[206:207], v[222:223]
v_pk_mul_f32 v[208:209], v[208:209], v[222:223]
v_pk_mul_f32 v[210:211], v[210:211], v[222:223]
v_pk_mul_f32 v[212:213], v[212:213], v[222:223]
v_cvt_pk_bf16_f32 v214, v206, v207
v_cvt_pk_bf16_f32 v215, v208, v209
v_cvt_pk_bf16_f32 v216, v210, v211
v_cvt_pk_bf16_f32 v217, v212, v213
buffer_store_dwordx4 v[214:217], v62, s[48:51], s44 offen offset:512 sc1
s_and_b32 s44, s43, 7
s_lshr_b32 s44, s44, 2
s_lshl_b32 s44, s44, 3
s_bfe_u32 s45, s43, 0x30003
s_add_u32 s44, s44, s45
s_sub_u32 s45, 15, s44
s_min_u32 s44, s44, s45
s_lshl_b32 s44, s44, 7
s_add_u32 s44, s44, 67913728
v_and_b32 v29, 31, v8
v_lshlrev_b32 v29, 2, v29
v_add_u32 v29, s44, v29
s_waitcnt lgkmcnt(0)
v_mfma_f32_32x32x16_bf16 v[96:111], a[224:227], a[128:131], v[160:175]
ds_read_b128 a[192:195], v49 offset:8192
v_exp_f32 v78, v78
v_add_f32 v52, v52, v77
v_cvt_pk_bf16_f32 v134, v76, v77
s_cmp_eq_u32 s82, 163840
s_cselect_b32 s82, 0, s82
v_mfma_f32_32x32x16_bf16 v[96:111], a[228:231], a[132:135], v[96:111]
ds_read_b128 a[196:199], v49 offset:9216
v_exp_f32 v79, v79
v_add_f32 v52, v52, v78
v_mfma_f32_32x32x16_bf16 v[96:111], a[232:235], a[136:139], v[96:111]
ds_read_b128 a[200:203], v49 offset:10240
v_add_f32 v52, v52, v79
v_cvt_pk_bf16_f32 v135, v78, v79
v_mfma_f32_32x32x16_bf16 v[96:111], a[236:239], a[140:143], v[96:111]
ds_read_b128 a[204:207], v49 offset:11264
v_mfma_f32_32x32x16_bf16 v[96:111], a[240:243], a[144:147], v[96:111]
ds_read_b128 a[208:211], v49 offset:12288
s_add_u32 s83, s83, 16384
v_mfma_f32_32x32x16_bf16 v[96:111], a[244:247], a[148:151], v[96:111]
ds_read_b128 a[212:215], v49 offset:13312
v_mfma_f32_32x32x16_bf16 v[96:111], a[248:251], a[152:155], v[96:111]
ds_read_b128 a[216:219], v49 offset:14336
s_cmp_eq_u32 s83, 163840
s_cselect_b32 s83, 0, s83
v_mfma_f32_32x32x16_bf16 v[96:111], a[252:255], a[156:159], v[96:111]
ds_read_b128 a[220:223], v49 offset:15360
v_add_u32 v49, s83, v48
s_waitcnt lgkmcnt(4)
v_mfma_f32_32x32x16_bf16 a[0:15], a[192:195], v[128:131], a[0:15]
ds_read_b128 a[224:227], v49 offset:0
s_add_u32 m0, s81, s82
v_mfma_f32_32x32x16_bf16 a[32:47], a[196:199], v[128:131], a[32:47]
buffer_load_dwordx4 v32, s[52:55], s76 offen sc1 lds
ds_read_b128 a[228:231], v49 offset:1024
v_mfma_f32_32x32x16_bf16 a[64:79], a[200:203], v[128:131], a[64:79]
ds_read_b128 a[232:235], v49 offset:2048
s_nop 0
v_exp_f32 v96, v96
v_mfma_f32_32x32x16_bf16 a[96:111], a[204:207], v[128:131], a[96:111]
ds_read_b128 a[236:239], v49 offset:3072
v_exp_f32 v97, v97
v_add_f32 v52, v52, v96
s_waitcnt lgkmcnt(4)
v_mfma_f32_32x32x16_bf16 a[0:15], a[208:211], v[132:135], a[0:15]
ds_read_b128 a[240:243], v49 offset:4096
v_exp_f32 v98, v98
v_add_f32 v52, v52, v97
v_cvt_pk_bf16_f32 v144, v96, v97
v_mfma_f32_32x32x16_bf16 a[32:47], a[212:215], v[132:135], a[32:47]
buffer_load_dwordx4 v32, s[52:55], s76 offen offset:1024 sc1 lds
ds_read_b128 a[244:247], v49 offset:5120
v_exp_f32 v99, v99
v_add_f32 v52, v52, v98
v_mfma_f32_32x32x16_bf16 a[64:79], a[216:219], v[132:135], a[64:79]
ds_read_b128 a[248:251], v49 offset:6144
v_exp_f32 v100, v100
v_add_f32 v52, v52, v99
v_cvt_pk_bf16_f32 v145, v98, v99
v_mfma_f32_32x32x16_bf16 a[96:111], a[220:223], v[132:135], a[96:111]
ds_read_b128 a[252:255], v49 offset:7168
v_exp_f32 v101, v101
v_add_f32 v52, v52, v100
s_waitcnt lgkmcnt(4)
v_mfma_f32_32x32x16_bf16 v[64:79], a[224:227], a[128:131], v[160:175]
ds_read_b128 a[192:195], v49 offset:8192
ds_read_b128 a[196:199], v49 offset:9216
v_exp_f32 v102, v102
v_add_f32 v52, v52, v101
v_cvt_pk_bf16_f32 v146, v100, v101
v_mfma_f32_32x32x16_bf16 v[64:79], a[228:231], a[132:135], v[64:79]
buffer_load_dwordx4 v32, s[52:55], s76 offen offset:2048 sc1 lds
ds_read_b128 a[200:203], v49 offset:10240
ds_read_b128 a[204:207], v49 offset:11264
v_exp_f32 v103, v103
v_add_f32 v52, v52, v102
s_add_u32 s83, s83, 16384
v_mfma_f32_32x32x16_bf16 v[64:79], a[232:235], a[136:139], v[64:79]
ds_read_b128 a[208:211], v49 offset:12288
ds_read_b128 a[212:215], v49 offset:13312
v_exp_f32 v104, v104
v_add_f32 v52, v52, v103
v_cvt_pk_bf16_f32 v147, v102, v103
s_cmp_eq_u32 s83, 163840
s_cselect_b32 s83, 0, s83
v_mfma_f32_32x32x16_bf16 v[64:79], a[236:239], a[140:143], v[64:79]
buffer_load_dwordx4 v32, s[52:55], s76 offen offset:3072 sc1 lds
ds_read_b128 a[216:219], v49 offset:14336
ds_read_b128 a[220:223], v49 offset:15360
v_exp_f32 v105, v105
v_add_f32 v52, v52, v104
v_add_u32 v49, s83, v48
s_waitcnt lgkmcnt(8)
v_mfma_f32_32x32x16_bf16 v[64:79], a[240:243], a[144:147], v[64:79]
v_exp_f32 v106, v106
v_add_f32 v52, v52, v105
v_cvt_pk_bf16_f32 v148, v104, v105
s_add_u32 s76, s76, 8192
s_add_u32 s82, s82, 16384
v_mfma_f32_32x32x16_bf16 v[64:79], a[244:247], a[148:151], v[64:79]
v_exp_f32 v107, v107
v_add_f32 v52, v52, v106
s_cmp_eq_u32 s82, 163840
s_cselect_b32 s82, 0, s82
v_mfma_f32_32x32x16_bf16 v[64:79], a[248:251], a[152:155], v[64:79]
v_exp_f32 v108, v108
v_add_f32 v52, v52, v107
v_cvt_pk_bf16_f32 v149, v106, v107
v_mfma_f32_32x32x16_bf16 v[64:79], a[252:255], a[156:159], v[64:79]
v_exp_f32 v109, v109
v_add_f32 v52, v52, v108
s_cmp_eq_u32 s90, 0
s_cbranch_scc1 L_hm0%=
s_waitcnt vmcnt(16) lgkmcnt(0)
s_barrier
v_mfma_f32_32x32x16_bf16 a[0:15], a[192:195], v[144:147], a[0:15]
ds_read_b128 a[224:227], v49 offset:0
v_exp_f32 v110, v110
v_add_f32 v52, v52, v109
v_cvt_pk_bf16_f32 v150, v108, v109
s_add_u32 m0, s81, s82
v_mfma_f32_32x32x16_bf16 a[32:47], a[196:199], v[144:147], a[32:47]
buffer_load_dwordx4 v32, s[52:55], s76 offen sc1 lds
ds_read_b128 a[228:231], v49 offset:1024
v_exp_f32 v111, v111
v_add_f32 v52, v52, v110
v_mfma_f32_32x32x16_bf16 a[64:79], a[200:203], v[144:147], a[64:79]
ds_read_b128 a[232:235], v49 offset:2048
v_exp_f32 v64, v64
v_add_f32 v52, v52, v111
v_cvt_pk_bf16_f32 v151, v110, v111
v_mfma_f32_32x32x16_bf16 a[96:111], a[204:207], v[144:147], a[96:111]
ds_read_b128 a[236:239], v49 offset:3072
v_exp_f32 v65, v65
v_add_f32 v52, v52, v64
v_mfma_f32_32x32x16_bf16 a[0:15], a[208:211], v[148:151], a[0:15]
ds_read_b128 a[240:243], v49 offset:4096
v_exp_f32 v66, v66
v_add_f32 v52, v52, v65
v_cvt_pk_bf16_f32 v128, v64, v65
v_mfma_f32_32x32x16_bf16 a[32:47], a[212:215], v[148:151], a[32:47]
buffer_load_dwordx4 v32, s[52:55], s76 offen offset:1024 sc1 lds
ds_read_b128 a[244:247], v49 offset:5120
v_exp_f32 v67, v67
v_add_f32 v52, v52, v66
v_mfma_f32_32x32x16_bf16 a[64:79], a[216:219], v[148:151], a[64:79]
ds_read_b128 a[248:251], v49 offset:6144
v_exp_f32 v68, v68
v_add_f32 v52, v52, v67
v_cvt_pk_bf16_f32 v129, v66, v67
v_mfma_f32_32x32x16_bf16 a[96:111], a[220:223], v[148:151], a[96:111]
ds_read_b128 a[252:255], v49 offset:7168
v_exp_f32 v69, v69
v_add_f32 v52, v52, v68
s_waitcnt lgkmcnt(4)
v_mfma_f32_32x32x16_bf16 v[96:111], a[224:227], a[128:131], v[160:175]
ds_read_b128 a[192:195], v49 offset:8192
v_exp_f32 v70, v70
v_add_f32 v52, v52, v69
v_cvt_pk_bf16_f32 v130, v68, v69
v_mfma_f32_32x32x16_bf16 v[96:111], a[228:231], a[132:135], v[96:111]
buffer_load_dwordx4 v32, s[52:55], s76 offen offset:2048 sc1 lds
ds_read_b128 a[196:199], v49 offset:9216
v_exp_f32 v71, v71
v_add_f32 v52, v52, v70
v_mfma_f32_32x32x16_bf16 v[96:111], a[232:235], a[136:139], v[96:111]
ds_read_b128 a[200:203], v49 offset:10240
v_exp_f32 v72, v72
v_add_f32 v52, v52, v71
v_cvt_pk_bf16_f32 v131, v70, v71
v_mfma_f32_32x32x16_bf16 v[96:111], a[236:239], a[140:143], v[96:111]
ds_read_b128 a[204:207], v49 offset:11264
v_exp_f32 v73, v73
v_add_f32 v52, v52, v72
s_waitcnt lgkmcnt(4)
v_mfma_f32_32x32x16_bf16 v[96:111], a[240:243], a[144:147], v[96:111]
ds_read_b128 a[208:211], v49 offset:12288
v_exp_f32 v74, v74
v_add_f32 v52, v52, v73
v_cvt_pk_bf16_f32 v132, v72, v73
s_add_u32 s83, s83, 16384
v_mfma_f32_32x32x16_bf16 v[96:111], a[244:247], a[148:151], v[96:111]
buffer_load_dwordx4 v32, s[52:55], s76 offen offset:3072 sc1 lds
ds_read_b128 a[212:215], v49 offset:13312
v_exp_f32 v75, v75
v_add_f32 v52, v52, v74
v_mfma_f32_32x32x16_bf16 v[96:111], a[248:251], a[152:155], v[96:111]
ds_read_b128 a[216:219], v49 offset:14336
v_exp_f32 v76, v76
v_add_f32 v52, v52, v75
v_cvt_pk_bf16_f32 v133, v74, v75
s_cmp_eq_u32 s83, 163840
s_cselect_b32 s83, 0, s83
s_add_u32 s76, s76, 8192
s_add_u32 s82, s82, 16384
v_mfma_f32_32x32x16_bf16 v[96:111], a[252:255], a[156:159], v[96:111]
ds_read_b128 a[220:223], v49 offset:15360
v_exp_f32 v77, v77
v_add_f32 v52, v52, v76
v_add_u32 v49, s83, v48
s_cmp_eq_u32 s82, 163840
s_cselect_b32 s82, 0, s82
s_waitcnt lgkmcnt(4)
v_mfma_f32_32x32x16_bf16 a[0:15], a[192:195], v[128:131], a[0:15]
ds_read_b128 a[224:227], v49 offset:0
v_exp_f32 v78, v78
v_add_f32 v52, v52, v77
v_cvt_pk_bf16_f32 v134, v76, v77
s_add_u32 m0, s81, s82
v_mfma_f32_32x32x16_bf16 a[32:47], a[196:199], v[128:131], a[32:47]
buffer_load_dwordx4 v32, s[52:55], s76 offen sc1 lds
ds_read_b128 a[228:231], v49 offset:1024
v_exp_f32 v79, v79
v_add_f32 v52, v52, v78
v_mfma_f32_32x32x16_bf16 a[64:79], a[200:203], v[128:131], a[64:79]
ds_read_b128 a[232:235], v49 offset:2048
v_exp_f32 v96, v96
v_add_f32 v52, v52, v79
v_cvt_pk_bf16_f32 v135, v78, v79
v_mfma_f32_32x32x16_bf16 a[96:111], a[204:207], v[128:131], a[96:111]
ds_read_b128 a[236:239], v49 offset:3072
v_exp_f32 v97, v97
v_add_f32 v52, v52, v96
s_waitcnt lgkmcnt(4)
v_mfma_f32_32x32x16_bf16 a[0:15], a[208:211], v[132:135], a[0:15]
ds_read_b128 a[240:243], v49 offset:4096
v_exp_f32 v98, v98
v_add_f32 v52, v52, v97
v_cvt_pk_bf16_f32 v144, v96, v97
v_mfma_f32_32x32x16_bf16 a[32:47], a[212:215], v[132:135], a[32:47]
buffer_load_dwordx4 v32, s[52:55], s76 offen offset:1024 sc1 lds
ds_read_b128 a[244:247], v49 offset:5120
v_exp_f32 v99, v99
v_add_f32 v52, v52, v98
v_mfma_f32_32x32x16_bf16 a[64:79], a[216:219], v[132:135], a[64:79]
ds_read_b128 a[248:251], v49 offset:6144
v_exp_f32 v100, v100
v_add_f32 v52, v52, v99
v_cvt_pk_bf16_f32 v145, v98, v99
v_mfma_f32_32x32x16_bf16 a[96:111], a[220:223], v[132:135], a[96:111]
ds_read_b128 a[252:255], v49 offset:7168
v_exp_f32 v101, v101
v_add_f32 v52, v52, v100
s_waitcnt lgkmcnt(4)
v_mfma_f32_32x32x16_bf16 v[64:79], a[224:227], a[128:131], v[160:175]
ds_read_b128 a[192:195], v49 offset:8192
ds_read_b128 a[196:199], v49 offset:9216
v_exp_f32 v102, v102
v_add_f32 v52, v52, v101
v_cvt_pk_bf16_f32 v146, v100, v101
v_mfma_f32_32x32x16_bf16 v[64:79], a[228:231], a[132:135], v[64:79]
buffer_load_dwordx4 v32, s[52:55], s76 offen offset:2048 sc1 lds
ds_read_b128 a[200:203], v49 offset:10240
ds_read_b128 a[204:207], v49 offset:11264
v_exp_f32 v103, v103
v_add_f32 v52, v52, v102
s_add_u32 s83, s83, 16384
v_mfma_f32_32x32x16_bf16 v[64:79], a[232:235], a[136:139], v[64:79]
ds_read_b128 a[208:211], v49 offset:12288
ds_read_b128 a[212:215], v49 offset:13312
v_exp_f32 v104, v104
v_add_f32 v52, v52, v103
v_cvt_pk_bf16_f32 v147, v102, v103
s_cmp_eq_u32 s83, 163840
s_cselect_b32 s83, 0, s83
v_mfma_f32_32x32x16_bf16 v[64:79], a[236:239], a[140:143], v[64:79]
buffer_load_dwordx4 v32, s[52:55], s76 offen offset:3072 sc1 lds
ds_read_b128 a[216:219], v49 offset:14336
ds_read_b128 a[220:223], v49 offset:15360
v_exp_f32 v105, v105
v_add_f32 v52, v52, v104
v_add_u32 v49, s83, v48
s_waitcnt lgkmcnt(8)
v_mfma_f32_32x32x16_bf16 v[64:79], a[240:243], a[144:147], v[64:79]
v_exp_f32 v106, v106
v_add_f32 v52, v52, v105
v_cvt_pk_bf16_f32 v148, v104, v105
s_add_u32 s76, s76, 8192
s_add_u32 s82, s82, 16384
v_mfma_f32_32x32x16_bf16 v[64:79], a[244:247], a[148:151], v[64:79]
v_exp_f32 v107, v107
v_add_f32 v52, v52, v106
s_cmp_eq_u32 s82, 163840
s_cselect_b32 s82, 0, s82
v_mfma_f32_32x32x16_bf16 v[64:79], a[248:251], a[152:155], v[64:79]
v_exp_f32 v108, v108
v_add_f32 v52, v52, v107
v_cvt_pk_bf16_f32 v149, v106, v107
v_mfma_f32_32x32x16_bf16 v[64:79], a[252:255], a[156:159], v[64:79]
v_exp_f32 v109, v109
v_add_f32 v52, v52, v108
s_lshr_b32 s90, s90, 1
s_cmp_eq_u32 s90, 0
s_cbranch_scc1 L_hm0%=
L_h%=:
s_waitcnt vmcnt(8) lgkmcnt(0)
s_barrier
v_mfma_f32_32x32x16_bf16 a[0:15], a[192:195], v[144:147], a[0:15]
ds_read_b128 a[224:227], v49 offset:0
v_exp_f32 v110, v110
v_add_f32 v52, v52, v109
v_cvt_pk_bf16_f32 v150, v108, v109
s_add_u32 m0, s81, s82
v_mfma_f32_32x32x16_bf16 a[32:47], a[196:199], v[144:147], a[32:47]
buffer_load_dwordx4 v32, s[52:55], s76 offen sc1 lds
ds_read_b128 a[228:231], v49 offset:1024
v_exp_f32 v111, v111
v_add_f32 v52, v52, v110
v_mfma_f32_32x32x16_bf16 a[64:79], a[200:203], v[144:147], a[64:79]
ds_read_b128 a[232:235], v49 offset:2048
v_exp_f32 v64, v64
v_add_f32 v52, v52, v111
v_cvt_pk_bf16_f32 v151, v110, v111
v_mfma_f32_32x32x16_bf16 a[96:111], a[204:207], v[144:147], a[96:111]
ds_read_b128 a[236:239], v49 offset:3072
v_exp_f32 v65, v65
v_add_f32 v52, v52, v64
v_mfma_f32_32x32x16_bf16 a[0:15], a[208:211], v[148:151], a[0:15]
ds_read_b128 a[240:243], v49 offset:4096
v_exp_f32 v66, v66
v_add_f32 v52, v52, v65
v_cvt_pk_bf16_f32 v128, v64, v65
v_mfma_f32_32x32x16_bf16 a[32:47], a[212:215], v[148:151], a[32:47]
buffer_load_dwordx4 v32, s[52:55], s76 offen offset:1024 sc1 lds
ds_read_b128 a[244:247], v49 offset:5120
v_exp_f32 v67, v67
v_add_f32 v52, v52, v66
v_mfma_f32_32x32x16_bf16 a[64:79], a[216:219], v[148:151], a[64:79]
ds_read_b128 a[248:251], v49 offset:6144
v_exp_f32 v68, v68
v_add_f32 v52, v52, v67
v_cvt_pk_bf16_f32 v129, v66, v67
v_mfma_f32_32x32x16_bf16 a[96:111], a[220:223], v[148:151], a[96:111]
ds_read_b128 a[252:255], v49 offset:7168
v_exp_f32 v69, v69
v_add_f32 v52, v52, v68
s_waitcnt lgkmcnt(4)
v_mfma_f32_32x32x16_bf16 v[96:111], a[224:227], a[128:131], v[160:175]
ds_read_b128 a[192:195], v49 offset:8192
v_exp_f32 v70, v70
v_add_f32 v52, v52, v69
v_cvt_pk_bf16_f32 v130, v68, v69
v_mfma_f32_32x32x16_bf16 v[96:111], a[228:231], a[132:135], v[96:111]
buffer_load_dwordx4 v32, s[52:55], s76 offen offset:2048 sc1 lds
ds_read_b128 a[196:199], v49 offset:9216
v_exp_f32 v71, v71
v_add_f32 v52, v52, v70
v_mfma_f32_32x32x16_bf16 v[96:111], a[232:235], a[136:139], v[96:111]
ds_read_b128 a[200:203], v49 offset:10240
v_exp_f32 v72, v72
v_add_f32 v52, v52, v71
v_cvt_pk_bf16_f32 v131, v70, v71
v_mfma_f32_32x32x16_bf16 v[96:111], a[236:239], a[140:143], v[96:111]
ds_read_b128 a[204:207], v49 offset:11264
v_exp_f32 v73, v73
v_add_f32 v52, v52, v72
s_waitcnt lgkmcnt(4)
v_mfma_f32_32x32x16_bf16 v[96:111], a[240:243], a[144:147], v[96:111]
ds_read_b128 a[208:211], v49 offset:12288
v_exp_f32 v74, v74
v_add_f32 v52, v52, v73
v_cvt_pk_bf16_f32 v132, v72, v73
s_add_u32 s83, s83, 16384
v_mfma_f32_32x32x16_bf16 v[96:111], a[244:247], a[148:151], v[96:111]
buffer_load_dwordx4 v32, s[52:55], s76 offen offset:3072 sc1 lds
ds_read_b128 a[212:215], v49 offset:13312
v_exp_f32 v75, v75
v_add_f32 v52, v52, v74
v_mfma_f32_32x32x16_bf16 v[96:111], a[248:251], a[152:155], v[96:111]
ds_read_b128 a[216:219], v49 offset:14336
v_exp_f32 v76, v76
v_add_f32 v52, v52, v75
v_cvt_pk_bf16_f32 v133, v74, v75
s_cmp_eq_u32 s83, 163840
s_cselect_b32 s83, 0, s83
s_add_u32 s76, s76, 8192
s_add_u32 s82, s82, 16384
v_mfma_f32_32x32x16_bf16 v[96:111], a[252:255], a[156:159], v[96:111]
ds_read_b128 a[220:223], v49 offset:15360
v_exp_f32 v77, v77
v_add_f32 v52, v52, v76
v_add_u32 v49, s83, v48
s_cmp_eq_u32 s82, 163840
s_cselect_b32 s82, 0, s82
s_waitcnt lgkmcnt(4)
v_mfma_f32_32x32x16_bf16 a[0:15], a[192:195], v[128:131], a[0:15]
ds_read_b128 a[224:227], v49 offset:0
v_exp_f32 v78, v78
v_add_f32 v52, v52, v77
v_cvt_pk_bf16_f32 v134, v76, v77
s_add_u32 m0, s81, s82
v_mfma_f32_32x32x16_bf16 a[32:47], a[196:199], v[128:131], a[32:47]
buffer_load_dwordx4 v32, s[52:55], s76 offen sc1 lds
ds_read_b128 a[228:231], v49 offset:1024
v_exp_f32 v79, v79
v_add_f32 v52, v52, v78
v_mfma_f32_32x32x16_bf16 a[64:79], a[200:203], v[128:131], a[64:79]
ds_read_b128 a[232:235], v49 offset:2048
v_exp_f32 v96, v96
v_add_f32 v52, v52, v79
v_cvt_pk_bf16_f32 v135, v78, v79
v_mfma_f32_32x32x16_bf16 a[96:111], a[204:207], v[128:131], a[96:111]
ds_read_b128 a[236:239], v49 offset:3072
v_exp_f32 v97, v97
v_add_f32 v52, v52, v96
s_waitcnt lgkmcnt(4)
v_mfma_f32_32x32x16_bf16 a[0:15], a[208:211], v[132:135], a[0:15]
ds_read_b128 a[240:243], v49 offset:4096
v_exp_f32 v98, v98
v_add_f32 v52, v52, v97
v_cvt_pk_bf16_f32 v144, v96, v97
v_mfma_f32_32x32x16_bf16 a[32:47], a[212:215], v[132:135], a[32:47]
buffer_load_dwordx4 v32, s[52:55], s76 offen offset:1024 sc1 lds
ds_read_b128 a[244:247], v49 offset:5120
v_exp_f32 v99, v99
v_add_f32 v52, v52, v98
v_mfma_f32_32x32x16_bf16 a[64:79], a[216:219], v[132:135], a[64:79]
ds_read_b128 a[248:251], v49 offset:6144
v_exp_f32 v100, v100
v_add_f32 v52, v52, v99
v_cvt_pk_bf16_f32 v145, v98, v99
v_mfma_f32_32x32x16_bf16 a[96:111], a[220:223], v[132:135], a[96:111]
ds_read_b128 a[252:255], v49 offset:7168
v_exp_f32 v101, v101
v_add_f32 v52, v52, v100
s_waitcnt lgkmcnt(4)
v_mfma_f32_32x32x16_bf16 v[64:79], a[224:227], a[128:131], v[160:175]
ds_read_b128 a[192:195], v49 offset:8192
ds_read_b128 a[196:199], v49 offset:9216
v_exp_f32 v102, v102
v_add_f32 v52, v52, v101
v_cvt_pk_bf16_f32 v146, v100, v101
v_mfma_f32_32x32x16_bf16 v[64:79], a[228:231], a[132:135], v[64:79]
buffer_load_dwordx4 v32, s[52:55], s76 offen offset:2048 sc1 lds
ds_read_b128 a[200:203], v49 offset:10240
ds_read_b128 a[204:207], v49 offset:11264
v_exp_f32 v103, v103
v_add_f32 v52, v52, v102
s_add_u32 s83, s83, 16384
v_mfma_f32_32x32x16_bf16 v[64:79], a[232:235], a[136:139], v[64:79]
ds_read_b128 a[208:211], v49 offset:12288
ds_read_b128 a[212:215], v49 offset:13312
v_exp_f32 v104, v104
v_add_f32 v52, v52, v103
v_cvt_pk_bf16_f32 v147, v102, v103
s_cmp_eq_u32 s83, 163840
s_cselect_b32 s83, 0, s83
v_mfma_f32_32x32x16_bf16 v[64:79], a[236:239], a[140:143], v[64:79]
buffer_load_dwordx4 v32, s[52:55], s76 offen offset:3072 sc1 lds
ds_read_b128 a[216:219], v49 offset:14336
ds_read_b128 a[220:223], v49 offset:15360
v_exp_f32 v105, v105
v_add_f32 v52, v52, v104
v_add_u32 v49, s83, v48
s_waitcnt lgkmcnt(8)
v_mfma_f32_32x32x16_bf16 v[64:79], a[240:243], a[144:147], v[64:79]
v_exp_f32 v106, v106
v_add_f32 v52, v52, v105
v_cvt_pk_bf16_f32 v148, v104, v105
s_add_u32 s76, s76, 8192
s_add_u32 s82, s82, 16384
v_mfma_f32_32x32x16_bf16 v[64:79], a[244:247], a[148:151], v[64:79]
v_exp_f32 v107, v107
v_add_f32 v52, v52, v106
s_cmp_eq_u32 s82, 163840
s_cselect_b32 s82, 0, s82
v_mfma_f32_32x32x16_bf16 v[64:79], a[248:251], a[152:155], v[64:79]
v_exp_f32 v108, v108
v_add_f32 v52, v52, v107
v_cvt_pk_bf16_f32 v149, v106, v107
v_mfma_f32_32x32x16_bf16 v[64:79], a[252:255], a[156:159], v[64:79]
v_exp_f32 v109, v109
v_add_f32 v52, v52, v108
s_waitcnt lgkmcnt(0)
v_mfma_f32_32x32x16_bf16 a[0:15], a[192:195], v[144:147], a[0:15]
ds_read_b128 a[224:227], v49 offset:0
v_exp_f32 v110, v110
v_add_f32 v52, v52, v109
v_cvt_pk_bf16_f32 v150, v108, v109
s_add_u32 m0, s81, s82
v_mfma_f32_32x32x16_bf16 a[32:47], a[196:199], v[144:147], a[32:47]
buffer_load_dwordx4 v32, s[52:55], s76 offen sc1 lds
ds_read_b128 a[228:231], v49 offset:1024
v_exp_f32 v111, v111
v_add_f32 v52, v52, v110
v_mfma_f32_32x32x16_bf16 a[64:79], a[200:203], v[144:147], a[64:79]
ds_read_b128 a[232:235], v49 offset:2048
v_exp_f32 v64, v64
v_add_f32 v52, v52, v111
v_cvt_pk_bf16_f32 v151, v110, v111
v_mfma_f32_32x32x16_bf16 a[96:111], a[204:207], v[144:147], a[96:111]
ds_read_b128 a[236:239], v49 offset:3072
v_exp_f32 v65, v65
v_add_f32 v52, v52, v64
v_mfma_f32_32x32x16_bf16 a[0:15], a[208:211], v[148:151], a[0:15]
ds_read_b128 a[240:243], v49 offset:4096
v_exp_f32 v66, v66
v_add_f32 v52, v52, v65
v_cvt_pk_bf16_f32 v128, v64, v65
v_mfma_f32_32x32x16_bf16 a[32:47], a[212:215], v[148:151], a[32:47]
buffer_load_dwordx4 v32, s[52:55], s76 offen offset:1024 sc1 lds
ds_read_b128 a[244:247], v49 offset:5120
v_exp_f32 v67, v67
v_add_f32 v52, v52, v66
v_mfma_f32_32x32x16_bf16 a[64:79], a[216:219], v[148:151], a[64:79]
ds_read_b128 a[248:251], v49 offset:6144
v_exp_f32 v68, v68
v_add_f32 v52, v52, v67
v_cvt_pk_bf16_f32 v129, v66, v67
v_mfma_f32_32x32x16_bf16 a[96:111], a[220:223], v[148:151], a[96:111]
ds_read_b128 a[252:255], v49 offset:7168
v_exp_f32 v69, v69
v_add_f32 v52, v52, v68
s_waitcnt lgkmcnt(4)
v_mfma_f32_32x32x16_bf16 v[96:111], a[224:227], a[128:131], v[160:175]
ds_read_b128 a[192:195], v49 offset:8192
v_exp_f32 v70, v70
v_add_f32 v52, v52, v69
v_cvt_pk_bf16_f32 v130, v68, v69
v_mfma_f32_32x32x16_bf16 v[96:111], a[228:231], a[132:135], v[96:111]
buffer_load_dwordx4 v32, s[52:55], s76 offen offset:2048 sc1 lds
ds_read_b128 a[196:199], v49 offset:9216
v_exp_f32 v71, v71
v_add_f32 v52, v52, v70
v_mfma_f32_32x32x16_bf16 v[96:111], a[232:235], a[136:139], v[96:111]
ds_read_b128 a[200:203], v49 offset:10240
v_exp_f32 v72, v72
v_add_f32 v52, v52, v71
v_cvt_pk_bf16_f32 v131, v70, v71
v_mfma_f32_32x32x16_bf16 v[96:111], a[236:239], a[140:143], v[96:111]
ds_read_b128 a[204:207], v49 offset:11264
v_exp_f32 v73, v73
v_add_f32 v52, v52, v72
s_waitcnt lgkmcnt(4)
v_mfma_f32_32x32x16_bf16 v[96:111], a[240:243], a[144:147], v[96:111]
ds_read_b128 a[208:211], v49 offset:12288
v_exp_f32 v74, v74
v_add_f32 v52, v52, v73
v_cvt_pk_bf16_f32 v132, v72, v73
s_add_u32 s83, s83, 16384
v_mfma_f32_32x32x16_bf16 v[96:111], a[244:247], a[148:151], v[96:111]
buffer_load_dwordx4 v32, s[52:55], s76 offen offset:3072 sc1 lds
ds_read_b128 a[212:215], v49 offset:13312
v_exp_f32 v75, v75
v_add_f32 v52, v52, v74
v_mfma_f32_32x32x16_bf16 v[96:111], a[248:251], a[152:155], v[96:111]
ds_read_b128 a[216:219], v49 offset:14336
v_exp_f32 v76, v76
v_add_f32 v52, v52, v75
v_cvt_pk_bf16_f32 v133, v74, v75
s_cmp_eq_u32 s83, 163840
s_cselect_b32 s83, 0, s83
s_add_u32 s76, s76, 8192
s_add_u32 s82, s82, 16384
v_mfma_f32_32x32x16_bf16 v[96:111], a[252:255], a[156:159], v[96:111]
ds_read_b128 a[220:223], v49 offset:15360
v_exp_f32 v77, v77
v_add_f32 v52, v52, v76
v_add_u32 v49, s83, v48
s_cmp_eq_u32 s82, 163840
s_cselect_b32 s82, 0, s82
s_waitcnt lgkmcnt(4)
v_mfma_f32_32x32x16_bf16 a[0:15], a[192:195], v[128:131], a[0:15]
ds_read_b128 a[224:227], v49 offset:0
v_exp_f32 v78, v78
v_add_f32 v52, v52, v77
v_cvt_pk_bf16_f32 v134, v76, v77
s_add_u32 m0, s81, s82
v_mfma_f32_32x32x16_bf16 a[32:47], a[196:199], v[128:131], a[32:47]
buffer_load_dwordx4 v32, s[52:55], s76 offen sc1 lds
ds_read_b128 a[228:231], v49 offset:1024
v_exp_f32 v79, v79
v_add_f32 v52, v52, v78
v_mfma_f32_32x32x16_bf16 a[64:79], a[200:203], v[128:131], a[64:79]
ds_read_b128 a[232:235], v49 offset:2048
v_exp_f32 v96, v96
v_add_f32 v52, v52, v79
v_cvt_pk_bf16_f32 v135, v78, v79
v_mfma_f32_32x32x16_bf16 a[96:111], a[204:207], v[128:131], a[96:111]
ds_read_b128 a[236:239], v49 offset:3072
v_exp_f32 v97, v97
v_add_f32 v52, v52, v96
s_waitcnt lgkmcnt(4)
v_mfma_f32_32x32x16_bf16 a[0:15], a[208:211], v[132:135], a[0:15]
ds_read_b128 a[240:243], v49 offset:4096
v_exp_f32 v98, v98
v_add_f32 v52, v52, v97
v_cvt_pk_bf16_f32 v144, v96, v97
v_mfma_f32_32x32x16_bf16 a[32:47], a[212:215], v[132:135], a[32:47]
buffer_load_dwordx4 v32, s[52:55], s76 offen offset:1024 sc1 lds
ds_read_b128 a[244:247], v49 offset:5120
v_exp_f32 v99, v99
v_add_f32 v52, v52, v98
v_mfma_f32_32x32x16_bf16 a[64:79], a[216:219], v[132:135], a[64:79]
ds_read_b128 a[248:251], v49 offset:6144
v_exp_f32 v100, v100
v_add_f32 v52, v52, v99
v_cvt_pk_bf16_f32 v145, v98, v99
v_mfma_f32_32x32x16_bf16 a[96:111], a[220:223], v[132:135], a[96:111]
ds_read_b128 a[252:255], v49 offset:7168
v_exp_f32 v101, v101
v_add_f32 v52, v52, v100
s_waitcnt lgkmcnt(4)
v_mfma_f32_32x32x16_bf16 v[64:79], a[224:227], a[128:131], v[160:175]
ds_read_b128 a[192:195], v49 offset:8192
ds_read_b128 a[196:199], v49 offset:9216
v_exp_f32 v102, v102
v_add_f32 v52, v52, v101
v_cvt_pk_bf16_f32 v146, v100, v101
v_mfma_f32_32x32x16_bf16 v[64:79], a[228:231], a[132:135], v[64:79]
buffer_load_dwordx4 v32, s[52:55], s76 offen offset:2048 sc1 lds
ds_read_b128 a[200:203], v49 offset:10240
ds_read_b128 a[204:207], v49 offset:11264
v_exp_f32 v103, v103
v_add_f32 v52, v52, v102
s_add_u32 s83, s83, 16384
v_mfma_f32_32x32x16_bf16 v[64:79], a[232:235], a[136:139], v[64:79]
ds_read_b128 a[208:211], v49 offset:12288
ds_read_b128 a[212:215], v49 offset:13312
v_exp_f32 v104, v104
v_add_f32 v52, v52, v103
v_cvt_pk_bf16_f32 v147, v102, v103
s_cmp_eq_u32 s83, 163840
s_cselect_b32 s83, 0, s83
s_sub_u32 s90, s90, 1
v_mfma_f32_32x32x16_bf16 v[64:79], a[236:239], a[140:143], v[64:79]
buffer_load_dwordx4 v32, s[52:55], s76 offen offset:3072 sc1 lds
ds_read_b128 a[216:219], v49 offset:14336
ds_read_b128 a[220:223], v49 offset:15360
v_exp_f32 v105, v105
v_add_f32 v52, v52, v104
v_add_u32 v49, s83, v48
s_waitcnt lgkmcnt(8)
v_mfma_f32_32x32x16_bf16 v[64:79], a[240:243], a[144:147], v[64:79]
v_exp_f32 v106, v106
v_add_f32 v52, v52, v105
v_cvt_pk_bf16_f32 v148, v104, v105
s_add_u32 s76, s76, 8192
s_add_u32 s82, s82, 16384
v_mfma_f32_32x32x16_bf16 v[64:79], a[244:247], a[148:151], v[64:79]
v_exp_f32 v107, v107
v_add_f32 v52, v52, v106
s_cmp_eq_u32 s82, 163840
s_cselect_b32 s82, 0, s82
v_mfma_f32_32x32x16_bf16 v[64:79], a[248:251], a[152:155], v[64:79]
v_exp_f32 v108, v108
v_add_f32 v52, v52, v107
v_cvt_pk_bf16_f32 v149, v106, v107
v_mfma_f32_32x32x16_bf16 v[64:79], a[252:255], a[156:159], v[64:79]
v_exp_f32 v109, v109
v_add_f32 v52, v52, v108
s_cmp_lg_u32 s90, 0
s_cbranch_scc1 L_h%=
L_hm0%=:
s_waitcnt vmcnt(16) lgkmcnt(0)
s_barrier
v_mfma_f32_32x32x16_bf16 a[0:15], a[192:195], v[144:147], a[0:15]
ds_read_b128 a[224:227], v49 offset:0
v_cmp_gt_i32_e64 s[92:93], 0, v58
v_exp_f32 v110, v110
v_add_f32 v52, v52, v109
v_cvt_pk_bf16_f32 v150, v108, v109
s_add_u32 m0, s81, s82
v_mfma_f32_32x32x16_bf16 a[32:47], a[196:199], v[144:147], a[32:47]
buffer_load_dwordx4 v32, s[52:55], s76 offen sc1 lds
buffer_load_dword v28, v29, s[48:51], 0 offen sc1
ds_read_b128 a[228:231], v49 offset:1024
v_cmp_gt_i32_e64 s[94:95], 1, v58
v_cndmask_b32_e64 v64, v64, v57, s[92:93]
v_exp_f32 v111, v111
v_add_f32 v52, v52, v110
s_and_b32 s44, s43, 7
v_mfma_f32_32x32x16_bf16 a[64:79], a[200:203], v[144:147], a[64:79]
ds_read_b128 a[232:235], v49 offset:2048
v_cmp_gt_i32_e64 s[96:97], 2, v58
v_cndmask_b32_e64 v65, v65, v57, s[94:95]
v_exp_f32 v64, v64
v_add_f32 v52, v52, v111
v_cvt_pk_bf16_f32 v151, v110, v111
s_and_b32 s100, s44, 3
v_mfma_f32_32x32x16_bf16 a[96:111], a[204:207], v[144:147], a[96:111]
ds_read_b128 a[236:239], v49 offset:3072
v_cmp_gt_i32_e64 s[98:99], 3, v58
v_cndmask_b32_e64 v66, v66, v57, s[96:97]
v_exp_f32 v65, v65
v_add_f32 v52, v52, v64
s_lshr_b32 s44, s44, 2
v_mfma_f32_32x32x16_bf16 a[0:15], a[208:211], v[148:151], a[0:15]
ds_read_b128 a[240:243], v49 offset:4096
v_cmp_gt_i32_e64 s[92:93], 16, v58
v_cndmask_b32_e64 v67, v67, v57, s[98:99]
v_exp_f32 v66, v66
v_add_f32 v52, v52, v65
v_cvt_pk_bf16_f32 v128, v64, v65
s_lshl_b32 s44, s44, 3
v_mfma_f32_32x32x16_bf16 a[32:47], a[212:215], v[148:151], a[32:47]
buffer_load_dwordx4 v32, s[52:55], s76 offen offset:1024 sc1 lds
ds_read_b128 a[244:247], v49 offset:5120
v_cmp_gt_i32_e64 s[94:95], 17, v58
v_cndmask_b32_e64 v68, v68, v57, s[92:93]
v_exp_f32 v67, v67
v_add_f32 v52, v52, v66
v_mfma_f32_32x32x16_bf16 a[64:79], a[216:219], v[148:151], a[64:79]
ds_read_b128 a[248:251], v49 offset:6144
v_cmp_gt_i32_e64 s[96:97], 18, v58
v_cndmask_b32_e64 v69, v69, v57, s[94:95]
v_exp_f32 v68, v68
v_add_f32 v52, v52, v67
v_cvt_pk_bf16_f32 v129, v66, v67
s_bfe_u32 s45, s43, 0x30003
v_mfma_f32_32x32x16_bf16 a[96:111], a[220:223], v[148:151], a[96:111]
ds_read_b128 a[252:255], v49 offset:7168
v_cmp_gt_i32_e64 s[98:99], 19, v58
v_cndmask_b32_e64 v70, v70, v57, s[96:97]
v_exp_f32 v69, v69
v_add_f32 v52, v52, v68
s_add_u32 s101, s44, s45
s_waitcnt lgkmcnt(4)
v_mfma_f32_32x32x16_bf16 v[96:111], a[224:227], a[128:131], v[160:175]
ds_read_b128 a[192:195], v49 offset:8192
v_cmp_gt_i32_e64 s[92:93], 8, v58
v_cndmask_b32_e64 v71, v71, v57, s[98:99]
v_exp_f32 v70, v70
v_add_f32 v52, v52, v69
v_cvt_pk_bf16_f32 v130, v68, v69
s_lshr_b32 s39, s43, 6
v_mfma_f32_32x32x16_bf16 v[96:111], a[228:231], a[132:135], v[96:111]
buffer_load_dwordx4 v32, s[52:55], s76 offen offset:2048 sc1 lds
ds_read_b128 a[196:199], v49 offset:9216
v_cmp_gt_i32_e64 s[94:95], 9, v58
v_cndmask_b32_e64 v72, v72, v57, s[92:93]
v_exp_f32 v71, v71
v_add_f32 v52, v52, v70
v_mfma_f32_32x32x16_bf16 v[96:111], a[232:235], a[136:139], v[96:111]
ds_read_b128 a[200:203], v49 offset:10240
v_cmp_gt_i32_e64 s[96:97], 10, v58
v_cndmask_b32_e64 v73, v73, v57, s[94:95]
v_exp_f32 v72, v72
v_add_f32 v52, v52, v71
v_cvt_pk_bf16_f32 v131, v70, v71
s_mul_i32 s44, s100, 448
v_mfma_f32_32x32x16_bf16 v[96:111], a[236:239], a[140:143], v[96:111]
ds_read_b128 a[204:207], v49 offset:11264
v_cmp_gt_i32_e64 s[98:99], 11, v58
v_cndmask_b32_e64 v74, v74, v57, s[96:97]
v_exp_f32 v73, v73
v_add_f32 v52, v52, v72
s_lshl_b32 s45, s38, 4
s_waitcnt lgkmcnt(4)
v_mfma_f32_32x32x16_bf16 v[96:111], a[240:243], a[144:147], v[96:111]
ds_read_b128 a[208:211], v49 offset:12288
v_cmp_gt_i32_e64 s[92:93], 24, v58
v_cndmask_b32_e64 v75, v75, v57, s[98:99]
v_exp_f32 v74, v74
v_add_f32 v52, v52, v73
v_cvt_pk_bf16_f32 v132, v72, v73
s_add_u32 s83, s83, 16384
s_add_u32 s44, s44, s45
v_mfma_f32_32x32x16_bf16 v[96:111], a[244:247], a[148:151], v[96:111]
buffer_load_dwordx4 v32, s[52:55], s76 offen offset:3072 sc1 lds
ds_read_b128 a[212:215], v49 offset:13312
v_cmp_gt_i32_e64 s[94:95], 25, v58
v_cndmask_b32_e64 v76, v76, v57, s[92:93]
v_exp_f32 v75, v75
v_add_f32 v52, v52, v74
v_mfma_f32_32x32x16_bf16 v[96:111], a[248:251], a[152:155], v[96:111]
ds_read_b128 a[216:219], v49 offset:14336
v_cmp_gt_i32_e64 s[96:97], 26, v58
v_cndmask_b32_e64 v77, v77, v57, s[94:95]
v_exp_f32 v76, v76
v_add_f32 v52, v52, v75
v_cvt_pk_bf16_f32 v133, v74, v75
s_cmp_eq_u32 s83, 163840
s_cselect_b32 s83, 0, s83
s_add_u32 s76, s76, 8192
s_add_u32 s82, s82, 16384
s_mul_i32 s86, s44, 1280
v_mfma_f32_32x32x16_bf16 v[96:111], a[252:255], a[156:159], v[96:111]
ds_read_b128 a[220:223], v49 offset:15360
v_cmp_gt_i32_e64 s[98:99], 27, v58
v_cndmask_b32_e64 v78, v78, v57, s[96:97]
v_exp_f32 v77, v77
v_add_f32 v52, v52, v76
v_add_u32 v49, s83, v48
s_cmp_eq_u32 s82, 163840
s_cselect_b32 s82, 0, s82
s_mul_i32 s44, s100, 14
s_waitcnt lgkmcnt(4)
v_mfma_f32_32x32x16_bf16 a[0:15], a[192:195], v[128:131], a[0:15]
ds_read_b128 a[224:227], v49 offset:0
v_cmp_gt_i32_e64 s[92:93], 0, v59
v_cndmask_b32_e64 v79, v79, v57, s[98:99]
v_exp_f32 v78, v78
v_add_f32 v52, v52, v77
v_cvt_pk_bf16_f32 v134, v76, v77
s_add_u32 m0, s81, s82
s_add_u32 s44, s44, 128
v_mfma_f32_32x32x16_bf16 a[32:47], a[196:199], v[128:131], a[32:47]
buffer_load_dwordx4 v32, s[52:55], s76 offen sc1 lds
ds_read_b128 a[228:231], v49 offset:1024
v_cmp_gt_i32_e64 s[94:95], 1, v59
v_cndmask_b32_e64 v96, v96, v57, s[92:93]
v_exp_f32 v79, v79
v_add_f32 v52, v52, v78
s_lshl_b32 s44, s44, 13
v_mfma_f32_32x32x16_bf16 a[64:79], a[200:203], v[128:131], a[64:79]
ds_read_b128 a[232:235], v49 offset:2048
v_cmp_gt_i32_e64 s[96:97], 2, v59
v_cndmask_b32_e64 v97, v97, v57, s[94:95]
v_exp_f32 v96, v96
v_add_f32 v52, v52, v79
v_cvt_pk_bf16_f32 v135, v78, v79
v_mfma_f32_32x32x16_bf16 a[96:111], a[204:207], v[128:131], a[96:111]
ds_read_b128 a[236:239], v49 offset:3072
v_cmp_gt_i32_e64 s[98:99], 3, v59
v_cndmask_b32_e64 v98, v98, v57, s[96:97]
v_exp_f32 v97, v97
v_add_f32 v52, v52, v96
s_lshl_b32 s45, s38, 11
s_waitcnt lgkmcnt(4)
v_mfma_f32_32x32x16_bf16 a[0:15], a[208:211], v[132:135], a[0:15]
ds_read_b128 a[240:243], v49 offset:4096
v_cmp_gt_i32_e64 s[92:93], 16, v59
v_cndmask_b32_e64 v99, v99, v57, s[98:99]
v_exp_f32 v98, v98
v_add_f32 v52, v52, v97
v_cvt_pk_bf16_f32 v144, v96, v97
s_add_u32 s84, s44, s45
v_mfma_f32_32x32x16_bf16 a[32:47], a[212:215], v[132:135], a[32:47]
buffer_load_dwordx4 v32, s[52:55], s76 offen offset:1024 sc1 lds
ds_read_b128 a[244:247], v49 offset:5120
v_cmp_gt_i32_e64 s[94:95], 17, v59
v_cndmask_b32_e64 v100, v100, v57, s[92:93]
v_exp_f32 v99, v99
v_add_f32 v52, v52, v98
s_mul_i32 s79, s38, 5120
v_mfma_f32_32x32x16_bf16 a[64:79], a[216:219], v[132:135], a[64:79]
ds_read_b128 a[248:251], v49 offset:6144
v_cmp_gt_i32_e64 s[96:97], 18, v59
v_cndmask_b32_e64 v101, v101, v57, s[94:95]
v_exp_f32 v100, v100
v_add_f32 v52, v52, v99
v_cvt_pk_bf16_f32 v145, v98, v99
v_mfma_f32_32x32x16_bf16 a[96:111], a[220:223], v[132:135], a[96:111]
ds_read_b128 a[252:255], v49 offset:7168
v_cmp_gt_i32_e64 s[98:99], 19, v59
v_cndmask_b32_e64 v102, v102, v57, s[96:97]
v_exp_f32 v101, v101
v_add_f32 v52, v52, v100
s_mul_i32 s44, s101, 1490944
s_waitcnt lgkmcnt(4)
v_mfma_f32_32x32x16_bf16 v[64:79], a[224:227], a[128:131], v[160:175]
ds_read_b128 a[192:195], v49 offset:8192
ds_read_b128 a[196:199], v49 offset:9216
v_cmp_gt_i32_e64 s[92:93], 8, v59
v_cndmask_b32_e64 v103, v103, v57, s[98:99]
v_exp_f32 v102, v102
v_add_f32 v52, v52, v101
v_cvt_pk_bf16_f32 v146, v100, v101
s_add_u32 s44, s44, 36192256
v_mfma_f32_32x32x16_bf16 v[64:79], a[228:231], a[132:135], v[64:79]
buffer_load_dwordx4 v32, s[52:55], s76 offen offset:2048 sc1 lds
ds_read_b128 a[200:203], v49 offset:10240
ds_read_b128 a[204:207], v49 offset:11264
v_cmp_gt_i32_e64 s[94:95], 9, v59
v_cndmask_b32_e64 v104, v104, v57, s[92:93]
v_exp_f32 v103, v103
v_add_f32 v52, v52, v102
s_add_u32 s83, s83, 16384
s_add_u32 s72, s48, s44
s_addc_u32 s73, s49, 0
v_mfma_f32_32x32x16_bf16 v[64:79], a[232:235], a[136:139], v[64:79]
ds_read_b128 a[208:211], v49 offset:12288
ds_read_b128 a[212:215], v49 offset:13312
v_cmp_gt_i32_e64 s[96:97], 10, v59
v_cndmask_b32_e64 v105, v105, v57, s[94:95]
v_exp_f32 v104, v104
v_add_f32 v52, v52, v103
v_cvt_pk_bf16_f32 v147, v102, v103
s_cmp_eq_u32 s83, 163840
s_cselect_b32 s83, 0, s83
v_mfma_f32_32x32x16_bf16 v[64:79], a[236:239], a[140:143], v[64:79]
buffer_load_dwordx4 v32, s[52:55], s76 offen offset:3072 sc1 lds
ds_read_b128 a[216:219], v49 offset:14336
ds_read_b128 a[220:223], v49 offset:15360
v_cmp_gt_i32_e64 s[98:99], 11, v59
v_cndmask_b32_e64 v106, v106, v57, s[96:97]
v_exp_f32 v105, v105
v_add_f32 v52, v52, v104
v_add_u32 v49, s83, v48
s_waitcnt lgkmcnt(8)
v_mfma_f32_32x32x16_bf16 v[64:79], a[240:243], a[144:147], v[64:79]
v_cmp_gt_i32_e64 s[92:93], 24, v59
v_cndmask_b32_e64 v107, v107, v57, s[98:99]
v_exp_f32 v106, v106
v_add_f32 v52, v52, v105
v_cvt_pk_bf16_f32 v148, v104, v105
s_add_u32 s76, s76, 8192
s_add_u32 s82, s82, 16384
s_mov_b32 s74, 1490944
v_mfma_f32_32x32x16_bf16 v[64:79], a[244:247], a[148:151], v[64:79]
v_cmp_gt_i32_e64 s[94:95], 25, v59
v_cndmask_b32_e64 v108, v108, v57, s[92:93]
v_exp_f32 v107, v107
v_add_f32 v52, v52, v106
s_cmp_eq_u32 s82, 163840
s_cselect_b32 s82, 0, s82
s_mov_b32 s75, 0x20000
v_mfma_f32_32x32x16_bf16 v[64:79], a[248:251], a[152:155], v[64:79]
v_cmp_gt_i32_e64 s[96:97], 26, v59
v_cndmask_b32_e64 v109, v109, v57, s[94:95]
v_exp_f32 v108, v108
v_add_f32 v52, v52, v107
v_cvt_pk_bf16_f32 v149, v106, v107
v_mfma_f32_32x32x16_bf16 v[64:79], a[252:255], a[156:159], v[64:79]
v_cmp_gt_i32_e64 s[98:99], 27, v59
v_cndmask_b32_e64 v110, v110, v57, s[96:97]
v_exp_f32 v109, v109
v_add_f32 v52, v52, v108
L_hm1%=:
s_waitcnt vmcnt(16) lgkmcnt(0)
s_barrier
s_waitcnt vmcnt(4)
v_cmp_ne_u32 vcc, s42, v28
s_nop 3
s_cmp_eq_u64 vcc, 0
s_cbranch_scc1 L_hd5%=
s_mov_b32 s45, 3000000
L_hl5%=:
buffer_load_dword v28, v29, s[48:51], 0 offen sc1
s_waitcnt vmcnt(0)
v_cmp_ne_u32 vcc, s42, v28
s_nop 3
s_cmp_eq_u64 vcc, 0
s_cbranch_scc1 L_hd5%=
s_sleep 1
s_sub_u32 s45, s45, 1
s_cmp_lg_u32 s45, 0
s_cbranch_scc1 L_hl5%=
L_hd5%=:
v_mfma_f32_32x32x16_bf16 a[0:15], a[192:195], v[144:147], a[0:15]
ds_read_b128 a[224:227], v49 offset:0
v_cmp_gt_i32_e64 s[92:93], 0, v60
v_cndmask_b32_e64 v111, v111, v57, s[98:99]
v_exp_f32 v110, v110
v_add_f32 v52, v52, v109
v_cvt_pk_bf16_f32 v150, v108, v109
s_add_u32 s85, s84, 8192
v_mfma_f32_32x32x16_bf16 a[32:47], a[196:199], v[144:147], a[32:47]
buffer_load_dwordx4 v[192:195], v32, s[72:75], s84 offen sc1
ds_read_b128 a[228:231], v49 offset:1024
v_cmp_gt_i32_e64 s[94:95], 1, v60
v_cndmask_b32_e64 v64, v64, v57, s[92:93]
v_exp_f32 v111, v111
v_add_f32 v52, v52, v110
v_mfma_f32_32x32x16_bf16 a[64:79], a[200:203], v[144:147], a[64:79]
buffer_load_dwordx4 v[196:199], v32, s[72:75], s84 offen offset:1024 sc1
ds_read_b128 a[232:235], v49 offset:2048
v_cmp_gt_i32_e64 s[96:97], 2, v60
v_cndmask_b32_e64 v65, v65, v57, s[94:95]
v_exp_f32 v64, v64
v_add_f32 v52, v52, v111
v_cvt_pk_bf16_f32 v151, v110, v111
v_mfma_f32_32x32x16_bf16 a[96:111], a[204:207], v[144:147], a[96:111]
buffer_load_dwordx4 v[200:203], v32, s[72:75], s85 offen sc1
ds_read_b128 a[236:239], v49 offset:3072
v_cmp_gt_i32_e64 s[98:99], 3, v60
v_cndmask_b32_e64 v66, v66, v57, s[96:97]
v_exp_f32 v65, v65
v_add_f32 v52, v52, v64
v_mfma_f32_32x32x16_bf16 a[0:15], a[208:211], v[148:151], a[0:15]
ds_read_b128 a[240:243], v49 offset:4096
v_cmp_gt_i32_e64 s[92:93], 16, v60
v_cndmask_b32_e64 v67, v67, v57, s[98:99]
v_exp_f32 v66, v66
v_add_f32 v52, v52, v65
v_cvt_pk_bf16_f32 v128, v64, v65
v_mfma_f32_32x32x16_bf16 a[32:47], a[212:215], v[148:151], a[32:47]
buffer_load_dwordx4 v[204:207], v32, s[72:75], s85 offen offset:1024 sc1
ds_read_b128 a[244:247], v49 offset:5120
v_cmp_gt_i32_e64 s[94:95], 17, v60
v_cndmask_b32_e64 v68, v68, v57, s[92:93]
v_exp_f32 v67, v67
v_add_f32 v52, v52, v66
s_add_u32 s84, s84, 16384
s_add_u32 s85, s84, 8192
v_mfma_f32_32x32x16_bf16 a[64:79], a[216:219], v[148:151], a[64:79]
buffer_load_dwordx4 v[208:211], v32, s[72:75], s84 offen sc1
ds_read_b128 a[248:251], v49 offset:6144
v_cmp_gt_i32_e64 s[96:97], 18, v60
v_cndmask_b32_e64 v69, v69, v57, s[94:95]
v_exp_f32 v68, v68
v_add_f32 v52, v52, v67
v_cvt_pk_bf16_f32 v129, v66, v67
v_mfma_f32_32x32x16_bf16 a[96:111], a[220:223], v[148:151], a[96:111]
buffer_load_dwordx4 v[212:215], v32, s[72:75], s84 offen offset:1024 sc1
ds_read_b128 a[252:255], v49 offset:7168
v_cmp_gt_i32_e64 s[98:99], 19, v60
v_cndmask_b32_e64 v70, v70, v57, s[96:97]
v_exp_f32 v69, v69
v_add_f32 v52, v52, v68
s_waitcnt lgkmcnt(4)
v_mfma_f32_32x32x16_bf16 v[96:111], a[224:227], a[128:131], v[160:175]
ds_read_b128 a[192:195], v49 offset:8192
v_cmp_gt_i32_e64 s[92:93], 8, v60
v_cndmask_b32_e64 v71, v71, v57, s[98:99]
v_exp_f32 v70, v70
v_add_f32 v52, v52, v69
v_cvt_pk_bf16_f32 v130, v68, v69
v_mfma_f32_32x32x16_bf16 v[96:111], a[228:231], a[132:135], v[96:111]
buffer_load_dwordx4 v[216:219], v32, s[72:75], s85 offen sc1
ds_read_b128 a[196:199], v49 offset:9216
v_cmp_gt_i32_e64 s[94:95], 9, v60
v_cndmask_b32_e64 v72, v72, v57, s[92:93]
v_exp_f32 v71, v71
v_add_f32 v52, v52, v70
v_mfma_f32_32x32x16_bf16 v[96:111], a[232:235], a[136:139], v[96:111]
buffer_load_dwordx4 v[220:223], v32, s[72:75], s85 offen offset:1024 sc1
ds_read_b128 a[200:203], v49 offset:10240
v_cmp_gt_i32_e64 s[96:97], 10, v60
v_cndmask_b32_e64 v73, v73, v57, s[94:95]
v_exp_f32 v72, v72
v_add_f32 v52, v52, v71
v_cvt_pk_bf16_f32 v131, v70, v71
v_mfma_f32_32x32x16_bf16 v[96:111], a[236:239], a[140:143], v[96:111]
ds_read_b128 a[204:207], v49 offset:11264
v_cmp_gt_i32_e64 s[98:99], 11, v60
v_cndmask_b32_e64 v74, v74, v57, s[96:97]
v_exp_f32 v73, v73
v_add_f32 v52, v52, v72
s_add_u32 s84, s84, 16384
s_add_u32 s85, s84, 8192
s_waitcnt lgkmcnt(4)
v_mfma_f32_32x32x16_bf16 v[96:111], a[240:243], a[144:147], v[96:111]
buffer_load_dwordx4 v[224:227], v32, s[72:75], s84 offen sc1
ds_read_b128 a[208:211], v49 offset:12288
v_cmp_gt_i32_e64 s[92:93], 24, v60
v_cndmask_b32_e64 v75, v75, v57, s[98:99]
v_exp_f32 v74, v74
v_add_f32 v52, v52, v73
v_cvt_pk_bf16_f32 v132, v72, v73
s_add_u32 s83, s83, 16384
v_mfma_f32_32x32x16_bf16 v[96:111], a[244:247], a[148:151], v[96:111]
buffer_load_dwordx4 v[228:231], v32, s[72:75], s84 offen offset:1024 sc1
ds_read_b128 a[212:215], v49 offset:13312
v_cmp_gt_i32_e64 s[94:95], 25, v60
v_cndmask_b32_e64 v76, v76, v57, s[92:93]
v_exp_f32 v75, v75
v_add_f32 v52, v52, v74
v_mfma_f32_32x32x16_bf16 v[96:111], a[248:251], a[152:155], v[96:111]
buffer_load_dwordx4 v[232:235], v32, s[72:75], s85 offen sc1
ds_read_b128 a[216:219], v49 offset:14336
v_cmp_gt_i32_e64 s[96:97], 26, v60
v_cndmask_b32_e64 v77, v77, v57, s[94:95]
v_exp_f32 v76, v76
v_add_f32 v52, v52, v75
v_cvt_pk_bf16_f32 v133, v74, v75
s_cmp_eq_u32 s83, 163840
s_cselect_b32 s83, 0, s83
v_mfma_f32_32x32x16_bf16 v[96:111], a[252:255], a[156:159], v[96:111]
ds_read_b128 a[220:223], v49 offset:15360
v_cmp_gt_i32_e64 s[98:99], 27, v60
v_cndmask_b32_e64 v78, v78, v57, s[96:97]
v_exp_f32 v77, v77
v_add_f32 v52, v52, v76
v_add_u32 v49, s83, v48
s_waitcnt lgkmcnt(4)
v_mfma_f32_32x32x16_bf16 a[0:15], a[192:195], v[128:131], a[0:15]
buffer_load_dwordx4 v[236:239], v32, s[72:75], s85 offen offset:1024 sc1
v_cmp_gt_i32_e64 s[92:93], 0, v61
v_cndmask_b32_e64 v79, v79, v57, s[98:99]
v_exp_f32 v78, v78
v_add_f32 v52, v52, v77
v_cvt_pk_bf16_f32 v134, v76, v77
s_add_u32 s84, s84, 16384
s_add_u32 s85, s84, 8192
v_mfma_f32_32x32x16_bf16 a[32:47], a[196:199], v[128:131], a[32:47]
buffer_load_dwordx4 v[240:243], v32, s[72:75], s84 offen sc1
v_cmp_gt_i32_e64 s[94:95], 1, v61
v_cndmask_b32_e64 v96, v96, v57, s[92:93]
v_exp_f32 v79, v79
v_add_f32 v52, v52, v78
v_mfma_f32_32x32x16_bf16 a[64:79], a[200:203], v[128:131], a[64:79]
buffer_load_dwordx4 v[244:247], v32, s[72:75], s84 offen offset:1024 sc1
v_cmp_gt_i32_e64 s[96:97], 2, v61
v_cndmask_b32_e64 v97, v97, v57, s[94:95]
v_exp_f32 v96, v96
v_add_f32 v52, v52, v79
v_cvt_pk_bf16_f32 v135, v78, v79
v_mfma_f32_32x32x16_bf16 a[96:111], a[204:207], v[128:131], a[96:111]
v_cmp_gt_i32_e64 s[98:99], 3, v61
v_cndmask_b32_e64 v98, v98, v57, s[96:97]
v_exp_f32 v97, v97
v_add_f32 v52, v52, v96
s_waitcnt lgkmcnt(0)
v_mfma_f32_32x32x16_bf16 a[0:15], a[208:211], v[132:135], a[0:15]
buffer_load_dwordx4 v[248:251], v32, s[72:75], s85 offen sc1
v_cmp_gt_i32_e64 s[92:93], 16, v61
v_cndmask_b32_e64 v99, v99, v57, s[98:99]
v_exp_f32 v98, v98
v_add_f32 v52, v52, v97
v_cvt_pk_bf16_f32 v144, v96, v97
v_mfma_f32_32x32x16_bf16 a[32:47], a[212:215], v[132:135], a[32:47]
buffer_load_dwordx4 v[252:255], v32, s[72:75], s85 offen offset:1024 sc1
v_cmp_gt_i32_e64 s[94:95], 17, v61
v_cndmask_b32_e64 v100, v100, v57, s[92:93]
v_exp_f32 v99, v99
v_add_f32 v52, v52, v98
v_mfma_f32_32x32x16_bf16 a[64:79], a[216:219], v[132:135], a[64:79]
v_cmp_gt_i32_e64 s[96:97], 18, v61
v_cndmask_b32_e64 v101, v101, v57, s[94:95]
v_exp_f32 v100, v100
v_add_f32 v52, v52, v99
v_cvt_pk_bf16_f32 v145, v98, v99
s_add_u32 s84, s84, 16384
v_mfma_f32_32x32x16_bf16 a[96:111], a[220:223], v[132:135], a[96:111]
v_cmp_gt_i32_e64 s[98:99], 19, v61
v_cndmask_b32_e64 v102, v102, v57, s[96:97]
v_exp_f32 v101, v101
v_add_f32 v52, v52, v100
ds_read_b128 a[192:195], v49 offset:8192
ds_read_b128 a[196:199], v49 offset:9216
ds_read_b128 a[200:203], v49 offset:10240
ds_read_b128 a[204:207], v49 offset:11264
ds_read_b128 a[208:211], v49 offset:12288
ds_read_b128 a[212:215], v49 offset:13312
ds_read_b128 a[216:219], v49 offset:14336
ds_read_b128 a[220:223], v49 offset:15360
v_cmp_gt_i32_e64 s[92:93], 8, v61
v_cndmask_b32_e64 v103, v103, v57, s[98:99]
v_exp_f32 v102, v102
v_add_f32 v52, v52, v101
v_cvt_pk_bf16_f32 v146, v100, v101
v_cmp_gt_i32_e64 s[94:95], 9, v61
v_cndmask_b32_e64 v104, v104, v57, s[92:93]
v_exp_f32 v103, v103
v_add_f32 v52, v52, v102
v_cmp_gt_i32_e64 s[96:97], 10, v61
v_cndmask_b32_e64 v105, v105, v57, s[94:95]
v_exp_f32 v104, v104
v_add_f32 v52, v52, v103
v_cvt_pk_bf16_f32 v147, v102, v103
v_cmp_gt_i32_e64 s[98:99], 11, v61
v_cndmask_b32_e64 v106, v106, v57, s[96:97]
v_exp_f32 v105, v105
v_add_f32 v52, v52, v104
v_cmp_gt_i32_e64 s[92:93], 24, v61
v_cndmask_b32_e64 v107, v107, v57, s[98:99]
v_exp_f32 v106, v106
v_add_f32 v52, v52, v105
v_cvt_pk_bf16_f32 v148, v104, v105
v_cmp_gt_i32_e64 s[94:95], 25, v61
v_cndmask_b32_e64 v108, v108, v57, s[92:93]
v_exp_f32 v107, v107
v_add_f32 v52, v52, v106
v_cmp_gt_i32_e64 s[96:97], 26, v61
v_cndmask_b32_e64 v109, v109, v57, s[94:95]
v_exp_f32 v108, v108
v_add_f32 v52, v52, v107
v_cvt_pk_bf16_f32 v149, v106, v107
v_cmp_gt_i32_e64 s[98:99], 27, v61
v_cndmask_b32_e64 v110, v110, v57, s[96:97]
v_exp_f32 v109, v109
v_add_f32 v52, v52, v108
v_cndmask_b32_e64 v111, v111, v57, s[98:99]
v_exp_f32 v110, v110
v_add_f32 v52, v52, v109
v_cvt_pk_bf16_f32 v150, v108, v109
v_exp_f32 v111, v111
v_add_f32 v52, v52, v110
v_add_f32 v52, v52, v111
v_cvt_pk_bf16_f32 v151, v110, v111
s_waitcnt lgkmcnt(0)
s_barrier
v_mfma_f32_32x32x16_bf16 a[0:15], a[192:195], v[144:147], a[0:15]
v_add_u32 v9, 0, v8
v_add_u32 v10, 96, v9
v_lshrrev_b32 v10, 8, v10
v_mul_u32_u24 v11, 0xa0, v10
v_sub_u32 v11, v9, v11
v_bfe_u32 v12, v11, 1, 3
v_lshlrev_b32 v38, 2, v10
v_xor_b32 v12, v12, v38
v_lshl_add_u32 v12, v10, 3, v12
v_mul_u32_u24 v12, 0x500, v12
v_lshrrev_b32 v11, 4, v11
v_mfma_f32_32x32x16_bf16 a[32:47], a[196:199], v[144:147], a[32:47]
v_lshl_add_u32 v12, v11, 5, v12
v_and_b32 v9, 1, v9
v_lshl_add_u32 v12, v9, 4, v12
v_add_u32 v33, 4096, v12
v_add_u32 v9, 64, v8
v_add_u32 v10, 96, v9
v_lshrrev_b32 v10, 8, v10
v_mul_u32_u24 v11, 0xa0, v10
v_sub_u32 v11, v9, v11
v_bfe_u32 v12, v11, 1, 3
v_lshlrev_b32 v38, 2, v10
v_mfma_f32_32x32x16_bf16 a[64:79], a[200:203], v[144:147], a[64:79]
v_xor_b32 v12, v12, v38
v_lshl_add_u32 v12, v10, 3, v12
v_mul_u32_u24 v12, 0x500, v12
v_lshrrev_b32 v11, 4, v11
v_lshl_add_u32 v12, v11, 5, v12
v_and_b32 v9, 1, v9
v_lshl_add_u32 v12, v9, 4, v12
v_add_u32 v34, 3072, v12
v_add_u32 v9, 128, v8
v_add_u32 v10, 96, v9
v_lshrrev_b32 v10, 8, v10
v_mfma_f32_32x32x16_bf16 a[96:111], a[204:207], v[144:147], a[96:111]
v_mul_u32_u24 v11, 0xa0, v10
v_sub_u32 v11, v9, v11
v_bfe_u32 v12, v11, 1, 3
v_lshlrev_b32 v38, 2, v10
v_xor_b32 v12, v12, v38
v_lshl_add_u32 v12, v10, 3, v12
v_mul_u32_u24 v12, 0x500, v12
v_lshrrev_b32 v11, 4, v11
v_lshl_add_u32 v12, v11, 5, v12
v_and_b32 v9, 1, v9
v_lshl_add_u32 v12, v9, 4, v12
v_mfma_f32_32x32x16_bf16 a[0:15], a[208:211], v[148:151], a[0:15]
v_add_u32 v35, 2048, v12
v_add_u32 v9, 192, v8
v_add_u32 v10, 96, v9
v_lshrrev_b32 v10, 8, v10
v_mul_u32_u24 v11, 0xa0, v10
v_sub_u32 v11, v9, v11
v_bfe_u32 v12, v11, 1, 3
v_lshlrev_b32 v38, 2, v10
v_xor_b32 v12, v12, v38
v_lshl_add_u32 v12, v10, 3, v12
v_mul_u32_u24 v12, 0x500, v12
v_mfma_f32_32x32x16_bf16 a[32:47], a[212:215], v[148:151], a[32:47]
v_lshrrev_b32 v11, 4, v11
v_lshl_add_u32 v12, v11, 5, v12
v_and_b32 v9, 1, v9
v_lshl_add_u32 v12, v9, 4, v12
v_add_u32 v36, 1024, v12
v_add_u32 v9, 256, v8
v_add_u32 v10, 96, v9
v_lshrrev_b32 v10, 8, v10
v_mul_u32_u24 v11, 0xa0, v10
v_sub_u32 v11, v9, v11
v_bfe_u32 v12, v11, 1, 3
v_mfma_f32_32x32x16_bf16 a[64:79], a[216:219], v[148:151], a[64:79]
v_lshlrev_b32 v38, 2, v10
v_xor_b32 v12, v12, v38
v_lshl_add_u32 v12, v10, 3, v12
v_mul_u32_u24 v12, 0x500, v12
v_lshrrev_b32 v11, 4, v11
v_lshl_add_u32 v12, v11, 5, v12
v_and_b32 v9, 1, v9
v_lshl_add_u32 v12, v9, 4, v12
v_add_u32 v37, 4096, v12
v_lshrrev_b32 v10, 4, v8
v_bfe_u32 v9, v8, 2, 2
v_mfma_f32_32x32x16_bf16 a[96:111], a[220:223], v[148:151], a[96:111]
v_and_b32 v11, 1, v10
v_lshl_add_u32 v9, v11, 2, v9
v_lshrrev_b32 v11, 1, v10
v_lshlrev_b32 v12, 2, v11
v_xor_b32 v9, v9, v12
v_lshlrev_b32 v9, 5, v9
v_mul_u32_u24 v11, 0xa00, v11
v_add_u32 v9, v9, v11
v_and_b32 v11, 3, v8
v_lshl_add_u32 v38, v11, 3, v9
s_load_dwordx2 s[52:53], s[36:37], 80
s_load_dwordx2 s[56:57], s[36:37], 0
s_load_dwordx2 s[60:61], s[36:37], 72
s_mul_i32 s44, s39, 320
s_sub_u32 s45, s44, 4096
s_ashr_i32 s46, s45, 31
s_add_u32 s64, s64, s45
s_addc_u32 s65, s65, s46
s_and_b32 s65, s65, 0xffff
s_mov_b32 s67, 0x20000
s_sub_u32 s66, 2215936, s44
s_ashr_i32 s46, s45, 31
s_add_u32 s68, s68, s45
s_addc_u32 s69, s69, s46
s_and_b32 s69, s69, 0xffff
s_mov_b32 s71, 0x20000
s_sub_u32 s70, 5246976, s44
s_waitcnt vmcnt(16)
s_nop 3
v_mov_b32 v9, v52
s_nop 1
v_permlane32_swap_b32 v52, v9
v_add_f32 v52, v52, v9
v_cmp_nge_f32 vcc, 0x6c800000, v52
v_rcp_f32 v94, v52
s_sub_u32 s44, 15, s41
s_mul_i32 s44, s44, 1490944
s_lshl_b32 s45, s40, 15
s_add_u32 s44, s44, s45
s_lshl_b32 s45, s38, 11
s_add_u32 s44, s44, s45
s_add_u32 s44, s44, 36192256
s_or_b32 s91, s91, vcc_lo
s_or_b32 s91, s91, vcc_hi
v_mov_b32 v95, v94
v_accvgpr_read_b32 v64, a0
v_accvgpr_read_b32 v65, a1
v_accvgpr_read_b32 v66, a2
v_accvgpr_read_b32 v67, a3
v_accvgpr_read_b32 v68, a8
v_accvgpr_read_b32 v69, a9
v_accvgpr_read_b32 v70, a10
v_accvgpr_read_b32 v71, a11
v_pk_mul_f32 v[64:65], v[64:65], v[94:95]
v_pk_mul_f32 v[66:67], v[66:67], v[94:95]
v_pk_mul_f32 v[68:69], v[68:69], v[94:95]
v_pk_mul_f32 v[70:71], v[70:71], v[94:95]
v_cvt_pk_bf16_f32 v72, v64, v65
v_cvt_pk_bf16_f32 v73, v66, v67
v_cvt_pk_bf16_f32 v74, v68, v69
v_cvt_pk_bf16_f32 v75, v70, v71
buffer_store_dwordx4 v[72:75], v62, s[48:51], s44 offen offset:0 sc1
s_add_u32 m0, s79, 0
s_nop 0
buffer_load_dwordx4 v33, s[64:67], s86 offen lds
buffer_load_dwordx4 v34, s[64:67], s86 offen offset:1024 lds
buffer_load_dwordx4 v35, s[64:67], s86 offen offset:2048 lds
v_accvgpr_read_b32 v78, a4
v_accvgpr_read_b32 v79, a5
v_accvgpr_read_b32 v80, a6
v_accvgpr_read_b32 v81, a7
v_accvgpr_read_b32 v82, a12
v_accvgpr_read_b32 v83, a13
v_accvgpr_read_b32 v84, a14
v_accvgpr_read_b32 v85, a15
v_pk_mul_f32 v[78:79], v[78:79], v[94:95]
v_pk_mul_f32 v[80:81], v[80:81], v[94:95]
v_pk_mul_f32 v[82:83], v[82:83], v[94:95]
v_pk_mul_f32 v[84:85], v[84:85], v[94:95]
v_cvt_pk_bf16_f32 v86, v78, v79
v_cvt_pk_bf16_f32 v87, v80, v81
v_cvt_pk_bf16_f32 v88, v82, v83
v_cvt_pk_bf16_f32 v89, v84, v85
buffer_store_dwordx4 v[86:89], v62, s[48:51], s44 offen offset:512 sc1
buffer_load_dwordx4 v36, s[64:67], s86 offen offset:3072 lds
s_add_u32 m0, s79, 4096
s_nop 0
buffer_load_dwordx4 v37, s[64:67], s86 offen lds
s_add_u32 s86, s86, 81920
s_add_u32 m0, s79, 20480
s_nop 0
buffer_load_dwordx4 v33, s[64:67], s86 offen lds
s_add_u32 s44, s44, 8192
v_accvgpr_read_b32 v64, a32
v_accvgpr_read_b32 v65, a33
v_accvgpr_read_b32 v66, a34
v_accvgpr_read_b32 v67, a35
v_accvgpr_read_b32 v68, a40
v_accvgpr_read_b32 v69, a41
v_accvgpr_read_b32 v70, a42
v_accvgpr_read_b32 v71, a43
v_pk_mul_f32 v[64:65], v[64:65], v[94:95]
v_pk_mul_f32 v[66:67], v[66:67], v[94:95]
v_pk_mul_f32 v[68:69], v[68:69], v[94:95]
v_pk_mul_f32 v[70:71], v[70:71], v[94:95]
v_cvt_pk_bf16_f32 v72, v64, v65
v_cvt_pk_bf16_f32 v73, v66, v67
v_cvt_pk_bf16_f32 v74, v68, v69
v_cvt_pk_bf16_f32 v75, v70, v71
buffer_store_dwordx4 v[72:75], v62, s[48:51], s44 offen offset:0 sc1
buffer_load_dwordx4 v34, s[64:67], s86 offen offset:1024 lds
buffer_load_dwordx4 v35, s[64:67], s86 offen offset:2048 lds
buffer_load_dwordx4 v36, s[64:67], s86 offen offset:3072 lds
v_accvgpr_read_b32 v78, a36
v_accvgpr_read_b32 v79, a37
v_accvgpr_read_b32 v80, a38
v_accvgpr_read_b32 v81, a39
v_accvgpr_read_b32 v82, a44
v_accvgpr_read_b32 v83, a45
v_accvgpr_read_b32 v84, a46
v_accvgpr_read_b32 v85, a47
v_pk_mul_f32 v[78:79], v[78:79], v[94:95]
v_pk_mul_f32 v[80:81], v[80:81], v[94:95]
v_pk_mul_f32 v[82:83], v[82:83], v[94:95]
v_pk_mul_f32 v[84:85], v[84:85], v[94:95]
v_cvt_pk_bf16_f32 v86, v78, v79
v_cvt_pk_bf16_f32 v87, v80, v81
v_cvt_pk_bf16_f32 v88, v82, v83
v_cvt_pk_bf16_f32 v89, v84, v85
buffer_store_dwordx4 v[86:89], v62, s[48:51], s44 offen offset:512 sc1
s_add_u32 m0, s79, 24576
s_nop 0
buffer_load_dwordx4 v37, s[64:67], s86 offen lds
s_add_u32 s86, s86, 81920
s_add_u32 m0, s79, 40960
s_nop 0
buffer_load_dwordx4 v33, s[64:67], s86 offen lds
buffer_load_dwordx4 v34, s[64:67], s86 offen offset:1024 lds
s_add_u32 s44, s44, 8192
v_accvgpr_read_b32 v64, a64
v_accvgpr_read_b32 v65, a65
v_accvgpr_read_b32 v66, a66
v_accvgpr_read_b32 v67, a67
v_accvgpr_read_b32 v68, a72
v_accvgpr_read_b32 v69, a73
v_accvgpr_read_b32 v70, a74
v_accvgpr_read_b32 v71, a75
v_pk_mul_f32 v[64:65], v[64:65], v[94:95]
v_pk_mul_f32 v[66:67], v[66:67], v[94:95]
v_pk_mul_f32 v[68:69], v[68:69], v[94:95]
v_pk_mul_f32 v[70:71], v[70:71], v[94:95]
v_cvt_pk_bf16_f32 v72, v64, v65
v_cvt_pk_bf16_f32 v73, v66, v67
v_cvt_pk_bf16_f32 v74, v68, v69
v_cvt_pk_bf16_f32 v75, v70, v71
buffer_store_dwordx4 v[72:75], v62, s[48:51], s44 offen offset:0 sc1
buffer_load_dwordx4 v35, s[64:67], s86 offen offset:2048 lds
buffer_load_dwordx4 v36, s[64:67], s86 offen offset:3072 lds
s_add_u32 m0, s79, 45056
s_nop 0
buffer_load_dwordx4 v37, s[64:67], s86 offen lds
s_add_u32 s86, s86, 81920
v_accvgpr_read_b32 v78, a68
v_accvgpr_read_b32 v79, a69
v_accvgpr_read_b32 v80, a70
v_accvgpr_read_b32 v81, a71
v_accvgpr_read_b32 v82, a76
v_accvgpr_read_b32 v83, a77
v_accvgpr_read_b32 v84, a78
v_accvgpr_read_b32 v85, a79
v_pk_mul_f32 v[78:79], v[78:79], v[94:95]
v_pk_mul_f32 v[80:81], v[80:81], v[94:95]
v_pk_mul_f32 v[82:83], v[82:83], v[94:95]
v_pk_mul_f32 v[84:85], v[84:85], v[94:95]
v_cvt_pk_bf16_f32 v86, v78, v79
v_cvt_pk_bf16_f32 v87, v80, v81
v_cvt_pk_bf16_f32 v88, v82, v83
v_cvt_pk_bf16_f32 v89, v84, v85
buffer_store_dwordx4 v[86:89], v62, s[48:51], s44 offen offset:512 sc1
s_add_u32 m0, s79, 61440
s_nop 0
buffer_load_dwordx4 v33, s[64:67], s86 offen lds
buffer_load_dwordx4 v34, s[64:67], s86 offen offset:1024 lds
buffer_load_dwordx4 v35, s[64:67], s86 offen offset:2048 lds
s_add_u32 s44, s44, 8192
v_accvgpr_read_b32 v64, a96
v_accvgpr_read_b32 v65, a97
v_accvgpr_read_b32 v66, a98
v_accvgpr_read_b32 v67, a99
v_accvgpr_read_b32 v68, a104
v_accvgpr_read_b32 v69, a105
v_accvgpr_read_b32 v70, a106
v_accvgpr_read_b32 v71, a107
v_pk_mul_f32 v[64:65], v[64:65], v[94:95]
v_pk_mul_f32 v[66:67], v[66:67], v[94:95]
v_pk_mul_f32 v[68:69], v[68:69], v[94:95]
v_pk_mul_f32 v[70:71], v[70:71], v[94:95]
v_cvt_pk_bf16_f32 v72, v64, v65
v_cvt_pk_bf16_f32 v73, v66, v67
v_cvt_pk_bf16_f32 v74, v68, v69
v_cvt_pk_bf16_f32 v75, v70, v71
buffer_store_dwordx4 v[72:75], v62, s[48:51], s44 offen offset:0 sc1
buffer_load_dwordx4 v36, s[64:67], s86 offen offset:3072 lds
s_add_u32 m0, s79, 65536
s_nop 0
buffer_load_dwordx4 v37, s[64:67], s86 offen lds
s_add_u32 s86, s86, 81920
s_add_u32 m0, s79, 81920
s_nop 0
buffer_load_dwordx4 v33, s[64:67], s86 offen lds
v_accvgpr_read_b32 v78, a100
v_accvgpr_read_b32 v79, a101
v_accvgpr_read_b32 v80, a102
v_accvgpr_read_b32 v81, a103
v_accvgpr_read_b32 v82, a108
v_accvgpr_read_b32 v83, a109
v_accvgpr_read_b32 v84, a110
v_accvgpr_read_b32 v85, a111
v_pk_mul_f32 v[78:79], v[78:79], v[94:95]
v_pk_mul_f32 v[80:81], v[80:81], v[94:95]
v_pk_mul_f32 v[82:83], v[82:83], v[94:95]
v_pk_mul_f32 v[84:85], v[84:85], v[94:95]
v_cvt_pk_bf16_f32 v86, v78, v79
v_cvt_pk_bf16_f32 v87, v80, v81
v_cvt_pk_bf16_f32 v88, v82, v83
v_cvt_pk_bf16_f32 v89, v84, v85
buffer_store_dwordx4 v[86:89], v62, s[48:51], s44 offen offset:512 sc1
buffer_load_dwordx4 v34, s[64:67], s86 offen offset:1024 lds
buffer_load_dwordx4 v35, s[64:67], s86 offen offset:2048 lds
buffer_load_dwordx4 v36, s[64:67], s86 offen offset:3072 lds
s_add_u32 m0, s79, 86016
s_nop 0
buffer_load_dwordx4 v37, s[64:67], s86 offen lds
s_add_u32 s86, s86, 81920
s_waitcnt lgkmcnt(0)
s_and_b32 s53, s53, 0xffff
s_mov_b32 s54, 2621440
s_mov_b32 s55, 0x20000
s_and_b32 s57, s57, 0xffff
s_mov_b32 s58, 2621440
s_mov_b32 s59, 0x20000
s_and_b32 s61, s61, 0xffff
s_mov_b32 s62, 1280
s_mov_b32 s63, 0x20000
s_branch L_p3end%=
L_tm0%=:
s_waitcnt vmcnt(16) lgkmcnt(0)
s_barrier
v_mfma_f32_32x32x16_bf16 a[0:15], a[192:195], v[144:147], a[0:15]
ds_read_b128 a[224:227], v49 offset:0
v_cmp_gt_i32_e64 s[92:93], 0, v58
v_cndmask_b32_e64 v126, v126, v57, s[96:97]
v_add_f32 v53, v53, v124
v_exp_f32 v125, v125
v_mfma_f32_32x32x16_bf16 a[16:31], a[192:195], v[152:155], a[16:31]
ds_read_b128 a[228:231], v49 offset:1024
v_cmp_gt_i32_e64 s[94:95], 1, v58
v_cndmask_b32_e64 v127, v127, v57, s[98:99]
v_cndmask_b32_e64 v64, v64, v57, s[92:93]
v_add_f32 v53, v53, v125
v_exp_f32 v126, v126
v_cvt_pk_bf16_f32 v158, v124, v125
v_mfma_f32_32x32x16_bf16 a[32:47], a[196:199], v[144:147], a[32:47]
ds_read_b128 a[232:235], v49 offset:2048
v_cmp_gt_i32_e64 s[96:97], 2, v58
v_cndmask_b32_e64 v65, v65, v57, s[94:95]
v_exp_f32 v64, v64
v_add_f32 v53, v53, v126
v_exp_f32 v127, v127
v_mfma_f32_32x32x16_bf16 a[48:63], a[196:199], v[152:155], a[48:63]
ds_read_b128 a[236:239], v49 offset:3072
v_cmp_gt_i32_e64 s[98:99], 3, v58
v_cndmask_b32_e64 v66, v66, v57, s[96:97]
v_exp_f32 v65, v65
v_add_f32 v53, v53, v127
v_add_f32 v52, v52, v64
v_cvt_pk_bf16_f32 v159, v126, v127
v_mfma_f32_32x32x16_bf16 a[64:79], a[200:203], v[144:147], a[64:79]
ds_read_b128 a[240:243], v49 offset:4096
v_cmp_gt_i32_e64 s[92:93], 16, v58
v_cndmask_b32_e64 v67, v67, v57, s[98:99]
v_exp_f32 v66, v66
v_add_f32 v52, v52, v65
v_cvt_pk_bf16_f32 v128, v64, v65
v_mfma_f32_32x32x16_bf16 a[80:95], a[200:203], v[152:155], a[80:95]
ds_read_b128 a[244:247], v49 offset:5120
v_cmp_gt_i32_e64 s[94:95], 17, v58
v_cndmask_b32_e64 v68, v68, v57, s[92:93]
v_exp_f32 v67, v67
v_add_f32 v52, v52, v66
v_mfma_f32_32x32x16_bf16 a[96:111], a[204:207], v[144:147], a[96:111]
ds_read_b128 a[248:251], v49 offset:6144
v_cmp_gt_i32_e64 s[96:97], 18, v58
v_cndmask_b32_e64 v69, v69, v57, s[94:95]
v_exp_f32 v68, v68
v_add_f32 v52, v52, v67
v_cvt_pk_bf16_f32 v129, v66, v67
v_mfma_f32_32x32x16_bf16 a[112:127], a[204:207], v[152:155], a[112:127]
ds_read_b128 a[252:255], v49 offset:7168
v_cmp_gt_i32_e64 s[98:99], 19, v58
v_cndmask_b32_e64 v70, v70, v57, s[96:97]
v_exp_f32 v69, v69
v_add_f32 v52, v52, v68
s_add_u32 m0, s81, s82
v_mfma_f32_32x32x16_bf16 a[0:15], a[208:211], v[148:151], a[0:15]
buffer_load_dwordx4 v32, s[52:55], s76 offen sc1 lds
v_cmp_gt_i32_e64 s[92:93], 8, v58
v_cndmask_b32_e64 v71, v71, v57, s[98:99]
v_exp_f32 v70, v70
v_add_f32 v52, v52, v69
v_cvt_pk_bf16_f32 v130, v68, v69
v_mfma_f32_32x32x16_bf16 a[16:31], a[208:211], v[156:159], a[16:31]
v_cmp_gt_i32_e64 s[94:95], 9, v58
v_cndmask_b32_e64 v72, v72, v57, s[92:93]
v_exp_f32 v71, v71
v_add_f32 v52, v52, v70
v_mfma_f32_32x32x16_bf16 a[32:47], a[212:215], v[148:151], a[32:47]
buffer_load_dwordx4 v32, s[52:55], s76 offen offset:1024 sc1 lds
v_cmp_gt_i32_e64 s[96:97], 10, v58
v_cndmask_b32_e64 v73, v73, v57, s[94:95]
v_exp_f32 v72, v72
v_add_f32 v52, v52, v71
v_cvt_pk_bf16_f32 v131, v70, v71
v_mfma_f32_32x32x16_bf16 a[48:63], a[212:215], v[156:159], a[48:63]
v_cmp_gt_i32_e64 s[98:99], 11, v58
v_cndmask_b32_e64 v74, v74, v57, s[96:97]
v_exp_f32 v73, v73
v_add_f32 v52, v52, v72
v_mfma_f32_32x32x16_bf16 a[64:79], a[216:219], v[148:151], a[64:79]
buffer_load_dwordx4 v32, s[52:55], s76 offen offset:2048 sc1 lds
v_cmp_gt_i32_e64 s[92:93], 24, v58
v_cndmask_b32_e64 v75, v75, v57, s[98:99]
v_exp_f32 v74, v74
v_add_f32 v52, v52, v73
v_cvt_pk_bf16_f32 v132, v72, v73
v_mfma_f32_32x32x16_bf16 a[80:95], a[216:219], v[156:159], a[80:95]
v_cmp_gt_i32_e64 s[94:95], 25, v58
v_cndmask_b32_e64 v76, v76, v57, s[92:93]
v_exp_f32 v75, v75
v_add_f32 v52, v52, v74
v_mfma_f32_32x32x16_bf16 a[96:111], a[220:223], v[148:151], a[96:111]
buffer_load_dwordx4 v32, s[52:55], s76 offen offset:3072 sc1 lds
v_cmp_gt_i32_e64 s[96:97], 26, v58
v_cndmask_b32_e64 v77, v77, v57, s[94:95]
v_exp_f32 v76, v76
v_add_f32 v52, v52, v75
v_cvt_pk_bf16_f32 v133, v74, v75
v_mfma_f32_32x32x16_bf16 a[112:127], a[220:223], v[156:159], a[112:127]
v_cmp_gt_i32_e64 s[98:99], 27, v58
v_cndmask_b32_e64 v78, v78, v57, s[96:97]
v_exp_f32 v77, v77
v_add_f32 v52, v52, v76
s_add_u32 s76, s76, 8192
s_add_u32 s82, s82, 16384
s_nop 7
s_nop 3
v_mov_b32 v9, v53
s_nop 1
v_permlane32_swap_b32 v53, v9
v_add_f32 v53, v53, v9
v_cmp_nge_f32 vcc, 0x6c800000, v53
v_rcp_f32 v222, v53
s_mov_b32 s44, s41
s_mul_i32 s44, s44, 1490944
s_lshl_b32 s45, s40, 15
s_add_u32 s44, s44, s45
s_lshl_b32 s45, s38, 11
s_add_u32 s44, s44, s45
s_add_u32 s44, s44, 36192256
s_or_b32 s91, s91, vcc_lo
s_or_b32 s91, s91, vcc_hi
v_mov_b32 v223, v222
v_accvgpr_read_b32 v192, a16
v_accvgpr_read_b32 v193, a17
v_accvgpr_read_b32 v194, a18
v_accvgpr_read_b32 v195, a19
v_accvgpr_read_b32 v196, a24
v_accvgpr_read_b32 v197, a25
v_accvgpr_read_b32 v198, a26
v_accvgpr_read_b32 v199, a27
v_pk_mul_f32 v[192:193], v[192:193], v[222:223]
v_pk_mul_f32 v[194:195], v[194:195], v[222:223]
v_pk_mul_f32 v[196:197], v[196:197], v[222:223]
v_pk_mul_f32 v[198:199], v[198:199], v[222:223]
v_cvt_pk_bf16_f32 v200, v192, v193
v_cvt_pk_bf16_f32 v201, v194, v195
v_cvt_pk_bf16_f32 v202, v196, v197
v_cvt_pk_bf16_f32 v203, v198, v199
buffer_store_dwordx4 v[200:203], v62, s[48:51], s44 offen offset:0 sc1
v_accvgpr_read_b32 v206, a20
v_accvgpr_read_b32 v207, a21
v_accvgpr_read_b32 v208, a22
v_accvgpr_read_b32 v209, a23
v_accvgpr_read_b32 v210, a28
v_accvgpr_read_b32 v211, a29
v_accvgpr_read_b32 v212, a30
v_accvgpr_read_b32 v213, a31
v_pk_mul_f32 v[206:207], v[206:207], v[222:223]
v_pk_mul_f32 v[208:209], v[208:209], v[222:223]
v_pk_mul_f32 v[210:211], v[210:211], v[222:223]
v_pk_mul_f32 v[212:213], v[212:213], v[222:223]
v_cvt_pk_bf16_f32 v214, v206, v207
v_cvt_pk_bf16_f32 v215, v208, v209
v_cvt_pk_bf16_f32 v216, v210, v211
v_cvt_pk_bf16_f32 v217, v212, v213
buffer_store_dwordx4 v[214:217], v62, s[48:51], s44 offen offset:512 sc1
s_add_u32 s44, s44, 8192
v_accvgpr_read_b32 v192, a48
v_accvgpr_read_b32 v193, a49
v_accvgpr_read_b32 v194, a50
v_accvgpr_read_b32 v195, a51
v_accvgpr_read_b32 v196, a56
v_accvgpr_read_b32 v197, a57
v_accvgpr_read_b32 v198, a58
v_accvgpr_read_b32 v199, a59
v_pk_mul_f32 v[192:193], v[192:193], v[222:223]
v_pk_mul_f32 v[194:195], v[194:195], v[222:223]
v_pk_mul_f32 v[196:197], v[196:197], v[222:223]
v_pk_mul_f32 v[198:199], v[198:199], v[222:223]
v_cvt_pk_bf16_f32 v200, v192, v193
v_cvt_pk_bf16_f32 v201, v194, v195
v_cvt_pk_bf16_f32 v202, v196, v197
v_cvt_pk_bf16_f32 v203, v198, v199
buffer_store_dwordx4 v[200:203], v62, s[48:51], s44 offen offset:0 sc1
v_accvgpr_read_b32 v206, a52
v_accvgpr_read_b32 v207, a53
v_accvgpr_read_b32 v208, a54
v_accvgpr_read_b32 v209, a55
v_accvgpr_read_b32 v210, a60
v_accvgpr_read_b32 v211, a61
v_accvgpr_read_b32 v212, a62
v_accvgpr_read_b32 v213, a63
v_pk_mul_f32 v[206:207], v[206:207], v[222:223]
v_pk_mul_f32 v[208:209], v[208:209], v[222:223]
v_pk_mul_f32 v[210:211], v[210:211], v[222:223]
v_pk_mul_f32 v[212:213], v[212:213], v[222:223]
v_cvt_pk_bf16_f32 v214, v206, v207
v_cvt_pk_bf16_f32 v215, v208, v209
v_cvt_pk_bf16_f32 v216, v210, v211
v_cvt_pk_bf16_f32 v217, v212, v213
buffer_store_dwordx4 v[214:217], v62, s[48:51], s44 offen offset:512 sc1
s_add_u32 s44, s44, 8192
v_accvgpr_read_b32 v192, a80
v_accvgpr_read_b32 v193, a81
v_accvgpr_read_b32 v194, a82
v_accvgpr_read_b32 v195, a83
v_accvgpr_read_b32 v196, a88
v_accvgpr_read_b32 v197, a89
v_accvgpr_read_b32 v198, a90
v_accvgpr_read_b32 v199, a91
v_pk_mul_f32 v[192:193], v[192:193], v[222:223]
v_pk_mul_f32 v[194:195], v[194:195], v[222:223]
v_pk_mul_f32 v[196:197], v[196:197], v[222:223]
v_pk_mul_f32 v[198:199], v[198:199], v[222:223]
v_cvt_pk_bf16_f32 v200, v192, v193
v_cvt_pk_bf16_f32 v201, v194, v195
v_cvt_pk_bf16_f32 v202, v196, v197
v_cvt_pk_bf16_f32 v203, v198, v199
buffer_store_dwordx4 v[200:203], v62, s[48:51], s44 offen offset:0 sc1
v_accvgpr_read_b32 v206, a84
v_accvgpr_read_b32 v207, a85
v_accvgpr_read_b32 v208, a86
v_accvgpr_read_b32 v209, a87
v_accvgpr_read_b32 v210, a92
v_accvgpr_read_b32 v211, a93
v_accvgpr_read_b32 v212, a94
v_accvgpr_read_b32 v213, a95
v_pk_mul_f32 v[206:207], v[206:207], v[222:223]
v_pk_mul_f32 v[208:209], v[208:209], v[222:223]
v_pk_mul_f32 v[210:211], v[210:211], v[222:223]
v_pk_mul_f32 v[212:213], v[212:213], v[222:223]
v_cvt_pk_bf16_f32 v214, v206, v207
v_cvt_pk_bf16_f32 v215, v208, v209
v_cvt_pk_bf16_f32 v216, v210, v211
v_cvt_pk_bf16_f32 v217, v212, v213
buffer_store_dwordx4 v[214:217], v62, s[48:51], s44 offen offset:512 sc1
s_add_u32 s44, s44, 8192
v_accvgpr_read_b32 v192, a112
v_accvgpr_read_b32 v193, a113
v_accvgpr_read_b32 v194, a114
v_accvgpr_read_b32 v195, a115
v_accvgpr_read_b32 v196, a120
v_accvgpr_read_b32 v197, a121
v_accvgpr_read_b32 v198, a122
v_accvgpr_read_b32 v199, a123
v_pk_mul_f32 v[192:193], v[192:193], v[222:223]
v_pk_mul_f32 v[194:195], v[194:195], v[222:223]
v_pk_mul_f32 v[196:197], v[196:197], v[222:223]
v_pk_mul_f32 v[198:199], v[198:199], v[222:223]
v_cvt_pk_bf16_f32 v200, v192, v193
v_cvt_pk_bf16_f32 v201, v194, v195
v_cvt_pk_bf16_f32 v202, v196, v197
v_cvt_pk_bf16_f32 v203, v198, v199
buffer_store_dwordx4 v[200:203], v62, s[48:51], s44 offen offset:0 sc1
v_accvgpr_read_b32 v206, a116
v_accvgpr_read_b32 v207, a117
v_accvgpr_read_b32 v208, a118
v_accvgpr_read_b32 v209, a119
v_accvgpr_read_b32 v210, a124
v_accvgpr_read_b32 v211, a125
v_accvgpr_read_b32 v212, a126
v_accvgpr_read_b32 v213, a127
v_pk_mul_f32 v[206:207], v[206:207], v[222:223]
v_pk_mul_f32 v[208:209], v[208:209], v[222:223]
v_pk_mul_f32 v[210:211], v[210:211], v[222:223]
v_pk_mul_f32 v[212:213], v[212:213], v[222:223]
v_cvt_pk_bf16_f32 v214, v206, v207
v_cvt_pk_bf16_f32 v215, v208, v209
v_cvt_pk_bf16_f32 v216, v210, v211
v_cvt_pk_bf16_f32 v217, v212, v213
buffer_store_dwordx4 v[214:217], v62, s[48:51], s44 offen offset:512 sc1
s_and_b32 s44, s43, 7
s_lshr_b32 s44, s44, 2
s_lshl_b32 s44, s44, 3
s_bfe_u32 s45, s43, 0x30003
s_add_u32 s44, s44, s45
s_sub_u32 s45, 15, s44
s_min_u32 s44, s44, s45
s_lshl_b32 s44, s44, 7
s_add_u32 s44, s44, 67913728
v_and_b32 v29, 31, v8
v_lshlrev_b32 v29, 2, v29
v_add_u32 v29, s44, v29
s_waitcnt lgkmcnt(0)
v_mfma_f32_32x32x16_bf16 v[96:111], a[224:227], a[128:131], v[160:175]
ds_read_b128 a[192:195], v49 offset:8192
v_cndmask_b32_e64 v79, v79, v57, s[98:99]
v_exp_f32 v78, v78
v_add_f32 v52, v52, v77
v_cvt_pk_bf16_f32 v134, v76, v77
s_cmp_eq_u32 s82, 163840
s_cselect_b32 s82, 0, s82
v_mfma_f32_32x32x16_bf16 v[96:111], a[228:231], a[132:135], v[96:111]
buffer_load_dword v28, v29, s[48:51], 0 offen sc1
ds_read_b128 a[196:199], v49 offset:9216
v_exp_f32 v79, v79
v_add_f32 v52, v52, v78
s_and_b32 s44, s43, 7
v_mfma_f32_32x32x16_bf16 v[96:111], a[232:235], a[136:139], v[96:111]
ds_read_b128 a[200:203], v49 offset:10240
v_add_f32 v52, v52, v79
v_cvt_pk_bf16_f32 v135, v78, v79
s_and_b32 s100, s44, 3
v_mfma_f32_32x32x16_bf16 v[96:111], a[236:239], a[140:143], v[96:111]
ds_read_b128 a[204:207], v49 offset:11264
s_lshr_b32 s44, s44, 2
v_mfma_f32_32x32x16_bf16 v[96:111], a[240:243], a[144:147], v[96:111]
ds_read_b128 a[208:211], v49 offset:12288
s_add_u32 s83, s83, 16384
s_lshl_b32 s44, s44, 3
v_mfma_f32_32x32x16_bf16 v[96:111], a[244:247], a[148:151], v[96:111]
ds_read_b128 a[212:215], v49 offset:13312
s_bfe_u32 s45, s43, 0x30003
v_mfma_f32_32x32x16_bf16 v[96:111], a[248:251], a[152:155], v[96:111]
ds_read_b128 a[216:219], v49 offset:14336
s_cmp_eq_u32 s83, 163840
s_cselect_b32 s83, 0, s83
s_add_u32 s101, s44, s45
v_mfma_f32_32x32x16_bf16 v[96:111], a[252:255], a[156:159], v[96:111]
ds_read_b128 a[220:223], v49 offset:15360
v_add_u32 v49, s83, v48
s_lshr_b32 s39, s43, 6
s_waitcnt lgkmcnt(4)
v_mfma_f32_32x32x16_bf16 a[0:15], a[192:195], v[128:131], a[0:15]
ds_read_b128 a[224:227], v49 offset:0
v_cmp_gt_i32_e64 s[92:93], 0, v59
s_add_u32 m0, s81, s82
s_mul_i32 s44, s100, 448
v_mfma_f32_32x32x16_bf16 a[32:47], a[196:199], v[128:131], a[32:47]
buffer_load_dwordx4 v32, s[52:55], s76 offen sc1 lds
ds_read_b128 a[228:231], v49 offset:1024
v_cmp_gt_i32_e64 s[94:95], 1, v59
v_cndmask_b32_e64 v96, v96, v57, s[92:93]
s_lshl_b32 s45, s38, 4
v_mfma_f32_32x32x16_bf16 a[64:79], a[200:203], v[128:131], a[64:79]
ds_read_b128 a[232:235], v49 offset:2048
v_cmp_gt_i32_e64 s[96:97], 2, v59
v_cndmask_b32_e64 v97, v97, v57, s[94:95]
v_exp_f32 v96, v96
s_add_u32 s44, s44, s45
v_mfma_f32_32x32x16_bf16 a[96:111], a[204:207], v[128:131], a[96:111]
ds_read_b128 a[236:239], v49 offset:3072
v_cmp_gt_i32_e64 s[98:99], 3, v59
v_cndmask_b32_e64 v98, v98, v57, s[96:97]
v_exp_f32 v97, v97
v_add_f32 v52, v52, v96
s_mul_i32 s86, s44, 1280
s_waitcnt lgkmcnt(4)
v_mfma_f32_32x32x16_bf16 a[0:15], a[208:211], v[132:135], a[0:15]
ds_read_b128 a[240:243], v49 offset:4096
v_cmp_gt_i32_e64 s[92:93], 16, v59
v_cndmask_b32_e64 v99, v99, v57, s[98:99]
v_exp_f32 v98, v98
v_add_f32 v52, v52, v97
v_cvt_pk_bf16_f32 v144, v96, v97
s_mul_i32 s44, s100, 14
v_mfma_f32_32x32x16_bf16 a[32:47], a[212:215], v[132:135], a[32:47]
buffer_load_dwordx4 v32, s[52:55], s76 offen offset:1024 sc1 lds
ds_read_b128 a[244:247], v49 offset:5120
v_cmp_gt_i32_e64 s[94:95], 17, v59
v_cndmask_b32_e64 v100, v100, v57, s[92:93]
v_exp_f32 v99, v99
v_add_f32 v52, v52, v98
s_add_u32 s44, s44, 128
v_mfma_f32_32x32x16_bf16 a[64:79], a[216:219], v[132:135], a[64:79]
ds_read_b128 a[248:251], v49 offset:6144
v_cmp_gt_i32_e64 s[96:97], 18, v59
v_cndmask_b32_e64 v101, v101, v57, s[94:95]
v_exp_f32 v100, v100
v_add_f32 v52, v52, v99
v_cvt_pk_bf16_f32 v145, v98, v99
s_lshl_b32 s44, s44, 13
v_mfma_f32_32x32x16_bf16 a[96:111], a[220:223], v[132:135], a[96:111]
ds_read_b128 a[252:255], v49 offset:7168
v_cmp_gt_i32_e64 s[98:99], 19, v59
v_cndmask_b32_e64 v102, v102, v57, s[96:97]
v_exp_f32 v101, v101
v_add_f32 v52, v52, v100
s_lshl_b32 s45, s38, 11
s_waitcnt lgkmcnt(4)
v_mfma_f32_32x32x16_bf16 v[64:79], a[224:227], a[128:131], v[160:175]
ds_read_b128 a[192:195], v49 offset:8192
ds_read_b128 a[196:199], v49 offset:9216
v_cmp_gt_i32_e64 s[92:93], 8, v59
v_cndmask_b32_e64 v103, v103, v57, s[98:99]
v_exp_f32 v102, v102
v_add_f32 v52, v52, v101
v_cvt_pk_bf16_f32 v146, v100, v101
s_add_u32 s84, s44, s45
v_mfma_f32_32x32x16_bf16 v[64:79], a[228:231], a[132:135], v[64:79]
buffer_load_dwordx4 v32, s[52:55], s76 offen offset:2048 sc1 lds
ds_read_b128 a[200:203], v49 offset:10240
ds_read_b128 a[204:207], v49 offset:11264
v_cmp_gt_i32_e64 s[94:95], 9, v59
v_cndmask_b32_e64 v104, v104, v57, s[92:93]
v_exp_f32 v103, v103
v_add_f32 v52, v52, v102
s_add_u32 s83, s83, 16384
s_mul_i32 s79, s38, 5120
v_mfma_f32_32x32x16_bf16 v[64:79], a[232:235], a[136:139], v[64:79]
ds_read_b128 a[208:211], v49 offset:12288
ds_read_b128 a[212:215], v49 offset:13312
v_cmp_gt_i32_e64 s[96:97], 10, v59
v_cndmask_b32_e64 v105, v105, v57, s[94:95]
v_exp_f32 v104, v104
v_add_f32 v52, v52, v103
v_cvt_pk_bf16_f32 v147, v102, v103
s_cmp_eq_u32 s83, 163840
s_cselect_b32 s83, 0, s83
s_mul_i32 s44, s101, 1490944
v_mfma_f32_32x32x16_bf16 v[64:79], a[236:239], a[140:143], v[64:79]
buffer_load_dwordx4 v32, s[52:55], s76 offen offset:3072 sc1 lds
ds_read_b128 a[216:219], v49 offset:14336
ds_read_b128 a[220:223], v49 offset:15360
v_cmp_gt_i32_e64 s[98:99], 11, v59
v_cndmask_b32_e64 v106, v106, v57, s[96:97]
v_exp_f32 v105, v105
v_add_f32 v52, v52, v104
v_add_u32 v49, s83, v48
s_add_u32 s44, s44, 36192256
s_waitcnt lgkmcnt(8)
v_mfma_f32_32x32x16_bf16 v[64:79], a[240:243], a[144:147], v[64:79]
v_cmp_gt_i32_e64 s[92:93], 24, v59
v_cndmask_b32_e64 v107, v107, v57, s[98:99]
v_exp_f32 v106, v106
v_add_f32 v52, v52, v105
v_cvt_pk_bf16_f32 v148, v104, v105
s_add_u32 s76, s76, 8192
s_add_u32 s82, s82, 16384
s_add_u32 s72, s48, s44
s_addc_u32 s73, s49, 0
v_mfma_f32_32x32x16_bf16 v[64:79], a[244:247], a[148:151], v[64:79]
v_cmp_gt_i32_e64 s[94:95], 25, v59
v_cndmask_b32_e64 v108, v108, v57, s[92:93]
v_exp_f32 v107, v107
v_add_f32 v52, v52, v106
s_cmp_eq_u32 s82, 163840
s_cselect_b32 s82, 0, s82
v_mfma_f32_32x32x16_bf16 v[64:79], a[248:251], a[152:155], v[64:79]
v_cmp_gt_i32_e64 s[96:97], 26, v59
v_cndmask_b32_e64 v109, v109, v57, s[94:95]
v_exp_f32 v108, v108
v_add_f32 v52, v52, v107
v_cvt_pk_bf16_f32 v149, v106, v107
s_mov_b32 s74, 1490944
v_mfma_f32_32x32x16_bf16 v[64:79], a[252:255], a[156:159], v[64:79]
v_cmp_gt_i32_e64 s[98:99], 27, v59
v_cndmask_b32_e64 v110, v110, v57, s[96:97]
v_exp_f32 v109, v109
v_add_f32 v52, v52, v108
s_mov_b32 s75, 0x20000
s_branch L_hm1%=
L_lpa%=:
s_mov_b32 s78, 1
s_lshl_b32 s44, s41, 5
s_add_u32 s44, s44, s40
s_lshl_b32 s44, s44, 2
s_add_u32 s44, s44, 67913728
s_cmp_lg_u32 s38, 0
s_cbranch_scc1 L_pb6%=
v_mov_b32 v9, s42
v_mov_b32 v10, 0
buffer_store_dword v9, v10, s[48:51], s44 offen sc1
L_pb6%=:
s_branch L_lpba%=
L_lpb%=:
s_mov_b32 s78, 1
s_lshl_b32 s44, s41, 5
s_add_u32 s44, s44, s40
s_lshl_b32 s44, s44, 2
s_add_u32 s44, s44, 67913728
s_cmp_lg_u32 s38, 0
s_cbranch_scc1 L_pb7%=
v_mov_b32 v9, s42
v_mov_b32 v10, 0
buffer_store_dword v9, v10, s[48:51], s44 offen sc1
L_pb7%=:
s_branch L_lpbb%=
L_p3end%=:
s_lshl_b32 s44, s38, 2
s_add_u32 s44, s44, 163824
v_mov_b32 v9, s44
v_mov_b32 v10, s91
ds_write_b32 v9, v10
v_mov_b32 v9, 163824
s_waitcnt lgkmcnt(0)
s_barrier
ds_read_b128 v[12:15], v9
s_waitcnt lgkmcnt(0)
v_or_b32 v12, v12, v13
v_or_b32 v14, v14, v15
v_or_b32 v12, v12, v14
s_nop 1
v_readfirstlane_b32 s44, v12
s_barrier
s_cmp_lg_u32 s44, 0
s_cbranch_scc1 L_leave%=
s_waitcnt vmcnt(44)
s_add_u32 m0, s79, 102400
s_nop 0
buffer_load_dwordx4 v33, s[64:67], s86 offen lds
buffer_load_dwordx4 v34, s[64:67], s86 offen offset:1024 lds
buffer_load_dwordx4 v35, s[64:67], s86 offen offset:2048 lds
buffer_load_dwordx4 v36, s[64:67], s86 offen offset:3072 lds
s_add_u32 m0, s79, 106496
s_nop 0
buffer_load_dwordx4 v37, s[64:67], s86 offen lds
s_add_u32 s86, s86, 81920
s_add_u32 m0, s79, 122880
s_nop 0
buffer_load_dwordx4 v33, s[64:67], s86 offen lds
buffer_load_dwordx4 v34, s[64:67], s86 offen offset:1024 lds
buffer_load_dwordx4 v35, s[64:67], s86 offen offset:2048 lds
buffer_load_dwordx4 v36, s[64:67], s86 offen offset:3072 lds
s_add_u32 m0, s79, 126976
s_nop 0
buffer_load_dwordx4 v37, s[64:67], s86 offen lds
s_add_u32 s86, s86, 81920
s_add_u32 s85, s84, 8192
buffer_load_dwordx4 v[144:147], v32, s[72:75], s84 offen sc1
buffer_load_dwordx4 v[148:151], v32, s[72:75], s84 offen offset:1024 sc1
buffer_load_dwordx4 v[152:155], v32, s[72:75], s85 offen sc1
buffer_load_dwordx4 v[156:159], v32, s[72:75], s85 offen offset:1024 sc1
s_add_u32 s84, s84, 16384
L_p4go%=:
s_waitcnt vmcnt(40)
s_barrier
v_mov_b32 v39, v38
ds_read_b64_tr_b16 v[64:65], v39 offset:0
ds_read_b64_tr_b16 v[66:67], v39 offset:5120
ds_read_b64_tr_b16 v[68:69], v39 offset:256
ds_read_b64_tr_b16 v[70:71], v39 offset:5376
ds_read_b64_tr_b16 v[72:73], v39 offset:512
ds_read_b64_tr_b16 v[74:75], v39 offset:5632
ds_read_b64_tr_b16 v[76:77], v39 offset:768
ds_read_b64_tr_b16 v[78:79], v39 offset:5888
ds_read_b64_tr_b16 v[80:81], v39 offset:1024
ds_read_b64_tr_b16 v[82:83], v39 offset:6144
ds_read_b64_tr_b16 v[84:85], v39 offset:1280
ds_read_b64_tr_b16 v[86:87], v39 offset:6400
ds_read_b64_tr_b16 v[88:89], v39 offset:1536
ds_read_b64_tr_b16 v[90:91], v39 offset:6656
ds_read_b64_tr_b16 v[92:93], v39 offset:1792
ds_read_b64_tr_b16 v[94:95], v39 offset:6912
ds_read_b64_tr_b16 v[96:97], v39 offset:2048
ds_read_b64_tr_b16 v[98:99], v39 offset:7168
ds_read_b64_tr_b16 v[100:101], v39 offset:2304
ds_read_b64_tr_b16 v[102:103], v39 offset:7424
s_waitcnt lgkmcnt(0)
s_waitcnt vmcnt(18)
s_barrier
s_lshl_b32 s44, s41, 5
s_add_u32 s44, s44, s40
s_lshl_b32 s44, s44, 2
s_add_u32 s44, s44, 67914752
s_cmp_lg_u32 s38, 0
s_cbranch_scc1 L_pb12%=
v_mov_b32 v9, s42
v_mov_b32 v10, 0
buffer_store_dword v9, v10, s[48:51], s44 offen sc1
L_pb12%=:
s_waitcnt lgkmcnt(12)
v_mfma_f32_16x16x32_bf16 a[0:3], v[64:67], v[192:195], 0
ds_read_b64_tr_b16 v[104:105], v39 offset:10240
v_mfma_f32_16x16x32_bf16 a[4:7], v[68:71], v[192:195], 0
ds_read_b64_tr_b16 v[106:107], v39 offset:15360
s_mov_b64 s[64:65], s[68:69]
s_mov_b32 s66, s70
s_lshl_b32 s44, s100, 10
s_lshl_b32 s45, s38, 4
s_add_u32 s44, s44, s45
s_mul_i32 s86, s44, 1280
s_add_u32 m0, s79, 143360
v_mfma_f32_16x16x32_bf16 a[8:11], v[72:75], v[192:195], 0
buffer_load_dwordx4 v33, s[64:67], s86 offen lds
ds_read_b64_tr_b16 v[108:109], v39 offset:10496
v_mfma_f32_16x16x32_bf16 a[12:15], v[76:79], v[192:195], 0
ds_read_b64_tr_b16 v[110:111], v39 offset:15616
s_waitcnt lgkmcnt(10)
v_mfma_f32_16x16x32_bf16 a[16:19], v[80:83], v[192:195], 0
ds_read_b64_tr_b16 v[112:113], v39 offset:10752
v_mfma_f32_16x16x32_bf16 a[20:23], v[84:87], v[192:195], 0
ds_read_b64_tr_b16 v[114:115], v39 offset:15872
v_mfma_f32_16x16x32_bf16 a[24:27], v[88:91], v[192:195], 0
buffer_load_dwordx4 v34, s[64:67], s86 offen offset:1024 lds
ds_read_b64_tr_b16 v[116:117], v39 offset:11008
s_waitcnt lgkmcnt(7)
v_mfma_f32_16x16x32_bf16 a[28:31], v[92:95], v[192:195], 0
ds_read_b64_tr_b16 v[118:119], v39 offset:16128
v_mfma_f32_16x16x32_bf16 a[32:35], v[96:99], v[192:195], 0
ds_read_b64_tr_b16 v[120:121], v39 offset:11264
v_mfma_f32_16x16x32_bf16 a[36:39], v[100:103], v[192:195], 0
ds_read_b64_tr_b16 v[122:123], v39 offset:16384
v_mfma_f32_16x16x32_bf16 a[40:43], v[64:67], v[196:199], 0
buffer_load_dwordx4 v35, s[64:67], s86 offen offset:2048 lds
ds_read_b64_tr_b16 v[124:125], v39 offset:11520
v_mfma_f32_16x16x32_bf16 a[44:47], v[68:71], v[196:199], 0
ds_read_b64_tr_b16 v[126:127], v39 offset:16640
v_mfma_f32_16x16x32_bf16 a[48:51], v[72:75], v[196:199], 0
ds_read_b64_tr_b16 v[128:129], v39 offset:11776
v_mfma_f32_16x16x32_bf16 a[52:55], v[76:79], v[196:199], 0
ds_read_b64_tr_b16 v[130:131], v39 offset:16896
v_mfma_f32_16x16x32_bf16 a[56:59], v[80:83], v[196:199], 0
buffer_load_dwordx4 v36, s[64:67], s86 offen offset:3072 lds
ds_read_b64_tr_b16 v[132:133], v39 offset:12032
v_mfma_f32_16x16x32_bf16 a[60:63], v[84:87], v[196:199], 0
ds_read_b64_tr_b16 v[134:135], v39 offset:17152
v_mfma_f32_16x16x32_bf16 a[64:67], v[88:91], v[196:199], 0
ds_read_b64_tr_b16 v[136:137], v39 offset:12288
v_mfma_f32_16x16x32_bf16 a[68:71], v[92:95], v[196:199], 0
ds_read_b64_tr_b16 v[138:139], v39 offset:17408
s_add_u32 m0, s79, 147456
v_mfma_f32_16x16x32_bf16 a[72:75], v[96:99], v[196:199], 0
buffer_load_dwordx4 v37, s[64:67], s86 offen lds
ds_read_b64_tr_b16 v[140:141], v39 offset:12544
v_mfma_f32_16x16x32_bf16 a[76:79], v[100:103], v[196:199], 0
ds_read_b64_tr_b16 v[142:143], v39 offset:17664
s_add_u32 s86, s86, 81920
v_add_u32 v39, 20480, v38
s_waitcnt lgkmcnt(12)
v_mfma_f32_16x16x32_bf16 a[0:3], v[104:107], v[200:203], a[0:3]
ds_read_b64_tr_b16 v[64:65], v39 offset:0
v_mfma_f32_16x16x32_bf16 a[4:7], v[108:111], v[200:203], a[4:7]
ds_read_b64_tr_b16 v[66:67], v39 offset:5120
v_mfma_f32_16x16x32_bf16 a[8:11], v[112:115], v[200:203], a[8:11]
ds_read_b64_tr_b16 v[68:69], v39 offset:256
s_add_u32 s85, s84, 8192
v_mfma_f32_16x16x32_bf16 a[12:15], v[116:119], v[200:203], a[12:15]
buffer_load_dwordx4 v[160:163], v32, s[72:75], s84 offen sc1
ds_read_b64_tr_b16 v[70:71], v39 offset:5376
s_waitcnt lgkmcnt(10)
v_mfma_f32_16x16x32_bf16 a[16:19], v[120:123], v[200:203], a[16:19]
ds_read_b64_tr_b16 v[72:73], v39 offset:512
v_mfma_f32_16x16x32_bf16 a[20:23], v[124:127], v[200:203], a[20:23]
ds_read_b64_tr_b16 v[74:75], v39 offset:5632
v_mfma_f32_16x16x32_bf16 a[24:27], v[128:131], v[200:203], a[24:27]
ds_read_b64_tr_b16 v[76:77], v39 offset:768
s_waitcnt lgkmcnt(7)
v_mfma_f32_16x16x32_bf16 a[28:31], v[132:135], v[200:203], a[28:31]
ds_read_b64_tr_b16 v[78:79], v39 offset:5888
v_mfma_f32_16x16x32_bf16 a[32:35], v[136:139], v[200:203], a[32:35]
buffer_load_dwordx4 v[164:167], v32, s[72:75], s84 offen offset:1024 sc1
ds_read_b64_tr_b16 v[80:81], v39 offset:1024
v_mfma_f32_16x16x32_bf16 a[36:39], v[140:143], v[200:203], a[36:39]
ds_read_b64_tr_b16 v[82:83], v39 offset:6144
v_mfma_f32_16x16x32_bf16 a[40:43], v[104:107], v[204:207], a[40:43]
ds_read_b64_tr_b16 v[84:85], v39 offset:1280
v_mfma_f32_16x16x32_bf16 a[44:47], v[108:111], v[204:207], a[44:47]
ds_read_b64_tr_b16 v[86:87], v39 offset:6400
v_mfma_f32_16x16x32_bf16 a[48:51], v[112:115], v[204:207], a[48:51]
buffer_load_dwordx4 v[168:171], v32, s[72:75], s85 offen sc1
ds_read_b64_tr_b16 v[88:89], v39 offset:1536
v_mfma_f32_16x16x32_bf16 a[52:55], v[116:119], v[204:207], a[52:55]
ds_read_b64_tr_b16 v[90:91], v39 offset:6656
v_mfma_f32_16x16x32_bf16 a[56:59], v[120:123], v[204:207], a[56:59]
ds_read_b64_tr_b16 v[92:93], v39 offset:1792
v_mfma_f32_16x16x32_bf16 a[60:63], v[124:127], v[204:207], a[60:63]
ds_read_b64_tr_b16 v[94:95], v39 offset:6912
v_mfma_f32_16x16x32_bf16 a[64:67], v[128:131], v[204:207], a[64:67]
ds_read_b64_tr_b16 v[96:97], v39 offset:2048
v_mfma_f32_16x16x32_bf16 a[68:71], v[132:135], v[204:207], a[68:71]
buffer_load_dwordx4 v[172:175], v32, s[72:75], s85 offen offset:1024 sc1
ds_read_b64_tr_b16 v[98:99], v39 offset:7168
v_mfma_f32_16x16x32_bf16 a[72:75], v[136:139], v[204:207], a[72:75]
ds_read_b64_tr_b16 v[100:101], v39 offset:2304
s_add_u32 s84, s84, 16384
v_mfma_f32_16x16x32_bf16 a[76:79], v[140:143], v[204:207], a[76:79]
ds_read_b64_tr_b16 v[102:103], v39 offset:7424
s_waitcnt vmcnt(36)
s_barrier
s_waitcnt lgkmcnt(12)
v_mfma_f32_16x16x32_bf16 a[0:3], v[64:67], v[208:211], a[0:3]
ds_read_b64_tr_b16 v[104:105], v39 offset:10240
v_mfma_f32_16x16x32_bf16 a[4:7], v[68:71], v[208:211], a[4:7]
ds_read_b64_tr_b16 v[106:107], v39 offset:15360
s_add_u32 m0, s79, 0
v_mfma_f32_16x16x32_bf16 a[8:11], v[72:75], v[208:211], a[8:11]
buffer_load_dwordx4 v33, s[64:67], s86 offen lds
ds_read_b64_tr_b16 v[108:109], v39 offset:10496
v_mfma_f32_16x16x32_bf16 a[12:15], v[76:79], v[208:211], a[12:15]
ds_read_b64_tr_b16 v[110:111], v39 offset:15616
s_waitcnt lgkmcnt(10)
v_mfma_f32_16x16x32_bf16 a[16:19], v[80:83], v[208:211], a[16:19]
ds_read_b64_tr_b16 v[112:113], v39 offset:10752
v_mfma_f32_16x16x32_bf16 a[20:23], v[84:87], v[208:211], a[20:23]
ds_read_b64_tr_b16 v[114:115], v39 offset:15872
v_mfma_f32_16x16x32_bf16 a[24:27], v[88:91], v[208:211], a[24:27]
buffer_load_dwordx4 v34, s[64:67], s86 offen offset:1024 lds
ds_read_b64_tr_b16 v[116:117], v39 offset:11008
s_waitcnt lgkmcnt(7)
v_mfma_f32_16x16x32_bf16 a[28:31], v[92:95], v[208:211], a[28:31]
ds_read_b64_tr_b16 v[118:119], v39 offset:16128
v_mfma_f32_16x16x32_bf16 a[32:35], v[96:99], v[208:211], a[32:35]
ds_read_b64_tr_b16 v[120:121], v39 offset:11264
v_mfma_f32_16x16x32_bf16 a[36:39], v[100:103], v[208:211], a[36:39]
ds_read_b64_tr_b16 v[122:123], v39 offset:16384
v_mfma_f32_16x16x32_bf16 a[40:43], v[64:67], v[212:215], a[40:43]
buffer_load_dwordx4 v35, s[64:67], s86 offen offset:2048 lds
ds_read_b64_tr_b16 v[124:125], v39 offset:11520
v_mfma_f32_16x16x32_bf16 a[44:47], v[68:71], v[212:215], a[44:47]
ds_read_b64_tr_b16 v[126:127], v39 offset:16640
v_mfma_f32_16x16x32_bf16 a[48:51], v[72:75], v[212:215], a[48:51]
ds_read_b64_tr_b16 v[128:129], v39 offset:11776
v_mfma_f32_16x16x32_bf16 a[52:55], v[76:79], v[212:215], a[52:55]
ds_read_b64_tr_b16 v[130:131], v39 offset:16896
v_mfma_f32_16x16x32_bf16 a[56:59], v[80:83], v[212:215], a[56:59]
buffer_load_dwordx4 v36, s[64:67], s86 offen offset:3072 lds
ds_read_b64_tr_b16 v[132:133], v39 offset:12032
v_mfma_f32_16x16x32_bf16 a[60:63], v[84:87], v[212:215], a[60:63]
ds_read_b64_tr_b16 v[134:135], v39 offset:17152
v_mfma_f32_16x16x32_bf16 a[64:67], v[88:91], v[212:215], a[64:67]
ds_read_b64_tr_b16 v[136:137], v39 offset:12288
v_mfma_f32_16x16x32_bf16 a[68:71], v[92:95], v[212:215], a[68:71]
ds_read_b64_tr_b16 v[138:139], v39 offset:17408
s_add_u32 m0, s79, 4096
v_mfma_f32_16x16x32_bf16 a[72:75], v[96:99], v[212:215], a[72:75]
buffer_load_dwordx4 v37, s[64:67], s86 offen lds
ds_read_b64_tr_b16 v[140:141], v39 offset:12544
v_mfma_f32_16x16x32_bf16 a[76:79], v[100:103], v[212:215], a[76:79]
ds_read_b64_tr_b16 v[142:143], v39 offset:17664
s_add_u32 s86, s86, 81920
v_add_u32 v39, 40960, v38
s_waitcnt lgkmcnt(12)
v_mfma_f32_16x16x32_bf16 a[0:3], v[104:107], v[216:219], a[0:3]
ds_read_b64_tr_b16 v[64:65], v39 offset:0
v_mfma_f32_16x16x32_bf16 a[4:7], v[108:111], v[216:219], a[4:7]
ds_read_b64_tr_b16 v[66:67], v39 offset:5120
v_mfma_f32_16x16x32_bf16 a[8:11], v[112:115], v[216:219], a[8:11]
ds_read_b64_tr_b16 v[68:69], v39 offset:256
s_add_u32 s85, s84, 8192
v_mfma_f32_16x16x32_bf16 a[12:15], v[116:119], v[216:219], a[12:15]
buffer_load_dwordx4 v[192:195], v32, s[72:75], s84 offen sc1
ds_read_b64_tr_b16 v[70:71], v39 offset:5376
s_waitcnt lgkmcnt(10)
v_mfma_f32_16x16x32_bf16 a[16:19], v[120:123], v[216:219], a[16:19]
ds_read_b64_tr_b16 v[72:73], v39 offset:512
v_mfma_f32_16x16x32_bf16 a[20:23], v[124:127], v[216:219], a[20:23]
ds_read_b64_tr_b16 v[74:75], v39 offset:5632
v_mfma_f32_16x16x32_bf16 a[24:27], v[128:131], v[216:219], a[24:27]
ds_read_b64_tr_b16 v[76:77], v39 offset:768
s_waitcnt lgkmcnt(7)
v_mfma_f32_16x16x32_bf16 a[28:31], v[132:135], v[216:219], a[28:31]
ds_read_b64_tr_b16 v[78:79], v39 offset:5888
v_mfma_f32_16x16x32_bf16 a[32:35], v[136:139], v[216:219], a[32:35]
buffer_load_dwordx4 v[196:199], v32, s[72:75], s84 offen offset:1024 sc1
ds_read_b64_tr_b16 v[80:81], v39 offset:1024
v_mfma_f32_16x16x32_bf16 a[36:39], v[140:143], v[216:219], a[36:39]
ds_read_b64_tr_b16 v[82:83], v39 offset:6144
v_mfma_f32_16x16x32_bf16 a[40:43], v[104:107], v[220:223], a[40:43]
ds_read_b64_tr_b16 v[84:85], v39 offset:1280
v_mfma_f32_16x16x32_bf16 a[44:47], v[108:111], v[220:223], a[44:47]
ds_read_b64_tr_b16 v[86:87], v39 offset:6400
v_mfma_f32_16x16x32_bf16 a[48:51], v[112:115], v[220:223], a[48:51]
buffer_load_dwordx4 v[200:203], v32, s[72:75], s85 offen sc1
ds_read_b64_tr_b16 v[88:89], v39 offset:1536
v_mfma_f32_16x16x32_bf16 a[52:55], v[116:119], v[220:223], a[52:55]
ds_read_b64_tr_b16 v[90:91], v39 offset:6656
v_mfma_f32_16x16x32_bf16 a[56:59], v[120:123], v[220:223], a[56:59]
ds_read_b64_tr_b16 v[92:93], v39 offset:1792
v_mfma_f32_16x16x32_bf16 a[60:63], v[124:127], v[220:223], a[60:63]
ds_read_b64_tr_b16 v[94:95], v39 offset:6912
v_mfma_f32_16x16x32_bf16 a[64:67], v[128:131], v[220:223], a[64:67]
ds_read_b64_tr_b16 v[96:97], v39 offset:2048
v_mfma_f32_16x16x32_bf16 a[68:71], v[132:135], v[220:223], a[68:71]
buffer_load_dwordx4 v[204:207], v32, s[72:75], s85 offen offset:1024 sc1
ds_read_b64_tr_b16 v[98:99], v39 offset:7168
v_mfma_f32_16x16x32_bf16 a[72:75], v[136:139], v[220:223], a[72:75]
ds_read_b64_tr_b16 v[100:101], v39 offset:2304
s_add_u32 s84, s84, 16384
v_mfma_f32_16x16x32_bf16 a[76:79], v[140:143], v[220:223], a[76:79]
ds_read_b64_tr_b16 v[102:103], v39 offset:7424
s_waitcnt vmcnt(38)
s_barrier
s_waitcnt lgkmcnt(12)
v_mfma_f32_16x16x32_bf16 a[0:3], v[64:67], v[224:227], a[0:3]
ds_read_b64_tr_b16 v[104:105], v39 offset:10240
v_mfma_f32_16x16x32_bf16 a[4:7], v[68:71], v[224:227], a[4:7]
ds_read_b64_tr_b16 v[106:107], v39 offset:15360
s_add_u32 m0, s79, 20480
v_mfma_f32_16x16x32_bf16 a[8:11], v[72:75], v[224:227], a[8:11]
buffer_load_dwordx4 v33, s[64:67], s86 offen lds
ds_read_b64_tr_b16 v[108:109], v39 offset:10496
v_mfma_f32_16x16x32_bf16 a[12:15], v[76:79], v[224:227], a[12:15]
ds_read_b64_tr_b16 v[110:111], v39 offset:15616
s_waitcnt lgkmcnt(10)
v_mfma_f32_16x16x32_bf16 a[16:19], v[80:83], v[224:227], a[16:19]
ds_read_b64_tr_b16 v[112:113], v39 offset:10752
v_mfma_f32_16x16x32_bf16 a[20:23], v[84:87], v[224:227], a[20:23]
ds_read_b64_tr_b16 v[114:115], v39 offset:15872
v_mfma_f32_16x16x32_bf16 a[24:27], v[88:91], v[224:227], a[24:27]
buffer_load_dwordx4 v34, s[64:67], s86 offen offset:1024 lds
ds_read_b64_tr_b16 v[116:117], v39 offset:11008
s_waitcnt lgkmcnt(7)
v_mfma_f32_16x16x32_bf16 a[28:31], v[92:95], v[224:227], a[28:31]
ds_read_b64_tr_b16 v[118:119], v39 offset:16128
v_mfma_f32_16x16x32_bf16 a[32:35], v[96:99], v[224:227], a[32:35]
ds_read_b64_tr_b16 v[120:121], v39 offset:11264
v_mfma_f32_16x16x32_bf16 a[36:39], v[100:103], v[224:227], a[36:39]
ds_read_b64_tr_b16 v[122:123], v39 offset:16384
v_mfma_f32_16x16x32_bf16 a[40:43], v[64:67], v[228:231], a[40:43]
buffer_load_dwordx4 v35, s[64:67], s86 offen offset:2048 lds
ds_read_b64_tr_b16 v[124:125], v39 offset:11520
v_mfma_f32_16x16x32_bf16 a[44:47], v[68:71], v[228:231], a[44:47]
ds_read_b64_tr_b16 v[126:127], v39 offset:16640
v_mfma_f32_16x16x32_bf16 a[48:51], v[72:75], v[228:231], a[48:51]
ds_read_b64_tr_b16 v[128:129], v39 offset:11776
v_mfma_f32_16x16x32_bf16 a[52:55], v[76:79], v[228:231], a[52:55]
ds_read_b64_tr_b16 v[130:131], v39 offset:16896
v_mfma_f32_16x16x32_bf16 a[56:59], v[80:83], v[228:231], a[56:59]
buffer_load_dwordx4 v36, s[64:67], s86 offen offset:3072 lds
ds_read_b64_tr_b16 v[132:133], v39 offset:12032
v_mfma_f32_16x16x32_bf16 a[60:63], v[84:87], v[228:231], a[60:63]
ds_read_b64_tr_b16 v[134:135], v39 offset:17152
v_mfma_f32_16x16x32_bf16 a[64:67], v[88:91], v[228:231], a[64:67]
ds_read_b64_tr_b16 v[136:137], v39 offset:12288
v_mfma_f32_16x16x32_bf16 a[68:71], v[92:95], v[228:231], a[68:71]
ds_read_b64_tr_b16 v[138:139], v39 offset:17408
s_add_u32 m0, s79, 24576
v_mfma_f32_16x16x32_bf16 a[72:75], v[96:99], v[228:231], a[72:75]
buffer_load_dwordx4 v37, s[64:67], s86 offen lds
ds_read_b64_tr_b16 v[140:141], v39 offset:12544
v_mfma_f32_16x16x32_bf16 a[76:79], v[100:103], v[228:231], a[76:79]
ds_read_b64_tr_b16 v[142:143], v39 offset:17664
s_add_u32 s86, s86, 81920
v_add_u32 v39, 61440, v38
s_waitcnt lgkmcnt(12)
v_mfma_f32_16x16x32_bf16 a[0:3], v[104:107], v[232:235], a[0:3]
ds_read_b64_tr_b16 v[64:65], v39 offset:0
v_mfma_f32_16x16x32_bf16 a[4:7], v[108:111], v[232:235], a[4:7]
ds_read_b64_tr_b16 v[66:67], v39 offset:5120
v_mfma_f32_16x16x32_bf16 a[8:11], v[112:115], v[232:235], a[8:11]
ds_read_b64_tr_b16 v[68:69], v39 offset:256
v_mfma_f32_16x16x32_bf16 a[12:15], v[116:119], v[232:235], a[12:15]
ds_read_b64_tr_b16 v[70:71], v39 offset:5376
s_waitcnt lgkmcnt(10)
v_mfma_f32_16x16x32_bf16 a[16:19], v[120:123], v[232:235], a[16:19]
ds_read_b64_tr_b16 v[72:73], v39 offset:512
v_mfma_f32_16x16x32_bf16 a[20:23], v[124:127], v[232:235], a[20:23]
ds_read_b64_tr_b16 v[74:75], v39 offset:5632
v_mfma_f32_16x16x32_bf16 a[24:27], v[128:131], v[232:235], a[24:27]
ds_read_b64_tr_b16 v[76:77], v39 offset:768
s_waitcnt lgkmcnt(7)
v_mfma_f32_16x16x32_bf16 a[28:31], v[132:135], v[232:235], a[28:31]
ds_read_b64_tr_b16 v[78:79], v39 offset:5888
v_mfma_f32_16x16x32_bf16 a[32:35], v[136:139], v[232:235], a[32:35]
ds_read_b64_tr_b16 v[80:81], v39 offset:1024
v_mfma_f32_16x16x32_bf16 a[36:39], v[140:143], v[232:235], a[36:39]
ds_read_b64_tr_b16 v[82:83], v39 offset:6144
v_mfma_f32_16x16x32_bf16 a[40:43], v[104:107], v[236:239], a[40:43]
ds_read_b64_tr_b16 v[84:85], v39 offset:1280
v_mfma_f32_16x16x32_bf16 a[44:47], v[108:111], v[236:239], a[44:47]
ds_read_b64_tr_b16 v[86:87], v39 offset:6400
v_mfma_f32_16x16x32_bf16 a[48:51], v[112:115], v[236:239], a[48:51]
ds_read_b64_tr_b16 v[88:89], v39 offset:1536
v_mfma_f32_16x16x32_bf16 a[52:55], v[116:119], v[236:239], a[52:55]
ds_read_b64_tr_b16 v[90:91], v39 offset:6656
v_mfma_f32_16x16x32_bf16 a[56:59], v[120:123], v[236:239], a[56:59]
ds_read_b64_tr_b16 v[92:93], v39 offset:1792
v_mfma_f32_16x16x32_bf16 a[60:63], v[124:127], v[236:239], a[60:63]
ds_read_b64_tr_b16 v[94:95], v39 offset:6912
v_mfma_f32_16x16x32_bf16 a[64:67], v[128:131], v[236:239], a[64:67]
ds_read_b64_tr_b16 v[96:97], v39 offset:2048
v_mfma_f32_16x16x32_bf16 a[68:71], v[132:135], v[236:239], a[68:71]
ds_read_b64_tr_b16 v[98:99], v39 offset:7168
v_mfma_f32_16x16x32_bf16 a[72:75], v[136:139], v[236:239], a[72:75]
ds_read_b64_tr_b16 v[100:101], v39 offset:2304
v_mfma_f32_16x16x32_bf16 a[76:79], v[140:143], v[236:239], a[76:79]
ds_read_b64_tr_b16 v[102:103], v39 offset:7424
s_waitcnt vmcnt(37)
s_barrier
s_waitcnt lgkmcnt(12)
v_mfma_f32_16x16x32_bf16 a[0:3], v[64:67], v[240:243], a[0:3]
ds_read_b64_tr_b16 v[104:105], v39 offset:10240
v_mfma_f32_16x16x32_bf16 a[4:7], v[68:71], v[240:243], a[4:7]
ds_read_b64_tr_b16 v[106:107], v39 offset:15360
s_add_u32 m0, s79, 40960
v_mfma_f32_16x16x32_bf16 a[8:11], v[72:75], v[240:243], a[8:11]
buffer_load_dwordx4 v33, s[64:67], s86 offen lds
ds_read_b64_tr_b16 v[108:109], v39 offset:10496
v_mfma_f32_16x16x32_bf16 a[12:15], v[76:79], v[240:243], a[12:15]
ds_read_b64_tr_b16 v[110:111], v39 offset:15616
s_waitcnt lgkmcnt(10)
v_mfma_f32_16x16x32_bf16 a[16:19], v[80:83], v[240:243], a[16:19]
ds_read_b64_tr_b16 v[112:113], v39 offset:10752
v_mfma_f32_16x16x32_bf16 a[20:23], v[84:87], v[240:243], a[20:23]
ds_read_b64_tr_b16 v[114:115], v39 offset:15872
v_mfma_f32_16x16x32_bf16 a[24:27], v[88:91], v[240:243], a[24:27]
buffer_load_dwordx4 v34, s[64:67], s86 offen offset:1024 lds
ds_read_b64_tr_b16 v[116:117], v39 offset:11008
s_waitcnt lgkmcnt(7)
v_mfma_f32_16x16x32_bf16 a[28:31], v[92:95], v[240:243], a[28:31]
ds_read_b64_tr_b16 v[118:119], v39 offset:16128
v_mfma_f32_16x16x32_bf16 a[32:35], v[96:99], v[240:243], a[32:35]
ds_read_b64_tr_b16 v[120:121], v39 offset:11264
v_mfma_f32_16x16x32_bf16 a[36:39], v[100:103], v[240:243], a[36:39]
ds_read_b64_tr_b16 v[122:123], v39 offset:16384
v_mfma_f32_16x16x32_bf16 a[40:43], v[64:67], v[244:247], a[40:43]
buffer_load_dwordx4 v35, s[64:67], s86 offen offset:2048 lds
ds_read_b64_tr_b16 v[124:125], v39 offset:11520
v_mfma_f32_16x16x32_bf16 a[44:47], v[68:71], v[244:247], a[44:47]
ds_read_b64_tr_b16 v[126:127], v39 offset:16640
v_mfma_f32_16x16x32_bf16 a[48:51], v[72:75], v[244:247], a[48:51]
ds_read_b64_tr_b16 v[128:129], v39 offset:11776
v_mfma_f32_16x16x32_bf16 a[52:55], v[76:79], v[244:247], a[52:55]
ds_read_b64_tr_b16 v[130:131], v39 offset:16896
v_mfma_f32_16x16x32_bf16 a[56:59], v[80:83], v[244:247], a[56:59]
buffer_load_dwordx4 v36, s[64:67], s86 offen offset:3072 lds
ds_read_b64_tr_b16 v[132:133], v39 offset:12032
v_mfma_f32_16x16x32_bf16 a[60:63], v[84:87], v[244:247], a[60:63]
ds_read_b64_tr_b16 v[134:135], v39 offset:17152
v_mfma_f32_16x16x32_bf16 a[64:67], v[88:91], v[244:247], a[64:67]
ds_read_b64_tr_b16 v[136:137], v39 offset:12288
v_mfma_f32_16x16x32_bf16 a[68:71], v[92:95], v[244:247], a[68:71]
ds_read_b64_tr_b16 v[138:139], v39 offset:17408
s_add_u32 m0, s79, 45056
v_mfma_f32_16x16x32_bf16 a[72:75], v[96:99], v[244:247], a[72:75]
buffer_load_dwordx4 v37, s[64:67], s86 offen lds
ds_read_b64_tr_b16 v[140:141], v39 offset:12544
v_mfma_f32_16x16x32_bf16 a[76:79], v[100:103], v[244:247], a[76:79]
ds_read_b64_tr_b16 v[142:143], v39 offset:17664
s_add_u32 s86, s86, 81920
v_add_u32 v39, 81920, v38
s_waitcnt lgkmcnt(12)
v_mfma_f32_16x16x32_bf16 a[0:3], v[104:107], v[248:251], a[0:3]
ds_read_b64_tr_b16 v[64:65], v39 offset:0
v_mfma_f32_16x16x32_bf16 a[4:7], v[108:111], v[248:251], a[4:7]
ds_read_b64_tr_b16 v[66:67], v39 offset:5120
v_mfma_f32_16x16x32_bf16 a[8:11], v[112:115], v[248:251], a[8:11]
ds_read_b64_tr_b16 v[68:69], v39 offset:256
v_mfma_f32_16x16x32_bf16 a[12:15], v[116:119], v[248:251], a[12:15]
ds_read_b64_tr_b16 v[70:71], v39 offset:5376
s_waitcnt lgkmcnt(10)
v_mfma_f32_16x16x32_bf16 a[16:19], v[120:123], v[248:251], a[16:19]
ds_read_b64_tr_b16 v[72:73], v39 offset:512
v_mfma_f32_16x16x32_bf16 a[20:23], v[124:127], v[248:251], a[20:23]
ds_read_b64_tr_b16 v[74:75], v39 offset:5632
v_mfma_f32_16x16x32_bf16 a[24:27], v[128:131], v[248:251], a[24:27]
ds_read_b64_tr_b16 v[76:77], v39 offset:768
s_waitcnt lgkmcnt(7)
v_mfma_f32_16x16x32_bf16 a[28:31], v[132:135], v[248:251], a[28:31]
ds_read_b64_tr_b16 v[78:79], v39 offset:5888
v_mfma_f32_16x16x32_bf16 a[32:35], v[136:139], v[248:251], a[32:35]
ds_read_b64_tr_b16 v[80:81], v39 offset:1024
v_mfma_f32_16x16x32_bf16 a[36:39], v[140:143], v[248:251], a[36:39]
ds_read_b64_tr_b16 v[82:83], v39 offset:6144
v_mfma_f32_16x16x32_bf16 a[40:43], v[104:107], v[252:255], a[40:43]
ds_read_b64_tr_b16 v[84:85], v39 offset:1280
v_mfma_f32_16x16x32_bf16 a[44:47], v[108:111], v[252:255], a[44:47]
ds_read_b64_tr_b16 v[86:87], v39 offset:6400
v_mfma_f32_16x16x32_bf16 a[48:51], v[112:115], v[252:255], a[48:51]
ds_read_b64_tr_b16 v[88:89], v39 offset:1536
v_mfma_f32_16x16x32_bf16 a[52:55], v[116:119], v[252:255], a[52:55]
ds_read_b64_tr_b16 v[90:91], v39 offset:6656
v_mfma_f32_16x16x32_bf16 a[56:59], v[120:123], v[252:255], a[56:59]
ds_read_b64_tr_b16 v[92:93], v39 offset:1792
v_mfma_f32_16x16x32_bf16 a[60:63], v[124:127], v[252:255], a[60:63]
ds_read_b64_tr_b16 v[94:95], v39 offset:6912
v_mfma_f32_16x16x32_bf16 a[64:67], v[128:131], v[252:255], a[64:67]
ds_read_b64_tr_b16 v[96:97], v39 offset:2048
v_mfma_f32_16x16x32_bf16 a[68:71], v[132:135], v[252:255], a[68:71]
ds_read_b64_tr_b16 v[98:99], v39 offset:7168
v_mfma_f32_16x16x32_bf16 a[72:75], v[136:139], v[252:255], a[72:75]
ds_read_b64_tr_b16 v[100:101], v39 offset:2304
v_mfma_f32_16x16x32_bf16 a[76:79], v[140:143], v[252:255], a[76:79]
ds_read_b64_tr_b16 v[102:103], v39 offset:7424
s_waitcnt vmcnt(28)
s_barrier
s_waitcnt lgkmcnt(12)
v_mfma_f32_16x16x32_bf16 a[0:3], v[64:67], v[144:147], a[0:3]
ds_read_b64_tr_b16 v[104:105], v39 offset:10240
v_mfma_f32_16x16x32_bf16 a[4:7], v[68:71], v[144:147], a[4:7]
ds_read_b64_tr_b16 v[106:107], v39 offset:15360
s_add_u32 m0, s79, 61440
v_mfma_f32_16x16x32_bf16 a[8:11], v[72:75], v[144:147], a[8:11]
buffer_load_dwordx4 v33, s[64:67], s86 offen lds
ds_read_b64_tr_b16 v[108:109], v39 offset:10496
v_mfma_f32_16x16x32_bf16 a[12:15], v[76:79], v[144:147], a[12:15]
ds_read_b64_tr_b16 v[110:111], v39 offset:15616
s_waitcnt lgkmcnt(10)
v_mfma_f32_16x16x32_bf16 a[16:19], v[80:83], v[144:147], a[16:19]
ds_read_b64_tr_b16 v[112:113], v39 offset:10752
v_mfma_f32_16x16x32_bf16 a[20:23], v[84:87], v[144:147], a[20:23]
ds_read_b64_tr_b16 v[114:115], v39 offset:15872
v_mfma_f32_16x16x32_bf16 a[24:27], v[88:91], v[144:147], a[24:27]
buffer_load_dwordx4 v34, s[64:67], s86 offen offset:1024 lds
ds_read_b64_tr_b16 v[116:117], v39 offset:11008
s_waitcnt lgkmcnt(7)
v_mfma_f32_16x16x32_bf16 a[28:31], v[92:95], v[144:147], a[28:31]
ds_read_b64_tr_b16 v[118:119], v39 offset:16128
v_mfma_f32_16x16x32_bf16 a[32:35], v[96:99], v[144:147], a[32:35]
ds_read_b64_tr_b16 v[120:121], v39 offset:11264
v_mfma_f32_16x16x32_bf16 a[36:39], v[100:103], v[144:147], a[36:39]
ds_read_b64_tr_b16 v[122:123], v39 offset:16384
v_mfma_f32_16x16x32_bf16 a[40:43], v[64:67], v[148:151], a[40:43]
buffer_load_dwordx4 v35, s[64:67], s86 offen offset:2048 lds
ds_read_b64_tr_b16 v[124:125], v39 offset:11520
v_mfma_f32_16x16x32_bf16 a[44:47], v[68:71], v[148:151], a[44:47]
ds_read_b64_tr_b16 v[126:127], v39 offset:16640
v_mfma_f32_16x16x32_bf16 a[48:51], v[72:75], v[148:151], a[48:51]
ds_read_b64_tr_b16 v[128:129], v39 offset:11776
v_mfma_f32_16x16x32_bf16 a[52:55], v[76:79], v[148:151], a[52:55]
ds_read_b64_tr_b16 v[130:131], v39 offset:16896
v_mfma_f32_16x16x32_bf16 a[56:59], v[80:83], v[148:151], a[56:59]
buffer_load_dwordx4 v36, s[64:67], s86 offen offset:3072 lds
ds_read_b64_tr_b16 v[132:133], v39 offset:12032
v_mfma_f32_16x16x32_bf16 a[60:63], v[84:87], v[148:151], a[60:63]
ds_read_b64_tr_b16 v[134:135], v39 offset:17152
v_mfma_f32_16x16x32_bf16 a[64:67], v[88:91], v[148:151], a[64:67]
ds_read_b64_tr_b16 v[136:137], v39 offset:12288
v_mfma_f32_16x16x32_bf16 a[68:71], v[92:95], v[148:151], a[68:71]
ds_read_b64_tr_b16 v[138:139], v39 offset:17408
s_add_u32 m0, s79, 65536
v_mfma_f32_16x16x32_bf16 a[72:75], v[96:99], v[148:151], a[72:75]
buffer_load_dwordx4 v37, s[64:67], s86 offen lds
ds_read_b64_tr_b16 v[140:141], v39 offset:12544
v_mfma_f32_16x16x32_bf16 a[76:79], v[100:103], v[148:151], a[76:79]
ds_read_b64_tr_b16 v[142:143], v39 offset:17664
s_add_u32 s86, s86, 81920
v_add_u32 v39, 102400, v38
s_waitcnt lgkmcnt(12)
v_mfma_f32_16x16x32_bf16 a[0:3], v[104:107], v[152:155], a[0:3]
ds_read_b64_tr_b16 v[64:65], v39 offset:0
v_mfma_f32_16x16x32_bf16 a[4:7], v[108:111], v[152:155], a[4:7]
ds_read_b64_tr_b16 v[66:67], v39 offset:5120
v_mfma_f32_16x16x32_bf16 a[8:11], v[112:115], v[152:155], a[8:11]
ds_read_b64_tr_b16 v[68:69], v39 offset:256
v_mfma_f32_16x16x32_bf16 a[12:15], v[116:119], v[152:155], a[12:15]
ds_read_b64_tr_b16 v[70:71], v39 offset:5376
s_waitcnt lgkmcnt(10)
v_mfma_f32_16x16x32_bf16 a[16:19], v[120:123], v[152:155], a[16:19]
ds_read_b64_tr_b16 v[72:73], v39 offset:512
v_mfma_f32_16x16x32_bf16 a[20:23], v[124:127], v[152:155], a[20:23]
ds_read_b64_tr_b16 v[74:75], v39 offset:5632
v_mfma_f32_16x16x32_bf16 a[24:27], v[128:131], v[152:155], a[24:27]
ds_read_b64_tr_b16 v[76:77], v39 offset:768
s_waitcnt lgkmcnt(7)
v_mfma_f32_16x16x32_bf16 a[28:31], v[132:135], v[152:155], a[28:31]
ds_read_b64_tr_b16 v[78:79], v39 offset:5888
v_mfma_f32_16x16x32_bf16 a[32:35], v[136:139], v[152:155], a[32:35]
ds_read_b64_tr_b16 v[80:81], v39 offset:1024
v_mfma_f32_16x16x32_bf16 a[36:39], v[140:143], v[152:155], a[36:39]
ds_read_b64_tr_b16 v[82:83], v39 offset:6144
v_mfma_f32_16x16x32_bf16 a[40:43], v[104:107], v[156:159], a[40:43]
ds_read_b64_tr_b16 v[84:85], v39 offset:1280
v_mfma_f32_16x16x32_bf16 a[44:47], v[108:111], v[156:159], a[44:47]
ds_read_b64_tr_b16 v[86:87], v39 offset:6400
v_mfma_f32_16x16x32_bf16 a[48:51], v[112:115], v[156:159], a[48:51]
ds_read_b64_tr_b16 v[88:89], v39 offset:1536
v_mfma_f32_16x16x32_bf16 a[52:55], v[116:119], v[156:159], a[52:55]
ds_read_b64_tr_b16 v[90:91], v39 offset:6656
v_mfma_f32_16x16x32_bf16 a[56:59], v[120:123], v[156:159], a[56:59]
ds_read_b64_tr_b16 v[92:93], v39 offset:1792
v_mfma_f32_16x16x32_bf16 a[60:63], v[124:127], v[156:159], a[60:63]
ds_read_b64_tr_b16 v[94:95], v39 offset:6912
v_mfma_f32_16x16x32_bf16 a[64:67], v[128:131], v[156:159], a[64:67]
ds_read_b64_tr_b16 v[96:97], v39 offset:2048
v_mfma_f32_16x16x32_bf16 a[68:71], v[132:135], v[156:159], a[68:71]
ds_read_b64_tr_b16 v[98:99], v39 offset:7168
v_mfma_f32_16x16x32_bf16 a[72:75], v[136:139], v[156:159], a[72:75]
ds_read_b64_tr_b16 v[100:101], v39 offset:2304
v_mfma_f32_16x16x32_bf16 a[76:79], v[140:143], v[156:159], a[76:79]
ds_read_b64_tr_b16 v[102:103], v39 offset:7424
s_waitcnt vmcnt(24)
s_sub_u32 s44, 15, s101
s_min_u32 s44, s44, s101
s_lshl_b32 s44, s44, 7
s_lshl_b32 s45, s100, 5
s_add_u32 s44, s44, s45
s_add_u32 s44, s44, 67914752
v_and_b32 v9, 7, v8
v_lshlrev_b32 v9, 2, v9
s_cmp_lg_u32 s38, 0
s_cbranch_scc1 L_pd13%=
s_mov_b32 s45, 3000000
L_pl13%=:
buffer_load_dword v10, v9, s[48:51], s44 offen sc1
s_waitcnt vmcnt(0)
v_cmp_ne_u32 vcc, s42, v10
s_nop 3
s_cmp_eq_u64 vcc, 0
s_cbranch_scc1 L_pd13%=
s_sleep 1
s_sub_u32 s45, s45, 1
s_cmp_lg_u32 s45, 0
s_cbranch_scc1 L_pl13%=
L_pd13%=:
s_barrier
s_waitcnt lgkmcnt(12)
v_mfma_f32_16x16x32_bf16 a[0:3], v[64:67], v[160:163], a[0:3]
ds_read_b64_tr_b16 v[104:105], v39 offset:10240
v_mfma_f32_16x16x32_bf16 a[4:7], v[68:71], v[160:163], a[4:7]
ds_read_b64_tr_b16 v[106:107], v39 offset:15360
s_add_u32 m0, s79, 81920
v_mfma_f32_16x16x32_bf16 a[8:11], v[72:75], v[160:163], a[8:11]
buffer_load_dwordx4 v33, s[64:67], s86 offen lds
ds_read_b64_tr_b16 v[108:109], v39 offset:10496
v_mfma_f32_16x16x32_bf16 a[12:15], v[76:79], v[160:163], a[12:15]
ds_read_b64_tr_b16 v[110:111], v39 offset:15616
s_waitcnt lgkmcnt(10)
v_mfma_f32_16x16x32_bf16 a[16:19], v[80:83], v[160:163], a[16:19]
ds_read_b64_tr_b16 v[112:113], v39 offset:10752
v_mfma_f32_16x16x32_bf16 a[20:23], v[84:87], v[160:163], a[20:23]
ds_read_b64_tr_b16 v[114:115], v39 offset:15872
v_mfma_f32_16x16x32_bf16 a[24:27], v[88:91], v[160:163], a[24:27]
buffer_load_dwordx4 v34, s[64:67], s86 offen offset:1024 lds
ds_read_b64_tr_b16 v[116:117], v39 offset:11008
s_waitcnt lgkmcnt(7)
v_mfma_f32_16x16x32_bf16 a[28:31], v[92:95], v[160:163], a[28:31]
ds_read_b64_tr_b16 v[118:119], v39 offset:16128
v_mfma_f32_16x16x32_bf16 a[32:35], v[96:99], v[160:163], a[32:35]
ds_read_b64_tr_b16 v[120:121], v39 offset:11264
v_mfma_f32_16x16x32_bf16 a[36:39], v[100:103], v[160:163], a[36:39]
ds_read_b64_tr_b16 v[122:123], v39 offset:16384
v_mfma_f32_16x16x32_bf16 a[40:43], v[64:67], v[164:167], a[40:43]
buffer_load_dwordx4 v35, s[64:67], s86 offen offset:2048 lds
ds_read_b64_tr_b16 v[124:125], v39 offset:11520
v_mfma_f32_16x16x32_bf16 a[44:47], v[68:71], v[164:167], a[44:47]
ds_read_b64_tr_b16 v[126:127], v39 offset:16640
v_mfma_f32_16x16x32_bf16 a[48:51], v[72:75], v[164:167], a[48:51]
ds_read_b64_tr_b16 v[128:129], v39 offset:11776
v_mfma_f32_16x16x32_bf16 a[52:55], v[76:79], v[164:167], a[52:55]
ds_read_b64_tr_b16 v[130:131], v39 offset:16896
v_mfma_f32_16x16x32_bf16 a[56:59], v[80:83], v[164:167], a[56:59]
buffer_load_dwordx4 v36, s[64:67], s86 offen offset:3072 lds
ds_read_b64_tr_b16 v[132:133], v39 offset:12032
v_mfma_f32_16x16x32_bf16 a[60:63], v[84:87], v[164:167], a[60:63]
ds_read_b64_tr_b16 v[134:135], v39 offset:17152
v_mfma_f32_16x16x32_bf16 a[64:67], v[88:91], v[164:167], a[64:67]
ds_read_b64_tr_b16 v[136:137], v39 offset:12288
v_mfma_f32_16x16x32_bf16 a[68:71], v[92:95], v[164:167], a[68:71]
ds_read_b64_tr_b16 v[138:139], v39 offset:17408
s_add_u32 m0, s79, 86016
v_mfma_f32_16x16x32_bf16 a[72:75], v[96:99], v[164:167], a[72:75]
buffer_load_dwordx4 v37, s[64:67], s86 offen lds
ds_read_b64_tr_b16 v[140:141], v39 offset:12544
v_mfma_f32_16x16x32_bf16 a[76:79], v[100:103], v[164:167], a[76:79]
ds_read_b64_tr_b16 v[142:143], v39 offset:17664
s_add_u32 s86, s86, 81920
v_add_u32 v39, 122880, v38
s_waitcnt lgkmcnt(12)
v_mfma_f32_16x16x32_bf16 a[0:3], v[104:107], v[168:171], a[0:3]
ds_read_b64_tr_b16 v[64:65], v39 offset:0
v_mfma_f32_16x16x32_bf16 a[4:7], v[108:111], v[168:171], a[4:7]
ds_read_b64_tr_b16 v[66:67], v39 offset:5120
v_mfma_f32_16x16x32_bf16 a[8:11], v[112:115], v[168:171], a[8:11]
ds_read_b64_tr_b16 v[68:69], v39 offset:256
s_lshl_b32 s44, s100, 18
s_lshl_b32 s45, s38, 11
v_mfma_f32_16x16x32_bf16 a[12:15], v[116:119], v[168:171], a[12:15]
ds_read_b64_tr_b16 v[70:71], v39 offset:5376
s_add_u32 s84, s44, s45
s_waitcnt lgkmcnt(10)
v_mfma_f32_16x16x32_bf16 a[16:19], v[120:123], v[168:171], a[16:19]
buffer_load_dwordx4 v[208:211], v32, s[72:75], s84 offen sc1
ds_read_b64_tr_b16 v[72:73], v39 offset:512
s_add_u32 s85, s84, 8192
v_mfma_f32_16x16x32_bf16 a[20:23], v[124:127], v[168:171], a[20:23]
buffer_load_dwordx4 v[212:215], v32, s[72:75], s84 offen offset:1024 sc1
ds_read_b64_tr_b16 v[74:75], v39 offset:5632
v_mfma_f32_16x16x32_bf16 a[24:27], v[128:131], v[168:171], a[24:27]
buffer_load_dwordx4 v[216:219], v32, s[72:75], s85 offen sc1
buffer_load_dwordx4 v[220:223], v32, s[72:75], s85 offen offset:1024 sc1
ds_read_b64_tr_b16 v[76:77], v39 offset:768
s_waitcnt lgkmcnt(7)
v_mfma_f32_16x16x32_bf16 a[28:31], v[132:135], v[168:171], a[28:31]
ds_read_b64_tr_b16 v[78:79], v39 offset:5888
s_add_u32 s84, s84, 16384
v_mfma_f32_16x16x32_bf16 a[32:35], v[136:139], v[168:171], a[32:35]
buffer_load_dwordx4 v[224:227], v32, s[72:75], s84 offen sc1
ds_read_b64_tr_b16 v[80:81], v39 offset:1024
s_add_u32 s85, s84, 8192
v_mfma_f32_16x16x32_bf16 a[36:39], v[140:143], v[168:171], a[36:39]
buffer_load_dwordx4 v[228:231], v32, s[72:75], s84 offen offset:1024 sc1
ds_read_b64_tr_b16 v[82:83], v39 offset:6144
v_mfma_f32_16x16x32_bf16 a[40:43], v[104:107], v[172:175], a[40:43]
buffer_load_dwordx4 v[232:235], v32, s[72:75], s85 offen sc1
buffer_load_dwordx4 v[236:239], v32, s[72:75], s85 offen offset:1024 sc1
ds_read_b64_tr_b16 v[84:85], v39 offset:1280
v_mfma_f32_16x16x32_bf16 a[44:47], v[108:111], v[172:175], a[44:47]
ds_read_b64_tr_b16 v[86:87], v39 offset:6400
s_add_u32 s84, s84, 16384
v_mfma_f32_16x16x32_bf16 a[48:51], v[112:115], v[172:175], a[48:51]
buffer_load_dwordx4 v[240:243], v32, s[72:75], s84 offen sc1
ds_read_b64_tr_b16 v[88:89], v39 offset:1536
s_add_u32 s85, s84, 8192
v_mfma_f32_16x16x32_bf16 a[52:55], v[116:119], v[172:175], a[52:55]
buffer_load_dwordx4 v[244:247], v32, s[72:75], s84 offen offset:1024 sc1
ds_read_b64_tr_b16 v[90:91], v39 offset:6656
v_mfma_f32_16x16x32_bf16 a[56:59], v[120:123], v[172:175], a[56:59]
buffer_load_dwordx4 v[248:251], v32, s[72:75], s85 offen sc1
buffer_load_dwordx4 v[252:255], v32, s[72:75], s85 offen offset:1024 sc1
ds_read_b64_tr_b16 v[92:93], v39 offset:1792
v_mfma_f32_16x16x32_bf16 a[60:63], v[124:127], v[172:175], a[60:63]
ds_read_b64_tr_b16 v[94:95], v39 offset:6912
s_add_u32 s84, s84, 16384
v_mfma_f32_16x16x32_bf16 a[64:67], v[128:131], v[172:175], a[64:67]
buffer_load_dwordx4 v[144:147], v32, s[72:75], s84 offen sc1
ds_read_b64_tr_b16 v[96:97], v39 offset:2048
s_add_u32 s85, s84, 8192
v_mfma_f32_16x16x32_bf16 a[68:71], v[132:135], v[172:175], a[68:71]
buffer_load_dwordx4 v[148:151], v32, s[72:75], s84 offen offset:1024 sc1
ds_read_b64_tr_b16 v[98:99], v39 offset:7168
v_mfma_f32_16x16x32_bf16 a[72:75], v[136:139], v[172:175], a[72:75]
buffer_load_dwordx4 v[152:155], v32, s[72:75], s85 offen sc1
buffer_load_dwordx4 v[156:159], v32, s[72:75], s85 offen offset:1024 sc1
ds_read_b64_tr_b16 v[100:101], v39 offset:2304
v_mfma_f32_16x16x32_bf16 a[76:79], v[140:143], v[172:175], a[76:79]
ds_read_b64_tr_b16 v[102:103], v39 offset:7424
s_add_u32 s84, s84, 16384
s_waitcnt vmcnt(36)
s_barrier
s_waitcnt lgkmcnt(12)
v_mfma_f32_16x16x32_bf16 a[0:3], v[64:67], v[192:195], a[0:3]
ds_read_b64_tr_b16 v[104:105], v39 offset:10240
v_mfma_f32_16x16x32_bf16 a[4:7], v[68:71], v[192:195], a[4:7]
ds_read_b64_tr_b16 v[106:107], v39 offset:15360
s_add_u32 m0, s79, 102400
v_mfma_f32_16x16x32_bf16 a[8:11], v[72:75], v[192:195], a[8:11]
buffer_load_dwordx4 v33, s[64:67], s86 offen lds
ds_read_b64_tr_b16 v[108:109], v39 offset:10496
v_mfma_f32_16x16x32_bf16 a[12:15], v[76:79], v[192:195], a[12:15]
ds_read_b64_tr_b16 v[110:111], v39 offset:15616
s_waitcnt lgkmcnt(10)
v_mfma_f32_16x16x32_bf16 a[16:19], v[80:83], v[192:195], a[16:19]
ds_read_b64_tr_b16 v[112:113], v39 offset:10752
v_mfma_f32_16x16x32_bf16 a[20:23], v[84:87], v[192:195], a[20:23]
ds_read_b64_tr_b16 v[114:115], v39 offset:15872
v_mfma_f32_16x16x32_bf16 a[24:27], v[88:91], v[192:195], a[24:27]
buffer_load_dwordx4 v34, s[64:67], s86 offen offset:1024 lds
ds_read_b64_tr_b16 v[116:117], v39 offset:11008
s_waitcnt lgkmcnt(7)
v_mfma_f32_16x16x32_bf16 a[28:31], v[92:95], v[192:195], a[28:31]
ds_read_b64_tr_b16 v[118:119], v39 offset:16128
v_mfma_f32_16x16x32_bf16 a[32:35], v[96:99], v[192:195], a[32:35]
ds_read_b64_tr_b16 v[120:121], v39 offset:11264
v_mfma_f32_16x16x32_bf16 a[36:39], v[100:103], v[192:195], a[36:39]
ds_read_b64_tr_b16 v[122:123], v39 offset:16384
v_mfma_f32_16x16x32_bf16 a[40:43], v[64:67], v[196:199], a[40:43]
buffer_load_dwordx4 v35, s[64:67], s86 offen offset:2048 lds
ds_read_b64_tr_b16 v[124:125], v39 offset:11520
v_mfma_f32_16x16x32_bf16 a[44:47], v[68:71], v[196:199], a[44:47]
ds_read_b64_tr_b16 v[126:127], v39 offset:16640
v_mfma_f32_16x16x32_bf16 a[48:51], v[72:75], v[196:199], a[48:51]
ds_read_b64_tr_b16 v[128:129], v39 offset:11776
v_mfma_f32_16x16x32_bf16 a[52:55], v[76:79], v[196:199], a[52:55]
ds_read_b64_tr_b16 v[130:131], v39 offset:16896
v_mfma_f32_16x16x32_bf16 a[56:59], v[80:83], v[196:199], a[56:59]
buffer_load_dwordx4 v36, s[64:67], s86 offen offset:3072 lds
ds_read_b64_tr_b16 v[132:133], v39 offset:12032
v_mfma_f32_16x16x32_bf16 a[60:63], v[84:87], v[196:199], a[60:63]
ds_read_b64_tr_b16 v[134:135], v39 offset:17152
v_mfma_f32_16x16x32_bf16 a[64:67], v[88:91], v[196:199], a[64:67]
ds_read_b64_tr_b16 v[136:137], v39 offset:12288
v_mfma_f32_16x16x32_bf16 a[68:71], v[92:95], v[196:199], a[68:71]
ds_read_b64_tr_b16 v[138:139], v39 offset:17408
s_add_u32 m0, s79, 106496
v_mfma_f32_16x16x32_bf16 a[72:75], v[96:99], v[196:199], a[72:75]
buffer_load_dwordx4 v37, s[64:67], s86 offen lds
ds_read_b64_tr_b16 v[140:141], v39 offset:12544
v_mfma_f32_16x16x32_bf16 a[76:79], v[100:103], v[196:199], a[76:79]
ds_read_b64_tr_b16 v[142:143], v39 offset:17664
s_add_u32 s86, s86, 81920
v_add_u32 v39, 143360, v38
s_waitcnt lgkmcnt(12)
v_mfma_f32_16x16x32_bf16 a[0:3], v[104:107], v[200:203], a[0:3]
ds_read_b64_tr_b16 v[64:65], v39 offset:0
v_mfma_f32_16x16x32_bf16 a[4:7], v[108:111], v[200:203], a[4:7]
ds_read_b64_tr_b16 v[66:67], v39 offset:5120
v_mfma_f32_16x16x32_bf16 a[8:11], v[112:115], v[200:203], a[8:11]
ds_read_b64_tr_b16 v[68:69], v39 offset:256
s_add_u32 s85, s84, 8192
v_mfma_f32_16x16x32_bf16 a[12:15], v[116:119], v[200:203], a[12:15]
buffer_load_dwordx4 v[160:163], v32, s[72:75], s84 offen sc1
ds_read_b64_tr_b16 v[70:71], v39 offset:5376
s_waitcnt lgkmcnt(10)
v_mfma_f32_16x16x32_bf16 a[16:19], v[120:123], v[200:203], a[16:19]
ds_read_b64_tr_b16 v[72:73], v39 offset:512
v_mfma_f32_16x16x32_bf16 a[20:23], v[124:127], v[200:203], a[20:23]
ds_read_b64_tr_b16 v[74:75], v39 offset:5632
v_mfma_f32_16x16x32_bf16 a[24:27], v[128:131], v[200:203], a[24:27]
ds_read_b64_tr_b16 v[76:77], v39 offset:768
s_waitcnt lgkmcnt(7)
v_mfma_f32_16x16x32_bf16 a[28:31], v[132:135], v[200:203], a[28:31]
ds_read_b64_tr_b16 v[78:79], v39 offset:5888
v_mfma_f32_16x16x32_bf16 a[32:35], v[136:139], v[200:203], a[32:35]
buffer_load_dwordx4 v[164:167], v32, s[72:75], s84 offen offset:1024 sc1
ds_read_b64_tr_b16 v[80:81], v39 offset:1024
v_mfma_f32_16x16x32_bf16 a[36:39], v[140:143], v[200:203], a[36:39]
ds_read_b64_tr_b16 v[82:83], v39 offset:6144
v_mfma_f32_16x16x32_bf16 a[40:43], v[104:107], v[204:207], a[40:43]
ds_read_b64_tr_b16 v[84:85], v39 offset:1280
v_mfma_f32_16x16x32_bf16 a[44:47], v[108:111], v[204:207], a[44:47]
ds_read_b64_tr_b16 v[86:87], v39 offset:6400
v_mfma_f32_16x16x32_bf16 a[48:51], v[112:115], v[204:207], a[48:51]
buffer_load_dwordx4 v[168:171], v32, s[72:75], s85 offen sc1
ds_read_b64_tr_b16 v[88:89], v39 offset:1536
v_mfma_f32_16x16x32_bf16 a[52:55], v[116:119], v[204:207], a[52:55]
ds_read_b64_tr_b16 v[90:91], v39 offset:6656
v_mfma_f32_16x16x32_bf16 a[56:59], v[120:123], v[204:207], a[56:59]
ds_read_b64_tr_b16 v[92:93], v39 offset:1792
v_mfma_f32_16x16x32_bf16 a[60:63], v[124:127], v[204:207], a[60:63]
ds_read_b64_tr_b16 v[94:95], v39 offset:6912
v_mfma_f32_16x16x32_bf16 a[64:67], v[128:131], v[204:207], a[64:67]
ds_read_b64_tr_b16 v[96:97], v39 offset:2048
v_mfma_f32_16x16x32_bf16 a[68:71], v[132:135], v[204:207], a[68:71]
buffer_load_dwordx4 v[172:175], v32, s[72:75], s85 offen offset:1024 sc1
ds_read_b64_tr_b16 v[98:99], v39 offset:7168
v_mfma_f32_16x16x32_bf16 a[72:75], v[136:139], v[204:207], a[72:75]
ds_read_b64_tr_b16 v[100:101], v39 offset:2304
s_add_u32 s84, s84, 16384
v_mfma_f32_16x16x32_bf16 a[76:79], v[140:143], v[204:207], a[76:79]
ds_read_b64_tr_b16 v[102:103], v39 offset:7424
s_waitcnt vmcnt(21)
s_barrier
s_waitcnt lgkmcnt(12)
v_mfma_f32_16x16x32_bf16 a[0:3], v[64:67], v[208:211], a[0:3]
ds_read_b64_tr_b16 v[104:105], v39 offset:10240
v_mfma_f32_16x16x32_bf16 a[4:7], v[68:71], v[208:211], a[4:7]
ds_read_b64_tr_b16 v[106:107], v39 offset:15360
s_add_u32 m0, s79, 122880
v_mfma_f32_16x16x32_bf16 a[8:11], v[72:75], v[208:211], a[8:11]
buffer_load_dwordx4 v33, s[64:67], s86 offen lds
ds_read_b64_tr_b16 v[108:109], v39 offset:10496
v_mfma_f32_16x16x32_bf16 a[12:15], v[76:79], v[208:211], a[12:15]
ds_read_b64_tr_b16 v[110:111], v39 offset:15616
s_waitcnt lgkmcnt(10)
v_mfma_f32_16x16x32_bf16 a[16:19], v[80:83], v[208:211], a[16:19]
ds_read_b64_tr_b16 v[112:113], v39 offset:10752
v_mfma_f32_16x16x32_bf16 a[20:23], v[84:87], v[208:211], a[20:23]
ds_read_b64_tr_b16 v[114:115], v39 offset:15872
v_mfma_f32_16x16x32_bf16 a[24:27], v[88:91], v[208:211], a[24:27]
buffer_load_dwordx4 v34, s[64:67], s86 offen offset:1024 lds
ds_read_b64_tr_b16 v[116:117], v39 offset:11008
s_waitcnt lgkmcnt(7)
v_mfma_f32_16x16x32_bf16 a[28:31], v[92:95], v[208:211], a[28:31]
ds_read_b64_tr_b16 v[118:119], v39 offset:16128
v_mfma_f32_16x16x32_bf16 a[32:35], v[96:99], v[208:211], a[32:35]
ds_read_b64_tr_b16 v[120:121], v39 offset:11264
v_mfma_f32_16x16x32_bf16 a[36:39], v[100:103], v[208:211], a[36:39]
ds_read_b64_tr_b16 v[122:123], v39 offset:16384
v_mfma_f32_16x16x32_bf16 a[40:43], v[64:67], v[212:215], a[40:43]
buffer_load_dwordx4 v35, s[64:67], s86 offen offset:2048 lds
ds_read_b64_tr_b16 v[124:125], v39 offset:11520
v_mfma_f32_16x16x32_bf16 a[44:47], v[68:71], v[212:215], a[44:47]
ds_read_b64_tr_b16 v[126:127], v39 offset:16640
v_mfma_f32_16x16x32_bf16 a[48:51], v[72:75], v[212:215], a[48:51]
ds_read_b64_tr_b16 v[128:129], v39 offset:11776
v_mfma_f32_16x16x32_bf16 a[52:55], v[76:79], v[212:215], a[52:55]
ds_read_b64_tr_b16 v[130:131], v39 offset:16896
v_mfma_f32_16x16x32_bf16 a[56:59], v[80:83], v[212:215], a[56:59]
buffer_load_dwordx4 v36, s[64:67], s86 offen offset:3072 lds
ds_read_b64_tr_b16 v[132:133], v39 offset:12032
v_mfma_f32_16x16x32_bf16 a[60:63], v[84:87], v[212:215], a[60:63]
ds_read_b64_tr_b16 v[134:135], v39 offset:17152
v_mfma_f32_16x16x32_bf16 a[64:67], v[88:91], v[212:215], a[64:67]
ds_read_b64_tr_b16 v[136:137], v39 offset:12288
v_mfma_f32_16x16x32_bf16 a[68:71], v[92:95], v[212:215], a[68:71]
ds_read_b64_tr_b16 v[138:139], v39 offset:17408
s_add_u32 m0, s79, 126976
v_mfma_f32_16x16x32_bf16 a[72:75], v[96:99], v[212:215], a[72:75]
buffer_load_dwordx4 v37, s[64:67], s86 offen lds
ds_read_b64_tr_b16 v[140:141], v39 offset:12544
v_mfma_f32_16x16x32_bf16 a[76:79], v[100:103], v[212:215], a[76:79]
ds_read_b64_tr_b16 v[142:143], v39 offset:17664
s_add_u32 s86, s86, 81920
v_add_u32 v39, 0, v38
s_waitcnt lgkmcnt(12)
v_mfma_f32_16x16x32_bf16 a[0:3], v[104:107], v[216:219], a[0:3]
ds_read_b64_tr_b16 v[64:65], v39 offset:0
v_mfma_f32_16x16x32_bf16 a[4:7], v[108:111], v[216:219], a[4:7]
ds_read_b64_tr_b16 v[66:67], v39 offset:5120
v_mfma_f32_16x16x32_bf16 a[8:11], v[112:115], v[216:219], a[8:11]
ds_read_b64_tr_b16 v[68:69], v39 offset:256
s_add_u32 s85, s84, 8192
v_mfma_f32_16x16x32_bf16 a[12:15], v[116:119], v[216:219], a[12:15]
buffer_load_dwordx4 v[192:195], v32, s[72:75], s84 offen sc1
ds_read_b64_tr_b16 v[70:71], v39 offset:5376
s_waitcnt lgkmcnt(10)
v_mfma_f32_16x16x32_bf16 a[16:19], v[120:123], v[216:219], a[16:19]
ds_read_b64_tr_b16 v[72:73], v39 offset:512
v_mfma_f32_16x16x32_bf16 a[20:23], v[124:127], v[216:219], a[20:23]
ds_read_b64_tr_b16 v[74:75], v39 offset:5632
v_mfma_f32_16x16x32_bf16 a[24:27], v[128:131], v[216:219], a[24:27]
ds_read_b64_tr_b16 v[76:77], v39 offset:768
s_waitcnt lgkmcnt(7)
v_mfma_f32_16x16x32_bf16 a[28:31], v[132:135], v[216:219], a[28:31]
ds_read_b64_tr_b16 v[78:79], v39 offset:5888
v_mfma_f32_16x16x32_bf16 a[32:35], v[136:139], v[216:219], a[32:35]
buffer_load_dwordx4 v[196:199], v32, s[72:75], s84 offen offset:1024 sc1
ds_read_b64_tr_b16 v[80:81], v39 offset:1024
v_mfma_f32_16x16x32_bf16 a[36:39], v[140:143], v[216:219], a[36:39]
ds_read_b64_tr_b16 v[82:83], v39 offset:6144
v_mfma_f32_16x16x32_bf16 a[40:43], v[104:107], v[220:223], a[40:43]
ds_read_b64_tr_b16 v[84:85], v39 offset:1280
v_mfma_f32_16x16x32_bf16 a[44:47], v[108:111], v[220:223], a[44:47]
ds_read_b64_tr_b16 v[86:87], v39 offset:6400
v_mfma_f32_16x16x32_bf16 a[48:51], v[112:115], v[220:223], a[48:51]
buffer_load_dwordx4 v[200:203], v32, s[72:75], s85 offen sc1
ds_read_b64_tr_b16 v[88:89], v39 offset:1536
v_mfma_f32_16x16x32_bf16 a[52:55], v[116:119], v[220:223], a[52:55]
ds_read_b64_tr_b16 v[90:91], v39 offset:6656
v_mfma_f32_16x16x32_bf16 a[56:59], v[120:123], v[220:223], a[56:59]
ds_read_b64_tr_b16 v[92:93], v39 offset:1792
v_mfma_f32_16x16x32_bf16 a[60:63], v[124:127], v[220:223], a[60:63]
ds_read_b64_tr_b16 v[94:95], v39 offset:6912
v_mfma_f32_16x16x32_bf16 a[64:67], v[128:131], v[220:223], a[64:67]
ds_read_b64_tr_b16 v[96:97], v39 offset:2048
v_mfma_f32_16x16x32_bf16 a[68:71], v[132:135], v[220:223], a[68:71]
buffer_load_dwordx4 v[204:207], v32, s[72:75], s85 offen offset:1024 sc1
ds_read_b64_tr_b16 v[98:99], v39 offset:7168
v_mfma_f32_16x16x32_bf16 a[72:75], v[136:139], v[220:223], a[72:75]
ds_read_b64_tr_b16 v[100:101], v39 offset:2304
s_add_u32 s84, s84, 16384
v_mfma_f32_16x16x32_bf16 a[76:79], v[140:143], v[220:223], a[76:79]
ds_read_b64_tr_b16 v[102:103], v39 offset:7424
s_waitcnt vmcnt(26)
s_barrier
s_waitcnt lgkmcnt(12)
v_mfma_f32_16x16x32_bf16 a[0:3], v[64:67], v[224:227], a[0:3]
ds_read_b64_tr_b16 v[104:105], v39 offset:10240
v_mfma_f32_16x16x32_bf16 a[4:7], v[68:71], v[224:227], a[4:7]
ds_read_b64_tr_b16 v[106:107], v39 offset:15360
s_add_u32 m0, s79, 143360
v_mfma_f32_16x16x32_bf16 a[8:11], v[72:75], v[224:227], a[8:11]
buffer_load_dwordx4 v33, s[64:67], s86 offen lds
ds_read_b64_tr_b16 v[108:109], v39 offset:10496
v_mfma_f32_16x16x32_bf16 a[12:15], v[76:79], v[224:227], a[12:15]
ds_read_b64_tr_b16 v[110:111], v39 offset:15616
s_waitcnt lgkmcnt(10)
v_mfma_f32_16x16x32_bf16 a[16:19], v[80:83], v[224:227], a[16:19]
ds_read_b64_tr_b16 v[112:113], v39 offset:10752
v_mfma_f32_16x16x32_bf16 a[20:23], v[84:87], v[224:227], a[20:23]
ds_read_b64_tr_b16 v[114:115], v39 offset:15872
v_mfma_f32_16x16x32_bf16 a[24:27], v[88:91], v[224:227], a[24:27]
buffer_load_dwordx4 v34, s[64:67], s86 offen offset:1024 lds
ds_read_b64_tr_b16 v[116:117], v39 offset:11008
s_waitcnt lgkmcnt(7)
v_mfma_f32_16x16x32_bf16 a[28:31], v[92:95], v[224:227], a[28:31]
ds_read_b64_tr_b16 v[118:119], v39 offset:16128
v_mfma_f32_16x16x32_bf16 a[32:35], v[96:99], v[224:227], a[32:35]
ds_read_b64_tr_b16 v[120:121], v39 offset:11264
v_mfma_f32_16x16x32_bf16 a[36:39], v[100:103], v[224:227], a[36:39]
ds_read_b64_tr_b16 v[122:123], v39 offset:16384
v_mfma_f32_16x16x32_bf16 a[40:43], v[64:67], v[228:231], a[40:43]
buffer_load_dwordx4 v35, s[64:67], s86 offen offset:2048 lds
ds_read_b64_tr_b16 v[124:125], v39 offset:11520
v_mfma_f32_16x16x32_bf16 a[44:47], v[68:71], v[228:231], a[44:47]
ds_read_b64_tr_b16 v[126:127], v39 offset:16640
v_mfma_f32_16x16x32_bf16 a[48:51], v[72:75], v[228:231], a[48:51]
ds_read_b64_tr_b16 v[128:129], v39 offset:11776
v_mfma_f32_16x16x32_bf16 a[52:55], v[76:79], v[228:231], a[52:55]
ds_read_b64_tr_b16 v[130:131], v39 offset:16896
v_mfma_f32_16x16x32_bf16 a[56:59], v[80:83], v[228:231], a[56:59]
buffer_load_dwordx4 v36, s[64:67], s86 offen offset:3072 lds
ds_read_b64_tr_b16 v[132:133], v39 offset:12032
v_mfma_f32_16x16x32_bf16 a[60:63], v[84:87], v[228:231], a[60:63]
ds_read_b64_tr_b16 v[134:135], v39 offset:17152
v_mfma_f32_16x16x32_bf16 a[64:67], v[88:91], v[228:231], a[64:67]
ds_read_b64_tr_b16 v[136:137], v39 offset:12288
v_mfma_f32_16x16x32_bf16 a[68:71], v[92:95], v[228:231], a[68:71]
ds_read_b64_tr_b16 v[138:139], v39 offset:17408
s_add_u32 m0, s79, 147456
v_mfma_f32_16x16x32_bf16 a[72:75], v[96:99], v[228:231], a[72:75]
buffer_load_dwordx4 v37, s[64:67], s86 offen lds
ds_read_b64_tr_b16 v[140:141], v39 offset:12544
v_mfma_f32_16x16x32_bf16 a[76:79], v[100:103], v[228:231], a[76:79]
ds_read_b64_tr_b16 v[142:143], v39 offset:17664
s_add_u32 s86, s86, 81920
v_add_u32 v39, 20480, v38
s_waitcnt lgkmcnt(12)
v_mfma_f32_16x16x32_bf16 a[0:3], v[104:107], v[232:235], a[0:3]
ds_read_b64_tr_b16 v[64:65], v39 offset:0
v_mfma_f32_16x16x32_bf16 a[4:7], v[108:111], v[232:235], a[4:7]
ds_read_b64_tr_b16 v[66:67], v39 offset:5120
v_mfma_f32_16x16x32_bf16 a[8:11], v[112:115], v[232:235], a[8:11]
ds_read_b64_tr_b16 v[68:69], v39 offset:256
s_add_u32 s85, s84, 8192
v_mfma_f32_16x16x32_bf16 a[12:15], v[116:119], v[232:235], a[12:15]
buffer_load_dwordx4 v[208:211], v32, s[72:75], s84 offen sc1
ds_read_b64_tr_b16 v[70:71], v39 offset:5376
s_waitcnt lgkmcnt(10)
v_mfma_f32_16x16x32_bf16 a[16:19], v[120:123], v[232:235], a[16:19]
ds_read_b64_tr_b16 v[72:73], v39 offset:512
v_mfma_f32_16x16x32_bf16 a[20:23], v[124:127], v[232:235], a[20:23]
ds_read_b64_tr_b16 v[74:75], v39 offset:5632
v_mfma_f32_16x16x32_bf16 a[24:27], v[128:131], v[232:235], a[24:27]
ds_read_b64_tr_b16 v[76:77], v39 offset:768
s_waitcnt lgkmcnt(7)
v_mfma_f32_16x16x32_bf16 a[28:31], v[132:135], v[232:235], a[28:31]
ds_read_b64_tr_b16 v[78:79], v39 offset:5888
v_mfma_f32_16x16x32_bf16 a[32:35], v[136:139], v[232:235], a[32:35]
buffer_load_dwordx4 v[212:215], v32, s[72:75], s84 offen offset:1024 sc1
ds_read_b64_tr_b16 v[80:81], v39 offset:1024
v_mfma_f32_16x16x32_bf16 a[36:39], v[140:143], v[232:235], a[36:39]
ds_read_b64_tr_b16 v[82:83], v39 offset:6144
v_mfma_f32_16x16x32_bf16 a[40:43], v[104:107], v[236:239], a[40:43]
ds_read_b64_tr_b16 v[84:85], v39 offset:1280
v_mfma_f32_16x16x32_bf16 a[44:47], v[108:111], v[236:239], a[44:47]
ds_read_b64_tr_b16 v[86:87], v39 offset:6400
v_mfma_f32_16x16x32_bf16 a[48:51], v[112:115], v[236:239], a[48:51]
buffer_load_dwordx4 v[216:219], v32, s[72:75], s85 offen sc1
ds_read_b64_tr_b16 v[88:89], v39 offset:1536
v_mfma_f32_16x16x32_bf16 a[52:55], v[116:119], v[236:239], a[52:55]
ds_read_b64_tr_b16 v[90:91], v39 offset:6656
v_mfma_f32_16x16x32_bf16 a[56:59], v[120:123], v[236:239], a[56:59]
ds_read_b64_tr_b16 v[92:93], v39 offset:1792
v_mfma_f32_16x16x32_bf16 a[60:63], v[124:127], v[236:239], a[60:63]
ds_read_b64_tr_b16 v[94:95], v39 offset:6912
v_mfma_f32_16x16x32_bf16 a[64:67], v[128:131], v[236:239], a[64:67]
ds_read_b64_tr_b16 v[96:97], v39 offset:2048
v_mfma_f32_16x16x32_bf16 a[68:71], v[132:135], v[236:239], a[68:71]
buffer_load_dwordx4 v[220:223], v32, s[72:75], s85 offen offset:1024 sc1
ds_read_b64_tr_b16 v[98:99], v39 offset:7168
v_mfma_f32_16x16x32_bf16 a[72:75], v[136:139], v[236:239], a[72:75]
ds_read_b64_tr_b16 v[100:101], v39 offset:2304
s_add_u32 s84, s84, 16384
v_mfma_f32_16x16x32_bf16 a[76:79], v[140:143], v[236:239], a[76:79]
ds_read_b64_tr_b16 v[102:103], v39 offset:7424
s_waitcnt vmcnt(31)
s_barrier
s_waitcnt lgkmcnt(12)
v_mfma_f32_16x16x32_bf16 a[0:3], v[64:67], v[240:243], a[0:3]
ds_read_b64_tr_b16 v[104:105], v39 offset:10240
v_mfma_f32_16x16x32_bf16 a[4:7], v[68:71], v[240:243], a[4:7]
ds_read_b64_tr_b16 v[106:107], v39 offset:15360
s_add_u32 m0, s79, 0
v_mfma_f32_16x16x32_bf16 a[8:11], v[72:75], v[240:243], a[8:11]
buffer_load_dwordx4 v33, s[64:67], s86 offen lds
ds_read_b64_tr_b16 v[108:109], v39 offset:10496
v_mfma_f32_16x16x32_bf16 a[12:15], v[76:79], v[240:243], a[12:15]
ds_read_b64_tr_b16 v[110:111], v39 offset:15616
s_waitcnt lgkmcnt(10)
v_mfma_f32_16x16x32_bf16 a[16:19], v[80:83], v[240:243], a[16:19]
ds_read_b64_tr_b16 v[112:113], v39 offset:10752
v_mfma_f32_16x16x32_bf16 a[20:23], v[84:87], v[240:243], a[20:23]
ds_read_b64_tr_b16 v[114:115], v39 offset:15872
v_mfma_f32_16x16x32_bf16 a[24:27], v[88:91], v[240:243], a[24:27]
buffer_load_dwordx4 v34, s[64:67], s86 offen offset:1024 lds
ds_read_b64_tr_b16 v[116:117], v39 offset:11008
s_waitcnt lgkmcnt(7)
v_mfma_f32_16x16x32_bf16 a[28:31], v[92:95], v[240:243], a[28:31]
ds_read_b64_tr_b16 v[118:119], v39 offset:16128
v_mfma_f32_16x16x32_bf16 a[32:35], v[96:99], v[240:243], a[32:35]
ds_read_b64_tr_b16 v[120:121], v39 offset:11264
v_mfma_f32_16x16x32_bf16 a[36:39], v[100:103], v[240:243], a[36:39]
ds_read_b64_tr_b16 v[122:123], v39 offset:16384
v_mfma_f32_16x16x32_bf16 a[40:43], v[64:67], v[244:247], a[40:43]
buffer_load_dwordx4 v35, s[64:67], s86 offen offset:2048 lds
ds_read_b64_tr_b16 v[124:125], v39 offset:11520
v_mfma_f32_16x16x32_bf16 a[44:47], v[68:71], v[244:247], a[44:47]
ds_read_b64_tr_b16 v[126:127], v39 offset:16640
v_mfma_f32_16x16x32_bf16 a[48:51], v[72:75], v[244:247], a[48:51]
ds_read_b64_tr_b16 v[128:129], v39 offset:11776
v_mfma_f32_16x16x32_bf16 a[52:55], v[76:79], v[244:247], a[52:55]
ds_read_b64_tr_b16 v[130:131], v39 offset:16896
v_mfma_f32_16x16x32_bf16 a[56:59], v[80:83], v[244:247], a[56:59]
buffer_load_dwordx4 v36, s[64:67], s86 offen offset:3072 lds
ds_read_b64_tr_b16 v[132:133], v39 offset:12032
v_mfma_f32_16x16x32_bf16 a[60:63], v[84:87], v[244:247], a[60:63]
ds_read_b64_tr_b16 v[134:135], v39 offset:17152
v_mfma_f32_16x16x32_bf16 a[64:67], v[88:91], v[244:247], a[64:67]
ds_read_b64_tr_b16 v[136:137], v39 offset:12288
v_mfma_f32_16x16x32_bf16 a[68:71], v[92:95], v[244:247], a[68:71]
ds_read_b64_tr_b16 v[138:139], v39 offset:17408
s_add_u32 m0, s79, 4096
v_mfma_f32_16x16x32_bf16 a[72:75], v[96:99], v[244:247], a[72:75]
buffer_load_dwordx4 v37, s[64:67], s86 offen lds
ds_read_b64_tr_b16 v[140:141], v39 offset:12544
v_mfma_f32_16x16x32_bf16 a[76:79], v[100:103], v[244:247], a[76:79]
ds_read_b64_tr_b16 v[142:143], v39 offset:17664
s_add_u32 s86, s86, 81920
v_add_u32 v39, 40960, v38
s_waitcnt lgkmcnt(12)
v_mfma_f32_16x16x32_bf16 a[0:3], v[104:107], v[248:251], a[0:3]
ds_read_b64_tr_b16 v[64:65], v39 offset:0
v_mfma_f32_16x16x32_bf16 a[4:7], v[108:111], v[248:251], a[4:7]
ds_read_b64_tr_b16 v[66:67], v39 offset:5120
v_mfma_f32_16x16x32_bf16 a[8:11], v[112:115], v[248:251], a[8:11]
ds_read_b64_tr_b16 v[68:69], v39 offset:256
s_add_u32 s85, s84, 8192
v_mfma_f32_16x16x32_bf16 a[12:15], v[116:119], v[248:251], a[12:15]
buffer_load_dwordx4 v[224:227], v32, s[72:75], s84 offen sc1
ds_read_b64_tr_b16 v[70:71], v39 offset:5376
s_waitcnt lgkmcnt(10)
v_mfma_f32_16x16x32_bf16 a[16:19], v[120:123], v[248:251], a[16:19]
ds_read_b64_tr_b16 v[72:73], v39 offset:512
v_mfma_f32_16x16x32_bf16 a[20:23], v[124:127], v[248:251], a[20:23]
ds_read_b64_tr_b16 v[74:75], v39 offset:5632
v_mfma_f32_16x16x32_bf16 a[24:27], v[128:131], v[248:251], a[24:27]
ds_read_b64_tr_b16 v[76:77], v39 offset:768
s_waitcnt lgkmcnt(7)
v_mfma_f32_16x16x32_bf16 a[28:31], v[132:135], v[248:251], a[28:31]
ds_read_b64_tr_b16 v[78:79], v39 offset:5888
v_mfma_f32_16x16x32_bf16 a[32:35], v[136:139], v[248:251], a[32:35]
buffer_load_dwordx4 v[228:231], v32, s[72:75], s84 offen offset:1024 sc1
ds_read_b64_tr_b16 v[80:81], v39 offset:1024
v_mfma_f32_16x16x32_bf16 a[36:39], v[140:143], v[248:251], a[36:39]
ds_read_b64_tr_b16 v[82:83], v39 offset:6144
v_mfma_f32_16x16x32_bf16 a[40:43], v[104:107], v[252:255], a[40:43]
ds_read_b64_tr_b16 v[84:85], v39 offset:1280
v_mfma_f32_16x16x32_bf16 a[44:47], v[108:111], v[252:255], a[44:47]
ds_read_b64_tr_b16 v[86:87], v39 offset:6400
v_mfma_f32_16x16x32_bf16 a[48:51], v[112:115], v[252:255], a[48:51]
buffer_load_dwordx4 v[232:235], v32, s[72:75], s85 offen sc1
ds_read_b64_tr_b16 v[88:89], v39 offset:1536
v_mfma_f32_16x16x32_bf16 a[52:55], v[116:119], v[252:255], a[52:55]
ds_read_b64_tr_b16 v[90:91], v39 offset:6656
v_mfma_f32_16x16x32_bf16 a[56:59], v[120:123], v[252:255], a[56:59]
ds_read_b64_tr_b16 v[92:93], v39 offset:1792
v_mfma_f32_16x16x32_bf16 a[60:63], v[124:127], v[252:255], a[60:63]
ds_read_b64_tr_b16 v[94:95], v39 offset:6912
v_mfma_f32_16x16x32_bf16 a[64:67], v[128:131], v[252:255], a[64:67]
ds_read_b64_tr_b16 v[96:97], v39 offset:2048
v_mfma_f32_16x16x32_bf16 a[68:71], v[132:135], v[252:255], a[68:71]
buffer_load_dwordx4 v[236:239], v32, s[72:75], s85 offen offset:1024 sc1
ds_read_b64_tr_b16 v[98:99], v39 offset:7168
v_mfma_f32_16x16x32_bf16 a[72:75], v[136:139], v[252:255], a[72:75]
ds_read_b64_tr_b16 v[100:101], v39 offset:2304
s_add_u32 s84, s84, 16384
v_mfma_f32_16x16x32_bf16 a[76:79], v[140:143], v[252:255], a[76:79]
ds_read_b64_tr_b16 v[102:103], v39 offset:7424
s_waitcnt vmcnt(36)
s_barrier
s_waitcnt lgkmcnt(12)
v_mfma_f32_16x16x32_bf16 a[0:3], v[64:67], v[144:147], a[0:3]
ds_read_b64_tr_b16 v[104:105], v39 offset:10240
v_mfma_f32_16x16x32_bf16 a[4:7], v[68:71], v[144:147], a[4:7]
ds_read_b64_tr_b16 v[106:107], v39 offset:15360
s_add_u32 m0, s79, 20480
v_mfma_f32_16x16x32_bf16 a[8:11], v[72:75], v[144:147], a[8:11]
buffer_load_dwordx4 v33, s[64:67], s86 offen lds
ds_read_b64_tr_b16 v[108:109], v39 offset:10496
v_mfma_f32_16x16x32_bf16 a[12:15], v[76:79], v[144:147], a[12:15]
ds_read_b64_tr_b16 v[110:111], v39 offset:15616
s_waitcnt lgkmcnt(10)
v_mfma_f32_16x16x32_bf16 a[16:19], v[80:83], v[144:147], a[16:19]
ds_read_b64_tr_b16 v[112:113], v39 offset:10752
v_mfma_f32_16x16x32_bf16 a[20:23], v[84:87], v[144:147], a[20:23]
ds_read_b64_tr_b16 v[114:115], v39 offset:15872
v_mfma_f32_16x16x32_bf16 a[24:27], v[88:91], v[144:147], a[24:27]
buffer_load_dwordx4 v34, s[64:67], s86 offen offset:1024 lds
ds_read_b64_tr_b16 v[116:117], v39 offset:11008
s_waitcnt lgkmcnt(7)
v_mfma_f32_16x16x32_bf16 a[28:31], v[92:95], v[144:147], a[28:31]
ds_read_b64_tr_b16 v[118:119], v39 offset:16128
v_mfma_f32_16x16x32_bf16 a[32:35], v[96:99], v[144:147], a[32:35]
ds_read_b64_tr_b16 v[120:121], v39 offset:11264
v_mfma_f32_16x16x32_bf16 a[36:39], v[100:103], v[144:147], a[36:39]
ds_read_b64_tr_b16 v[122:123], v39 offset:16384
v_mfma_f32_16x16x32_bf16 a[40:43], v[64:67], v[148:151], a[40:43]
buffer_load_dwordx4 v35, s[64:67], s86 offen offset:2048 lds
ds_read_b64_tr_b16 v[124:125], v39 offset:11520
v_mfma_f32_16x16x32_bf16 a[44:47], v[68:71], v[148:151], a[44:47]
ds_read_b64_tr_b16 v[126:127], v39 offset:16640
v_mfma_f32_16x16x32_bf16 a[48:51], v[72:75], v[148:151], a[48:51]
ds_read_b64_tr_b16 v[128:129], v39 offset:11776
v_mfma_f32_16x16x32_bf16 a[52:55], v[76:79], v[148:151], a[52:55]
ds_read_b64_tr_b16 v[130:131], v39 offset:16896
v_mfma_f32_16x16x32_bf16 a[56:59], v[80:83], v[148:151], a[56:59]
buffer_load_dwordx4 v36, s[64:67], s86 offen offset:3072 lds
ds_read_b64_tr_b16 v[132:133], v39 offset:12032
v_mfma_f32_16x16x32_bf16 a[60:63], v[84:87], v[148:151], a[60:63]
ds_read_b64_tr_b16 v[134:135], v39 offset:17152
v_mfma_f32_16x16x32_bf16 a[64:67], v[88:91], v[148:151], a[64:67]
ds_read_b64_tr_b16 v[136:137], v39 offset:12288
v_mfma_f32_16x16x32_bf16 a[68:71], v[92:95], v[148:151], a[68:71]
ds_read_b64_tr_b16 v[138:139], v39 offset:17408
s_add_u32 m0, s79, 24576
v_mfma_f32_16x16x32_bf16 a[72:75], v[96:99], v[148:151], a[72:75]
buffer_load_dwordx4 v37, s[64:67], s86 offen lds
ds_read_b64_tr_b16 v[140:141], v39 offset:12544
v_mfma_f32_16x16x32_bf16 a[76:79], v[100:103], v[148:151], a[76:79]
ds_read_b64_tr_b16 v[142:143], v39 offset:17664
s_add_u32 s86, s86, 81920
v_add_u32 v39, 61440, v38
s_waitcnt lgkmcnt(12)
v_mfma_f32_16x16x32_bf16 a[0:3], v[104:107], v[152:155], a[0:3]
ds_read_b64_tr_b16 v[64:65], v39 offset:0
v_mfma_f32_16x16x32_bf16 a[4:7], v[108:111], v[152:155], a[4:7]
ds_read_b64_tr_b16 v[66:67], v39 offset:5120
v_mfma_f32_16x16x32_bf16 a[8:11], v[112:115], v[152:155], a[8:11]
ds_read_b64_tr_b16 v[68:69], v39 offset:256
s_add_u32 s85, s84, 8192
v_mfma_f32_16x16x32_bf16 a[12:15], v[116:119], v[152:155], a[12:15]
buffer_load_dwordx4 v[240:243], v32, s[72:75], s84 offen sc1
ds_read_b64_tr_b16 v[70:71], v39 offset:5376
s_waitcnt lgkmcnt(10)
v_mfma_f32_16x16x32_bf16 a[16:19], v[120:123], v[152:155], a[16:19]
ds_read_b64_tr_b16 v[72:73], v39 offset:512
v_mfma_f32_16x16x32_bf16 a[20:23], v[124:127], v[152:155], a[20:23]
ds_read_b64_tr_b16 v[74:75], v39 offset:5632
v_mfma_f32_16x16x32_bf16 a[24:27], v[128:131], v[152:155], a[24:27]
ds_read_b64_tr_b16 v[76:77], v39 offset:768
s_waitcnt lgkmcnt(7)
v_mfma_f32_16x16x32_bf16 a[28:31], v[132:135], v[152:155], a[28:31]
ds_read_b64_tr_b16 v[78:79], v39 offset:5888
v_mfma_f32_16x16x32_bf16 a[32:35], v[136:139], v[152:155], a[32:35]
buffer_load_dwordx4 v[244:247], v32, s[72:75], s84 offen offset:1024 sc1
ds_read_b64_tr_b16 v[80:81], v39 offset:1024
v_mfma_f32_16x16x32_bf16 a[36:39], v[140:143], v[152:155], a[36:39]
ds_read_b64_tr_b16 v[82:83], v39 offset:6144
v_mfma_f32_16x16x32_bf16 a[40:43], v[104:107], v[156:159], a[40:43]
ds_read_b64_tr_b16 v[84:85], v39 offset:1280
v_mfma_f32_16x16x32_bf16 a[44:47], v[108:111], v[156:159], a[44:47]
ds_read_b64_tr_b16 v[86:87], v39 offset:6400
v_mfma_f32_16x16x32_bf16 a[48:51], v[112:115], v[156:159], a[48:51]
buffer_load_dwordx4 v[248:251], v32, s[72:75], s85 offen sc1
ds_read_b64_tr_b16 v[88:89], v39 offset:1536
v_mfma_f32_16x16x32_bf16 a[52:55], v[116:119], v[156:159], a[52:55]
ds_read_b64_tr_b16 v[90:91], v39 offset:6656
v_mfma_f32_16x16x32_bf16 a[56:59], v[120:123], v[156:159], a[56:59]
ds_read_b64_tr_b16 v[92:93], v39 offset:1792
v_mfma_f32_16x16x32_bf16 a[60:63], v[124:127], v[156:159], a[60:63]
ds_read_b64_tr_b16 v[94:95], v39 offset:6912
v_mfma_f32_16x16x32_bf16 a[64:67], v[128:131], v[156:159], a[64:67]
ds_read_b64_tr_b16 v[96:97], v39 offset:2048
v_mfma_f32_16x16x32_bf16 a[68:71], v[132:135], v[156:159], a[68:71]
buffer_load_dwordx4 v[252:255], v32, s[72:75], s85 offen offset:1024 sc1
ds_read_b64_tr_b16 v[98:99], v39 offset:7168
v_mfma_f32_16x16x32_bf16 a[72:75], v[136:139], v[156:159], a[72:75]
ds_read_b64_tr_b16 v[100:101], v39 offset:2304
s_add_u32 s84, s84, 16384
v_mfma_f32_16x16x32_bf16 a[76:79], v[140:143], v[156:159], a[76:79]
ds_read_b64_tr_b16 v[102:103], v39 offset:7424
s_waitcnt vmcnt(36)
s_barrier
s_waitcnt lgkmcnt(12)
v_mfma_f32_16x16x32_bf16 a[0:3], v[64:67], v[160:163], a[0:3]
ds_read_b64_tr_b16 v[104:105], v39 offset:10240
v_mfma_f32_16x16x32_bf16 a[4:7], v[68:71], v[160:163], a[4:7]
ds_read_b64_tr_b16 v[106:107], v39 offset:15360
s_add_u32 m0, s79, 40960
v_mfma_f32_16x16x32_bf16 a[8:11], v[72:75], v[160:163], a[8:11]
buffer_load_dwordx4 v33, s[64:67], s86 offen lds
ds_read_b64_tr_b16 v[108:109], v39 offset:10496
v_mfma_f32_16x16x32_bf16 a[12:15], v[76:79], v[160:163], a[12:15]
ds_read_b64_tr_b16 v[110:111], v39 offset:15616
s_waitcnt lgkmcnt(10)
v_mfma_f32_16x16x32_bf16 a[16:19], v[80:83], v[160:163], a[16:19]
ds_read_b64_tr_b16 v[112:113], v39 offset:10752
v_mfma_f32_16x16x32_bf16 a[20:23], v[84:87], v[160:163], a[20:23]
ds_read_b64_tr_b16 v[114:115], v39 offset:15872
v_mfma_f32_16x16x32_bf16 a[24:27], v[88:91], v[160:163], a[24:27]
buffer_load_dwordx4 v34, s[64:67], s86 offen offset:1024 lds
ds_read_b64_tr_b16 v[116:117], v39 offset:11008
s_waitcnt lgkmcnt(7)
v_mfma_f32_16x16x32_bf16 a[28:31], v[92:95], v[160:163], a[28:31]
ds_read_b64_tr_b16 v[118:119], v39 offset:16128
v_mfma_f32_16x16x32_bf16 a[32:35], v[96:99], v[160:163], a[32:35]
ds_read_b64_tr_b16 v[120:121], v39 offset:11264
v_mfma_f32_16x16x32_bf16 a[36:39], v[100:103], v[160:163], a[36:39]
ds_read_b64_tr_b16 v[122:123], v39 offset:16384
v_mfma_f32_16x16x32_bf16 a[40:43], v[64:67], v[164:167], a[40:43]
buffer_load_dwordx4 v35, s[64:67], s86 offen offset:2048 lds
ds_read_b64_tr_b16 v[124:125], v39 offset:11520
v_mfma_f32_16x16x32_bf16 a[44:47], v[68:71], v[164:167], a[44:47]
ds_read_b64_tr_b16 v[126:127], v39 offset:16640
v_mfma_f32_16x16x32_bf16 a[48:51], v[72:75], v[164:167], a[48:51]
ds_read_b64_tr_b16 v[128:129], v39 offset:11776
v_mfma_f32_16x16x32_bf16 a[52:55], v[76:79], v[164:167], a[52:55]
ds_read_b64_tr_b16 v[130:131], v39 offset:16896
v_mfma_f32_16x16x32_bf16 a[56:59], v[80:83], v[164:167], a[56:59]
buffer_load_dwordx4 v36, s[64:67], s86 offen offset:3072 lds
ds_read_b64_tr_b16 v[132:133], v39 offset:12032
v_mfma_f32_16x16x32_bf16 a[60:63], v[84:87], v[164:167], a[60:63]
ds_read_b64_tr_b16 v[134:135], v39 offset:17152
v_mfma_f32_16x16x32_bf16 a[64:67], v[88:91], v[164:167], a[64:67]
ds_read_b64_tr_b16 v[136:137], v39 offset:12288
v_mfma_f32_16x16x32_bf16 a[68:71], v[92:95], v[164:167], a[68:71]
ds_read_b64_tr_b16 v[138:139], v39 offset:17408
s_add_u32 m0, s79, 45056
v_mfma_f32_16x16x32_bf16 a[72:75], v[96:99], v[164:167], a[72:75]
buffer_load_dwordx4 v37, s[64:67], s86 offen lds
ds_read_b64_tr_b16 v[140:141], v39 offset:12544
v_mfma_f32_16x16x32_bf16 a[76:79], v[100:103], v[164:167], a[76:79]
ds_read_b64_tr_b16 v[142:143], v39 offset:17664
s_add_u32 s86, s86, 81920
v_add_u32 v39, 81920, v38
s_waitcnt lgkmcnt(12)
v_mfma_f32_16x16x32_bf16 a[0:3], v[104:107], v[168:171], a[0:3]
ds_read_b64_tr_b16 v[64:65], v39 offset:0
v_mfma_f32_16x16x32_bf16 a[4:7], v[108:111], v[168:171], a[4:7]
ds_read_b64_tr_b16 v[66:67], v39 offset:5120
v_mfma_f32_16x16x32_bf16 a[8:11], v[112:115], v[168:171], a[8:11]
ds_read_b64_tr_b16 v[68:69], v39 offset:256
s_add_u32 s85, s84, 8192
v_mfma_f32_16x16x32_bf16 a[12:15], v[116:119], v[168:171], a[12:15]
buffer_load_dwordx4 v[144:147], v32, s[72:75], s84 offen sc1
ds_read_b64_tr_b16 v[70:71], v39 offset:5376
s_waitcnt lgkmcnt(10)
v_mfma_f32_16x16x32_bf16 a[16:19], v[120:123], v[168:171], a[16:19]
ds_read_b64_tr_b16 v[72:73], v39 offset:512
v_mfma_f32_16x16x32_bf16 a[20:23], v[124:127], v[168:171], a[20:23]
ds_read_b64_tr_b16 v[74:75], v39 offset:5632
v_mfma_f32_16x16x32_bf16 a[24:27], v[128:131], v[168:171], a[24:27]
ds_read_b64_tr_b16 v[76:77], v39 offset:768
s_waitcnt lgkmcnt(7)
v_mfma_f32_16x16x32_bf16 a[28:31], v[132:135], v[168:171], a[28:31]
ds_read_b64_tr_b16 v[78:79], v39 offset:5888
v_mfma_f32_16x16x32_bf16 a[32:35], v[136:139], v[168:171], a[32:35]
buffer_load_dwordx4 v[148:151], v32, s[72:75], s84 offen offset:1024 sc1
ds_read_b64_tr_b16 v[80:81], v39 offset:1024
v_mfma_f32_16x16x32_bf16 a[36:39], v[140:143], v[168:171], a[36:39]
ds_read_b64_tr_b16 v[82:83], v39 offset:6144
v_mfma_f32_16x16x32_bf16 a[40:43], v[104:107], v[172:175], a[40:43]
ds_read_b64_tr_b16 v[84:85], v39 offset:1280
v_mfma_f32_16x16x32_bf16 a[44:47], v[108:111], v[172:175], a[44:47]
ds_read_b64_tr_b16 v[86:87], v39 offset:6400
v_mfma_f32_16x16x32_bf16 a[48:51], v[112:115], v[172:175], a[48:51]
buffer_load_dwordx4 v[152:155], v32, s[72:75], s85 offen sc1
ds_read_b64_tr_b16 v[88:89], v39 offset:1536
v_mfma_f32_16x16x32_bf16 a[52:55], v[116:119], v[172:175], a[52:55]
ds_read_b64_tr_b16 v[90:91], v39 offset:6656
v_mfma_f32_16x16x32_bf16 a[56:59], v[120:123], v[172:175], a[56:59]
ds_read_b64_tr_b16 v[92:93], v39 offset:1792
v_mfma_f32_16x16x32_bf16 a[60:63], v[124:127], v[172:175], a[60:63]
ds_read_b64_tr_b16 v[94:95], v39 offset:6912
v_mfma_f32_16x16x32_bf16 a[64:67], v[128:131], v[172:175], a[64:67]
ds_read_b64_tr_b16 v[96:97], v39 offset:2048
v_mfma_f32_16x16x32_bf16 a[68:71], v[132:135], v[172:175], a[68:71]
buffer_load_dwordx4 v[156:159], v32, s[72:75], s85 offen offset:1024 sc1
ds_read_b64_tr_b16 v[98:99], v39 offset:7168
v_mfma_f32_16x16x32_bf16 a[72:75], v[136:139], v[172:175], a[72:75]
ds_read_b64_tr_b16 v[100:101], v39 offset:2304
s_add_u32 s84, s84, 16384
v_mfma_f32_16x16x32_bf16 a[76:79], v[140:143], v[172:175], a[76:79]
ds_read_b64_tr_b16 v[102:103], v39 offset:7424
s_waitcnt vmcnt(36)
s_barrier
s_waitcnt lgkmcnt(12)
v_mfma_f32_16x16x32_bf16 a[0:3], v[64:67], v[192:195], a[0:3]
ds_read_b64_tr_b16 v[104:105], v39 offset:10240
v_mfma_f32_16x16x32_bf16 a[4:7], v[68:71], v[192:195], a[4:7]
ds_read_b64_tr_b16 v[106:107], v39 offset:15360
s_add_u32 m0, s79, 61440
v_mfma_f32_16x16x32_bf16 a[8:11], v[72:75], v[192:195], a[8:11]
buffer_load_dwordx4 v33, s[64:67], s86 offen lds
ds_read_b64_tr_b16 v[108:109], v39 offset:10496
v_mfma_f32_16x16x32_bf16 a[12:15], v[76:79], v[192:195], a[12:15]
ds_read_b64_tr_b16 v[110:111], v39 offset:15616
s_waitcnt lgkmcnt(10)
v_mfma_f32_16x16x32_bf16 a[16:19], v[80:83], v[192:195], a[16:19]
ds_read_b64_tr_b16 v[112:113], v39 offset:10752
v_mfma_f32_16x16x32_bf16 a[20:23], v[84:87], v[192:195], a[20:23]
ds_read_b64_tr_b16 v[114:115], v39 offset:15872
v_mfma_f32_16x16x32_bf16 a[24:27], v[88:91], v[192:195], a[24:27]
buffer_load_dwordx4 v34, s[64:67], s86 offen offset:1024 lds
ds_read_b64_tr_b16 v[116:117], v39 offset:11008
s_waitcnt lgkmcnt(7)
v_mfma_f32_16x16x32_bf16 a[28:31], v[92:95], v[192:195], a[28:31]
ds_read_b64_tr_b16 v[118:119], v39 offset:16128
v_mfma_f32_16x16x32_bf16 a[32:35], v[96:99], v[192:195], a[32:35]
ds_read_b64_tr_b16 v[120:121], v39 offset:11264
v_mfma_f32_16x16x32_bf16 a[36:39], v[100:103], v[192:195], a[36:39]
ds_read_b64_tr_b16 v[122:123], v39 offset:16384
v_mfma_f32_16x16x32_bf16 a[40:43], v[64:67], v[196:199], a[40:43]
buffer_load_dwordx4 v35, s[64:67], s86 offen offset:2048 lds
ds_read_b64_tr_b16 v[124:125], v39 offset:11520
v_mfma_f32_16x16x32_bf16 a[44:47], v[68:71], v[196:199], a[44:47]
ds_read_b64_tr_b16 v[126:127], v39 offset:16640
v_mfma_f32_16x16x32_bf16 a[48:51], v[72:75], v[196:199], a[48:51]
ds_read_b64_tr_b16 v[128:129], v39 offset:11776
v_mfma_f32_16x16x32_bf16 a[52:55], v[76:79], v[196:199], a[52:55]
ds_read_b64_tr_b16 v[130:131], v39 offset:16896
v_mfma_f32_16x16x32_bf16 a[56:59], v[80:83], v[196:199], a[56:59]
buffer_load_dwordx4 v36, s[64:67], s86 offen offset:3072 lds
ds_read_b64_tr_b16 v[132:133], v39 offset:12032
v_mfma_f32_16x16x32_bf16 a[60:63], v[84:87], v[196:199], a[60:63]
ds_read_b64_tr_b16 v[134:135], v39 offset:17152
v_mfma_f32_16x16x32_bf16 a[64:67], v[88:91], v[196:199], a[64:67]
ds_read_b64_tr_b16 v[136:137], v39 offset:12288
v_mfma_f32_16x16x32_bf16 a[68:71], v[92:95], v[196:199], a[68:71]
ds_read_b64_tr_b16 v[138:139], v39 offset:17408
s_add_u32 m0, s79, 65536
v_mfma_f32_16x16x32_bf16 a[72:75], v[96:99], v[196:199], a[72:75]
buffer_load_dwordx4 v37, s[64:67], s86 offen lds
ds_read_b64_tr_b16 v[140:141], v39 offset:12544
v_mfma_f32_16x16x32_bf16 a[76:79], v[100:103], v[196:199], a[76:79]
ds_read_b64_tr_b16 v[142:143], v39 offset:17664
s_add_u32 s86, s86, 81920
v_add_u32 v39, 102400, v38
s_waitcnt lgkmcnt(12)
v_mfma_f32_16x16x32_bf16 a[0:3], v[104:107], v[200:203], a[0:3]
ds_read_b64_tr_b16 v[64:65], v39 offset:0
v_mfma_f32_16x16x32_bf16 a[4:7], v[108:111], v[200:203], a[4:7]
ds_read_b64_tr_b16 v[66:67], v39 offset:5120
v_mfma_f32_16x16x32_bf16 a[8:11], v[112:115], v[200:203], a[8:11]
ds_read_b64_tr_b16 v[68:69], v39 offset:256
s_add_u32 s85, s84, 8192
v_mfma_f32_16x16x32_bf16 a[12:15], v[116:119], v[200:203], a[12:15]
buffer_load_dwordx4 v[160:163], v32, s[72:75], s84 offen sc1
ds_read_b64_tr_b16 v[70:71], v39 offset:5376
s_waitcnt lgkmcnt(10)
v_mfma_f32_16x16x32_bf16 a[16:19], v[120:123], v[200:203], a[16:19]
ds_read_b64_tr_b16 v[72:73], v39 offset:512
v_mfma_f32_16x16x32_bf16 a[20:23], v[124:127], v[200:203], a[20:23]
ds_read_b64_tr_b16 v[74:75], v39 offset:5632
v_mfma_f32_16x16x32_bf16 a[24:27], v[128:131], v[200:203], a[24:27]
ds_read_b64_tr_b16 v[76:77], v39 offset:768
s_waitcnt lgkmcnt(7)
v_mfma_f32_16x16x32_bf16 a[28:31], v[132:135], v[200:203], a[28:31]
ds_read_b64_tr_b16 v[78:79], v39 offset:5888
v_mfma_f32_16x16x32_bf16 a[32:35], v[136:139], v[200:203], a[32:35]
buffer_load_dwordx4 v[164:167], v32, s[72:75], s84 offen offset:1024 sc1
ds_read_b64_tr_b16 v[80:81], v39 offset:1024
v_mfma_f32_16x16x32_bf16 a[36:39], v[140:143], v[200:203], a[36:39]
ds_read_b64_tr_b16 v[82:83], v39 offset:6144
v_mfma_f32_16x16x32_bf16 a[40:43], v[104:107], v[204:207], a[40:43]
ds_read_b64_tr_b16 v[84:85], v39 offset:1280
v_mfma_f32_16x16x32_bf16 a[44:47], v[108:111], v[204:207], a[44:47]
ds_read_b64_tr_b16 v[86:87], v39 offset:6400
v_mfma_f32_16x16x32_bf16 a[48:51], v[112:115], v[204:207], a[48:51]
buffer_load_dwordx4 v[168:171], v32, s[72:75], s85 offen sc1
ds_read_b64_tr_b16 v[88:89], v39 offset:1536
v_mfma_f32_16x16x32_bf16 a[52:55], v[116:119], v[204:207], a[52:55]
ds_read_b64_tr_b16 v[90:91], v39 offset:6656
v_mfma_f32_16x16x32_bf16 a[56:59], v[120:123], v[204:207], a[56:59]
ds_read_b64_tr_b16 v[92:93], v39 offset:1792
v_mfma_f32_16x16x32_bf16 a[60:63], v[124:127], v[204:207], a[60:63]
ds_read_b64_tr_b16 v[94:95], v39 offset:6912
v_mfma_f32_16x16x32_bf16 a[64:67], v[128:131], v[204:207], a[64:67]
ds_read_b64_tr_b16 v[96:97], v39 offset:2048
v_mfma_f32_16x16x32_bf16 a[68:71], v[132:135], v[204:207], a[68:71]
buffer_load_dwordx4 v[172:175], v32, s[72:75], s85 offen offset:1024 sc1
ds_read_b64_tr_b16 v[98:99], v39 offset:7168
v_mfma_f32_16x16x32_bf16 a[72:75], v[136:139], v[204:207], a[72:75]
ds_read_b64_tr_b16 v[100:101], v39 offset:2304
s_add_u32 s84, s84, 16384
v_mfma_f32_16x16x32_bf16 a[76:79], v[140:143], v[204:207], a[76:79]
ds_read_b64_tr_b16 v[102:103], v39 offset:7424
s_waitcnt vmcnt(36)
s_barrier
s_waitcnt lgkmcnt(12)
v_mfma_f32_16x16x32_bf16 a[0:3], v[64:67], v[208:211], a[0:3]
ds_read_b64_tr_b16 v[104:105], v39 offset:10240
v_mfma_f32_16x16x32_bf16 a[4:7], v[68:71], v[208:211], a[4:7]
ds_read_b64_tr_b16 v[106:107], v39 offset:15360
s_add_u32 m0, s79, 81920
v_mfma_f32_16x16x32_bf16 a[8:11], v[72:75], v[208:211], a[8:11]
buffer_load_dwordx4 v33, s[64:67], s86 offen lds
ds_read_b64_tr_b16 v[108:109], v39 offset:10496
v_mfma_f32_16x16x32_bf16 a[12:15], v[76:79], v[208:211], a[12:15]
ds_read_b64_tr_b16 v[110:111], v39 offset:15616
s_waitcnt lgkmcnt(10)
v_mfma_f32_16x16x32_bf16 a[16:19], v[80:83], v[208:211], a[16:19]
ds_read_b64_tr_b16 v[112:113], v39 offset:10752
v_mfma_f32_16x16x32_bf16 a[20:23], v[84:87], v[208:211], a[20:23]
ds_read_b64_tr_b16 v[114:115], v39 offset:15872
v_mfma_f32_16x16x32_bf16 a[24:27], v[88:91], v[208:211], a[24:27]
buffer_load_dwordx4 v34, s[64:67], s86 offen offset:1024 lds
ds_read_b64_tr_b16 v[116:117], v39 offset:11008
s_waitcnt lgkmcnt(7)
v_mfma_f32_16x16x32_bf16 a[28:31], v[92:95], v[208:211], a[28:31]
ds_read_b64_tr_b16 v[118:119], v39 offset:16128
v_mfma_f32_16x16x32_bf16 a[32:35], v[96:99], v[208:211], a[32:35]
ds_read_b64_tr_b16 v[120:121], v39 offset:11264
v_mfma_f32_16x16x32_bf16 a[36:39], v[100:103], v[208:211], a[36:39]
ds_read_b64_tr_b16 v[122:123], v39 offset:16384
v_mfma_f32_16x16x32_bf16 a[40:43], v[64:67], v[212:215], a[40:43]
buffer_load_dwordx4 v35, s[64:67], s86 offen offset:2048 lds
ds_read_b64_tr_b16 v[124:125], v39 offset:11520
v_mfma_f32_16x16x32_bf16 a[44:47], v[68:71], v[212:215], a[44:47]
ds_read_b64_tr_b16 v[126:127], v39 offset:16640
v_mfma_f32_16x16x32_bf16 a[48:51], v[72:75], v[212:215], a[48:51]
ds_read_b64_tr_b16 v[128:129], v39 offset:11776
v_mfma_f32_16x16x32_bf16 a[52:55], v[76:79], v[212:215], a[52:55]
ds_read_b64_tr_b16 v[130:131], v39 offset:16896
v_mfma_f32_16x16x32_bf16 a[56:59], v[80:83], v[212:215], a[56:59]
buffer_load_dwordx4 v36, s[64:67], s86 offen offset:3072 lds
ds_read_b64_tr_b16 v[132:133], v39 offset:12032
v_mfma_f32_16x16x32_bf16 a[60:63], v[84:87], v[212:215], a[60:63]
ds_read_b64_tr_b16 v[134:135], v39 offset:17152
v_mfma_f32_16x16x32_bf16 a[64:67], v[88:91], v[212:215], a[64:67]
ds_read_b64_tr_b16 v[136:137], v39 offset:12288
v_mfma_f32_16x16x32_bf16 a[68:71], v[92:95], v[212:215], a[68:71]
ds_read_b64_tr_b16 v[138:139], v39 offset:17408
s_add_u32 m0, s79, 86016
v_mfma_f32_16x16x32_bf16 a[72:75], v[96:99], v[212:215], a[72:75]
buffer_load_dwordx4 v37, s[64:67], s86 offen lds
ds_read_b64_tr_b16 v[140:141], v39 offset:12544
v_mfma_f32_16x16x32_bf16 a[76:79], v[100:103], v[212:215], a[76:79]
ds_read_b64_tr_b16 v[142:143], v39 offset:17664
s_add_u32 s86, s86, 81920
v_add_u32 v39, 122880, v38
s_waitcnt lgkmcnt(12)
v_mfma_f32_16x16x32_bf16 a[0:3], v[104:107], v[216:219], a[0:3]
ds_read_b64_tr_b16 v[64:65], v39 offset:0
v_mfma_f32_16x16x32_bf16 a[4:7], v[108:111], v[216:219], a[4:7]
ds_read_b64_tr_b16 v[66:67], v39 offset:5120
v_mfma_f32_16x16x32_bf16 a[8:11], v[112:115], v[216:219], a[8:11]
ds_read_b64_tr_b16 v[68:69], v39 offset:256
s_add_u32 s85, s84, 8192
v_mfma_f32_16x16x32_bf16 a[12:15], v[116:119], v[216:219], a[12:15]
buffer_load_dwordx4 v[192:195], v32, s[72:75], s84 offen sc1
ds_read_b64_tr_b16 v[70:71], v39 offset:5376
s_waitcnt lgkmcnt(10)
v_mfma_f32_16x16x32_bf16 a[16:19], v[120:123], v[216:219], a[16:19]
ds_read_b64_tr_b16 v[72:73], v39 offset:512
v_mfma_f32_16x16x32_bf16 a[20:23], v[124:127], v[216:219], a[20:23]
ds_read_b64_tr_b16 v[74:75], v39 offset:5632
v_mfma_f32_16x16x32_bf16 a[24:27], v[128:131], v[216:219], a[24:27]
ds_read_b64_tr_b16 v[76:77], v39 offset:768
s_waitcnt lgkmcnt(7)
v_mfma_f32_16x16x32_bf16 a[28:31], v[132:135], v[216:219], a[28:31]
ds_read_b64_tr_b16 v[78:79], v39 offset:5888
v_mfma_f32_16x16x32_bf16 a[32:35], v[136:139], v[216:219], a[32:35]
buffer_load_dwordx4 v[196:199], v32, s[72:75], s84 offen offset:1024 sc1
ds_read_b64_tr_b16 v[80:81], v39 offset:1024
v_mfma_f32_16x16x32_bf16 a[36:39], v[140:143], v[216:219], a[36:39]
ds_read_b64_tr_b16 v[82:83], v39 offset:6144
v_mfma_f32_16x16x32_bf16 a[40:43], v[104:107], v[220:223], a[40:43]
ds_read_b64_tr_b16 v[84:85], v39 offset:1280
v_mfma_f32_16x16x32_bf16 a[44:47], v[108:111], v[220:223], a[44:47]
ds_read_b64_tr_b16 v[86:87], v39 offset:6400
v_mfma_f32_16x16x32_bf16 a[48:51], v[112:115], v[220:223], a[48:51]
buffer_load_dwordx4 v[200:203], v32, s[72:75], s85 offen sc1
ds_read_b64_tr_b16 v[88:89], v39 offset:1536
v_mfma_f32_16x16x32_bf16 a[52:55], v[116:119], v[220:223], a[52:55]
ds_read_b64_tr_b16 v[90:91], v39 offset:6656
v_mfma_f32_16x16x32_bf16 a[56:59], v[120:123], v[220:223], a[56:59]
ds_read_b64_tr_b16 v[92:93], v39 offset:1792
v_mfma_f32_16x16x32_bf16 a[60:63], v[124:127], v[220:223], a[60:63]
ds_read_b64_tr_b16 v[94:95], v39 offset:6912
v_mfma_f32_16x16x32_bf16 a[64:67], v[128:131], v[220:223], a[64:67]
ds_read_b64_tr_b16 v[96:97], v39 offset:2048
v_mfma_f32_16x16x32_bf16 a[68:71], v[132:135], v[220:223], a[68:71]
buffer_load_dwordx4 v[204:207], v32, s[72:75], s85 offen offset:1024 sc1
ds_read_b64_tr_b16 v[98:99], v39 offset:7168
v_mfma_f32_16x16x32_bf16 a[72:75], v[136:139], v[220:223], a[72:75]
ds_read_b64_tr_b16 v[100:101], v39 offset:2304
s_add_u32 s84, s84, 16384
v_mfma_f32_16x16x32_bf16 a[76:79], v[140:143], v[220:223], a[76:79]
ds_read_b64_tr_b16 v[102:103], v39 offset:7424
s_waitcnt vmcnt(36)
s_barrier
s_waitcnt lgkmcnt(12)
v_mfma_f32_16x16x32_bf16 a[0:3], v[64:67], v[224:227], a[0:3]
ds_read_b64_tr_b16 v[104:105], v39 offset:10240
v_mfma_f32_16x16x32_bf16 a[4:7], v[68:71], v[224:227], a[4:7]
ds_read_b64_tr_b16 v[106:107], v39 offset:15360
s_add_u32 m0, s79, 102400
v_mfma_f32_16x16x32_bf16 a[8:11], v[72:75], v[224:227], a[8:11]
buffer_load_dwordx4 v33, s[64:67], s86 offen lds
ds_read_b64_tr_b16 v[108:109], v39 offset:10496
v_mfma_f32_16x16x32_bf16 a[12:15], v[76:79], v[224:227], a[12:15]
ds_read_b64_tr_b16 v[110:111], v39 offset:15616
s_waitcnt lgkmcnt(10)
v_mfma_f32_16x16x32_bf16 a[16:19], v[80:83], v[224:227], a[16:19]
ds_read_b64_tr_b16 v[112:113], v39 offset:10752
v_mfma_f32_16x16x32_bf16 a[20:23], v[84:87], v[224:227], a[20:23]
ds_read_b64_tr_b16 v[114:115], v39 offset:15872
v_mfma_f32_16x16x32_bf16 a[24:27], v[88:91], v[224:227], a[24:27]
buffer_load_dwordx4 v34, s[64:67], s86 offen offset:1024 lds
ds_read_b64_tr_b16 v[116:117], v39 offset:11008
s_waitcnt lgkmcnt(7)
v_mfma_f32_16x16x32_bf16 a[28:31], v[92:95], v[224:227], a[28:31]
ds_read_b64_tr_b16 v[118:119], v39 offset:16128
v_mfma_f32_16x16x32_bf16 a[32:35], v[96:99], v[224:227], a[32:35]
ds_read_b64_tr_b16 v[120:121], v39 offset:11264
v_mfma_f32_16x16x32_bf16 a[36:39], v[100:103], v[224:227], a[36:39]
ds_read_b64_tr_b16 v[122:123], v39 offset:16384
v_mfma_f32_16x16x32_bf16 a[40:43], v[64:67], v[228:231], a[40:43]
buffer_load_dwordx4 v35, s[64:67], s86 offen offset:2048 lds
ds_read_b64_tr_b16 v[124:125], v39 offset:11520
v_mfma_f32_16x16x32_bf16 a[44:47], v[68:71], v[228:231], a[44:47]
ds_read_b64_tr_b16 v[126:127], v39 offset:16640
v_mfma_f32_16x16x32_bf16 a[48:51], v[72:75], v[228:231], a[48:51]
ds_read_b64_tr_b16 v[128:129], v39 offset:11776
v_mfma_f32_16x16x32_bf16 a[52:55], v[76:79], v[228:231], a[52:55]
ds_read_b64_tr_b16 v[130:131], v39 offset:16896
v_mfma_f32_16x16x32_bf16 a[56:59], v[80:83], v[228:231], a[56:59]
buffer_load_dwordx4 v36, s[64:67], s86 offen offset:3072 lds
ds_read_b64_tr_b16 v[132:133], v39 offset:12032
v_mfma_f32_16x16x32_bf16 a[60:63], v[84:87], v[228:231], a[60:63]
ds_read_b64_tr_b16 v[134:135], v39 offset:17152
v_mfma_f32_16x16x32_bf16 a[64:67], v[88:91], v[228:231], a[64:67]
ds_read_b64_tr_b16 v[136:137], v39 offset:12288
v_mfma_f32_16x16x32_bf16 a[68:71], v[92:95], v[228:231], a[68:71]
ds_read_b64_tr_b16 v[138:139], v39 offset:17408
s_add_u32 m0, s79, 106496
v_mfma_f32_16x16x32_bf16 a[72:75], v[96:99], v[228:231], a[72:75]
buffer_load_dwordx4 v37, s[64:67], s86 offen lds
ds_read_b64_tr_b16 v[140:141], v39 offset:12544
v_mfma_f32_16x16x32_bf16 a[76:79], v[100:103], v[228:231], a[76:79]
ds_read_b64_tr_b16 v[142:143], v39 offset:17664
s_add_u32 s86, s86, 81920
v_add_u32 v39, 143360, v38
s_waitcnt lgkmcnt(12)
v_mfma_f32_16x16x32_bf16 a[0:3], v[104:107], v[232:235], a[0:3]
ds_read_b64_tr_b16 v[64:65], v39 offset:0
v_mfma_f32_16x16x32_bf16 a[4:7], v[108:111], v[232:235], a[4:7]
ds_read_b64_tr_b16 v[66:67], v39 offset:5120
v_mfma_f32_16x16x32_bf16 a[8:11], v[112:115], v[232:235], a[8:11]
ds_read_b64_tr_b16 v[68:69], v39 offset:256
s_add_u32 s85, s84, 8192
v_mfma_f32_16x16x32_bf16 a[12:15], v[116:119], v[232:235], a[12:15]
buffer_load_dwordx4 v[208:211], v32, s[72:75], s84 offen sc1
ds_read_b64_tr_b16 v[70:71], v39 offset:5376
s_waitcnt lgkmcnt(10)
v_mfma_f32_16x16x32_bf16 a[16:19], v[120:123], v[232:235], a[16:19]
ds_read_b64_tr_b16 v[72:73], v39 offset:512
v_mfma_f32_16x16x32_bf16 a[20:23], v[124:127], v[232:235], a[20:23]
ds_read_b64_tr_b16 v[74:75], v39 offset:5632
v_mfma_f32_16x16x32_bf16 a[24:27], v[128:131], v[232:235], a[24:27]
ds_read_b64_tr_b16 v[76:77], v39 offset:768
s_waitcnt lgkmcnt(7)
v_mfma_f32_16x16x32_bf16 a[28:31], v[132:135], v[232:235], a[28:31]
ds_read_b64_tr_b16 v[78:79], v39 offset:5888
v_mfma_f32_16x16x32_bf16 a[32:35], v[136:139], v[232:235], a[32:35]
buffer_load_dwordx4 v[212:215], v32, s[72:75], s84 offen offset:1024 sc1
ds_read_b64_tr_b16 v[80:81], v39 offset:1024
v_mfma_f32_16x16x32_bf16 a[36:39], v[140:143], v[232:235], a[36:39]
ds_read_b64_tr_b16 v[82:83], v39 offset:6144
v_mfma_f32_16x16x32_bf16 a[40:43], v[104:107], v[236:239], a[40:43]
ds_read_b64_tr_b16 v[84:85], v39 offset:1280
v_mfma_f32_16x16x32_bf16 a[44:47], v[108:111], v[236:239], a[44:47]
ds_read_b64_tr_b16 v[86:87], v39 offset:6400
v_mfma_f32_16x16x32_bf16 a[48:51], v[112:115], v[236:239], a[48:51]
buffer_load_dwordx4 v[216:219], v32, s[72:75], s85 offen sc1
ds_read_b64_tr_b16 v[88:89], v39 offset:1536
v_mfma_f32_16x16x32_bf16 a[52:55], v[116:119], v[236:239], a[52:55]
ds_read_b64_tr_b16 v[90:91], v39 offset:6656
v_mfma_f32_16x16x32_bf16 a[56:59], v[120:123], v[236:239], a[56:59]
ds_read_b64_tr_b16 v[92:93], v39 offset:1792
v_mfma_f32_16x16x32_bf16 a[60:63], v[124:127], v[236:239], a[60:63]
ds_read_b64_tr_b16 v[94:95], v39 offset:6912
v_mfma_f32_16x16x32_bf16 a[64:67], v[128:131], v[236:239], a[64:67]
ds_read_b64_tr_b16 v[96:97], v39 offset:2048
v_mfma_f32_16x16x32_bf16 a[68:71], v[132:135], v[236:239], a[68:71]
buffer_load_dwordx4 v[220:223], v32, s[72:75], s85 offen offset:1024 sc1
ds_read_b64_tr_b16 v[98:99], v39 offset:7168
v_mfma_f32_16x16x32_bf16 a[72:75], v[136:139], v[236:239], a[72:75]
ds_read_b64_tr_b16 v[100:101], v39 offset:2304
s_add_u32 s84, s84, 16384
v_mfma_f32_16x16x32_bf16 a[76:79], v[140:143], v[236:239], a[76:79]
ds_read_b64_tr_b16 v[102:103], v39 offset:7424
s_waitcnt vmcnt(36)
s_barrier
s_waitcnt lgkmcnt(12)
v_mfma_f32_16x16x32_bf16 a[0:3], v[64:67], v[240:243], a[0:3]
ds_read_b64_tr_b16 v[104:105], v39 offset:10240
v_mfma_f32_16x16x32_bf16 a[4:7], v[68:71], v[240:243], a[4:7]
ds_read_b64_tr_b16 v[106:107], v39 offset:15360
s_add_u32 m0, s79, 122880
v_mfma_f32_16x16x32_bf16 a[8:11], v[72:75], v[240:243], a[8:11]
buffer_load_dwordx4 v33, s[64:67], s86 offen lds
ds_read_b64_tr_b16 v[108:109], v39 offset:10496
v_mfma_f32_16x16x32_bf16 a[12:15], v[76:79], v[240:243], a[12:15]
ds_read_b64_tr_b16 v[110:111], v39 offset:15616
s_waitcnt lgkmcnt(10)
v_mfma_f32_16x16x32_bf16 a[16:19], v[80:83], v[240:243], a[16:19]
ds_read_b64_tr_b16 v[112:113], v39 offset:10752
v_mfma_f32_16x16x32_bf16 a[20:23], v[84:87], v[240:243], a[20:23]
ds_read_b64_tr_b16 v[114:115], v39 offset:15872
v_mfma_f32_16x16x32_bf16 a[24:27], v[88:91], v[240:243], a[24:27]
buffer_load_dwordx4 v34, s[64:67], s86 offen offset:1024 lds
ds_read_b64_tr_b16 v[116:117], v39 offset:11008
s_waitcnt lgkmcnt(7)
v_mfma_f32_16x16x32_bf16 a[28:31], v[92:95], v[240:243], a[28:31]
ds_read_b64_tr_b16 v[118:119], v39 offset:16128
v_mfma_f32_16x16x32_bf16 a[32:35], v[96:99], v[240:243], a[32:35]
ds_read_b64_tr_b16 v[120:121], v39 offset:11264
v_mfma_f32_16x16x32_bf16 a[36:39], v[100:103], v[240:243], a[36:39]
ds_read_b64_tr_b16 v[122:123], v39 offset:16384
v_mfma_f32_16x16x32_bf16 a[40:43], v[64:67], v[244:247], a[40:43]
buffer_load_dwordx4 v35, s[64:67], s86 offen offset:2048 lds
ds_read_b64_tr_b16 v[124:125], v39 offset:11520
v_mfma_f32_16x16x32_bf16 a[44:47], v[68:71], v[244:247], a[44:47]
ds_read_b64_tr_b16 v[126:127], v39 offset:16640
v_mfma_f32_16x16x32_bf16 a[48:51], v[72:75], v[244:247], a[48:51]
ds_read_b64_tr_b16 v[128:129], v39 offset:11776
v_mfma_f32_16x16x32_bf16 a[52:55], v[76:79], v[244:247], a[52:55]
ds_read_b64_tr_b16 v[130:131], v39 offset:16896
v_mfma_f32_16x16x32_bf16 a[56:59], v[80:83], v[244:247], a[56:59]
buffer_load_dwordx4 v36, s[64:67], s86 offen offset:3072 lds
ds_read_b64_tr_b16 v[132:133], v39 offset:12032
v_mfma_f32_16x16x32_bf16 a[60:63], v[84:87], v[244:247], a[60:63]
ds_read_b64_tr_b16 v[134:135], v39 offset:17152
v_mfma_f32_16x16x32_bf16 a[64:67], v[88:91], v[244:247], a[64:67]
ds_read_b64_tr_b16 v[136:137], v39 offset:12288
v_mfma_f32_16x16x32_bf16 a[68:71], v[92:95], v[244:247], a[68:71]
ds_read_b64_tr_b16 v[138:139], v39 offset:17408
s_add_u32 m0, s79, 126976
v_mfma_f32_16x16x32_bf16 a[72:75], v[96:99], v[244:247], a[72:75]
buffer_load_dwordx4 v37, s[64:67], s86 offen lds
ds_read_b64_tr_b16 v[140:141], v39 offset:12544
v_mfma_f32_16x16x32_bf16 a[76:79], v[100:103], v[244:247], a[76:79]
ds_read_b64_tr_b16 v[142:143], v39 offset:17664
s_add_u32 s86, s86, 81920
v_add_u32 v39, 0, v38
s_waitcnt lgkmcnt(12)
v_mfma_f32_16x16x32_bf16 a[0:3], v[104:107], v[248:251], a[0:3]
ds_read_b64_tr_b16 v[64:65], v39 offset:0
v_mfma_f32_16x16x32_bf16 a[4:7], v[108:111], v[248:251], a[4:7]
ds_read_b64_tr_b16 v[66:67], v39 offset:5120
v_mfma_f32_16x16x32_bf16 a[8:11], v[112:115], v[248:251], a[8:11]
ds_read_b64_tr_b16 v[68:69], v39 offset:256
s_add_u32 s85, s84, 8192
v_mfma_f32_16x16x32_bf16 a[12:15], v[116:119], v[248:251], a[12:15]
buffer_load_dwordx4 v[224:227], v32, s[72:75], s84 offen sc1
ds_read_b64_tr_b16 v[70:71], v39 offset:5376
s_waitcnt lgkmcnt(10)
v_mfma_f32_16x16x32_bf16 a[16:19], v[120:123], v[248:251], a[16:19]
ds_read_b64_tr_b16 v[72:73], v39 offset:512
v_mfma_f32_16x16x32_bf16 a[20:23], v[124:127], v[248:251], a[20:23]
ds_read_b64_tr_b16 v[74:75], v39 offset:5632
v_mfma_f32_16x16x32_bf16 a[24:27], v[128:131], v[248:251], a[24:27]
ds_read_b64_tr_b16 v[76:77], v39 offset:768
s_waitcnt lgkmcnt(7)
v_mfma_f32_16x16x32_bf16 a[28:31], v[132:135], v[248:251], a[28:31]
ds_read_b64_tr_b16 v[78:79], v39 offset:5888
v_mfma_f32_16x16x32_bf16 a[32:35], v[136:139], v[248:251], a[32:35]
buffer_load_dwordx4 v[228:231], v32, s[72:75], s84 offen offset:1024 sc1
ds_read_b64_tr_b16 v[80:81], v39 offset:1024
v_mfma_f32_16x16x32_bf16 a[36:39], v[140:143], v[248:251], a[36:39]
ds_read_b64_tr_b16 v[82:83], v39 offset:6144
v_mfma_f32_16x16x32_bf16 a[40:43], v[104:107], v[252:255], a[40:43]
ds_read_b64_tr_b16 v[84:85], v39 offset:1280
v_mfma_f32_16x16x32_bf16 a[44:47], v[108:111], v[252:255], a[44:47]
ds_read_b64_tr_b16 v[86:87], v39 offset:6400
v_mfma_f32_16x16x32_bf16 a[48:51], v[112:115], v[252:255], a[48:51]
buffer_load_dwordx4 v[232:235], v32, s[72:75], s85 offen sc1
ds_read_b64_tr_b16 v[88:89], v39 offset:1536
v_mfma_f32_16x16x32_bf16 a[52:55], v[116:119], v[252:255], a[52:55]
ds_read_b64_tr_b16 v[90:91], v39 offset:6656
v_mfma_f32_16x16x32_bf16 a[56:59], v[120:123], v[252:255], a[56:59]
ds_read_b64_tr_b16 v[92:93], v39 offset:1792
v_mfma_f32_16x16x32_bf16 a[60:63], v[124:127], v[252:255], a[60:63]
ds_read_b64_tr_b16 v[94:95], v39 offset:6912
v_mfma_f32_16x16x32_bf16 a[64:67], v[128:131], v[252:255], a[64:67]
ds_read_b64_tr_b16 v[96:97], v39 offset:2048
v_mfma_f32_16x16x32_bf16 a[68:71], v[132:135], v[252:255], a[68:71]
buffer_load_dwordx4 v[236:239], v32, s[72:75], s85 offen offset:1024 sc1
ds_read_b64_tr_b16 v[98:99], v39 offset:7168
v_mfma_f32_16x16x32_bf16 a[72:75], v[136:139], v[252:255], a[72:75]
ds_read_b64_tr_b16 v[100:101], v39 offset:2304
s_add_u32 s84, s84, 16384
v_mfma_f32_16x16x32_bf16 a[76:79], v[140:143], v[252:255], a[76:79]
ds_read_b64_tr_b16 v[102:103], v39 offset:7424
s_waitcnt vmcnt(36)
s_barrier
s_waitcnt lgkmcnt(12)
v_mfma_f32_16x16x32_bf16 a[0:3], v[64:67], v[144:147], a[0:3]
ds_read_b64_tr_b16 v[104:105], v39 offset:10240
v_mfma_f32_16x16x32_bf16 a[4:7], v[68:71], v[144:147], a[4:7]
ds_read_b64_tr_b16 v[106:107], v39 offset:15360
v_mfma_f32_16x16x32_bf16 a[8:11], v[72:75], v[144:147], a[8:11]
ds_read_b64_tr_b16 v[108:109], v39 offset:10496
v_mfma_f32_16x16x32_bf16 a[12:15], v[76:79], v[144:147], a[12:15]
ds_read_b64_tr_b16 v[110:111], v39 offset:15616
s_waitcnt lgkmcnt(10)
v_mfma_f32_16x16x32_bf16 a[16:19], v[80:83], v[144:147], a[16:19]
ds_read_b64_tr_b16 v[112:113], v39 offset:10752
v_mfma_f32_16x16x32_bf16 a[20:23], v[84:87], v[144:147], a[20:23]
ds_read_b64_tr_b16 v[114:115], v39 offset:15872
v_mfma_f32_16x16x32_bf16 a[24:27], v[88:91], v[144:147], a[24:27]
ds_read_b64_tr_b16 v[116:117], v39 offset:11008
s_waitcnt lgkmcnt(7)
v_mfma_f32_16x16x32_bf16 a[28:31], v[92:95], v[144:147], a[28:31]
ds_read_b64_tr_b16 v[118:119], v39 offset:16128
v_mfma_f32_16x16x32_bf16 a[32:35], v[96:99], v[144:147], a[32:35]
ds_read_b64_tr_b16 v[120:121], v39 offset:11264
v_mfma_f32_16x16x32_bf16 a[36:39], v[100:103], v[144:147], a[36:39]
ds_read_b64_tr_b16 v[122:123], v39 offset:16384
v_mfma_f32_16x16x32_bf16 a[40:43], v[64:67], v[148:151], a[40:43]
ds_read_b64_tr_b16 v[124:125], v39 offset:11520
v_mfma_f32_16x16x32_bf16 a[44:47], v[68:71], v[148:151], a[44:47]
ds_read_b64_tr_b16 v[126:127], v39 offset:16640
v_mfma_f32_16x16x32_bf16 a[48:51], v[72:75], v[148:151], a[48:51]
ds_read_b64_tr_b16 v[128:129], v39 offset:11776
v_mfma_f32_16x16x32_bf16 a[52:55], v[76:79], v[148:151], a[52:55]
ds_read_b64_tr_b16 v[130:131], v39 offset:16896
v_mfma_f32_16x16x32_bf16 a[56:59], v[80:83], v[148:151], a[56:59]
ds_read_b64_tr_b16 v[132:133], v39 offset:12032
v_mfma_f32_16x16x32_bf16 a[60:63], v[84:87], v[148:151], a[60:63]
ds_read_b64_tr_b16 v[134:135], v39 offset:17152
v_mfma_f32_16x16x32_bf16 a[64:67], v[88:91], v[148:151], a[64:67]
ds_read_b64_tr_b16 v[136:137], v39 offset:12288
v_mfma_f32_16x16x32_bf16 a[68:71], v[92:95], v[148:151], a[68:71]
ds_read_b64_tr_b16 v[138:139], v39 offset:17408
v_mfma_f32_16x16x32_bf16 a[72:75], v[96:99], v[148:151], a[72:75]
ds_read_b64_tr_b16 v[140:141], v39 offset:12544
v_mfma_f32_16x16x32_bf16 a[76:79], v[100:103], v[148:151], a[76:79]
ds_read_b64_tr_b16 v[142:143], v39 offset:17664
v_add_u32 v39, 20480, v38
s_waitcnt lgkmcnt(12)
v_mfma_f32_16x16x32_bf16 a[0:3], v[104:107], v[152:155], a[0:3]
ds_read_b64_tr_b16 v[64:65], v39 offset:0
v_mfma_f32_16x16x32_bf16 a[4:7], v[108:111], v[152:155], a[4:7]
ds_read_b64_tr_b16 v[66:67], v39 offset:5120
v_mfma_f32_16x16x32_bf16 a[8:11], v[112:115], v[152:155], a[8:11]
ds_read_b64_tr_b16 v[68:69], v39 offset:256
s_add_u32 s85, s84, 8192
v_mfma_f32_16x16x32_bf16 a[12:15], v[116:119], v[152:155], a[12:15]
buffer_load_dwordx4 v[240:243], v32, s[72:75], s84 offen sc1
ds_read_b64_tr_b16 v[70:71], v39 offset:5376
s_waitcnt lgkmcnt(10)
v_mfma_f32_16x16x32_bf16 a[16:19], v[120:123], v[152:155], a[16:19]
ds_read_b64_tr_b16 v[72:73], v39 offset:512
v_mfma_f32_16x16x32_bf16 a[20:23], v[124:127], v[152:155], a[20:23]
ds_read_b64_tr_b16 v[74:75], v39 offset:5632
v_mfma_f32_16x16x32_bf16 a[24:27], v[128:131], v[152:155], a[24:27]
ds_read_b64_tr_b16 v[76:77], v39 offset:768
s_waitcnt lgkmcnt(7)
v_mfma_f32_16x16x32_bf16 a[28:31], v[132:135], v[152:155], a[28:31]
ds_read_b64_tr_b16 v[78:79], v39 offset:5888
v_mfma_f32_16x16x32_bf16 a[32:35], v[136:139], v[152:155], a[32:35]
buffer_load_dwordx4 v[244:247], v32, s[72:75], s84 offen offset:1024 sc1
ds_read_b64_tr_b16 v[80:81], v39 offset:1024
v_mfma_f32_16x16x32_bf16 a[36:39], v[140:143], v[152:155], a[36:39]
ds_read_b64_tr_b16 v[82:83], v39 offset:6144
v_mfma_f32_16x16x32_bf16 a[40:43], v[104:107], v[156:159], a[40:43]
ds_read_b64_tr_b16 v[84:85], v39 offset:1280
v_mfma_f32_16x16x32_bf16 a[44:47], v[108:111], v[156:159], a[44:47]
ds_read_b64_tr_b16 v[86:87], v39 offset:6400
v_mfma_f32_16x16x32_bf16 a[48:51], v[112:115], v[156:159], a[48:51]
buffer_load_dwordx4 v[248:251], v32, s[72:75], s85 offen sc1
ds_read_b64_tr_b16 v[88:89], v39 offset:1536
v_mfma_f32_16x16x32_bf16 a[52:55], v[116:119], v[156:159], a[52:55]
ds_read_b64_tr_b16 v[90:91], v39 offset:6656
v_mfma_f32_16x16x32_bf16 a[56:59], v[120:123], v[156:159], a[56:59]
ds_read_b64_tr_b16 v[92:93], v39 offset:1792
v_mfma_f32_16x16x32_bf16 a[60:63], v[124:127], v[156:159], a[60:63]
ds_read_b64_tr_b16 v[94:95], v39 offset:6912
v_mfma_f32_16x16x32_bf16 a[64:67], v[128:131], v[156:159], a[64:67]
ds_read_b64_tr_b16 v[96:97], v39 offset:2048
v_mfma_f32_16x16x32_bf16 a[68:71], v[132:135], v[156:159], a[68:71]
buffer_load_dwordx4 v[252:255], v32, s[72:75], s85 offen offset:1024 sc1
ds_read_b64_tr_b16 v[98:99], v39 offset:7168
v_mfma_f32_16x16x32_bf16 a[72:75], v[136:139], v[156:159], a[72:75]
ds_read_b64_tr_b16 v[100:101], v39 offset:2304
s_add_u32 s84, s84, 16384
v_mfma_f32_16x16x32_bf16 a[76:79], v[140:143], v[156:159], a[76:79]
ds_read_b64_tr_b16 v[102:103], v39 offset:7424
s_waitcnt vmcnt(31)
s_barrier
s_waitcnt lgkmcnt(12)
v_mfma_f32_16x16x32_bf16 a[0:3], v[64:67], v[160:163], a[0:3]
ds_read_b64_tr_b16 v[104:105], v39 offset:10240
v_mfma_f32_16x16x32_bf16 a[4:7], v[68:71], v[160:163], a[4:7]
ds_read_b64_tr_b16 v[106:107], v39 offset:15360
v_mfma_f32_16x16x32_bf16 a[8:11], v[72:75], v[160:163], a[8:11]
ds_read_b64_tr_b16 v[108:109], v39 offset:10496
v_mfma_f32_16x16x32_bf16 a[12:15], v[76:79], v[160:163], a[12:15]
ds_read_b64_tr_b16 v[110:111], v39 offset:15616
s_waitcnt lgkmcnt(10)
v_mfma_f32_16x16x32_bf16 a[16:19], v[80:83], v[160:163], a[16:19]
ds_read_b64_tr_b16 v[112:113], v39 offset:10752
v_mfma_f32_16x16x32_bf16 a[20:23], v[84:87], v[160:163], a[20:23]
ds_read_b64_tr_b16 v[114:115], v39 offset:15872
v_mfma_f32_16x16x32_bf16 a[24:27], v[88:91], v[160:163], a[24:27]
ds_read_b64_tr_b16 v[116:117], v39 offset:11008
s_waitcnt lgkmcnt(7)
v_mfma_f32_16x16x32_bf16 a[28:31], v[92:95], v[160:163], a[28:31]
ds_read_b64_tr_b16 v[118:119], v39 offset:16128
v_mfma_f32_16x16x32_bf16 a[32:35], v[96:99], v[160:163], a[32:35]
ds_read_b64_tr_b16 v[120:121], v39 offset:11264
v_mfma_f32_16x16x32_bf16 a[36:39], v[100:103], v[160:163], a[36:39]
ds_read_b64_tr_b16 v[122:123], v39 offset:16384
v_mfma_f32_16x16x32_bf16 a[40:43], v[64:67], v[164:167], a[40:43]
ds_read_b64_tr_b16 v[124:125], v39 offset:11520
v_mfma_f32_16x16x32_bf16 a[44:47], v[68:71], v[164:167], a[44:47]
ds_read_b64_tr_b16 v[126:127], v39 offset:16640
v_mfma_f32_16x16x32_bf16 a[48:51], v[72:75], v[164:167], a[48:51]
ds_read_b64_tr_b16 v[128:129], v39 offset:11776
v_mfma_f32_16x16x32_bf16 a[52:55], v[76:79], v[164:167], a[52:55]
ds_read_b64_tr_b16 v[130:131], v39 offset:16896
v_mfma_f32_16x16x32_bf16 a[56:59], v[80:83], v[164:167], a[56:59]
ds_read_b64_tr_b16 v[132:133], v39 offset:12032
v_mfma_f32_16x16x32_bf16 a[60:63], v[84:87], v[164:167], a[60:63]
ds_read_b64_tr_b16 v[134:135], v39 offset:17152
v_mfma_f32_16x16x32_bf16 a[64:67], v[88:91], v[164:167], a[64:67]
ds_read_b64_tr_b16 v[136:137], v39 offset:12288
v_mfma_f32_16x16x32_bf16 a[68:71], v[92:95], v[164:167], a[68:71]
ds_read_b64_tr_b16 v[138:139], v39 offset:17408
v_mfma_f32_16x16x32_bf16 a[72:75], v[96:99], v[164:167], a[72:75]
ds_read_b64_tr_b16 v[140:141], v39 offset:12544
v_mfma_f32_16x16x32_bf16 a[76:79], v[100:103], v[164:167], a[76:79]
ds_read_b64_tr_b16 v[142:143], v39 offset:17664
v_add_u32 v39, 40960, v38
s_waitcnt lgkmcnt(12)
v_mfma_f32_16x16x32_bf16 a[0:3], v[104:107], v[168:171], a[0:3]
ds_read_b64_tr_b16 v[64:65], v39 offset:0
v_mfma_f32_16x16x32_bf16 a[4:7], v[108:111], v[168:171], a[4:7]
ds_read_b64_tr_b16 v[66:67], v39 offset:5120
v_mfma_f32_16x16x32_bf16 a[8:11], v[112:115], v[168:171], a[8:11]
ds_read_b64_tr_b16 v[68:69], v39 offset:256
s_add_u32 s85, s84, 8192
v_mfma_f32_16x16x32_bf16 a[12:15], v[116:119], v[168:171], a[12:15]
buffer_load_dwordx4 v[144:147], v32, s[72:75], s84 offen sc1
ds_read_b64_tr_b16 v[70:71], v39 offset:5376
s_waitcnt lgkmcnt(10)
v_mfma_f32_16x16x32_bf16 a[16:19], v[120:123], v[168:171], a[16:19]
ds_read_b64_tr_b16 v[72:73], v39 offset:512
v_mfma_f32_16x16x32_bf16 a[20:23], v[124:127], v[168:171], a[20:23]
ds_read_b64_tr_b16 v[74:75], v39 offset:5632
v_mfma_f32_16x16x32_bf16 a[24:27], v[128:131], v[168:171], a[24:27]
ds_read_b64_tr_b16 v[76:77], v39 offset:768
s_waitcnt lgkmcnt(7)
v_mfma_f32_16x16x32_bf16 a[28:31], v[132:135], v[168:171], a[28:31]
ds_read_b64_tr_b16 v[78:79], v39 offset:5888
v_mfma_f32_16x16x32_bf16 a[32:35], v[136:139], v[168:171], a[32:35]
buffer_load_dwordx4 v[148:151], v32, s[72:75], s84 offen offset:1024 sc1
ds_read_b64_tr_b16 v[80:81], v39 offset:1024
v_mfma_f32_16x16x32_bf16 a[36:39], v[140:143], v[168:171], a[36:39]
ds_read_b64_tr_b16 v[82:83], v39 offset:6144
v_mfma_f32_16x16x32_bf16 a[40:43], v[104:107], v[172:175], a[40:43]
ds_read_b64_tr_b16 v[84:85], v39 offset:1280
v_mfma_f32_16x16x32_bf16 a[44:47], v[108:111], v[172:175], a[44:47]
ds_read_b64_tr_b16 v[86:87], v39 offset:6400
v_mfma_f32_16x16x32_bf16 a[48:51], v[112:115], v[172:175], a[48:51]
buffer_load_dwordx4 v[152:155], v32, s[72:75], s85 offen sc1
ds_read_b64_tr_b16 v[88:89], v39 offset:1536
v_mfma_f32_16x16x32_bf16 a[52:55], v[116:119], v[172:175], a[52:55]
ds_read_b64_tr_b16 v[90:91], v39 offset:6656
v_mfma_f32_16x16x32_bf16 a[56:59], v[120:123], v[172:175], a[56:59]
ds_read_b64_tr_b16 v[92:93], v39 offset:1792
v_mfma_f32_16x16x32_bf16 a[60:63], v[124:127], v[172:175], a[60:63]
ds_read_b64_tr_b16 v[94:95], v39 offset:6912
v_mfma_f32_16x16x32_bf16 a[64:67], v[128:131], v[172:175], a[64:67]
ds_read_b64_tr_b16 v[96:97], v39 offset:2048
v_mfma_f32_16x16x32_bf16 a[68:71], v[132:135], v[172:175], a[68:71]
buffer_load_dwordx4 v[156:159], v32, s[72:75], s85 offen offset:1024 sc1
ds_read_b64_tr_b16 v[98:99], v39 offset:7168
v_mfma_f32_16x16x32_bf16 a[72:75], v[136:139], v[172:175], a[72:75]
ds_read_b64_tr_b16 v[100:101], v39 offset:2304
s_add_u32 s84, s84, 16384
v_mfma_f32_16x16x32_bf16 a[76:79], v[140:143], v[172:175], a[76:79]
ds_read_b64_tr_b16 v[102:103], v39 offset:7424
s_waitcnt vmcnt(26)
s_barrier
s_waitcnt lgkmcnt(12)
v_mfma_f32_16x16x32_bf16 a[0:3], v[64:67], v[192:195], a[0:3]
ds_read_b64_tr_b16 v[104:105], v39 offset:10240
v_mfma_f32_16x16x32_bf16 a[4:7], v[68:71], v[192:195], a[4:7]
ds_read_b64_tr_b16 v[106:107], v39 offset:15360
v_mfma_f32_16x16x32_bf16 a[8:11], v[72:75], v[192:195], a[8:11]
ds_read_b64_tr_b16 v[108:109], v39 offset:10496
v_mfma_f32_16x16x32_bf16 a[12:15], v[76:79], v[192:195], a[12:15]
ds_read_b64_tr_b16 v[110:111], v39 offset:15616
s_waitcnt lgkmcnt(10)
v_mfma_f32_16x16x32_bf16 a[16:19], v[80:83], v[192:195], a[16:19]
ds_read_b64_tr_b16 v[112:113], v39 offset:10752
v_mfma_f32_16x16x32_bf16 a[20:23], v[84:87], v[192:195], a[20:23]
ds_read_b64_tr_b16 v[114:115], v39 offset:15872
v_mfma_f32_16x16x32_bf16 a[24:27], v[88:91], v[192:195], a[24:27]
ds_read_b64_tr_b16 v[116:117], v39 offset:11008
s_waitcnt lgkmcnt(7)
v_mfma_f32_16x16x32_bf16 a[28:31], v[92:95], v[192:195], a[28:31]
ds_read_b64_tr_b16 v[118:119], v39 offset:16128
v_mfma_f32_16x16x32_bf16 a[32:35], v[96:99], v[192:195], a[32:35]
ds_read_b64_tr_b16 v[120:121], v39 offset:11264
v_mfma_f32_16x16x32_bf16 a[36:39], v[100:103], v[192:195], a[36:39]
ds_read_b64_tr_b16 v[122:123], v39 offset:16384
v_mfma_f32_16x16x32_bf16 a[40:43], v[64:67], v[196:199], a[40:43]
ds_read_b64_tr_b16 v[124:125], v39 offset:11520
v_mfma_f32_16x16x32_bf16 a[44:47], v[68:71], v[196:199], a[44:47]
ds_read_b64_tr_b16 v[126:127], v39 offset:16640
v_mfma_f32_16x16x32_bf16 a[48:51], v[72:75], v[196:199], a[48:51]
ds_read_b64_tr_b16 v[128:129], v39 offset:11776
v_mfma_f32_16x16x32_bf16 a[52:55], v[76:79], v[196:199], a[52:55]
ds_read_b64_tr_b16 v[130:131], v39 offset:16896
v_mfma_f32_16x16x32_bf16 a[56:59], v[80:83], v[196:199], a[56:59]
ds_read_b64_tr_b16 v[132:133], v39 offset:12032
v_mfma_f32_16x16x32_bf16 a[60:63], v[84:87], v[196:199], a[60:63]
ds_read_b64_tr_b16 v[134:135], v39 offset:17152
v_mfma_f32_16x16x32_bf16 a[64:67], v[88:91], v[196:199], a[64:67]
ds_read_b64_tr_b16 v[136:137], v39 offset:12288
v_mfma_f32_16x16x32_bf16 a[68:71], v[92:95], v[196:199], a[68:71]
ds_read_b64_tr_b16 v[138:139], v39 offset:17408
v_mfma_f32_16x16x32_bf16 a[72:75], v[96:99], v[196:199], a[72:75]
ds_read_b64_tr_b16 v[140:141], v39 offset:12544
v_mfma_f32_16x16x32_bf16 a[76:79], v[100:103], v[196:199], a[76:79]
ds_read_b64_tr_b16 v[142:143], v39 offset:17664
v_add_u32 v39, 61440, v38
s_waitcnt lgkmcnt(12)
v_mfma_f32_16x16x32_bf16 a[0:3], v[104:107], v[200:203], a[0:3]
ds_read_b64_tr_b16 v[64:65], v39 offset:0
v_mfma_f32_16x16x32_bf16 a[4:7], v[108:111], v[200:203], a[4:7]
ds_read_b64_tr_b16 v[66:67], v39 offset:5120
v_mfma_f32_16x16x32_bf16 a[8:11], v[112:115], v[200:203], a[8:11]
ds_read_b64_tr_b16 v[68:69], v39 offset:256
v_mfma_f32_16x16x32_bf16 a[12:15], v[116:119], v[200:203], a[12:15]
ds_read_b64_tr_b16 v[70:71], v39 offset:5376
s_waitcnt lgkmcnt(10)
v_mfma_f32_16x16x32_bf16 a[16:19], v[120:123], v[200:203], a[16:19]
ds_read_b64_tr_b16 v[72:73], v39 offset:512
v_mfma_f32_16x16x32_bf16 a[20:23], v[124:127], v[200:203], a[20:23]
ds_read_b64_tr_b16 v[74:75], v39 offset:5632
v_mfma_f32_16x16x32_bf16 a[24:27], v[128:131], v[200:203], a[24:27]
ds_read_b64_tr_b16 v[76:77], v39 offset:768
s_waitcnt lgkmcnt(7)
v_mfma_f32_16x16x32_bf16 a[28:31], v[132:135], v[200:203], a[28:31]
ds_read_b64_tr_b16 v[78:79], v39 offset:5888
v_mfma_f32_16x16x32_bf16 a[32:35], v[136:139], v[200:203], a[32:35]
ds_read_b64_tr_b16 v[80:81], v39 offset:1024
v_mfma_f32_16x16x32_bf16 a[36:39], v[140:143], v[200:203], a[36:39]
ds_read_b64_tr_b16 v[82:83], v39 offset:6144
v_mfma_f32_16x16x32_bf16 a[40:43], v[104:107], v[204:207], a[40:43]
ds_read_b64_tr_b16 v[84:85], v39 offset:1280
v_mfma_f32_16x16x32_bf16 a[44:47], v[108:111], v[204:207], a[44:47]
ds_read_b64_tr_b16 v[86:87], v39 offset:6400
v_mfma_f32_16x16x32_bf16 a[48:51], v[112:115], v[204:207], a[48:51]
ds_read_b64_tr_b16 v[88:89], v39 offset:1536
v_mfma_f32_16x16x32_bf16 a[52:55], v[116:119], v[204:207], a[52:55]
ds_read_b64_tr_b16 v[90:91], v39 offset:6656
v_mfma_f32_16x16x32_bf16 a[56:59], v[120:123], v[204:207], a[56:59]
ds_read_b64_tr_b16 v[92:93], v39 offset:1792
v_mfma_f32_16x16x32_bf16 a[60:63], v[124:127], v[204:207], a[60:63]
ds_read_b64_tr_b16 v[94:95], v39 offset:6912
v_mfma_f32_16x16x32_bf16 a[64:67], v[128:131], v[204:207], a[64:67]
ds_read_b64_tr_b16 v[96:97], v39 offset:2048
v_mfma_f32_16x16x32_bf16 a[68:71], v[132:135], v[204:207], a[68:71]
ds_read_b64_tr_b16 v[98:99], v39 offset:7168
v_mfma_f32_16x16x32_bf16 a[72:75], v[136:139], v[204:207], a[72:75]
ds_read_b64_tr_b16 v[100:101], v39 offset:2304
v_mfma_f32_16x16x32_bf16 a[76:79], v[140:143], v[204:207], a[76:79]
ds_read_b64_tr_b16 v[102:103], v39 offset:7424
s_waitcnt vmcnt(17)
s_barrier
s_waitcnt lgkmcnt(12)
v_mfma_f32_16x16x32_bf16 a[0:3], v[64:67], v[208:211], a[0:3]
ds_read_b64_tr_b16 v[104:105], v39 offset:10240
v_mfma_f32_16x16x32_bf16 a[4:7], v[68:71], v[208:211], a[4:7]
ds_read_b64_tr_b16 v[106:107], v39 offset:15360
v_mfma_f32_16x16x32_bf16 a[8:11], v[72:75], v[208:211], a[8:11]
ds_read_b64_tr_b16 v[108:109], v39 offset:10496
v_mfma_f32_16x16x32_bf16 a[12:15], v[76:79], v[208:211], a[12:15]
ds_read_b64_tr_b16 v[110:111], v39 offset:15616
s_waitcnt lgkmcnt(10)
v_mfma_f32_16x16x32_bf16 a[16:19], v[80:83], v[208:211], a[16:19]
ds_read_b64_tr_b16 v[112:113], v39 offset:10752
v_mfma_f32_16x16x32_bf16 a[20:23], v[84:87], v[208:211], a[20:23]
ds_read_b64_tr_b16 v[114:115], v39 offset:15872
v_mfma_f32_16x16x32_bf16 a[24:27], v[88:91], v[208:211], a[24:27]
ds_read_b64_tr_b16 v[116:117], v39 offset:11008
s_waitcnt lgkmcnt(7)
v_mfma_f32_16x16x32_bf16 a[28:31], v[92:95], v[208:211], a[28:31]
ds_read_b64_tr_b16 v[118:119], v39 offset:16128
s_lshl_b32 s44, s101, 2
s_add_u32 s44, s44, s39
v_mfma_f32_16x16x32_bf16 a[32:35], v[96:99], v[208:211], a[32:35]
ds_read_b64_tr_b16 v[120:121], v39 offset:11264
s_mul_i32 s89, s44, 122880
v_mfma_f32_16x16x32_bf16 a[36:39], v[100:103], v[208:211], a[36:39]
ds_read_b64_tr_b16 v[122:123], v39 offset:16384
s_mul_i32 s44, s38, 2560
v_mfma_f32_16x16x32_bf16 a[40:43], v[64:67], v[212:215], a[40:43]
ds_read_b64_tr_b16 v[124:125], v39 offset:11520
s_add_u32 s89, s89, s44
v_mfma_f32_16x16x32_bf16 a[44:47], v[68:71], v[212:215], a[44:47]
ds_read_b64_tr_b16 v[126:127], v39 offset:16640
s_add_u32 s89, s89, 60047360
v_and_b32 v60, 15, v8
v_mfma_f32_16x16x32_bf16 a[48:51], v[72:75], v[212:215], a[48:51]
ds_read_b64_tr_b16 v[128:129], v39 offset:11776
v_mul_u32_u24 v60, 0x500, v60
v_mfma_f32_16x16x32_bf16 a[52:55], v[76:79], v[212:215], a[52:55]
ds_read_b64_tr_b16 v[130:131], v39 offset:16896
v_lshrrev_b32 v61, 4, v8
v_mfma_f32_16x16x32_bf16 a[56:59], v[80:83], v[212:215], a[56:59]
ds_read_b64_tr_b16 v[132:133], v39 offset:12032
v_lshlrev_b32 v61, 3, v61
v_mfma_f32_16x16x32_bf16 a[60:63], v[84:87], v[212:215], a[60:63]
ds_read_b64_tr_b16 v[134:135], v39 offset:17152
v_add_u32 v60, v60, v61
s_lshl_b32 s44, s101, 7
v_mfma_f32_16x16x32_bf16 a[64:67], v[88:91], v[212:215], a[64:67]
ds_read_b64_tr_b16 v[136:137], v39 offset:12288
s_lshl_b32 s45, s38, 5
v_mfma_f32_16x16x32_bf16 a[68:71], v[92:95], v[212:215], a[68:71]
ds_read_b64_tr_b16 v[138:139], v39 offset:17408
s_add_u32 s44, s44, s45
v_mfma_f32_16x16x32_bf16 a[72:75], v[96:99], v[212:215], a[72:75]
ds_read_b64_tr_b16 v[140:141], v39 offset:12544
s_lshr_b32 s45, s100, 1
v_mfma_f32_16x16x32_bf16 a[76:79], v[100:103], v[212:215], a[76:79]
ds_read_b64_tr_b16 v[142:143], v39 offset:17664
s_lshl_b32 s45, s45, 4
v_add_u32 v39, 81920, v38
s_waitcnt lgkmcnt(12)
v_mfma_f32_16x16x32_bf16 a[0:3], v[104:107], v[216:219], a[0:3]
ds_read_b64_tr_b16 v[64:65], v39 offset:0
v_mfma_f32_16x16x32_bf16 a[4:7], v[108:111], v[216:219], a[4:7]
ds_read_b64_tr_b16 v[66:67], v39 offset:5120
v_mfma_f32_16x16x32_bf16 a[8:11], v[112:115], v[216:219], a[8:11]
ds_read_b64_tr_b16 v[68:69], v39 offset:256
v_mfma_f32_16x16x32_bf16 a[12:15], v[116:119], v[216:219], a[12:15]
ds_read_b64_tr_b16 v[70:71], v39 offset:5376
s_waitcnt lgkmcnt(10)
v_mfma_f32_16x16x32_bf16 a[16:19], v[120:123], v[216:219], a[16:19]
ds_read_b64_tr_b16 v[72:73], v39 offset:512
v_mfma_f32_16x16x32_bf16 a[20:23], v[124:127], v[216:219], a[20:23]
ds_read_b64_tr_b16 v[74:75], v39 offset:5632
v_mfma_f32_16x16x32_bf16 a[24:27], v[128:131], v[216:219], a[24:27]
ds_read_b64_tr_b16 v[76:77], v39 offset:768
s_waitcnt lgkmcnt(7)
v_mfma_f32_16x16x32_bf16 a[28:31], v[132:135], v[216:219], a[28:31]
ds_read_b64_tr_b16 v[78:79], v39 offset:5888
v_mfma_f32_16x16x32_bf16 a[32:35], v[136:139], v[216:219], a[32:35]
ds_read_b64_tr_b16 v[80:81], v39 offset:1024
v_mfma_f32_16x16x32_bf16 a[36:39], v[140:143], v[216:219], a[36:39]
ds_read_b64_tr_b16 v[82:83], v39 offset:6144
v_mfma_f32_16x16x32_bf16 a[40:43], v[104:107], v[220:223], a[40:43]
ds_read_b64_tr_b16 v[84:85], v39 offset:1280
v_mfma_f32_16x16x32_bf16 a[44:47], v[108:111], v[220:223], a[44:47]
ds_read_b64_tr_b16 v[86:87], v39 offset:6400
v_mfma_f32_16x16x32_bf16 a[48:51], v[112:115], v[220:223], a[48:51]
ds_read_b64_tr_b16 v[88:89], v39 offset:1536
v_mfma_f32_16x16x32_bf16 a[52:55], v[116:119], v[220:223], a[52:55]
ds_read_b64_tr_b16 v[90:91], v39 offset:6656
v_mfma_f32_16x16x32_bf16 a[56:59], v[120:123], v[220:223], a[56:59]
ds_read_b64_tr_b16 v[92:93], v39 offset:1792
v_mfma_f32_16x16x32_bf16 a[60:63], v[124:127], v[220:223], a[60:63]
ds_read_b64_tr_b16 v[94:95], v39 offset:6912
v_mfma_f32_16x16x32_bf16 a[64:67], v[128:131], v[220:223], a[64:67]
ds_read_b64_tr_b16 v[96:97], v39 offset:2048
v_mfma_f32_16x16x32_bf16 a[68:71], v[132:135], v[220:223], a[68:71]
ds_read_b64_tr_b16 v[98:99], v39 offset:7168
v_mfma_f32_16x16x32_bf16 a[72:75], v[136:139], v[220:223], a[72:75]
ds_read_b64_tr_b16 v[100:101], v39 offset:2304
v_mfma_f32_16x16x32_bf16 a[76:79], v[140:143], v[220:223], a[76:79]
ds_read_b64_tr_b16 v[102:103], v39 offset:7424
s_waitcnt vmcnt(8)
s_barrier
s_waitcnt lgkmcnt(12)
v_mfma_f32_16x16x32_bf16 a[0:3], v[64:67], v[224:227], a[0:3]
ds_read_b64_tr_b16 v[104:105], v39 offset:10240
v_mfma_f32_16x16x32_bf16 a[4:7], v[68:71], v[224:227], a[4:7]
ds_read_b64_tr_b16 v[106:107], v39 offset:15360
v_mfma_f32_16x16x32_bf16 a[8:11], v[72:75], v[224:227], a[8:11]
ds_read_b64_tr_b16 v[108:109], v39 offset:10496
v_mfma_f32_16x16x32_bf16 a[12:15], v[76:79], v[224:227], a[12:15]
ds_read_b64_tr_b16 v[110:111], v39 offset:15616
s_waitcnt lgkmcnt(10)
v_mfma_f32_16x16x32_bf16 a[16:19], v[80:83], v[224:227], a[16:19]
ds_read_b64_tr_b16 v[112:113], v39 offset:10752
v_mfma_f32_16x16x32_bf16 a[20:23], v[84:87], v[224:227], a[20:23]
ds_read_b64_tr_b16 v[114:115], v39 offset:15872
v_mfma_f32_16x16x32_bf16 a[24:27], v[88:91], v[224:227], a[24:27]
ds_read_b64_tr_b16 v[116:117], v39 offset:11008
s_waitcnt lgkmcnt(7)
v_mfma_f32_16x16x32_bf16 a[28:31], v[92:95], v[224:227], a[28:31]
ds_read_b64_tr_b16 v[118:119], v39 offset:16128
s_add_u32 s44, s44, s45
s_mul_i32 s44, s44, 1280
v_mfma_f32_16x16x32_bf16 a[32:35], v[96:99], v[224:227], a[32:35]
ds_read_b64_tr_b16 v[120:121], v39 offset:11264
s_mul_i32 s45, s39, 320
v_mfma_f32_16x16x32_bf16 a[36:39], v[100:103], v[224:227], a[36:39]
ds_read_b64_tr_b16 v[122:123], v39 offset:16384
s_and_b32 s46, s100, 1
v_mfma_f32_16x16x32_bf16 a[40:43], v[64:67], v[228:231], a[40:43]
ds_read_b64_tr_b16 v[124:125], v39 offset:11520
s_mul_i32 s46, s46, 160
s_add_u32 s91, s45, s46
v_mfma_f32_16x16x32_bf16 a[44:47], v[68:71], v[228:231], a[44:47]
ds_read_b64_tr_b16 v[126:127], v39 offset:16640
s_add_u32 s90, s44, s91
v_mfma_f32_16x16x32_bf16 a[48:51], v[72:75], v[228:231], a[48:51]
buffer_load_dwordx2 v[40:41], v60, s[56:59], s90 offen offset:0
ds_read_b64_tr_b16 v[128:129], v39 offset:11776
v_mfma_f32_16x16x32_bf16 a[52:55], v[76:79], v[228:231], a[52:55]
buffer_load_dwordx2 v[50:51], v61, s[60:63], s91 offen offset:0
buffer_load_dwordx2 v[42:43], v60, s[56:59], s90 offen offset:32
ds_read_b64_tr_b16 v[130:131], v39 offset:16896
v_mfma_f32_16x16x32_bf16 a[56:59], v[80:83], v[228:231], a[56:59]
buffer_load_dwordx2 v[52:53], v61, s[60:63], s91 offen offset:32
ds_read_b64_tr_b16 v[132:133], v39 offset:12032
v_mfma_f32_16x16x32_bf16 a[60:63], v[84:87], v[228:231], a[60:63]
buffer_load_dwordx2 v[44:45], v60, s[56:59], s90 offen offset:64
ds_read_b64_tr_b16 v[134:135], v39 offset:17152
v_mfma_f32_16x16x32_bf16 a[64:67], v[88:91], v[228:231], a[64:67]
buffer_load_dwordx2 v[54:55], v61, s[60:63], s91 offen offset:64
buffer_load_dwordx2 v[46:47], v60, s[56:59], s90 offen offset:96
ds_read_b64_tr_b16 v[136:137], v39 offset:12288
v_mfma_f32_16x16x32_bf16 a[68:71], v[92:95], v[228:231], a[68:71]
buffer_load_dwordx2 v[56:57], v61, s[60:63], s91 offen offset:96
ds_read_b64_tr_b16 v[138:139], v39 offset:17408
v_mfma_f32_16x16x32_bf16 a[72:75], v[96:99], v[228:231], a[72:75]
buffer_load_dwordx2 v[48:49], v60, s[56:59], s90 offen offset:128
ds_read_b64_tr_b16 v[140:141], v39 offset:12544
v_mfma_f32_16x16x32_bf16 a[76:79], v[100:103], v[228:231], a[76:79]
buffer_load_dwordx2 v[58:59], v61, s[60:63], s91 offen offset:128
ds_read_b64_tr_b16 v[142:143], v39 offset:17664
v_add_u32 v39, 102400, v38
s_waitcnt lgkmcnt(12)
v_mfma_f32_16x16x32_bf16 a[0:3], v[104:107], v[232:235], a[0:3]
ds_read_b64_tr_b16 v[64:65], v39 offset:0
v_mfma_f32_16x16x32_bf16 a[4:7], v[108:111], v[232:235], a[4:7]
ds_read_b64_tr_b16 v[66:67], v39 offset:5120
v_mfma_f32_16x16x32_bf16 a[8:11], v[112:115], v[232:235], a[8:11]
ds_read_b64_tr_b16 v[68:69], v39 offset:256
v_mfma_f32_16x16x32_bf16 a[12:15], v[116:119], v[232:235], a[12:15]
ds_read_b64_tr_b16 v[70:71], v39 offset:5376
s_waitcnt lgkmcnt(10)
v_mfma_f32_16x16x32_bf16 a[16:19], v[120:123], v[232:235], a[16:19]
ds_read_b64_tr_b16 v[72:73], v39 offset:512
v_mfma_f32_16x16x32_bf16 a[20:23], v[124:127], v[232:235], a[20:23]
ds_read_b64_tr_b16 v[74:75], v39 offset:5632
v_mfma_f32_16x16x32_bf16 a[24:27], v[128:131], v[232:235], a[24:27]
ds_read_b64_tr_b16 v[76:77], v39 offset:768
s_waitcnt lgkmcnt(7)
v_mfma_f32_16x16x32_bf16 a[28:31], v[132:135], v[232:235], a[28:31]
ds_read_b64_tr_b16 v[78:79], v39 offset:5888
v_mfma_f32_16x16x32_bf16 a[32:35], v[136:139], v[232:235], a[32:35]
ds_read_b64_tr_b16 v[80:81], v39 offset:1024
v_mfma_f32_16x16x32_bf16 a[36:39], v[140:143], v[232:235], a[36:39]
ds_read_b64_tr_b16 v[82:83], v39 offset:6144
v_mfma_f32_16x16x32_bf16 a[40:43], v[104:107], v[236:239], a[40:43]
ds_read_b64_tr_b16 v[84:85], v39 offset:1280
v_mfma_f32_16x16x32_bf16 a[44:47], v[108:111], v[236:239], a[44:47]
ds_read_b64_tr_b16 v[86:87], v39 offset:6400
v_mfma_f32_16x16x32_bf16 a[48:51], v[112:115], v[236:239], a[48:51]
ds_read_b64_tr_b16 v[88:89], v39 offset:1536
v_mfma_f32_16x16x32_bf16 a[52:55], v[116:119], v[236:239], a[52:55]
ds_read_b64_tr_b16 v[90:91], v39 offset:6656
v_mfma_f32_16x16x32_bf16 a[56:59], v[120:123], v[236:239], a[56:59]
ds_read_b64_tr_b16 v[92:93], v39 offset:1792
v_mfma_f32_16x16x32_bf16 a[60:63], v[124:127], v[236:239], a[60:63]
ds_read_b64_tr_b16 v[94:95], v39 offset:6912
v_mfma_f32_16x16x32_bf16 a[64:67], v[128:131], v[236:239], a[64:67]
ds_read_b64_tr_b16 v[96:97], v39 offset:2048
v_mfma_f32_16x16x32_bf16 a[68:71], v[132:135], v[236:239], a[68:71]
ds_read_b64_tr_b16 v[98:99], v39 offset:7168
v_mfma_f32_16x16x32_bf16 a[72:75], v[136:139], v[236:239], a[72:75]
ds_read_b64_tr_b16 v[100:101], v39 offset:2304
v_mfma_f32_16x16x32_bf16 a[76:79], v[140:143], v[236:239], a[76:79]
ds_read_b64_tr_b16 v[102:103], v39 offset:7424
s_waitcnt vmcnt(14)
s_barrier
s_waitcnt lgkmcnt(12)
v_mfma_f32_16x16x32_bf16 a[0:3], v[64:67], v[240:243], a[0:3]
ds_read_b64_tr_b16 v[104:105], v39 offset:10240
v_mfma_f32_16x16x32_bf16 a[4:7], v[68:71], v[240:243], a[4:7]
ds_read_b64_tr_b16 v[106:107], v39 offset:15360
v_mfma_f32_16x16x32_bf16 a[8:11], v[72:75], v[240:243], a[8:11]
ds_read_b64_tr_b16 v[108:109], v39 offset:10496
v_mfma_f32_16x16x32_bf16 a[12:15], v[76:79], v[240:243], a[12:15]
ds_read_b64_tr_b16 v[110:111], v39 offset:15616
s_waitcnt lgkmcnt(10)
v_mfma_f32_16x16x32_bf16 a[16:19], v[80:83], v[240:243], a[16:19]
ds_read_b64_tr_b16 v[112:113], v39 offset:10752
v_mfma_f32_16x16x32_bf16 a[20:23], v[84:87], v[240:243], a[20:23]
ds_read_b64_tr_b16 v[114:115], v39 offset:15872
v_mfma_f32_16x16x32_bf16 a[24:27], v[88:91], v[240:243], a[24:27]
ds_read_b64_tr_b16 v[116:117], v39 offset:11008
s_waitcnt lgkmcnt(7)
v_mfma_f32_16x16x32_bf16 a[28:31], v[92:95], v[240:243], a[28:31]
ds_read_b64_tr_b16 v[118:119], v39 offset:16128
v_mfma_f32_16x16x32_bf16 a[32:35], v[96:99], v[240:243], a[32:35]
ds_read_b64_tr_b16 v[120:121], v39 offset:11264
v_mfma_f32_16x16x32_bf16 a[36:39], v[100:103], v[240:243], a[36:39]
ds_read_b64_tr_b16 v[122:123], v39 offset:16384
v_mfma_f32_16x16x32_bf16 a[40:43], v[64:67], v[244:247], a[40:43]
ds_read_b64_tr_b16 v[124:125], v39 offset:11520
v_mfma_f32_16x16x32_bf16 a[44:47], v[68:71], v[244:247], a[44:47]
ds_read_b64_tr_b16 v[126:127], v39 offset:16640
v_mfma_f32_16x16x32_bf16 a[48:51], v[72:75], v[244:247], a[48:51]
ds_read_b64_tr_b16 v[128:129], v39 offset:11776
v_mfma_f32_16x16x32_bf16 a[52:55], v[76:79], v[244:247], a[52:55]
ds_read_b64_tr_b16 v[130:131], v39 offset:16896
v_mfma_f32_16x16x32_bf16 a[56:59], v[80:83], v[244:247], a[56:59]
ds_read_b64_tr_b16 v[132:133], v39 offset:12032
v_mfma_f32_16x16x32_bf16 a[60:63], v[84:87], v[244:247], a[60:63]
ds_read_b64_tr_b16 v[134:135], v39 offset:17152
v_mfma_f32_16x16x32_bf16 a[64:67], v[88:91], v[244:247], a[64:67]
ds_read_b64_tr_b16 v[136:137], v39 offset:12288
v_mfma_f32_16x16x32_bf16 a[68:71], v[92:95], v[244:247], a[68:71]
ds_read_b64_tr_b16 v[138:139], v39 offset:17408
v_mfma_f32_16x16x32_bf16 a[72:75], v[96:99], v[244:247], a[72:75]
ds_read_b64_tr_b16 v[140:141], v39 offset:12544
v_mfma_f32_16x16x32_bf16 a[76:79], v[100:103], v[244:247], a[76:79]
ds_read_b64_tr_b16 v[142:143], v39 offset:17664
v_add_u32 v39, 122880, v38
s_waitcnt lgkmcnt(12)
v_mfma_f32_16x16x32_bf16 a[0:3], v[104:107], v[248:251], a[0:3]
ds_read_b64_tr_b16 v[64:65], v39 offset:0
v_mfma_f32_16x16x32_bf16 a[4:7], v[108:111], v[248:251], a[4:7]
ds_read_b64_tr_b16 v[66:67], v39 offset:5120
v_mfma_f32_16x16x32_bf16 a[8:11], v[112:115], v[248:251], a[8:11]
ds_read_b64_tr_b16 v[68:69], v39 offset:256
v_mfma_f32_16x16x32_bf16 a[12:15], v[116:119], v[248:251], a[12:15]
ds_read_b64_tr_b16 v[70:71], v39 offset:5376
s_waitcnt lgkmcnt(10)
v_mfma_f32_16x16x32_bf16 a[16:19], v[120:123], v[248:251], a[16:19]
ds_read_b64_tr_b16 v[72:73], v39 offset:512
v_mfma_f32_16x16x32_bf16 a[20:23], v[124:127], v[248:251], a[20:23]
ds_read_b64_tr_b16 v[74:75], v39 offset:5632
v_mfma_f32_16x16x32_bf16 a[24:27], v[128:131], v[248:251], a[24:27]
ds_read_b64_tr_b16 v[76:77], v39 offset:768
s_waitcnt lgkmcnt(7)
v_mfma_f32_16x16x32_bf16 a[28:31], v[132:135], v[248:251], a[28:31]
ds_read_b64_tr_b16 v[78:79], v39 offset:5888
v_mfma_f32_16x16x32_bf16 a[32:35], v[136:139], v[248:251], a[32:35]
ds_read_b64_tr_b16 v[80:81], v39 offset:1024
v_mfma_f32_16x16x32_bf16 a[36:39], v[140:143], v[248:251], a[36:39]
ds_read_b64_tr_b16 v[82:83], v39 offset:6144
v_mfma_f32_16x16x32_bf16 a[40:43], v[104:107], v[252:255], a[40:43]
ds_read_b64_tr_b16 v[84:85], v39 offset:1280
v_mfma_f32_16x16x32_bf16 a[44:47], v[108:111], v[252:255], a[44:47]
ds_read_b64_tr_b16 v[86:87], v39 offset:6400
v_mfma_f32_16x16x32_bf16 a[48:51], v[112:115], v[252:255], a[48:51]
ds_read_b64_tr_b16 v[88:89], v39 offset:1536
v_mfma_f32_16x16x32_bf16 a[52:55], v[116:119], v[252:255], a[52:55]
ds_read_b64_tr_b16 v[90:91], v39 offset:6656
v_mfma_f32_16x16x32_bf16 a[56:59], v[120:123], v[252:255], a[56:59]
ds_read_b64_tr_b16 v[92:93], v39 offset:1792
v_mfma_f32_16x16x32_bf16 a[60:63], v[124:127], v[252:255], a[60:63]
ds_read_b64_tr_b16 v[94:95], v39 offset:6912
v_mfma_f32_16x16x32_bf16 a[64:67], v[128:131], v[252:255], a[64:67]
ds_read_b64_tr_b16 v[96:97], v39 offset:2048
v_mfma_f32_16x16x32_bf16 a[68:71], v[132:135], v[252:255], a[68:71]
ds_read_b64_tr_b16 v[98:99], v39 offset:7168
v_mfma_f32_16x16x32_bf16 a[72:75], v[136:139], v[252:255], a[72:75]
ds_read_b64_tr_b16 v[100:101], v39 offset:2304
v_mfma_f32_16x16x32_bf16 a[76:79], v[140:143], v[252:255], a[76:79]
ds_read_b64_tr_b16 v[102:103], v39 offset:7424
s_waitcnt vmcnt(10)
s_barrier
s_waitcnt lgkmcnt(12)
v_mfma_f32_16x16x32_bf16 a[0:3], v[64:67], v[144:147], a[0:3]
ds_read_b64_tr_b16 v[104:105], v39 offset:10240
v_mfma_f32_16x16x32_bf16 a[4:7], v[68:71], v[144:147], a[4:7]
ds_read_b64_tr_b16 v[106:107], v39 offset:15360
v_mfma_f32_16x16x32_bf16 a[8:11], v[72:75], v[144:147], a[8:11]
ds_read_b64_tr_b16 v[108:109], v39 offset:10496
v_mfma_f32_16x16x32_bf16 a[12:15], v[76:79], v[144:147], a[12:15]
ds_read_b64_tr_b16 v[110:111], v39 offset:15616
s_waitcnt lgkmcnt(10)
v_mfma_f32_16x16x32_bf16 a[16:19], v[80:83], v[144:147], a[16:19]
ds_read_b64_tr_b16 v[112:113], v39 offset:10752
v_mfma_f32_16x16x32_bf16 a[20:23], v[84:87], v[144:147], a[20:23]
ds_read_b64_tr_b16 v[114:115], v39 offset:15872
v_mfma_f32_16x16x32_bf16 a[24:27], v[88:91], v[144:147], a[24:27]
ds_read_b64_tr_b16 v[116:117], v39 offset:11008
s_waitcnt lgkmcnt(7)
v_mfma_f32_16x16x32_bf16 a[28:31], v[92:95], v[144:147], a[28:31]
ds_read_b64_tr_b16 v[118:119], v39 offset:16128
v_mfma_f32_16x16x32_bf16 a[32:35], v[96:99], v[144:147], a[32:35]
ds_read_b64_tr_b16 v[120:121], v39 offset:11264
v_mfma_f32_16x16x32_bf16 a[36:39], v[100:103], v[144:147], a[36:39]
ds_read_b64_tr_b16 v[122:123], v39 offset:16384
v_mfma_f32_16x16x32_bf16 a[40:43], v[64:67], v[148:151], a[40:43]
ds_read_b64_tr_b16 v[124:125], v39 offset:11520
v_mfma_f32_16x16x32_bf16 a[44:47], v[68:71], v[148:151], a[44:47]
ds_read_b64_tr_b16 v[126:127], v39 offset:16640
v_mfma_f32_16x16x32_bf16 a[48:51], v[72:75], v[148:151], a[48:51]
ds_read_b64_tr_b16 v[128:129], v39 offset:11776
v_mfma_f32_16x16x32_bf16 a[52:55], v[76:79], v[148:151], a[52:55]
ds_read_b64_tr_b16 v[130:131], v39 offset:16896
v_mfma_f32_16x16x32_bf16 a[56:59], v[80:83], v[148:151], a[56:59]
ds_read_b64_tr_b16 v[132:133], v39 offset:12032
v_mfma_f32_16x16x32_bf16 a[60:63], v[84:87], v[148:151], a[60:63]
ds_read_b64_tr_b16 v[134:135], v39 offset:17152
v_mfma_f32_16x16x32_bf16 a[64:67], v[88:91], v[148:151], a[64:67]
ds_read_b64_tr_b16 v[136:137], v39 offset:12288
v_mfma_f32_16x16x32_bf16 a[68:71], v[92:95], v[148:151], a[68:71]
ds_read_b64_tr_b16 v[138:139], v39 offset:17408
v_mfma_f32_16x16x32_bf16 a[72:75], v[96:99], v[148:151], a[72:75]
ds_read_b64_tr_b16 v[140:141], v39 offset:12544
v_mfma_f32_16x16x32_bf16 a[76:79], v[100:103], v[148:151], a[76:79]
ds_read_b64_tr_b16 v[142:143], v39 offset:17664
s_waitcnt lgkmcnt(12)
v_mfma_f32_16x16x32_bf16 a[0:3], v[104:107], v[152:155], a[0:3]
v_mfma_f32_16x16x32_bf16 a[4:7], v[108:111], v[152:155], a[4:7]
v_mfma_f32_16x16x32_bf16 a[8:11], v[112:115], v[152:155], a[8:11]
v_mfma_f32_16x16x32_bf16 a[12:15], v[116:119], v[152:155], a[12:15]
s_waitcnt lgkmcnt(6)
v_mfma_f32_16x16x32_bf16 a[16:19], v[120:123], v[152:155], a[16:19]
v_mfma_f32_16x16x32_bf16 a[20:23], v[124:127], v[152:155], a[20:23]
v_mfma_f32_16x16x32_bf16 a[24:27], v[128:131], v[152:155], a[24:27]
s_waitcnt lgkmcnt(0)
v_mfma_f32_16x16x32_bf16 a[28:31], v[132:135], v[152:155], a[28:31]
v_mfma_f32_16x16x32_bf16 a[32:35], v[136:139], v[152:155], a[32:35]
v_mfma_f32_16x16x32_bf16 a[36:39], v[140:143], v[152:155], a[36:39]
v_mfma_f32_16x16x32_bf16 a[40:43], v[104:107], v[156:159], a[40:43]
v_mfma_f32_16x16x32_bf16 a[44:47], v[108:111], v[156:159], a[44:47]
v_mfma_f32_16x16x32_bf16 a[48:51], v[112:115], v[156:159], a[48:51]
v_mfma_f32_16x16x32_bf16 a[52:55], v[116:119], v[156:159], a[52:55]
v_mfma_f32_16x16x32_bf16 a[56:59], v[120:123], v[156:159], a[56:59]
v_mfma_f32_16x16x32_bf16 a[60:63], v[124:127], v[156:159], a[60:63]
v_mfma_f32_16x16x32_bf16 a[64:67], v[128:131], v[156:159], a[64:67]
v_mfma_f32_16x16x32_bf16 a[68:71], v[132:135], v[156:159], a[68:71]
v_mfma_f32_16x16x32_bf16 a[72:75], v[136:139], v[156:159], a[72:75]
v_mfma_f32_16x16x32_bf16 a[76:79], v[140:143], v[156:159], a[76:79]
s_nop 7
s_cmp_lg_u32 s100, 0
s_cbranch_scc1 L_k0%=
v_accvgpr_read_b32 v128, a20
v_accvgpr_read_b32 v129, a21
v_accvgpr_read_b32 v130, a22
v_accvgpr_read_b32 v131, a23
v_accvgpr_read_b32 v132, a24
v_accvgpr_read_b32 v133, a25
v_accvgpr_read_b32 v134, a26
v_accvgpr_read_b32 v135, a27
v_accvgpr_read_b32 v136, a28
v_accvgpr_read_b32 v137, a29
v_accvgpr_read_b32 v138, a30
v_accvgpr_read_b32 v139, a31
v_accvgpr_read_b32 v140, a32
v_accvgpr_read_b32 v141, a33
v_accvgpr_read_b32 v142, a34
v_accvgpr_read_b32 v143, a35
v_accvgpr_read_b32 v144, a36
v_accvgpr_read_b32 v145, a37
v_accvgpr_read_b32 v146, a38
v_accvgpr_read_b32 v147, a39
v_cvt_pk_bf16_f32 v148, v128, v129
v_cvt_pk_bf16_f32 v149, v130, v131
v_cvt_pk_bf16_f32 v150, v132, v133
v_cvt_pk_bf16_f32 v151, v134, v135
v_cvt_pk_bf16_f32 v152, v136, v137
v_cvt_pk_bf16_f32 v153, v138, v139
v_cvt_pk_bf16_f32 v154, v140, v141
v_cvt_pk_bf16_f32 v155, v142, v143
v_cvt_pk_bf16_f32 v156, v144, v145
v_cvt_pk_bf16_f32 v157, v146, v147
s_add_u32 s44, s89, 30720
buffer_store_dwordx4 v[148:151], v32, s[48:51], s44 offen sc1
buffer_store_dwordx4 v[152:155], v32, s[48:51], s44 offen offset:1024 sc1
v_lshrrev_b32 v9, 1, v32
buffer_store_dwordx2 v[156:157], v9, s[48:51], s44 offen offset:2048 sc1
v_accvgpr_read_b32 v160, a40
v_accvgpr_read_b32 v161, a41
v_accvgpr_read_b32 v162, a42
v_accvgpr_read_b32 v163, a43
v_accvgpr_read_b32 v164, a44
v_accvgpr_read_b32 v165, a45
v_accvgpr_read_b32 v166, a46
v_accvgpr_read_b32 v167, a47
v_accvgpr_read_b32 v168, a48
v_accvgpr_read_b32 v169, a49
v_accvgpr_read_b32 v170, a50
v_accvgpr_read_b32 v171, a51
v_accvgpr_read_b32 v172, a52
v_accvgpr_read_b32 v173, a53
v_accvgpr_read_b32 v174, a54
v_accvgpr_read_b32 v175, a55
v_accvgpr_read_b32 v176, a56
v_accvgpr_read_b32 v177, a57
v_accvgpr_read_b32 v178, a58
v_accvgpr_read_b32 v179, a59
v_cvt_pk_bf16_f32 v180, v160, v161
v_cvt_pk_bf16_f32 v181, v162, v163
v_cvt_pk_bf16_f32 v182, v164, v165
v_cvt_pk_bf16_f32 v183, v166, v167
v_cvt_pk_bf16_f32 v184, v168, v169
v_cvt_pk_bf16_f32 v185, v170, v171
v_cvt_pk_bf16_f32 v186, v172, v173
v_cvt_pk_bf16_f32 v187, v174, v175
v_cvt_pk_bf16_f32 v188, v176, v177
v_cvt_pk_bf16_f32 v189, v178, v179
s_add_u32 s44, s89, 61440
buffer_store_dwordx4 v[180:183], v32, s[48:51], s44 offen sc1
buffer_store_dwordx4 v[184:187], v32, s[48:51], s44 offen offset:1024 sc1
buffer_store_dwordx2 v[188:189], v9, s[48:51], s44 offen offset:2048 sc1
v_accvgpr_read_b32 v128, a60
v_accvgpr_read_b32 v129, a61
v_accvgpr_read_b32 v130, a62
v_accvgpr_read_b32 v131, a63
v_accvgpr_read_b32 v132, a64
v_accvgpr_read_b32 v133, a65
v_accvgpr_read_b32 v134, a66
v_accvgpr_read_b32 v135, a67
v_accvgpr_read_b32 v136, a68
v_accvgpr_read_b32 v137, a69
v_accvgpr_read_b32 v138, a70
v_accvgpr_read_b32 v139, a71
v_accvgpr_read_b32 v140, a72
v_accvgpr_read_b32 v141, a73
v_accvgpr_read_b32 v142, a74
v_accvgpr_read_b32 v143, a75
v_accvgpr_read_b32 v144, a76
v_accvgpr_read_b32 v145, a77
v_accvgpr_read_b32 v146, a78
v_accvgpr_read_b32 v147, a79
v_cvt_pk_bf16_f32 v148, v128, v129
v_cvt_pk_bf16_f32 v149, v130, v131
v_cvt_pk_bf16_f32 v150, v132, v133
v_cvt_pk_bf16_f32 v151, v134, v135
v_cvt_pk_bf16_f32 v152, v136, v137
v_cvt_pk_bf16_f32 v153, v138, v139
v_cvt_pk_bf16_f32 v154, v140, v141
v_cvt_pk_bf16_f32 v155, v142, v143
v_cvt_pk_bf16_f32 v156, v144, v145
v_cvt_pk_bf16_f32 v157, v146, v147
s_add_u32 s44, s89, 92160
buffer_store_dwordx4 v[148:151], v32, s[48:51], s44 offen sc1
buffer_store_dwordx4 v[152:155], v32, s[48:51], s44 offen offset:1024 sc1
buffer_store_dwordx2 v[156:157], v9, s[48:51], s44 offen offset:2048 sc1
s_waitcnt vmcnt(9)
v_accvgpr_read_b32 v100, a0
v_accvgpr_read_b32 v101, a1
v_accvgpr_read_b32 v102, a2
v_accvgpr_read_b32 v103, a3
v_accvgpr_read_b32 v104, a4
v_accvgpr_read_b32 v105, a5
v_accvgpr_read_b32 v106, a6
v_accvgpr_read_b32 v107, a7
v_accvgpr_read_b32 v108, a8
v_accvgpr_read_b32 v109, a9
v_accvgpr_read_b32 v110, a10
v_accvgpr_read_b32 v111, a11
v_accvgpr_read_b32 v112, a12
v_accvgpr_read_b32 v113, a13
v_accvgpr_read_b32 v114, a14
v_accvgpr_read_b32 v115, a15
v_accvgpr_read_b32 v116, a16
v_accvgpr_read_b32 v117, a17
v_accvgpr_read_b32 v118, a18
v_accvgpr_read_b32 v119, a19
v_lshlrev_b32 v120, 16, v40
v_and_b32 v121, 0xffff0000, v40
v_lshlrev_b32 v122, 16, v50
v_and_b32 v123, 0xffff0000, v50
v_add_f32 v120, v120, v122
v_add_f32 v121, v121, v123
v_add_f32 v100, v100, v120
v_add_f32 v101, v101, v121
v_lshlrev_b32 v120, 16, v41
v_and_b32 v121, 0xffff0000, v41
v_lshlrev_b32 v122, 16, v51
v_and_b32 v123, 0xffff0000, v51
v_add_f32 v120, v120, v122
v_add_f32 v121, v121, v123
v_add_f32 v102, v102, v120
v_add_f32 v103, v103, v121
v_lshlrev_b32 v120, 16, v42
v_and_b32 v121, 0xffff0000, v42
v_lshlrev_b32 v122, 16, v52
v_and_b32 v123, 0xffff0000, v52
v_add_f32 v120, v120, v122
v_add_f32 v121, v121, v123
v_add_f32 v104, v104, v120
v_add_f32 v105, v105, v121
v_lshlrev_b32 v120, 16, v43
v_and_b32 v121, 0xffff0000, v43
v_lshlrev_b32 v122, 16, v53
v_and_b32 v123, 0xffff0000, v53
v_add_f32 v120, v120, v122
v_add_f32 v121, v121, v123
v_add_f32 v106, v106, v120
v_add_f32 v107, v107, v121
v_lshlrev_b32 v120, 16, v44
v_and_b32 v121, 0xffff0000, v44
v_lshlrev_b32 v122, 16, v54
v_and_b32 v123, 0xffff0000, v54
v_add_f32 v120, v120, v122
v_add_f32 v121, v121, v123
v_add_f32 v108, v108, v120
v_add_f32 v109, v109, v121
v_lshlrev_b32 v120, 16, v45
v_and_b32 v121, 0xffff0000, v45
v_lshlrev_b32 v122, 16, v55
v_and_b32 v123, 0xffff0000, v55
v_add_f32 v120, v120, v122
v_add_f32 v121, v121, v123
v_add_f32 v110, v110, v120
v_add_f32 v111, v111, v121
v_lshlrev_b32 v120, 16, v46
v_and_b32 v121, 0xffff0000, v46
v_lshlrev_b32 v122, 16, v56
v_and_b32 v123, 0xffff0000, v56
v_add_f32 v120, v120, v122
v_add_f32 v121, v121, v123
v_add_f32 v112, v112, v120
v_add_f32 v113, v113, v121
v_lshlrev_b32 v120, 16, v47
v_and_b32 v121, 0xffff0000, v47
v_lshlrev_b32 v122, 16, v57
v_and_b32 v123, 0xffff0000, v57
v_add_f32 v120, v120, v122
v_add_f32 v121, v121, v123
v_add_f32 v114, v114, v120
v_add_f32 v115, v115, v121
v_lshlrev_b32 v120, 16, v48
v_and_b32 v121, 0xffff0000, v48
v_lshlrev_b32 v122, 16, v58
v_and_b32 v123, 0xffff0000, v58
v_add_f32 v120, v120, v122
v_add_f32 v121, v121, v123
v_add_f32 v116, v116, v120
v_add_f32 v117, v117, v121
v_lshlrev_b32 v120, 16, v49
v_and_b32 v121, 0xffff0000, v49
v_lshlrev_b32 v122, 16, v59
v_and_b32 v123, 0xffff0000, v59
v_add_f32 v120, v120, v122
v_add_f32 v121, v121, v123
v_add_f32 v118, v118, v120
v_add_f32 v119, v119, v121
s_lshl_b32 s44, s101, 2
s_add_u32 s44, s44, s39
s_lshl_b32 s44, s44, 6
s_lshl_b32 s45, s38, 2
s_add_u32 s44, s44, s45
s_add_u32 s44, s44, 67915776
s_add_u32 s46, s44, 0
v_mov_b32 v124, s42
v_mov_b32 v125, 0
v_and_b32 v126, 3, v8
v_lshlrev_b32 v126, 4, v126
s_waitcnt vmcnt(0)
buffer_store_dword v124, v125, s[48:51], s46 offen sc1
s_mov_b32 s45, 3000000
L_pp14%=:
buffer_load_dword v127, v126, s[48:51], s44 offen sc1
s_waitcnt vmcnt(0)
v_cmp_ne_u32 vcc, s42, v127
s_nop 3
s_cmp_eq_u64 vcc, 0
s_cbranch_scc1 L_pq14%=
s_sub_u32 s45, s45, 1
s_cmp_lg_u32 s45, 0
s_cbranch_scc1 L_pp14%=
L_pq14%=:
s_add_u32 s44, s89, 0
buffer_load_dwordx4 v[64:67], v32, s[48:51], s44 offen sc1
buffer_load_dwordx4 v[68:71], v32, s[48:51], s44 offen offset:1024 sc1
buffer_load_dwordx2 v[72:73], v9, s[48:51], s44 offen offset:2048 sc1
s_add_u32 s44, s44, 10240
buffer_load_dwordx4 v[74:77], v32, s[48:51], s44 offen sc1
buffer_load_dwordx4 v[78:81], v32, s[48:51], s44 offen offset:1024 sc1
buffer_load_dwordx2 v[82:83], v9, s[48:51], s44 offen offset:2048 sc1
s_add_u32 s44, s44, 10240
buffer_load_dwordx4 v[84:87], v32, s[48:51], s44 offen sc1
buffer_load_dwordx4 v[88:91], v32, s[48:51], s44 offen offset:1024 sc1
buffer_load_dwordx2 v[92:93], v9, s[48:51], s44 offen offset:2048 sc1
s_waitcnt vmcnt(6)
v_lshlrev_b32 v120, 16, v64
v_and_b32 v121, 0xffff0000, v64
v_add_f32 v100, v100, v120
v_add_f32 v101, v101, v121
v_lshlrev_b32 v120, 16, v65
v_and_b32 v121, 0xffff0000, v65
v_add_f32 v102, v102, v120
v_add_f32 v103, v103, v121
v_lshlrev_b32 v120, 16, v66
v_and_b32 v121, 0xffff0000, v66
v_add_f32 v104, v104, v120
v_add_f32 v105, v105, v121
v_lshlrev_b32 v120, 16, v67
v_and_b32 v121, 0xffff0000, v67
v_add_f32 v106, v106, v120
v_add_f32 v107, v107, v121
v_lshlrev_b32 v120, 16, v68
v_and_b32 v121, 0xffff0000, v68
v_add_f32 v108, v108, v120
v_add_f32 v109, v109, v121
v_lshlrev_b32 v120, 16, v69
v_and_b32 v121, 0xffff0000, v69
v_add_f32 v110, v110, v120
v_add_f32 v111, v111, v121
v_lshlrev_b32 v120, 16, v70
v_and_b32 v121, 0xffff0000, v70
v_add_f32 v112, v112, v120
v_add_f32 v113, v113, v121
v_lshlrev_b32 v120, 16, v71
v_and_b32 v121, 0xffff0000, v71
v_add_f32 v114, v114, v120
v_add_f32 v115, v115, v121
v_lshlrev_b32 v120, 16, v72
v_and_b32 v121, 0xffff0000, v72
v_add_f32 v116, v116, v120
v_add_f32 v117, v117, v121
v_lshlrev_b32 v120, 16, v73
v_and_b32 v121, 0xffff0000, v73
v_add_f32 v118, v118, v120
v_add_f32 v119, v119, v121
s_waitcnt vmcnt(3)
v_lshlrev_b32 v120, 16, v74
v_and_b32 v121, 0xffff0000, v74
v_add_f32 v100, v100, v120
v_add_f32 v101, v101, v121
v_lshlrev_b32 v120, 16, v75
v_and_b32 v121, 0xffff0000, v75
v_add_f32 v102, v102, v120
v_add_f32 v103, v103, v121
v_lshlrev_b32 v120, 16, v76
v_and_b32 v121, 0xffff0000, v76
v_add_f32 v104, v104, v120
v_add_f32 v105, v105, v121
v_lshlrev_b32 v120, 16, v77
v_and_b32 v121, 0xffff0000, v77
v_add_f32 v106, v106, v120
v_add_f32 v107, v107, v121
v_lshlrev_b32 v120, 16, v78
v_and_b32 v121, 0xffff0000, v78
v_add_f32 v108, v108, v120
v_add_f32 v109, v109, v121
v_lshlrev_b32 v120, 16, v79
v_and_b32 v121, 0xffff0000, v79
v_add_f32 v110, v110, v120
v_add_f32 v111, v111, v121
v_lshlrev_b32 v120, 16, v80
v_and_b32 v121, 0xffff0000, v80
v_add_f32 v112, v112, v120
v_add_f32 v113, v113, v121
v_lshlrev_b32 v120, 16, v81
v_and_b32 v121, 0xffff0000, v81
v_add_f32 v114, v114, v120
v_add_f32 v115, v115, v121
v_lshlrev_b32 v120, 16, v82
v_and_b32 v121, 0xffff0000, v82
v_add_f32 v116, v116, v120
v_add_f32 v117, v117, v121
v_lshlrev_b32 v120, 16, v83
v_and_b32 v121, 0xffff0000, v83
v_add_f32 v118, v118, v120
v_add_f32 v119, v119, v121
s_waitcnt vmcnt(0)
v_lshlrev_b32 v120, 16, v84
v_and_b32 v121, 0xffff0000, v84
v_add_f32 v100, v100, v120
v_add_f32 v101, v101, v121
v_lshlrev_b32 v120, 16, v85
v_and_b32 v121, 0xffff0000, v85
v_add_f32 v102, v102, v120
v_add_f32 v103, v103, v121
v_lshlrev_b32 v120, 16, v86
v_and_b32 v121, 0xffff0000, v86
v_add_f32 v104, v104, v120
v_add_f32 v105, v105, v121
v_lshlrev_b32 v120, 16, v87
v_and_b32 v121, 0xffff0000, v87
v_add_f32 v106, v106, v120
v_add_f32 v107, v107, v121
v_lshlrev_b32 v120, 16, v88
v_and_b32 v121, 0xffff0000, v88
v_add_f32 v108, v108, v120
v_add_f32 v109, v109, v121
v_lshlrev_b32 v120, 16, v89
v_and_b32 v121, 0xffff0000, v89
v_add_f32 v110, v110, v120
v_add_f32 v111, v111, v121
v_lshlrev_b32 v120, 16, v90
v_and_b32 v121, 0xffff0000, v90
v_add_f32 v112, v112, v120
v_add_f32 v113, v113, v121
v_lshlrev_b32 v120, 16, v91
v_and_b32 v121, 0xffff0000, v91
v_add_f32 v114, v114, v120
v_add_f32 v115, v115, v121
v_lshlrev_b32 v120, 16, v92
v_and_b32 v121, 0xffff0000, v92
v_add_f32 v116, v116, v120
v_add_f32 v117, v117, v121
v_lshlrev_b32 v120, 16, v93
v_and_b32 v121, 0xffff0000, v93
v_add_f32 v118, v118, v120
v_add_f32 v119, v119, v121
v_cvt_pk_bf16_f32 v94, v100, v101
v_cvt_pk_bf16_f32 v95, v102, v103
buffer_store_dwordx2 v[94:95], v60, s[52:55], s90 offen offset:0
v_cvt_pk_bf16_f32 v96, v104, v105
v_cvt_pk_bf16_f32 v97, v106, v107
buffer_store_dwordx2 v[96:97], v60, s[52:55], s90 offen offset:32
v_cvt_pk_bf16_f32 v94, v108, v109
v_cvt_pk_bf16_f32 v95, v110, v111
buffer_store_dwordx2 v[94:95], v60, s[52:55], s90 offen offset:64
v_cvt_pk_bf16_f32 v96, v112, v113
v_cvt_pk_bf16_f32 v97, v114, v115
buffer_store_dwordx2 v[96:97], v60, s[52:55], s90 offen offset:96
v_cvt_pk_bf16_f32 v94, v116, v117
v_cvt_pk_bf16_f32 v95, v118, v119
buffer_store_dwordx2 v[94:95], v60, s[52:55], s90 offen offset:128
s_branch L_kend%=
L_k0%=:
s_cmp_lg_u32 s100, 1
s_cbranch_scc1 L_k1%=
v_accvgpr_read_b32 v128, a0
v_accvgpr_read_b32 v129, a1
v_accvgpr_read_b32 v130, a2
v_accvgpr_read_b32 v131, a3
v_accvgpr_read_b32 v132, a4
v_accvgpr_read_b32 v133, a5
v_accvgpr_read_b32 v134, a6
v_accvgpr_read_b32 v135, a7
v_accvgpr_read_b32 v136, a8
v_accvgpr_read_b32 v137, a9
v_accvgpr_read_b32 v138, a10
v_accvgpr_read_b32 v139, a11
v_accvgpr_read_b32 v140, a12
v_accvgpr_read_b32 v141, a13
v_accvgpr_read_b32 v142, a14
v_accvgpr_read_b32 v143, a15
v_accvgpr_read_b32 v144, a16
v_accvgpr_read_b32 v145, a17
v_accvgpr_read_b32 v146, a18
v_accvgpr_read_b32 v147, a19
v_cvt_pk_bf16_f32 v148, v128, v129
v_cvt_pk_bf16_f32 v149, v130, v131
v_cvt_pk_bf16_f32 v150, v132, v133
v_cvt_pk_bf16_f32 v151, v134, v135
v_cvt_pk_bf16_f32 v152, v136, v137
v_cvt_pk_bf16_f32 v153, v138, v139
v_cvt_pk_bf16_f32 v154, v140, v141
v_cvt_pk_bf16_f32 v155, v142, v143
v_cvt_pk_bf16_f32 v156, v144, v145
v_cvt_pk_bf16_f32 v157, v146, v147
s_add_u32 s44, s89, 0
buffer_store_dwordx4 v[148:151], v32, s[48:51], s44 offen sc1
buffer_store_dwordx4 v[152:155], v32, s[48:51], s44 offen offset:1024 sc1
v_lshrrev_b32 v9, 1, v32
buffer_store_dwordx2 v[156:157], v9, s[48:51], s44 offen offset:2048 sc1
v_accvgpr_read_b32 v160, a40
v_accvgpr_read_b32 v161, a41
v_accvgpr_read_b32 v162, a42
v_accvgpr_read_b32 v163, a43
v_accvgpr_read_b32 v164, a44
v_accvgpr_read_b32 v165, a45
v_accvgpr_read_b32 v166, a46
v_accvgpr_read_b32 v167, a47
v_accvgpr_read_b32 v168, a48
v_accvgpr_read_b32 v169, a49
v_accvgpr_read_b32 v170, a50
v_accvgpr_read_b32 v171, a51
v_accvgpr_read_b32 v172, a52
v_accvgpr_read_b32 v173, a53
v_accvgpr_read_b32 v174, a54
v_accvgpr_read_b32 v175, a55
v_accvgpr_read_b32 v176, a56
v_accvgpr_read_b32 v177, a57
v_accvgpr_read_b32 v178, a58
v_accvgpr_read_b32 v179, a59
v_cvt_pk_bf16_f32 v180, v160, v161
v_cvt_pk_bf16_f32 v181, v162, v163
v_cvt_pk_bf16_f32 v182, v164, v165
v_cvt_pk_bf16_f32 v183, v166, v167
v_cvt_pk_bf16_f32 v184, v168, v169
v_cvt_pk_bf16_f32 v185, v170, v171
v_cvt_pk_bf16_f32 v186, v172, v173
v_cvt_pk_bf16_f32 v187, v174, v175
v_cvt_pk_bf16_f32 v188, v176, v177
v_cvt_pk_bf16_f32 v189, v178, v179
s_add_u32 s44, s89, 71680
buffer_store_dwordx4 v[180:183], v32, s[48:51], s44 offen sc1
buffer_store_dwordx4 v[184:187], v32, s[48:51], s44 offen offset:1024 sc1
buffer_store_dwordx2 v[188:189], v9, s[48:51], s44 offen offset:2048 sc1
v_accvgpr_read_b32 v128, a60
v_accvgpr_read_b32 v129, a61
v_accvgpr_read_b32 v130, a62
v_accvgpr_read_b32 v131, a63
v_accvgpr_read_b32 v132, a64
v_accvgpr_read_b32 v133, a65
v_accvgpr_read_b32 v134, a66
v_accvgpr_read_b32 v135, a67
v_accvgpr_read_b32 v136, a68
v_accvgpr_read_b32 v137, a69
v_accvgpr_read_b32 v138, a70
v_accvgpr_read_b32 v139, a71
v_accvgpr_read_b32 v140, a72
v_accvgpr_read_b32 v141, a73
v_accvgpr_read_b32 v142, a74
v_accvgpr_read_b32 v143, a75
v_accvgpr_read_b32 v144, a76
v_accvgpr_read_b32 v145, a77
v_accvgpr_read_b32 v146, a78
v_accvgpr_read_b32 v147, a79
v_cvt_pk_bf16_f32 v148, v128, v129
v_cvt_pk_bf16_f32 v149, v130, v131
v_cvt_pk_bf16_f32 v150, v132, v133
v_cvt_pk_bf16_f32 v151, v134, v135
v_cvt_pk_bf16_f32 v152, v136, v137
v_cvt_pk_bf16_f32 v153, v138, v139
v_cvt_pk_bf16_f32 v154, v140, v141
v_cvt_pk_bf16_f32 v155, v142, v143
v_cvt_pk_bf16_f32 v156, v144, v145
v_cvt_pk_bf16_f32 v157, v146, v147
s_add_u32 s44, s89, 102400
buffer_store_dwordx4 v[148:151], v32, s[48:51], s44 offen sc1
buffer_store_dwordx4 v[152:155], v32, s[48:51], s44 offen offset:1024 sc1
buffer_store_dwordx2 v[156:157], v9, s[48:51], s44 offen offset:2048 sc1
s_waitcnt vmcnt(9)
v_accvgpr_read_b32 v100, a20
v_accvgpr_read_b32 v101, a21
v_accvgpr_read_b32 v102, a22
v_accvgpr_read_b32 v103, a23
v_accvgpr_read_b32 v104, a24
v_accvgpr_read_b32 v105, a25
v_accvgpr_read_b32 v106, a26
v_accvgpr_read_b32 v107, a27
v_accvgpr_read_b32 v108, a28
v_accvgpr_read_b32 v109, a29
v_accvgpr_read_b32 v110, a30
v_accvgpr_read_b32 v111, a31
v_accvgpr_read_b32 v112, a32
v_accvgpr_read_b32 v113, a33
v_accvgpr_read_b32 v114, a34
v_accvgpr_read_b32 v115, a35
v_accvgpr_read_b32 v116, a36
v_accvgpr_read_b32 v117, a37
v_accvgpr_read_b32 v118, a38
v_accvgpr_read_b32 v119, a39
v_lshlrev_b32 v120, 16, v40
v_and_b32 v121, 0xffff0000, v40
v_lshlrev_b32 v122, 16, v50
v_and_b32 v123, 0xffff0000, v50
v_add_f32 v120, v120, v122
v_add_f32 v121, v121, v123
v_add_f32 v100, v100, v120
v_add_f32 v101, v101, v121
v_lshlrev_b32 v120, 16, v41
v_and_b32 v121, 0xffff0000, v41
v_lshlrev_b32 v122, 16, v51
v_and_b32 v123, 0xffff0000, v51
v_add_f32 v120, v120, v122
v_add_f32 v121, v121, v123
v_add_f32 v102, v102, v120
v_add_f32 v103, v103, v121
v_lshlrev_b32 v120, 16, v42
v_and_b32 v121, 0xffff0000, v42
v_lshlrev_b32 v122, 16, v52
v_and_b32 v123, 0xffff0000, v52
v_add_f32 v120, v120, v122
v_add_f32 v121, v121, v123
v_add_f32 v104, v104, v120
v_add_f32 v105, v105, v121
v_lshlrev_b32 v120, 16, v43
v_and_b32 v121, 0xffff0000, v43
v_lshlrev_b32 v122, 16, v53
v_and_b32 v123, 0xffff0000, v53
v_add_f32 v120, v120, v122
v_add_f32 v121, v121, v123
v_add_f32 v106, v106, v120
v_add_f32 v107, v107, v121
v_lshlrev_b32 v120, 16, v44
v_and_b32 v121, 0xffff0000, v44
v_lshlrev_b32 v122, 16, v54
v_and_b32 v123, 0xffff0000, v54
v_add_f32 v120, v120, v122
v_add_f32 v121, v121, v123
v_add_f32 v108, v108, v120
v_add_f32 v109, v109, v121
v_lshlrev_b32 v120, 16, v45
v_and_b32 v121, 0xffff0000, v45
v_lshlrev_b32 v122, 16, v55
v_and_b32 v123, 0xffff0000, v55
v_add_f32 v120, v120, v122
v_add_f32 v121, v121, v123
v_add_f32 v110, v110, v120
v_add_f32 v111, v111, v121
v_lshlrev_b32 v120, 16, v46
v_and_b32 v121, 0xffff0000, v46
v_lshlrev_b32 v122, 16, v56
v_and_b32 v123, 0xffff0000, v56
v_add_f32 v120, v120, v122
v_add_f32 v121, v121, v123
v_add_f32 v112, v112, v120
v_add_f32 v113, v113, v121
v_lshlrev_b32 v120, 16, v47
v_and_b32 v121, 0xffff0000, v47
v_lshlrev_b32 v122, 16, v57
v_and_b32 v123, 0xffff0000, v57
v_add_f32 v120, v120, v122
v_add_f32 v121, v121, v123
v_add_f32 v114, v114, v120
v_add_f32 v115, v115, v121
v_lshlrev_b32 v120, 16, v48
v_and_b32 v121, 0xffff0000, v48
v_lshlrev_b32 v122, 16, v58
v_and_b32 v123, 0xffff0000, v58
v_add_f32 v120, v120, v122
v_add_f32 v121, v121, v123
v_add_f32 v116, v116, v120
v_add_f32 v117, v117, v121
v_lshlrev_b32 v120, 16, v49
v_and_b32 v121, 0xffff0000, v49
v_lshlrev_b32 v122, 16, v59
v_and_b32 v123, 0xffff0000, v59
v_add_f32 v120, v120, v122
v_add_f32 v121, v121, v123
v_add_f32 v118, v118, v120
v_add_f32 v119, v119, v121
s_lshl_b32 s44, s101, 2
s_add_u32 s44, s44, s39
s_lshl_b32 s44, s44, 6
s_lshl_b32 s45, s38, 2
s_add_u32 s44, s44, s45
s_add_u32 s44, s44, 67915776
s_add_u32 s46, s44, 16
v_mov_b32 v124, s42
v_mov_b32 v125, 0
v_and_b32 v126, 3, v8
v_lshlrev_b32 v126, 4, v126
s_waitcnt vmcnt(0)
buffer_store_dword v124, v125, s[48:51], s46 offen sc1
s_mov_b32 s45, 3000000
L_pp15%=:
buffer_load_dword v127, v126, s[48:51], s44 offen sc1
s_waitcnt vmcnt(0)
v_cmp_ne_u32 vcc, s42, v127
s_nop 3
s_cmp_eq_u64 vcc, 0
s_cbranch_scc1 L_pq15%=
s_sub_u32 s45, s45, 1
s_cmp_lg_u32 s45, 0
s_cbranch_scc1 L_pp15%=
L_pq15%=:
s_add_u32 s44, s89, 30720
buffer_load_dwordx4 v[64:67], v32, s[48:51], s44 offen sc1
buffer_load_dwordx4 v[68:71], v32, s[48:51], s44 offen offset:1024 sc1
buffer_load_dwordx2 v[72:73], v9, s[48:51], s44 offen offset:2048 sc1
s_add_u32 s44, s44, 10240
buffer_load_dwordx4 v[74:77], v32, s[48:51], s44 offen sc1
buffer_load_dwordx4 v[78:81], v32, s[48:51], s44 offen offset:1024 sc1
buffer_load_dwordx2 v[82:83], v9, s[48:51], s44 offen offset:2048 sc1
s_add_u32 s44, s44, 10240
buffer_load_dwordx4 v[84:87], v32, s[48:51], s44 offen sc1
buffer_load_dwordx4 v[88:91], v32, s[48:51], s44 offen offset:1024 sc1
buffer_load_dwordx2 v[92:93], v9, s[48:51], s44 offen offset:2048 sc1
s_waitcnt vmcnt(6)
v_lshlrev_b32 v120, 16, v64
v_and_b32 v121, 0xffff0000, v64
v_add_f32 v100, v100, v120
v_add_f32 v101, v101, v121
v_lshlrev_b32 v120, 16, v65
v_and_b32 v121, 0xffff0000, v65
v_add_f32 v102, v102, v120
v_add_f32 v103, v103, v121
v_lshlrev_b32 v120, 16, v66
v_and_b32 v121, 0xffff0000, v66
v_add_f32 v104, v104, v120
v_add_f32 v105, v105, v121
v_lshlrev_b32 v120, 16, v67
v_and_b32 v121, 0xffff0000, v67
v_add_f32 v106, v106, v120
v_add_f32 v107, v107, v121
v_lshlrev_b32 v120, 16, v68
v_and_b32 v121, 0xffff0000, v68
v_add_f32 v108, v108, v120
v_add_f32 v109, v109, v121
v_lshlrev_b32 v120, 16, v69
v_and_b32 v121, 0xffff0000, v69
v_add_f32 v110, v110, v120
v_add_f32 v111, v111, v121
v_lshlrev_b32 v120, 16, v70
v_and_b32 v121, 0xffff0000, v70
v_add_f32 v112, v112, v120
v_add_f32 v113, v113, v121
v_lshlrev_b32 v120, 16, v71
v_and_b32 v121, 0xffff0000, v71
v_add_f32 v114, v114, v120
v_add_f32 v115, v115, v121
v_lshlrev_b32 v120, 16, v72
v_and_b32 v121, 0xffff0000, v72
v_add_f32 v116, v116, v120
v_add_f32 v117, v117, v121
v_lshlrev_b32 v120, 16, v73
v_and_b32 v121, 0xffff0000, v73
v_add_f32 v118, v118, v120
v_add_f32 v119, v119, v121
s_waitcnt vmcnt(3)
v_lshlrev_b32 v120, 16, v74
v_and_b32 v121, 0xffff0000, v74
v_add_f32 v100, v100, v120
v_add_f32 v101, v101, v121
v_lshlrev_b32 v120, 16, v75
v_and_b32 v121, 0xffff0000, v75
v_add_f32 v102, v102, v120
v_add_f32 v103, v103, v121
v_lshlrev_b32 v120, 16, v76
v_and_b32 v121, 0xffff0000, v76
v_add_f32 v104, v104, v120
v_add_f32 v105, v105, v121
v_lshlrev_b32 v120, 16, v77
v_and_b32 v121, 0xffff0000, v77
v_add_f32 v106, v106, v120
v_add_f32 v107, v107, v121
v_lshlrev_b32 v120, 16, v78
v_and_b32 v121, 0xffff0000, v78
v_add_f32 v108, v108, v120
v_add_f32 v109, v109, v121
v_lshlrev_b32 v120, 16, v79
v_and_b32 v121, 0xffff0000, v79
v_add_f32 v110, v110, v120
v_add_f32 v111, v111, v121
v_lshlrev_b32 v120, 16, v80
v_and_b32 v121, 0xffff0000, v80
v_add_f32 v112, v112, v120
v_add_f32 v113, v113, v121
v_lshlrev_b32 v120, 16, v81
v_and_b32 v121, 0xffff0000, v81
v_add_f32 v114, v114, v120
v_add_f32 v115, v115, v121
v_lshlrev_b32 v120, 16, v82
v_and_b32 v121, 0xffff0000, v82
v_add_f32 v116, v116, v120
v_add_f32 v117, v117, v121
v_lshlrev_b32 v120, 16, v83
v_and_b32 v121, 0xffff0000, v83
v_add_f32 v118, v118, v120
v_add_f32 v119, v119, v121
s_waitcnt vmcnt(0)
v_lshlrev_b32 v120, 16, v84
v_and_b32 v121, 0xffff0000, v84
v_add_f32 v100, v100, v120
v_add_f32 v101, v101, v121
v_lshlrev_b32 v120, 16, v85
v_and_b32 v121, 0xffff0000, v85
v_add_f32 v102, v102, v120
v_add_f32 v103, v103, v121
v_lshlrev_b32 v120, 16, v86
v_and_b32 v121, 0xffff0000, v86
v_add_f32 v104, v104, v120
v_add_f32 v105, v105, v121
v_lshlrev_b32 v120, 16, v87
v_and_b32 v121, 0xffff0000, v87
v_add_f32 v106, v106, v120
v_add_f32 v107, v107, v121
v_lshlrev_b32 v120, 16, v88
v_and_b32 v121, 0xffff0000, v88
v_add_f32 v108, v108, v120
v_add_f32 v109, v109, v121
v_lshlrev_b32 v120, 16, v89
v_and_b32 v121, 0xffff0000, v89
v_add_f32 v110, v110, v120
v_add_f32 v111, v111, v121
v_lshlrev_b32 v120, 16, v90
v_and_b32 v121, 0xffff0000, v90
v_add_f32 v112, v112, v120
v_add_f32 v113, v113, v121
v_lshlrev_b32 v120, 16, v91
v_and_b32 v121, 0xffff0000, v91
v_add_f32 v114, v114, v120
v_add_f32 v115, v115, v121
v_lshlrev_b32 v120, 16, v92
v_and_b32 v121, 0xffff0000, v92
v_add_f32 v116, v116, v120
v_add_f32 v117, v117, v121
v_lshlrev_b32 v120, 16, v93
v_and_b32 v121, 0xffff0000, v93
v_add_f32 v118, v118, v120
v_add_f32 v119, v119, v121
v_cvt_pk_bf16_f32 v94, v100, v101
v_cvt_pk_bf16_f32 v95, v102, v103
buffer_store_dwordx2 v[94:95], v60, s[52:55], s90 offen offset:0
v_cvt_pk_bf16_f32 v96, v104, v105
v_cvt_pk_bf16_f32 v97, v106, v107
buffer_store_dwordx2 v[96:97], v60, s[52:55], s90 offen offset:32
v_cvt_pk_bf16_f32 v94, v108, v109
v_cvt_pk_bf16_f32 v95, v110, v111
buffer_store_dwordx2 v[94:95], v60, s[52:55], s90 offen offset:64
v_cvt_pk_bf16_f32 v96, v112, v113
v_cvt_pk_bf16_f32 v97, v114, v115
buffer_store_dwordx2 v[96:97], v60, s[52:55], s90 offen offset:96
v_cvt_pk_bf16_f32 v94, v116, v117
v_cvt_pk_bf16_f32 v95, v118, v119
buffer_store_dwordx2 v[94:95], v60, s[52:55], s90 offen offset:128
s_branch L_kend%=
L_k1%=:
s_cmp_lg_u32 s100, 2
s_cbranch_scc1 L_k2%=
v_accvgpr_read_b32 v128, a0
v_accvgpr_read_b32 v129, a1
v_accvgpr_read_b32 v130, a2
v_accvgpr_read_b32 v131, a3
v_accvgpr_read_b32 v132, a4
v_accvgpr_read_b32 v133, a5
v_accvgpr_read_b32 v134, a6
v_accvgpr_read_b32 v135, a7
v_accvgpr_read_b32 v136, a8
v_accvgpr_read_b32 v137, a9
v_accvgpr_read_b32 v138, a10
v_accvgpr_read_b32 v139, a11
v_accvgpr_read_b32 v140, a12
v_accvgpr_read_b32 v141, a13
v_accvgpr_read_b32 v142, a14
v_accvgpr_read_b32 v143, a15
v_accvgpr_read_b32 v144, a16
v_accvgpr_read_b32 v145, a17
v_accvgpr_read_b32 v146, a18
v_accvgpr_read_b32 v147, a19
v_cvt_pk_bf16_f32 v148, v128, v129
v_cvt_pk_bf16_f32 v149, v130, v131
v_cvt_pk_bf16_f32 v150, v132, v133
v_cvt_pk_bf16_f32 v151, v134, v135
v_cvt_pk_bf16_f32 v152, v136, v137
v_cvt_pk_bf16_f32 v153, v138, v139
v_cvt_pk_bf16_f32 v154, v140, v141
v_cvt_pk_bf16_f32 v155, v142, v143
v_cvt_pk_bf16_f32 v156, v144, v145
v_cvt_pk_bf16_f32 v157, v146, v147
s_add_u32 s44, s89, 10240
buffer_store_dwordx4 v[148:151], v32, s[48:51], s44 offen sc1
buffer_store_dwordx4 v[152:155], v32, s[48:51], s44 offen offset:1024 sc1
v_lshrrev_b32 v9, 1, v32
buffer_store_dwordx2 v[156:157], v9, s[48:51], s44 offen offset:2048 sc1
v_accvgpr_read_b32 v160, a20
v_accvgpr_read_b32 v161, a21
v_accvgpr_read_b32 v162, a22
v_accvgpr_read_b32 v163, a23
v_accvgpr_read_b32 v164, a24
v_accvgpr_read_b32 v165, a25
v_accvgpr_read_b32 v166, a26
v_accvgpr_read_b32 v167, a27
v_accvgpr_read_b32 v168, a28
v_accvgpr_read_b32 v169, a29
v_accvgpr_read_b32 v170, a30
v_accvgpr_read_b32 v171, a31
v_accvgpr_read_b32 v172, a32
v_accvgpr_read_b32 v173, a33
v_accvgpr_read_b32 v174, a34
v_accvgpr_read_b32 v175, a35
v_accvgpr_read_b32 v176, a36
v_accvgpr_read_b32 v177, a37
v_accvgpr_read_b32 v178, a38
v_accvgpr_read_b32 v179, a39
v_cvt_pk_bf16_f32 v180, v160, v161
v_cvt_pk_bf16_f32 v181, v162, v163
v_cvt_pk_bf16_f32 v182, v164, v165
v_cvt_pk_bf16_f32 v183, v166, v167
v_cvt_pk_bf16_f32 v184, v168, v169
v_cvt_pk_bf16_f32 v185, v170, v171
v_cvt_pk_bf16_f32 v186, v172, v173
v_cvt_pk_bf16_f32 v187, v174, v175
v_cvt_pk_bf16_f32 v188, v176, v177
v_cvt_pk_bf16_f32 v189, v178, v179
s_add_u32 s44, s89, 40960
buffer_store_dwordx4 v[180:183], v32, s[48:51], s44 offen sc1
buffer_store_dwordx4 v[184:187], v32, s[48:51], s44 offen offset:1024 sc1
buffer_store_dwordx2 v[188:189], v9, s[48:51], s44 offen offset:2048 sc1
v_accvgpr_read_b32 v128, a60
v_accvgpr_read_b32 v129, a61
v_accvgpr_read_b32 v130, a62
v_accvgpr_read_b32 v131, a63
v_accvgpr_read_b32 v132, a64
v_accvgpr_read_b32 v133, a65
v_accvgpr_read_b32 v134, a66
v_accvgpr_read_b32 v135, a67
v_accvgpr_read_b32 v136, a68
v_accvgpr_read_b32 v137, a69
v_accvgpr_read_b32 v138, a70
v_accvgpr_read_b32 v139, a71
v_accvgpr_read_b32 v140, a72
v_accvgpr_read_b32 v141, a73
v_accvgpr_read_b32 v142, a74
v_accvgpr_read_b32 v143, a75
v_accvgpr_read_b32 v144, a76
v_accvgpr_read_b32 v145, a77
v_accvgpr_read_b32 v146, a78
v_accvgpr_read_b32 v147, a79
v_cvt_pk_bf16_f32 v148, v128, v129
v_cvt_pk_bf16_f32 v149, v130, v131
v_cvt_pk_bf16_f32 v150, v132, v133
v_cvt_pk_bf16_f32 v151, v134, v135
v_cvt_pk_bf16_f32 v152, v136, v137
v_cvt_pk_bf16_f32 v153, v138, v139
v_cvt_pk_bf16_f32 v154, v140, v141
v_cvt_pk_bf16_f32 v155, v142, v143
v_cvt_pk_bf16_f32 v156, v144, v145
v_cvt_pk_bf16_f32 v157, v146, v147
s_add_u32 s44, s89, 112640
buffer_store_dwordx4 v[148:151], v32, s[48:51], s44 offen sc1
buffer_store_dwordx4 v[152:155], v32, s[48:51], s44 offen offset:1024 sc1
buffer_store_dwordx2 v[156:157], v9, s[48:51], s44 offen offset:2048 sc1
s_waitcnt vmcnt(9)
v_accvgpr_read_b32 v100, a40
v_accvgpr_read_b32 v101, a41
v_accvgpr_read_b32 v102, a42
v_accvgpr_read_b32 v103, a43
v_accvgpr_read_b32 v104, a44
v_accvgpr_read_b32 v105, a45
v_accvgpr_read_b32 v106, a46
v_accvgpr_read_b32 v107, a47
v_accvgpr_read_b32 v108, a48
v_accvgpr_read_b32 v109, a49
v_accvgpr_read_b32 v110, a50
v_accvgpr_read_b32 v111, a51
v_accvgpr_read_b32 v112, a52
v_accvgpr_read_b32 v113, a53
v_accvgpr_read_b32 v114, a54
v_accvgpr_read_b32 v115, a55
v_accvgpr_read_b32 v116, a56
v_accvgpr_read_b32 v117, a57
v_accvgpr_read_b32 v118, a58
v_accvgpr_read_b32 v119, a59
v_lshlrev_b32 v120, 16, v40
v_and_b32 v121, 0xffff0000, v40
v_lshlrev_b32 v122, 16, v50
v_and_b32 v123, 0xffff0000, v50
v_add_f32 v120, v120, v122
v_add_f32 v121, v121, v123
v_add_f32 v100, v100, v120
v_add_f32 v101, v101, v121
v_lshlrev_b32 v120, 16, v41
v_and_b32 v121, 0xffff0000, v41
v_lshlrev_b32 v122, 16, v51
v_and_b32 v123, 0xffff0000, v51
v_add_f32 v120, v120, v122
v_add_f32 v121, v121, v123
v_add_f32 v102, v102, v120
v_add_f32 v103, v103, v121
v_lshlrev_b32 v120, 16, v42
v_and_b32 v121, 0xffff0000, v42
v_lshlrev_b32 v122, 16, v52
v_and_b32 v123, 0xffff0000, v52
v_add_f32 v120, v120, v122
v_add_f32 v121, v121, v123
v_add_f32 v104, v104, v120
v_add_f32 v105, v105, v121
v_lshlrev_b32 v120, 16, v43
v_and_b32 v121, 0xffff0000, v43
v_lshlrev_b32 v122, 16, v53
v_and_b32 v123, 0xffff0000, v53
v_add_f32 v120, v120, v122
v_add_f32 v121, v121, v123
v_add_f32 v106, v106, v120
v_add_f32 v107, v107, v121
v_lshlrev_b32 v120, 16, v44
v_and_b32 v121, 0xffff0000, v44
v_lshlrev_b32 v122, 16, v54
v_and_b32 v123, 0xffff0000, v54
v_add_f32 v120, v120, v122
v_add_f32 v121, v121, v123
v_add_f32 v108, v108, v120
v_add_f32 v109, v109, v121
v_lshlrev_b32 v120, 16, v45
v_and_b32 v121, 0xffff0000, v45
v_lshlrev_b32 v122, 16, v55
v_and_b32 v123, 0xffff0000, v55
v_add_f32 v120, v120, v122
v_add_f32 v121, v121, v123
v_add_f32 v110, v110, v120
v_add_f32 v111, v111, v121
v_lshlrev_b32 v120, 16, v46
v_and_b32 v121, 0xffff0000, v46
v_lshlrev_b32 v122, 16, v56
v_and_b32 v123, 0xffff0000, v56
v_add_f32 v120, v120, v122
v_add_f32 v121, v121, v123
v_add_f32 v112, v112, v120
v_add_f32 v113, v113, v121
v_lshlrev_b32 v120, 16, v47
v_and_b32 v121, 0xffff0000, v47
v_lshlrev_b32 v122, 16, v57
v_and_b32 v123, 0xffff0000, v57
v_add_f32 v120, v120, v122
v_add_f32 v121, v121, v123
v_add_f32 v114, v114, v120
v_add_f32 v115, v115, v121
v_lshlrev_b32 v120, 16, v48
v_and_b32 v121, 0xffff0000, v48
v_lshlrev_b32 v122, 16, v58
v_and_b32 v123, 0xffff0000, v58
v_add_f32 v120, v120, v122
v_add_f32 v121, v121, v123
v_add_f32 v116, v116, v120
v_add_f32 v117, v117, v121
v_lshlrev_b32 v120, 16, v49
v_and_b32 v121, 0xffff0000, v49
v_lshlrev_b32 v122, 16, v59
v_and_b32 v123, 0xffff0000, v59
v_add_f32 v120, v120, v122
v_add_f32 v121, v121, v123
v_add_f32 v118, v118, v120
v_add_f32 v119, v119, v121
s_lshl_b32 s44, s101, 2
s_add_u32 s44, s44, s39
s_lshl_b32 s44, s44, 6
s_lshl_b32 s45, s38, 2
s_add_u32 s44, s44, s45
s_add_u32 s44, s44, 67915776
s_add_u32 s46, s44, 32
v_mov_b32 v124, s42
v_mov_b32 v125, 0
v_and_b32 v126, 3, v8
v_lshlrev_b32 v126, 4, v126
s_waitcnt vmcnt(0)
buffer_store_dword v124, v125, s[48:51], s46 offen sc1
s_mov_b32 s45, 3000000
L_pp16%=:
buffer_load_dword v127, v126, s[48:51], s44 offen sc1
s_waitcnt vmcnt(0)
v_cmp_ne_u32 vcc, s42, v127
s_nop 3
s_cmp_eq_u64 vcc, 0
s_cbranch_scc1 L_pq16%=
s_sub_u32 s45, s45, 1
s_cmp_lg_u32 s45, 0
s_cbranch_scc1 L_pp16%=
L_pq16%=:
s_add_u32 s44, s89, 61440
buffer_load_dwordx4 v[64:67], v32, s[48:51], s44 offen sc1
buffer_load_dwordx4 v[68:71], v32, s[48:51], s44 offen offset:1024 sc1
buffer_load_dwordx2 v[72:73], v9, s[48:51], s44 offen offset:2048 sc1
s_add_u32 s44, s44, 10240
buffer_load_dwordx4 v[74:77], v32, s[48:51], s44 offen sc1
buffer_load_dwordx4 v[78:81], v32, s[48:51], s44 offen offset:1024 sc1
buffer_load_dwordx2 v[82:83], v9, s[48:51], s44 offen offset:2048 sc1
s_add_u32 s44, s44, 10240
buffer_load_dwordx4 v[84:87], v32, s[48:51], s44 offen sc1
buffer_load_dwordx4 v[88:91], v32, s[48:51], s44 offen offset:1024 sc1
buffer_load_dwordx2 v[92:93], v9, s[48:51], s44 offen offset:2048 sc1
s_waitcnt vmcnt(6)
v_lshlrev_b32 v120, 16, v64
v_and_b32 v121, 0xffff0000, v64
v_add_f32 v100, v100, v120
v_add_f32 v101, v101, v121
v_lshlrev_b32 v120, 16, v65
v_and_b32 v121, 0xffff0000, v65
v_add_f32 v102, v102, v120
v_add_f32 v103, v103, v121
v_lshlrev_b32 v120, 16, v66
v_and_b32 v121, 0xffff0000, v66
v_add_f32 v104, v104, v120
v_add_f32 v105, v105, v121
v_lshlrev_b32 v120, 16, v67
v_and_b32 v121, 0xffff0000, v67
v_add_f32 v106, v106, v120
v_add_f32 v107, v107, v121
v_lshlrev_b32 v120, 16, v68
v_and_b32 v121, 0xffff0000, v68
v_add_f32 v108, v108, v120
v_add_f32 v109, v109, v121
v_lshlrev_b32 v120, 16, v69
v_and_b32 v121, 0xffff0000, v69
v_add_f32 v110, v110, v120
v_add_f32 v111, v111, v121
v_lshlrev_b32 v120, 16, v70
v_and_b32 v121, 0xffff0000, v70
v_add_f32 v112, v112, v120
v_add_f32 v113, v113, v121
v_lshlrev_b32 v120, 16, v71
v_and_b32 v121, 0xffff0000, v71
v_add_f32 v114, v114, v120
v_add_f32 v115, v115, v121
v_lshlrev_b32 v120, 16, v72
v_and_b32 v121, 0xffff0000, v72
v_add_f32 v116, v116, v120
v_add_f32 v117, v117, v121
v_lshlrev_b32 v120, 16, v73
v_and_b32 v121, 0xffff0000, v73
v_add_f32 v118, v118, v120
v_add_f32 v119, v119, v121
s_waitcnt vmcnt(3)
v_lshlrev_b32 v120, 16, v74
v_and_b32 v121, 0xffff0000, v74
v_add_f32 v100, v100, v120
v_add_f32 v101, v101, v121
v_lshlrev_b32 v120, 16, v75
v_and_b32 v121, 0xffff0000, v75
v_add_f32 v102, v102, v120
v_add_f32 v103, v103, v121
v_lshlrev_b32 v120, 16, v76
v_and_b32 v121, 0xffff0000, v76
v_add_f32 v104, v104, v120
v_add_f32 v105, v105, v121
v_lshlrev_b32 v120, 16, v77
v_and_b32 v121, 0xffff0000, v77
v_add_f32 v106, v106, v120
v_add_f32 v107, v107, v121
v_lshlrev_b32 v120, 16, v78
v_and_b32 v121, 0xffff0000, v78
v_add_f32 v108, v108, v120
v_add_f32 v109, v109, v121
v_lshlrev_b32 v120, 16, v79
v_and_b32 v121, 0xffff0000, v79
v_add_f32 v110, v110, v120
v_add_f32 v111, v111, v121
v_lshlrev_b32 v120, 16, v80
v_and_b32 v121, 0xffff0000, v80
v_add_f32 v112, v112, v120
v_add_f32 v113, v113, v121
v_lshlrev_b32 v120, 16, v81
v_and_b32 v121, 0xffff0000, v81
v_add_f32 v114, v114, v120
v_add_f32 v115, v115, v121
v_lshlrev_b32 v120, 16, v82
v_and_b32 v121, 0xffff0000, v82
v_add_f32 v116, v116, v120
v_add_f32 v117, v117, v121
v_lshlrev_b32 v120, 16, v83
v_and_b32 v121, 0xffff0000, v83
v_add_f32 v118, v118, v120
v_add_f32 v119, v119, v121
s_waitcnt vmcnt(0)
v_lshlrev_b32 v120, 16, v84
v_and_b32 v121, 0xffff0000, v84
v_add_f32 v100, v100, v120
v_add_f32 v101, v101, v121
v_lshlrev_b32 v120, 16, v85
v_and_b32 v121, 0xffff0000, v85
v_add_f32 v102, v102, v120
v_add_f32 v103, v103, v121
v_lshlrev_b32 v120, 16, v86
v_and_b32 v121, 0xffff0000, v86
v_add_f32 v104, v104, v120
v_add_f32 v105, v105, v121
v_lshlrev_b32 v120, 16, v87
v_and_b32 v121, 0xffff0000, v87
v_add_f32 v106, v106, v120
v_add_f32 v107, v107, v121
v_lshlrev_b32 v120, 16, v88
v_and_b32 v121, 0xffff0000, v88
v_add_f32 v108, v108, v120
v_add_f32 v109, v109, v121
v_lshlrev_b32 v120, 16, v89
v_and_b32 v121, 0xffff0000, v89
v_add_f32 v110, v110, v120
v_add_f32 v111, v111, v121
v_lshlrev_b32 v120, 16, v90
v_and_b32 v121, 0xffff0000, v90
v_add_f32 v112, v112, v120
v_add_f32 v113, v113, v121
v_lshlrev_b32 v120, 16, v91
v_and_b32 v121, 0xffff0000, v91
v_add_f32 v114, v114, v120
v_add_f32 v115, v115, v121
v_lshlrev_b32 v120, 16, v92
v_and_b32 v121, 0xffff0000, v92
v_add_f32 v116, v116, v120
v_add_f32 v117, v117, v121
v_lshlrev_b32 v120, 16, v93
v_and_b32 v121, 0xffff0000, v93
v_add_f32 v118, v118, v120
v_add_f32 v119, v119, v121
v_cvt_pk_bf16_f32 v94, v100, v101
v_cvt_pk_bf16_f32 v95, v102, v103
buffer_store_dwordx2 v[94:95], v60, s[52:55], s90 offen offset:0
v_cvt_pk_bf16_f32 v96, v104, v105
v_cvt_pk_bf16_f32 v97, v106, v107
buffer_store_dwordx2 v[96:97], v60, s[52:55], s90 offen offset:32
v_cvt_pk_bf16_f32 v94, v108, v109
v_cvt_pk_bf16_f32 v95, v110, v111
buffer_store_dwordx2 v[94:95], v60, s[52:55], s90 offen offset:64
v_cvt_pk_bf16_f32 v96, v112, v113
v_cvt_pk_bf16_f32 v97, v114, v115
buffer_store_dwordx2 v[96:97], v60, s[52:55], s90 offen offset:96
v_cvt_pk_bf16_f32 v94, v116, v117
v_cvt_pk_bf16_f32 v95, v118, v119
buffer_store_dwordx2 v[94:95], v60, s[52:55], s90 offen offset:128
s_branch L_kend%=
L_k2%=:
s_cmp_lg_u32 s100, 3
s_cbranch_scc1 L_k3%=
v_accvgpr_read_b32 v128, a0
v_accvgpr_read_b32 v129, a1
v_accvgpr_read_b32 v130, a2
v_accvgpr_read_b32 v131, a3
v_accvgpr_read_b32 v132, a4
v_accvgpr_read_b32 v133, a5
v_accvgpr_read_b32 v134, a6
v_accvgpr_read_b32 v135, a7
v_accvgpr_read_b32 v136, a8
v_accvgpr_read_b32 v137, a9
v_accvgpr_read_b32 v138, a10
v_accvgpr_read_b32 v139, a11
v_accvgpr_read_b32 v140, a12
v_accvgpr_read_b32 v141, a13
v_accvgpr_read_b32 v142, a14
v_accvgpr_read_b32 v143, a15
v_accvgpr_read_b32 v144, a16
v_accvgpr_read_b32 v145, a17
v_accvgpr_read_b32 v146, a18
v_accvgpr_read_b32 v147, a19
v_cvt_pk_bf16_f32 v148, v128, v129
v_cvt_pk_bf16_f32 v149, v130, v131
v_cvt_pk_bf16_f32 v150, v132, v133
v_cvt_pk_bf16_f32 v151, v134, v135
v_cvt_pk_bf16_f32 v152, v136, v137
v_cvt_pk_bf16_f32 v153, v138, v139
v_cvt_pk_bf16_f32 v154, v140, v141
v_cvt_pk_bf16_f32 v155, v142, v143
v_cvt_pk_bf16_f32 v156, v144, v145
v_cvt_pk_bf16_f32 v157, v146, v147
s_add_u32 s44, s89, 20480
buffer_store_dwordx4 v[148:151], v32, s[48:51], s44 offen sc1
buffer_store_dwordx4 v[152:155], v32, s[48:51], s44 offen offset:1024 sc1
v_lshrrev_b32 v9, 1, v32
buffer_store_dwordx2 v[156:157], v9, s[48:51], s44 offen offset:2048 sc1
v_accvgpr_read_b32 v160, a20
v_accvgpr_read_b32 v161, a21
v_accvgpr_read_b32 v162, a22
v_accvgpr_read_b32 v163, a23
v_accvgpr_read_b32 v164, a24
v_accvgpr_read_b32 v165, a25
v_accvgpr_read_b32 v166, a26
v_accvgpr_read_b32 v167, a27
v_accvgpr_read_b32 v168, a28
v_accvgpr_read_b32 v169, a29
v_accvgpr_read_b32 v170, a30
v_accvgpr_read_b32 v171, a31
v_accvgpr_read_b32 v172, a32
v_accvgpr_read_b32 v173, a33
v_accvgpr_read_b32 v174, a34
v_accvgpr_read_b32 v175, a35
v_accvgpr_read_b32 v176, a36
v_accvgpr_read_b32 v177, a37
v_accvgpr_read_b32 v178, a38
v_accvgpr_read_b32 v179, a39
v_cvt_pk_bf16_f32 v180, v160, v161
v_cvt_pk_bf16_f32 v181, v162, v163
v_cvt_pk_bf16_f32 v182, v164, v165
v_cvt_pk_bf16_f32 v183, v166, v167
v_cvt_pk_bf16_f32 v184, v168, v169
v_cvt_pk_bf16_f32 v185, v170, v171
v_cvt_pk_bf16_f32 v186, v172, v173
v_cvt_pk_bf16_f32 v187, v174, v175
v_cvt_pk_bf16_f32 v188, v176, v177
v_cvt_pk_bf16_f32 v189, v178, v179
s_add_u32 s44, s89, 51200
buffer_store_dwordx4 v[180:183], v32, s[48:51], s44 offen sc1
buffer_store_dwordx4 v[184:187], v32, s[48:51], s44 offen offset:1024 sc1
buffer_store_dwordx2 v[188:189], v9, s[48:51], s44 offen offset:2048 sc1
v_accvgpr_read_b32 v128, a40
v_accvgpr_read_b32 v129, a41
v_accvgpr_read_b32 v130, a42
v_accvgpr_read_b32 v131, a43
v_accvgpr_read_b32 v132, a44
v_accvgpr_read_b32 v133, a45
v_accvgpr_read_b32 v134, a46
v_accvgpr_read_b32 v135, a47
v_accvgpr_read_b32 v136, a48
v_accvgpr_read_b32 v137, a49
v_accvgpr_read_b32 v138, a50
v_accvgpr_read_b32 v139, a51
v_accvgpr_read_b32 v140, a52
v_accvgpr_read_b32 v141, a53
v_accvgpr_read_b32 v142, a54
v_accvgpr_read_b32 v143, a55
v_accvgpr_read_b32 v144, a56
v_accvgpr_read_b32 v145, a57
v_accvgpr_read_b32 v146, a58
v_accvgpr_read_b32 v147, a59
v_cvt_pk_bf16_f32 v148, v128, v129
v_cvt_pk_bf16_f32 v149, v130, v131
v_cvt_pk_bf16_f32 v150, v132, v133
v_cvt_pk_bf16_f32 v151, v134, v135
v_cvt_pk_bf16_f32 v152, v136, v137
v_cvt_pk_bf16_f32 v153, v138, v139
v_cvt_pk_bf16_f32 v154, v140, v141
v_cvt_pk_bf16_f32 v155, v142, v143
v_cvt_pk_bf16_f32 v156, v144, v145
v_cvt_pk_bf16_f32 v157, v146, v147
s_add_u32 s44, s89, 81920
buffer_store_dwordx4 v[148:151], v32, s[48:51], s44 offen sc1
buffer_store_dwordx4 v[152:155], v32, s[48:51], s44 offen offset:1024 sc1
buffer_store_dwordx2 v[156:157], v9, s[48:51], s44 offen offset:2048 sc1
s_waitcnt vmcnt(9)
v_accvgpr_read_b32 v100, a60
v_accvgpr_read_b32 v101, a61
v_accvgpr_read_b32 v102, a62
v_accvgpr_read_b32 v103, a63
v_accvgpr_read_b32 v104, a64
v_accvgpr_read_b32 v105, a65
v_accvgpr_read_b32 v106, a66
v_accvgpr_read_b32 v107, a67
v_accvgpr_read_b32 v108, a68
v_accvgpr_read_b32 v109, a69
v_accvgpr_read_b32 v110, a70
v_accvgpr_read_b32 v111, a71
v_accvgpr_read_b32 v112, a72
v_accvgpr_read_b32 v113, a73
v_accvgpr_read_b32 v114, a74
v_accvgpr_read_b32 v115, a75
v_accvgpr_read_b32 v116, a76
v_accvgpr_read_b32 v117, a77
v_accvgpr_read_b32 v118, a78
v_accvgpr_read_b32 v119, a79
v_lshlrev_b32 v120, 16, v40
v_and_b32 v121, 0xffff0000, v40
v_lshlrev_b32 v122, 16, v50
v_and_b32 v123, 0xffff0000, v50
v_add_f32 v120, v120, v122
v_add_f32 v121, v121, v123
v_add_f32 v100, v100, v120
v_add_f32 v101, v101, v121
v_lshlrev_b32 v120, 16, v41
v_and_b32 v121, 0xffff0000, v41
v_lshlrev_b32 v122, 16, v51
v_and_b32 v123, 0xffff0000, v51
v_add_f32 v120, v120, v122
v_add_f32 v121, v121, v123
v_add_f32 v102, v102, v120
v_add_f32 v103, v103, v121
v_lshlrev_b32 v120, 16, v42
v_and_b32 v121, 0xffff0000, v42
v_lshlrev_b32 v122, 16, v52
v_and_b32 v123, 0xffff0000, v52
v_add_f32 v120, v120, v122
v_add_f32 v121, v121, v123
v_add_f32 v104, v104, v120
v_add_f32 v105, v105, v121
v_lshlrev_b32 v120, 16, v43
v_and_b32 v121, 0xffff0000, v43
v_lshlrev_b32 v122, 16, v53
v_and_b32 v123, 0xffff0000, v53
v_add_f32 v120, v120, v122
v_add_f32 v121, v121, v123
v_add_f32 v106, v106, v120
v_add_f32 v107, v107, v121
v_lshlrev_b32 v120, 16, v44
v_and_b32 v121, 0xffff0000, v44
v_lshlrev_b32 v122, 16, v54
v_and_b32 v123, 0xffff0000, v54
v_add_f32 v120, v120, v122
v_add_f32 v121, v121, v123
v_add_f32 v108, v108, v120
v_add_f32 v109, v109, v121
v_lshlrev_b32 v120, 16, v45
v_and_b32 v121, 0xffff0000, v45
v_lshlrev_b32 v122, 16, v55
v_and_b32 v123, 0xffff0000, v55
v_add_f32 v120, v120, v122
v_add_f32 v121, v121, v123
v_add_f32 v110, v110, v120
v_add_f32 v111, v111, v121
v_lshlrev_b32 v120, 16, v46
v_and_b32 v121, 0xffff0000, v46
v_lshlrev_b32 v122, 16, v56
v_and_b32 v123, 0xffff0000, v56
v_add_f32 v120, v120, v122
v_add_f32 v121, v121, v123
v_add_f32 v112, v112, v120
v_add_f32 v113, v113, v121
v_lshlrev_b32 v120, 16, v47
v_and_b32 v121, 0xffff0000, v47
v_lshlrev_b32 v122, 16, v57
v_and_b32 v123, 0xffff0000, v57
v_add_f32 v120, v120, v122
v_add_f32 v121, v121, v123
v_add_f32 v114, v114, v120
v_add_f32 v115, v115, v121
v_lshlrev_b32 v120, 16, v48
v_and_b32 v121, 0xffff0000, v48
v_lshlrev_b32 v122, 16, v58
v_and_b32 v123, 0xffff0000, v58
v_add_f32 v120, v120, v122
v_add_f32 v121, v121, v123
v_add_f32 v116, v116, v120
v_add_f32 v117, v117, v121
v_lshlrev_b32 v120, 16, v49
v_and_b32 v121, 0xffff0000, v49
v_lshlrev_b32 v122, 16, v59
v_and_b32 v123, 0xffff0000, v59
v_add_f32 v120, v120, v122
v_add_f32 v121, v121, v123
v_add_f32 v118, v118, v120
v_add_f32 v119, v119, v121
s_lshl_b32 s44, s101, 2
s_add_u32 s44, s44, s39
s_lshl_b32 s44, s44, 6
s_lshl_b32 s45, s38, 2
s_add_u32 s44, s44, s45
s_add_u32 s44, s44, 67915776
s_add_u32 s46, s44, 48
v_mov_b32 v124, s42
v_mov_b32 v125, 0
v_and_b32 v126, 3, v8
v_lshlrev_b32 v126, 4, v126
s_waitcnt vmcnt(0)
buffer_store_dword v124, v125, s[48:51], s46 offen sc1
s_mov_b32 s45, 3000000
L_pp17%=:
buffer_load_dword v127, v126, s[48:51], s44 offen sc1
s_waitcnt vmcnt(0)
v_cmp_ne_u32 vcc, s42, v127
s_nop 3
s_cmp_eq_u64 vcc, 0
s_cbranch_scc1 L_pq17%=
s_sub_u32 s45, s45, 1
s_cmp_lg_u32 s45, 0
s_cbranch_scc1 L_pp17%=
L_pq17%=:
s_add_u32 s44, s89, 92160
buffer_load_dwordx4 v[64:67], v32, s[48:51], s44 offen sc1
buffer_load_dwordx4 v[68:71], v32, s[48:51], s44 offen offset:1024 sc1
buffer_load_dwordx2 v[72:73], v9, s[48:51], s44 offen offset:2048 sc1
s_add_u32 s44, s44, 10240
buffer_load_dwordx4 v[74:77], v32, s[48:51], s44 offen sc1
buffer_load_dwordx4 v[78:81], v32, s[48:51], s44 offen offset:1024 sc1
buffer_load_dwordx2 v[82:83], v9, s[48:51], s44 offen offset:2048 sc1
s_add_u32 s44, s44, 10240
buffer_load_dwordx4 v[84:87], v32, s[48:51], s44 offen sc1
buffer_load_dwordx4 v[88:91], v32, s[48:51], s44 offen offset:1024 sc1
buffer_load_dwordx2 v[92:93], v9, s[48:51], s44 offen offset:2048 sc1
s_waitcnt vmcnt(6)
v_lshlrev_b32 v120, 16, v64
v_and_b32 v121, 0xffff0000, v64
v_add_f32 v100, v100, v120
v_add_f32 v101, v101, v121
v_lshlrev_b32 v120, 16, v65
v_and_b32 v121, 0xffff0000, v65
v_add_f32 v102, v102, v120
v_add_f32 v103, v103, v121
v_lshlrev_b32 v120, 16, v66
v_and_b32 v121, 0xffff0000, v66
v_add_f32 v104, v104, v120
v_add_f32 v105, v105, v121
v_lshlrev_b32 v120, 16, v67
v_and_b32 v121, 0xffff0000, v67
v_add_f32 v106, v106, v120
v_add_f32 v107, v107, v121
v_lshlrev_b32 v120, 16, v68
v_and_b32 v121, 0xffff0000, v68
v_add_f32 v108, v108, v120
v_add_f32 v109, v109, v121
v_lshlrev_b32 v120, 16, v69
v_and_b32 v121, 0xffff0000, v69
v_add_f32 v110, v110, v120
v_add_f32 v111, v111, v121
v_lshlrev_b32 v120, 16, v70
v_and_b32 v121, 0xffff0000, v70
v_add_f32 v112, v112, v120
v_add_f32 v113, v113, v121
v_lshlrev_b32 v120, 16, v71
v_and_b32 v121, 0xffff0000, v71
v_add_f32 v114, v114, v120
v_add_f32 v115, v115, v121
v_lshlrev_b32 v120, 16, v72
v_and_b32 v121, 0xffff0000, v72
v_add_f32 v116, v116, v120
v_add_f32 v117, v117, v121
v_lshlrev_b32 v120, 16, v73
v_and_b32 v121, 0xffff0000, v73
v_add_f32 v118, v118, v120
v_add_f32 v119, v119, v121
s_waitcnt vmcnt(3)
v_lshlrev_b32 v120, 16, v74
v_and_b32 v121, 0xffff0000, v74
v_add_f32 v100, v100, v120
v_add_f32 v101, v101, v121
v_lshlrev_b32 v120, 16, v75
v_and_b32 v121, 0xffff0000, v75
v_add_f32 v102, v102, v120
v_add_f32 v103, v103, v121
v_lshlrev_b32 v120, 16, v76
v_and_b32 v121, 0xffff0000, v76
v_add_f32 v104, v104, v120
v_add_f32 v105, v105, v121
v_lshlrev_b32 v120, 16, v77
v_and_b32 v121, 0xffff0000, v77
v_add_f32 v106, v106, v120
v_add_f32 v107, v107, v121
v_lshlrev_b32 v120, 16, v78
v_and_b32 v121, 0xffff0000, v78
v_add_f32 v108, v108, v120
v_add_f32 v109, v109, v121
v_lshlrev_b32 v120, 16, v79
v_and_b32 v121, 0xffff0000, v79
v_add_f32 v110, v110, v120
v_add_f32 v111, v111, v121
v_lshlrev_b32 v120, 16, v80
v_and_b32 v121, 0xffff0000, v80
v_add_f32 v112, v112, v120
v_add_f32 v113, v113, v121
v_lshlrev_b32 v120, 16, v81
v_and_b32 v121, 0xffff0000, v81
v_add_f32 v114, v114, v120
v_add_f32 v115, v115, v121
v_lshlrev_b32 v120, 16, v82
v_and_b32 v121, 0xffff0000, v82
v_add_f32 v116, v116, v120
v_add_f32 v117, v117, v121
v_lshlrev_b32 v120, 16, v83
v_and_b32 v121, 0xffff0000, v83
v_add_f32 v118, v118, v120
v_add_f32 v119, v119, v121
s_waitcnt vmcnt(0)
v_lshlrev_b32 v120, 16, v84
v_and_b32 v121, 0xffff0000, v84
v_add_f32 v100, v100, v120
v_add_f32 v101, v101, v121
v_lshlrev_b32 v120, 16, v85
v_and_b32 v121, 0xffff0000, v85
v_add_f32 v102, v102, v120
v_add_f32 v103, v103, v121
v_lshlrev_b32 v120, 16, v86
v_and_b32 v121, 0xffff0000, v86
v_add_f32 v104, v104, v120
v_add_f32 v105, v105, v121
v_lshlrev_b32 v120, 16, v87
v_and_b32 v121, 0xffff0000, v87
v_add_f32 v106, v106, v120
v_add_f32 v107, v107, v121
v_lshlrev_b32 v120, 16, v88
v_and_b32 v121, 0xffff0000, v88
v_add_f32 v108, v108, v120
v_add_f32 v109, v109, v121
v_lshlrev_b32 v120, 16, v89
v_and_b32 v121, 0xffff0000, v89
v_add_f32 v110, v110, v120
v_add_f32 v111, v111, v121
v_lshlrev_b32 v120, 16, v90
v_and_b32 v121, 0xffff0000, v90
v_add_f32 v112, v112, v120
v_add_f32 v113, v113, v121
v_lshlrev_b32 v120, 16, v91
v_and_b32 v121, 0xffff0000, v91
v_add_f32 v114, v114, v120
v_add_f32 v115, v115, v121
v_lshlrev_b32 v120, 16, v92
v_and_b32 v121, 0xffff0000, v92
v_add_f32 v116, v116, v120
v_add_f32 v117, v117, v121
v_lshlrev_b32 v120, 16, v93
v_and_b32 v121, 0xffff0000, v93
v_add_f32 v118, v118, v120
v_add_f32 v119, v119, v121
v_cvt_pk_bf16_f32 v94, v100, v101
v_cvt_pk_bf16_f32 v95, v102, v103
buffer_store_dwordx2 v[94:95], v60, s[52:55], s90 offen offset:0
v_cvt_pk_bf16_f32 v96, v104, v105
v_cvt_pk_bf16_f32 v97, v106, v107
buffer_store_dwordx2 v[96:97], v60, s[52:55], s90 offen offset:32
v_cvt_pk_bf16_f32 v94, v108, v109
v_cvt_pk_bf16_f32 v95, v110, v111
buffer_store_dwordx2 v[94:95], v60, s[52:55], s90 offen offset:64
v_cvt_pk_bf16_f32 v96, v112, v113
v_cvt_pk_bf16_f32 v97, v114, v115
buffer_store_dwordx2 v[96:97], v60, s[52:55], s90 offen offset:96
v_cvt_pk_bf16_f32 v94, v116, v117
v_cvt_pk_bf16_f32 v95, v118, v119
buffer_store_dwordx2 v[94:95], v60, s[52:55], s90 offen offset:128
s_branch L_kend%=
L_k3%=:
L_kend%=:
s_cmp_lg_u32 s43, 0
s_cbranch_scc1 L_fin%=
s_mov_b32 s44, 67919872
s_cmp_lg_u32 s38, 0
s_cbranch_scc1 L_pb18%=
v_mov_b32 v124, s42
v_mov_b32 v125, 0
buffer_store_dword v124, v125, s[48:51], s44 offen sc1
L_pb18%=:
L_fin%=:
s_mov_b32 %[again], 0
s_branch L_out%=
L_leave%=:
s_waitcnt vmcnt(0) lgkmcnt(0)
s_mov_b32 %[again], 1
s_branch L_out%=
L_resume%=:
s_mov_b64 s[36:37], %2
s_mov_b32 s38, %3
s_mov_b32 s43, %4
s_and_b32 s44, s43, 7
s_lshl_b32 s44, s44, 2
s_bfe_u32 s45, s43, 0x20003
s_or_b32 s40, s44, s45
s_lshr_b32 s41, s43, 5
s_load_dwordx2 s[48:49], s[36:37], 88
v_mbcnt_lo_u32_b32 v8, -1, 0
v_mbcnt_hi_u32_b32 v8, -1, v8
s_mov_b32 s42, %[tagio]
s_waitcnt lgkmcnt(0)
s_and_b32 s49, s49, 0xffff
s_mov_b32 s51, 0x20000
s_mov_b32 s50, 67920128
s_load_dwordx2 s[52:53], s[36:37], 80
s_load_dwordx2 s[56:57], s[36:37], 0
s_load_dwordx2 s[60:61], s[36:37], 72
s_load_dwordx2 s[64:65], s[36:37], 64
s_load_dwordx2 s[68:69], s[36:37], 40
v_lshlrev_b32 v32, 4, v8
s_and_b32 s44, s43, 7
s_and_b32 s100, s44, 3
s_lshr_b32 s44, s44, 2
s_lshl_b32 s44, s44, 3
s_bfe_u32 s45, s43, 0x30003
s_add_u32 s101, s44, s45
s_lshr_b32 s39, s43, 6
s_mul_i32 s44, s100, 448
s_lshl_b32 s45, s38, 4
s_add_u32 s44, s44, s45
s_mul_i32 s86, s44, 1280
s_mul_i32 s44, s100, 14
s_add_u32 s44, s44, 128
s_lshl_b32 s44, s44, 13
s_lshl_b32 s45, s38, 11
s_add_u32 s84, s44, s45
s_mul_i32 s79, s38, 5120
v_add_u32 v9, 0, v8
v_add_u32 v10, 96, v9
v_lshrrev_b32 v10, 8, v10
v_mul_u32_u24 v11, 0xa0, v10
v_sub_u32 v11, v9, v11
v_bfe_u32 v12, v11, 1, 3
v_lshlrev_b32 v38, 2, v10
v_xor_b32 v12, v12, v38
v_lshl_add_u32 v12, v10, 3, v12
v_mul_u32_u24 v12, 0x500, v12
v_lshrrev_b32 v11, 4, v11
v_lshl_add_u32 v12, v11, 5, v12
v_and_b32 v9, 1, v9
v_lshl_add_u32 v12, v9, 4, v12
v_add_u32 v33, 4096, v12
v_add_u32 v9, 64, v8
v_add_u32 v10, 96, v9
v_lshrrev_b32 v10, 8, v10
v_mul_u32_u24 v11, 0xa0, v10
v_sub_u32 v11, v9, v11
v_bfe_u32 v12, v11, 1, 3
v_lshlrev_b32 v38, 2, v10
v_xor_b32 v12, v12, v38
v_lshl_add_u32 v12, v10, 3, v12
v_mul_u32_u24 v12, 0x500, v12
v_lshrrev_b32 v11, 4, v11
v_lshl_add_u32 v12, v11, 5, v12
v_and_b32 v9, 1, v9
v_lshl_add_u32 v12, v9, 4, v12
v_add_u32 v34, 3072, v12
v_add_u32 v9, 128, v8
v_add_u32 v10, 96, v9
v_lshrrev_b32 v10, 8, v10
v_mul_u32_u24 v11, 0xa0, v10
v_sub_u32 v11, v9, v11
v_bfe_u32 v12, v11, 1, 3
v_lshlrev_b32 v38, 2, v10
v_xor_b32 v12, v12, v38
v_lshl_add_u32 v12, v10, 3, v12
v_mul_u32_u24 v12, 0x500, v12
v_lshrrev_b32 v11, 4, v11
v_lshl_add_u32 v12, v11, 5, v12
v_and_b32 v9, 1, v9
v_lshl_add_u32 v12, v9, 4, v12
v_add_u32 v35, 2048, v12
v_add_u32 v9, 192, v8
v_add_u32 v10, 96, v9
v_lshrrev_b32 v10, 8, v10
v_mul_u32_u24 v11, 0xa0, v10
v_sub_u32 v11, v9, v11
v_bfe_u32 v12, v11, 1, 3
v_lshlrev_b32 v38, 2, v10
v_xor_b32 v12, v12, v38
v_lshl_add_u32 v12, v10, 3, v12
v_mul_u32_u24 v12, 0x500, v12
v_lshrrev_b32 v11, 4, v11
v_lshl_add_u32 v12, v11, 5, v12
v_and_b32 v9, 1, v9
v_lshl_add_u32 v12, v9, 4, v12
v_add_u32 v36, 1024, v12
v_add_u32 v9, 256, v8
v_add_u32 v10, 96, v9
v_lshrrev_b32 v10, 8, v10
v_mul_u32_u24 v11, 0xa0, v10
v_sub_u32 v11, v9, v11
v_bfe_u32 v12, v11, 1, 3
v_lshlrev_b32 v38, 2, v10
v_xor_b32 v12, v12, v38
v_lshl_add_u32 v12, v10, 3, v12
v_mul_u32_u24 v12, 0x500, v12
v_lshrrev_b32 v11, 4, v11
v_lshl_add_u32 v12, v11, 5, v12
v_and_b32 v9, 1, v9
v_lshl_add_u32 v12, v9, 4, v12
v_add_u32 v37, 4096, v12
v_lshrrev_b32 v10, 4, v8
v_bfe_u32 v9, v8, 2, 2
v_and_b32 v11, 1, v10
v_lshl_add_u32 v9, v11, 2, v9
v_lshrrev_b32 v11, 1, v10
v_lshlrev_b32 v12, 2, v11
v_xor_b32 v9, v9, v12
v_lshlrev_b32 v9, 5, v9
v_mul_u32_u24 v11, 0xa00, v11
v_add_u32 v9, v9, v11
v_and_b32 v11, 3, v8
v_lshl_add_u32 v38, v11, 3, v9
s_waitcnt lgkmcnt(0)
s_mul_i32 s44, s101, 1490944
s_add_u32 s44, s44, 36192256
s_add_u32 s72, s48, s44
s_addc_u32 s73, s49, 0
s_mov_b32 s74, 1490944
s_mov_b32 s75, 0x20000
s_mul_i32 s44, s39, 320
s_sub_u32 s45, s44, 4096
s_ashr_i32 s46, s45, 31
s_add_u32 s64, s64, s45
s_addc_u32 s65, s65, s46
s_and_b32 s65, s65, 0xffff
s_mov_b32 s67, 0x20000
s_sub_u32 s66, 2215936, s44
s_ashr_i32 s46, s45, 31
s_add_u32 s68, s68, s45
s_addc_u32 s69, s69, s46
s_and_b32 s69, s69, 0xffff
s_mov_b32 s71, 0x20000
s_sub_u32 s70, 5246976, s44
s_and_b32 s53, s53, 0xffff
s_mov_b32 s54, 2621440
s_mov_b32 s55, 0x20000
s_and_b32 s57, s57, 0xffff
s_mov_b32 s58, 2621440
s_mov_b32 s59, 0x20000
s_and_b32 s61, s61, 0xffff
s_mov_b32 s62, 1280
s_mov_b32 s63, 0x20000
s_add_u32 s86, s86, 409600
s_add_u32 m0, s79, 102400
s_nop 0
buffer_load_dwordx4 v33, s[64:67], s86 offen lds
buffer_load_dwordx4 v34, s[64:67], s86 offen offset:1024 lds
buffer_load_dwordx4 v35, s[64:67], s86 offen offset:2048 lds
buffer_load_dwordx4 v36, s[64:67], s86 offen offset:3072 lds
s_add_u32 m0, s79, 106496
s_nop 0
buffer_load_dwordx4 v37, s[64:67], s86 offen lds
s_add_u32 s86, s86, 81920
s_add_u32 m0, s79, 122880
s_nop 0
buffer_load_dwordx4 v33, s[64:67], s86 offen lds
buffer_load_dwordx4 v34, s[64:67], s86 offen offset:1024 lds
buffer_load_dwordx4 v35, s[64:67], s86 offen offset:2048 lds
buffer_load_dwordx4 v36, s[64:67], s86 offen offset:3072 lds
s_add_u32 m0, s79, 126976
s_nop 0
buffer_load_dwordx4 v37, s[64:67], s86 offen lds
s_add_u32 s86, s86, 81920
s_add_u32 s85, s84, 8192
buffer_load_dwordx4 v[192:195], v32, s[72:75], s84 offen sc1
buffer_load_dwordx4 v[196:199], v32, s[72:75], s84 offen offset:1024 sc1
buffer_load_dwordx4 v[200:203], v32, s[72:75], s85 offen sc1
buffer_load_dwordx4 v[204:207], v32, s[72:75], s85 offen offset:1024 sc1
s_add_u32 s84, s84, 16384
s_add_u32 s85, s84, 8192
buffer_load_dwordx4 v[208:211], v32, s[72:75], s84 offen sc1
buffer_load_dwordx4 v[212:215], v32, s[72:75], s84 offen offset:1024 sc1
buffer_load_dwordx4 v[216:219], v32, s[72:75], s85 offen sc1
buffer_load_dwordx4 v[220:223], v32, s[72:75], s85 offen offset:1024 sc1
s_add_u32 s84, s84, 16384
s_add_u32 s85, s84, 8192
buffer_load_dwordx4 v[224:227], v32, s[72:75], s84 offen sc1
buffer_load_dwordx4 v[228:231], v32, s[72:75], s84 offen offset:1024 sc1
buffer_load_dwordx4 v[232:235], v32, s[72:75], s85 offen sc1
buffer_load_dwordx4 v[236:239], v32, s[72:75], s85 offen offset:1024 sc1
s_add_u32 s84, s84, 16384
s_add_u32 s85, s84, 8192
buffer_load_dwordx4 v[240:243], v32, s[72:75], s84 offen sc1
buffer_load_dwordx4 v[244:247], v32, s[72:75], s84 offen offset:1024 sc1
buffer_load_dwordx4 v[248:251], v32, s[72:75], s85 offen sc1
buffer_load_dwordx4 v[252:255], v32, s[72:75], s85 offen offset:1024 sc1
s_add_u32 s84, s84, 16384
s_add_u32 s85, s84, 8192
buffer_load_dwordx4 v[144:147], v32, s[72:75], s84 offen sc1
buffer_load_dwordx4 v[148:151], v32, s[72:75], s84 offen offset:1024 sc1
buffer_load_dwordx4 v[152:155], v32, s[72:75], s85 offen sc1
buffer_load_dwordx4 v[156:159], v32, s[72:75], s85 offen offset:1024 sc1
s_add_u32 s84, s84, 16384
s_waitcnt vmcnt(0)
s_branch L_p4go%=
L_out%=:
s_mov_b32 %[tagio], s42
fz_code_end:
)ASM" : [again]"=&s"(again),[tagio]"+s"(tag) : "s"(ka),"s"(wid),"s"(bid),[mode]"s"(mode) : "v8", "v9", "v10", "v11", "v12", "v13", "v14", "v15", "v16", "v17", "v18", "v19", "v20", "v21", "v22", "v23", "v24", "v25", "v26", "v27", "v28", "v29", "v30", "v31", "v32", "v33", "v34", "v35", "v36", "v37", "v38", "v39", "v40", "v41", "v42", "v43", "v44", "v45", "v46", "v47", "v48", "v49", "v50", "v51", "v52", "v53", "v54", "v55", "v56", "v57", "v58", "v59", "v60", "v61", "v62", "v63", "v64", "v65", "v66", "v67", "v68", "v69", "v70", "v71", "v72", "v73", "v74", "v75", "v76", "v77", "v78", "v79", "v80", "v81", "v82", "v83", "v84", "v85", "v86", "v87", "v88", "v89", "v90", "v91", "v92", "v93", "v94", "v95", "v96", "v97", "v98", "v99", "v100", "v101", "v102", "v103", "v104", "v105", "v106", "v107", "v108", "v109", "v110", "v111", "v112", "v113", "v114", "v115", "v116", "v117", "v118", "v119", "v120", "v121", "v122", "v123", "v124", "v125", "v126", "v127", "v128", "v129", "v130", "v131", "v132", "v133", "v134", "v135", "v136", "v137", "v138", "v139", "v140", "v141", "v142", "v143", "v144", "v145", "v146", "v147", "v148", "v149", "v150", "v151", "v152", "v153", "v154", "v155", "v156", "v157", "v158", "v159", "v160", "v161", "v162", "v163", "v164", "v165", "v166", "v167", "v168", "v169", "v170", "v171", "v172", "v173", "v174", "v175", "v176", "v177", "v178", "v179", "v180", "v181", "v182", "v183", "v184", "v185", "v186", "v187", "v188", "v189", "v190", "v191", "v192", "v193", "v194", "v195", "v196", "v197", "v198", "v199", "v200", "v201", "v202", "v203", "v204", "v205", "v206", "v207", "v208", "v209", "v210", "v211", "v212", "v213", "v214", "v215", "v216", "v217", "v218", "v219", "v220", "v221", "v222", "v223", "v224", "v225", "v226", "v227", "v228", "v229", "v230", "v231", "v232", "v233", "v234", "v235", "v236", "v237", "v238", "v239", "v240", "v241", "v242", "v243", "v244", "v245", "v246", "v247", "v248", "v249", "v250", "v251", "v252", "v253", "v254", "v255", "a0", "a1", "a2", "a3", "a4", "a5", "a6", "a7", "a8", "a9", "a10", "a11", "a12", "a13", "a14", "a15", "a16", "a17", "a18", "a19", "a20", "a21", "a22", "a23", "a24", "a25", "a26", "a27", "a28", "a29", "a30", "a31", "a32", "a33", "a34", "a35", "a36", "a37", "a38", "a39", "a40", "a41", "a42", "a43", "a44", "a45", "a46", "a47", "a48", "a49", "a50", "a51", "a52", "a53", "a54", "a55", "a56", "a57", "a58", "a59", "a60", "a61", "a62", "a63", "a64", "a65", "a66", "a67", "a68", "a69", "a70", "a71", "a72", "a73", "a74", "a75", "a76", "a77", "a78", "a79", "a80", "a81", "a82", "a83", "a84", "a85", "a86", "a87", "a88", "a89", "a90", "a91", "a92", "a93", "a94", "a95", "a96", "a97", "a98", "a99", "a100", "a101", "a102", "a103", "a104", "a105", "a106", "a107", "a108", "a109", "a110", "a111", "a112", "a113", "a114", "a115", "a116", "a117", "a118", "a119", "a120", "a121", "a122", "a123", "a124", "a125", "a126", "a127", "a128", "a129", "a130", "a131", "a132", "a133", "a134", "a135", "a136", "a137", "a138", "a139", "a140", "a141", "a142", "a143", "a144", "a145", "a146", "a147", "a148", "a149", "a150", "a151", "a152", "a153", "a154", "a155", "a156", "a157", "a158", "a159", "a160", "a161", "a162", "a163", "a164", "a165", "a166", "a167", "a168", "a169", "a170", "a171", "a172", "a173", "a174", "a175", "a176", "a177", "a178", "a179", "a180", "a181", "a182", "a183", "a184", "a185", "a186", "a187", "a188", "a189", "a190", "a191", "a192", "a193", "a194", "a195", "a196", "a197", "a198", "a199", "a200", "a201", "a202", "a203", "a204", "a205", "a206", "a207", "a208", "a209", "a210", "a211", "a212", "a213", "a214", "a215", "a216", "a217", "a218", "a219", "a220", "a221", "a222", "a223", "a224", "a225", "a226", "a227", "a228", "a229", "a230", "a231", "a232", "a233", "a234", "a235", "a236", "a237", "a238", "a239", "a240", "a241", "a242", "a243", "a244", "a245", "a246", "a247", "a248", "a249", "a250", "a251", "a252", "a253", "a254", "a255", "s36", "s37", "s38", "s39", "s40", "s41", "s42", "s43", "s44", "s45", "s46", "s47", "s48", "s49", "s50", "s51", "s52", "s53", "s54", "s55", "s56", "s57", "s58", "s59", "s60", "s61", "s62", "s63", "s64", "s65", "s66", "s67", "s68", "s69", "s70", "s71", "s72", "s73", "s74", "s75", "s76", "s77", "s78", "s79", "s80", "s81", "s82", "s83", "s84", "s85", "s86", "s87", "s88", "s89", "s90", "s91", "s92", "s93", "s94", "s95", "s96", "s97", "s98", "s99", "s100", "s101", "vcc", "scc", "memory");
  if(__builtin_expect(again==0,1)) break;
  slow_attention(ka,bid,wid);
  mode=1;

  } while(true);

}
extern "C" void kernel_launch(void* const* d_in, const int* in_sizes, int n_in, void* d_out, int out_size, void* d_ws, size_t ws_size, hipStream_t stream) {
  Args a; a.x=d_in[0]; a.ckv=d_in[1]; a.wq=d_in[2]; a.wuk=d_in[3]; a.wuv=d_in[4]; a.wo=d_in[5]; a.w1=d_in[7]; a.b1=d_in[8]; a.w2=d_in[9]; a.b2=d_in[10]; a.out=d_out; a.ws=(char*)d_ws+0u;
  (void)hipFuncSetAttribute((const void*)fused,hipFuncAttributeMaxDynamicSharedMemorySize,163840);
  fused<<<256,256,163840,stream>>>(a);
}
```
